```python
import torch

def _softmax(x, axis=-1):
    return torch.softmax(x, dim=axis)

def _gelu(x):
    return torch.nn.functional.gelu(x, approximate='tanh')

def _silu(x):
    return torch.nn.functional.silu(x)

def _sigmoid(x):
    return torch.sigmoid(x)

def _layernorm(x, axis=-1, eps=1e-05):
    m = x.mean(dim=axis, keepdim=True)
    v = x.var(dim=axis, keepdim=True, unbiased=False)
    return (x - m) / torch.sqrt(v + eps)

def _scatter_add(tgt, idx, src, axis=0):
    return tgt.clone().index_add_(axis, idx, src)

def _segment_sum(src, seg, n):
    out = torch.zeros((n,) + src.shape[1:], dtype=src.dtype)
    return out.index_add_(0, seg, src)

def _pad(x, pw):
    flat = tuple((v for pair in reversed(pw) for v in pair))
    return torch.nn.functional.pad(x, flat)
kernel_name = 'parallel_sum__compressed_kv_cla__mixer_gmlp__prompt'

def setup_inputs():

    _w = lambda a: (lambda t: t.to(torch.bfloat16) if t.is_floating_point() else t)(torch.as_tensor(a))

    def _A_setup():
        B, H, Sq, Sk, D, M = (1, 32, 2048, 2048, 128, 640)
        Dc = 4 * D
        return {'x': _w((lambda *s: torch.randn(*s, dtype=torch.bfloat16) * 0.05)(B, Sq, M)), 'c_kv': _w((lambda *s: torch.randn(*s, dtype=torch.bfloat16) * 1.6)(B, Sk, Dc)), 'w_q': _w((lambda *s: torch.randn(*s, dtype=torch.bfloat16) * 1.6)(M, H, D) * M ** (-0.5)), 'w_uk': _w((lambda *s: torch.randn(*s, dtype=torch.bfloat16) * 1.6)(Dc, H, D) * Dc ** (-0.5)), 'w_uv': _w((lambda *s: torch.randn(*s, dtype=torch.bfloat16) * 1.6)(Dc, H, D) * Dc ** (-0.5)), 'w_o': _w((lambda *s: torch.randn(*s, dtype=torch.bfloat16) * 1.6)(H, D, M) * (H * D) ** (-0.5))}
    _A_in = _A_setup()

    def _B_setup():
        B, S, D, F = (1, 2048, 640, 1728)
        Fh = F // 2
        return {'x': _w((lambda *s: torch.randn(*s, dtype=torch.bfloat16) * 0.05)(B, S, D)), 'w_in': _w((lambda *s: torch.randn(*s, dtype=torch.bfloat16) * 0.4)(D, 2 * Fh)), 'w_sp': _w(0.01 * (lambda *s: torch.randn(*s, dtype=torch.bfloat16) * 0.4)(S, S)), 'b_sp': _w(1.0 + 0.01 * (lambda *s: torch.randn(*s, dtype=torch.bfloat16) * 0.05)(S)), 'w_out': _w((lambda *s: torch.randn(*s, dtype=torch.bfloat16) * 0.05)(Fh, D))}
    _B_in = _B_setup()
    return {'A_x': _A_in['x'], 'A_c_kv': _A_in['c_kv'], 'A_w_q': _A_in['w_q'], 'A_w_uk': _A_in['w_uk'], 'A_w_uv': _A_in['w_uv'], 'A_w_o': _A_in['w_o'], 'B_x': _B_in['x'], 'B_w_in': _B_in['w_in'], 'B_w_sp': _B_in['w_sp'], 'B_b_sp': _B_in['b_sp'], 'B_w_out': _B_in['w_out']}

def reference(A_x, A_c_kv, A_w_q, A_w_uk, A_w_uv, A_w_o, B_x, B_w_in, B_w_sp, B_b_sp, B_w_out):

    def _A_ref(x, c_kv, w_q, w_uk, w_uv, w_o):
        B, Sq, M = (x.shape[0], x.shape[1], x.shape[2])
        Sk = c_kv.shape[1]
        H, D = (w_q.shape[1], w_q.shape[2])
        q = torch.einsum('bqm,mhd->bhqd', x, w_q)
        k = torch.einsum('bkc,chd->bhkd', c_kv, w_uk)
        v = torch.einsum('bkc,chd->bhkd', c_kv, w_uv)
        scores = torch.einsum('bhqd,bhkd->bhqk', q, k) * D ** (-0.5)
        off = Sk - Sq
        qi = torch.arange(Sq)[:, None]
        kj = torch.arange(Sk)[None, :]
        cmask = kj <= qi + off
        scores = torch.where(cmask[None, None], scores, -1000000000.0)
        p = _softmax(scores, axis=-1)
        ctx = torch.einsum('bhqk,bhkd->bhqd', p, v)
        return torch.einsum('bhqd,hdm->bqm', ctx, w_o)

    def _B_ref(x, w_in, w_sp, b_sp, w_out):
        B, S, D = x.shape
        F = w_in.shape[-1]
        Fh = F // 2
        z = _gelu(torch.einsum('bsd,df->bsf', x, w_in))
        z1 = z[..., :Fh].clone()
        z2 = _layernorm(z[..., Fh:], axis=-1)
        g = torch.einsum('ts,bsf->btf', w_sp, z2) + b_sp[None, :, None]
        return torch.einsum('bsf,fd->bsd', z1 * g, w_out)
    x = A_x
    xn = _layernorm(x, axis=-1)
    return x + _A_ref(xn, A_c_kv, A_w_q, A_w_uk, A_w_uv, A_w_o) + _B_ref(xn, B_w_in, B_w_sp, B_b_sp, B_w_out)
```

```cpp
#include <hip/hip_runtime.h>
#include <hip/hip_bf16.h>
#include <stdint.h>

typedef unsigned short u16;
typedef short bf16x8 __attribute__((ext_vector_type(8)));
typedef short s16x4 __attribute__((ext_vector_type(4)));
typedef float f32x2 __attribute__((ext_vector_type(2)));
typedef float f32x4 __attribute__((ext_vector_type(4)));
typedef float f32x16 __attribute__((ext_vector_type(16)));
typedef unsigned u32x2 __attribute__((ext_vector_type(2)));
typedef unsigned u32x4 __attribute__((ext_vector_type(4)));
typedef __bf16 bf16x2_t __attribute__((ext_vector_type(2)));
#define LAS __attribute__((address_space(3)))
#define DI __device__ __forceinline__
typedef __amdgpu_buffer_rsrc_t srd_t;

constexpr int S = 2048, DM = 640, H = 32, HD = 128, DC = 512, FF = 1728, FH = 864, FP = 896;
constexpr int CY = H * HD + FP;

constexpr size_t WS_XN = 0;
constexpr size_t WS_K = WS_XN + (size_t)S * DM * 2;
constexpr size_t WS_V = WS_K + (size_t)H * S * HD * 2;
constexpr size_t WS_Q = WS_V + (size_t)H * S * HD * 2;
constexpr size_t WS_Z1 = WS_Q + (size_t)H * S * HD * 2;
constexpr size_t WS_Z2 = WS_Z1 + (size_t)S * FP * 2;
constexpr size_t WS_Z2N = WS_Z2 + (size_t)S * FP * 2;
constexpr size_t WS_CY = WS_Z2N + (size_t)S * FP * 2;
constexpr size_t WS_PART = WS_CY + (size_t)S * CY * 2;
constexpr size_t WS_END = WS_PART + (size_t)3 * S * DM * 4;

DI unsigned cvtpk(float lo, float hi) { f32x2 v = {lo, hi}; bf16x2_t b = __builtin_convertvector(v, bf16x2_t); return __builtin_bit_cast(unsigned, b); }
DI float bf_lo(unsigned w) { return __uint_as_float(w << 16); }
DI float bf_hi(unsigned w) { return __uint_as_float(w & 0xffff0000u); }
DI int rfl(int x) { return __builtin_amdgcn_readfirstlane(x); }
DI srd_t mk_srd(const void* p, unsigned bytes) { return __builtin_amdgcn_make_buffer_rsrc((void*)p, (short)0, (int)bytes, 0x00020000); }
DI void dma16(srd_t srd, unsigned ldsaddr, unsigned voff, unsigned soff) {
    asm volatile("s_mov_b32 m0, %0\n\ts_nop 0\n\tbuffer_load_dwordx4 %1, %2, %3 offen lds" :: "s"(ldsaddr), "v"(voff), "s"(srd), "s"(soff) : "memory");
}
template <int N> DI void wait_vm() { asm volatile("s_waitcnt vmcnt(%0)" :: "i"(N) : "memory"); }
DI void bar() { asm volatile("s_waitcnt lgkmcnt(0)\n\ts_barrier" ::: "memory"); }
DI bf16x8 lds_rd128(unsigned a) { return *(const LAS bf16x8*)(uintptr_t)a; }
DI s16x4 lds_tr(unsigned a) { return __builtin_amdgcn_ds_read_tr16_b64_v4i16((LAS s16x4*)(uintptr_t)a); }
DI bf16x8 cat4(s16x4 a, s16x4 b) { return __builtin_shufflevector(a, b, 0, 1, 2, 3, 4, 5, 6, 7); }

__global__ __launch_bounds__(256) void k_ln(const u16* __restrict__ x, u16* __restrict__ xn) {
    const int row = blockIdx.x * 4 + (threadIdx.x >> 6), lane = threadIdx.x & 63;
    const unsigned* p = (const unsigned*)(x + (size_t)row * DM);
    unsigned w[5]; float s = 0.f;
#pragma unroll
    for (int i = 0; i < 5; ++i) { w[i] = p[lane + 64 * i]; s += bf_lo(w[i]) + bf_hi(w[i]); }
#pragma unroll
    for (int o = 32; o >= 1; o >>= 1) s += __shfl_xor(s, o);
    const float mean = s * (1.f / DM); float q = 0.f;
#pragma unroll
    for (int i = 0; i < 5; ++i) { const float a = bf_lo(w[i]) - mean, b = bf_hi(w[i]) - mean; q += a * a + b * b; }
#pragma unroll
    for (int o = 32; o >= 1; o >>= 1) q += __shfl_xor(q, o);
    const float rstd = 1.f / sqrtf(q * (1.f / DM) + 1e-5f);
    unsigned* o = (unsigned*)(xn + (size_t)row * DM);
#pragma unroll
    for (int i = 0; i < 5; ++i) o[lane + 64 * i] = cvtpk((bf_lo(w[i]) - mean) * rstd, (bf_hi(w[i]) - mean) * rstd);
}
__global__ __launch_bounds__(256) void k_ln2(const u16* __restrict__ z2, u16* __restrict__ z2n) {
    const int row = blockIdx.x * 4 + (threadIdx.x >> 6), lane = threadIdx.x & 63;
    const unsigned* p = (const unsigned*)(z2 + (size_t)row * FP);
    unsigned w[7]; float s = 0.f;
#pragma unroll
    for (int i = 0; i < 7; ++i) { const int c = lane + 64 * i; w[i] = c < FH / 2 ? p[c] : 0u; s += bf_lo(w[i]) + bf_hi(w[i]); }
#pragma unroll
    for (int o = 32; o >= 1; o >>= 1) s += __shfl_xor(s, o);
    const float mean = s * (1.f / FH); float q = 0.f;
#pragma unroll
    for (int i = 0; i < 7; ++i) { const int c = lane + 64 * i; const float a = bf_lo(w[i]) - mean, b = bf_hi(w[i]) - mean; if (c < FH / 2) q += a * a + b * b; }
#pragma unroll
    for (int o = 32; o >= 1; o >>= 1) q += __shfl_xor(q, o);
    const float rstd = 1.f / sqrtf(q * (1.f / FH) + 1e-5f);
    unsigned* o = (unsigned*)(z2n + (size_t)row * FP);
#pragma unroll
    for (int i = 0; i < 7; ++i) { const int c = lane + 64 * i; o[c] = c < FH / 2 ? cvtpk((bf_lo(w[i]) - mean) * rstd, (bf_hi(w[i]) - mean) * rstd) : 0u; }
}

template <int BM, int NB, int WM, int NST> struct Gemm {
    static constexpr int WN = 8 / WM, BN = NB * 128, MT = BM / WM / 16, NT = BN / WN / 16;
    static constexpr int ABYTES = BM * 128, STAGE = ABYTES + NB * 16384, PA = BM / 64, PB = 2 * NB, P = PA + PB, LDS = STAGE * NST;
    static_assert(NT >= 1 && (128 / 16) % NT == 0 || NT % 8 == 0, "wave n-range must stay inside one 128-col subtile or cover whole ones");
};
struct BSrc { srd_t srd; unsigned soff; };

template <class G, int NB>
DI void gemm_run(f32x4 (&acc)[G::MT][G::NT], unsigned lds0, srd_t srdA, unsigned soffA, unsigned lda2, const BSrc (&bs)[NB], unsigned ldb2, int nk) {
    constexpr int PA = G::PA, PB = G::PB, P = G::P, MT = G::MT, NT = G::NT, WN = G::WN;
    constexpr int NST = G::LDS / G::STAGE;
    const int tid = threadIdx.x, lane = tid & 63, wid = rfl(tid >> 6), wm = wid / WN, wn = wid % WN;
    unsigned voffA[PA], voffB[2];
#pragma unroll
    for (int p = 0; p < PA; ++p) { const int row = (wid * PA + p) * 8 + (lane >> 3); const int ch = (lane & 7) ^ ((row >> 1) & 7); voffA[p] = row * lda2 + ch * 16; }
#pragma unroll
    for (int p = 0; p < 2; ++p) { const int k = (wid * 2 + p) * 4 + (lane >> 4); const int sg = (k & 3) | (((k >> 3) & 1) << 2); const int ch = (lane & 15) ^ (2 * sg); voffB[p] = k * ldb2 + ch * 16; }
    auto issue = [&](int kt) {
        const unsigned st = lds0 + (unsigned)(kt % NST) * G::STAGE;
#pragma unroll
        for (int p = 0; p < PA; ++p) dma16(srdA, st + (wid * PA + p) * 1024, voffA[p], soffA + kt * 128);
#pragma unroll
        for (int j = 0; j < NB; ++j)
#pragma unroll
            for (int p = 0; p < 2; ++p) dma16(bs[j].srd, st + G::ABYTES + j * 16384 + (wid * 2 + p) * 1024, voffB[p], bs[j].soff + kt * 64 * ldb2);
    };
    const int fr = lane & 15, fg = lane >> 4;
    unsigned aoff[2];
#pragma unroll
    for (int ks = 0; ks < 2; ++ks) aoff[ks] = (wm * MT * 16 + fr) * 128 + (((4 * ks + fg) ^ (fr >> 1)) << 4);
    const int q = (lane & 15) >> 2, p4 = lane & 3, sg = q | ((fg & 1) << 2);
    unsigned boff[NT];
#pragma unroll
    for (int nt = 0; nt < NT; ++nt) { const int gn = wn * NT + nt; boff[nt] = G::ABYTES + (gn >> 3) * 16384 + (8 * fg + q) * 256 + 32 * ((gn & 7) ^ sg) + 8 * p4; }
#pragma unroll
    for (int i = 0; i < NST - 1; ++i) if (i < nk) issue(i);
    for (int kt = 0; kt < nk; ++kt) {
        const int rem = nk - 1 - kt;
        if (rem >= NST - 2) wait_vm<(NST - 2) * P>();
        else if (NST > 3 && rem == 1) wait_vm<P>();
        else if (NST > 4 && rem == 2) wait_vm<2 * P>();
        else wait_vm<0>();
        bar();
        if (kt + NST - 1 < nk) issue(kt + NST - 1);
        const unsigned st = lds0 + (unsigned)(kt % NST) * G::STAGE;
#pragma unroll
        for (int ks = 0; ks < 2; ++ks) {
            bf16x8 af[MT], bfr[NT];
#pragma unroll
            for (int nt = 0; nt < NT; ++nt) bfr[nt] = cat4(lds_tr(st + boff[nt] + ks * 8192), lds_tr(st + boff[nt] + ks * 8192 + 1024));
#pragma unroll
            for (int mt = 0; mt < MT; ++mt) af[mt] = lds_rd128(st + aoff[ks] + mt * 2048);
#pragma unroll
            for (int mt = 0; mt < MT; ++mt)
#pragma unroll
                for (int nt = 0; nt < NT; ++nt) acc[mt][nt] = __builtin_amdgcn_mfma_f32_16x16x32_bf16(bfr[nt], af[mt], acc[mt][nt], 0, 0, 0);
        }
    }
    bar();
}
DI u32x4 pack32(f32x4 a, f32x4 b) {
    unsigned a0 = cvtpk(a[0], a[1]), a1 = cvtpk(a[2], a[3]), b0 = cvtpk(b[0], b[1]), b1 = cvtpk(b[2], b[3]);
    auto r0 = __builtin_amdgcn_permlane32_swap(a0, b0, false, false); auto r1 = __builtin_amdgcn_permlane32_swap(a1, b1, false, false);
    auto s0 = __builtin_amdgcn_permlane16_swap(r0[0], r0[1], false, false); auto s1 = __builtin_amdgcn_permlane16_swap(r1[0], r1[1], false, false);
    return (u32x4){s0[0], s1[0], s0[1], s1[1]};
}
DI float gelu_t(float x) {
    const float u = x * (0.7978845608028654f + 0.035677408136300125f * x * x);
    const float e = __builtin_amdgcn_exp2f(u * -2.885390081777927f);
    return x * __builtin_amdgcn_rcpf(1.f + e);
}

using GKV = Gemm<256, 2, 2, 2>;
__global__ __launch_bounds__(512, 2) void k_kv(const u16* __restrict__ ckv, const u16* __restrict__ wuk, const u16* __restrict__ wuv, u16* __restrict__ Kb, u16* __restrict__ Vb) {
    extern __shared__ __attribute__((aligned(16))) char lds[];
    const int b = blockIdx.x, h = (b & 7) * 4 + ((b >> 3) & 3), j = b >> 5;
    f32x4 acc[GKV::MT][GKV::NT] = {};
    const BSrc bs[2] = {{mk_srd(wuk, DC * H * HD * 2), (unsigned)(h * HD * 2)}, {mk_srd(wuv, DC * H * HD * 2), (unsigned)(h * HD * 2)}};
    gemm_run<GKV, 2>(acc, (unsigned)(uintptr_t)lds, mk_srd(ckv, S * DC * 2), (unsigned)(j * 256 * DC * 2), DC * 2, bs, H * HD * 2, DC / 64);
    const int lane = threadIdx.x & 63, wid = rfl(threadIdx.x >> 6), wm = wid / GKV::WN, wn = wid % GKV::WN;
    u16* dst = (wn < 2 ? Kb : Vb) + ((size_t)h * S + j * 256 + wm * 128 + (lane & 15)) * HD + (wn & 1) * 64 + 8 * (lane >> 4);
#pragma unroll
    for (int mt = 0; mt < GKV::MT; ++mt)
#pragma unroll
        for (int n2 = 0; n2 < GKV::NT / 2; ++n2) *(u32x4*)(dst + mt * 16 * HD + n2 * 32) = pack32(acc[mt][2 * n2], acc[mt][2 * n2 + 1]);
}
using GQ = Gemm<256, 1, 4, 3>;
__global__ __launch_bounds__(512, 2) void k_q(const u16* __restrict__ xn, const u16* __restrict__ wq, u16* __restrict__ Qb) {
    extern __shared__ __attribute__((aligned(16))) char lds[];
    const int b = blockIdx.x, h = (b & 7) * 4 + ((b >> 3) & 3), j = b >> 5;
    f32x4 acc[GQ::MT][GQ::NT] = {};
    const BSrc bs[1] = {{mk_srd(wq, DM * H * HD * 2), (unsigned)(h * HD * 2)}};
    gemm_run<GQ, 1>(acc, (unsigned)(uintptr_t)lds, mk_srd(xn, S * DM * 2), (unsigned)(j * 256 * DM * 2), DM * 2, bs, H * HD * 2, DM / 64);
    const int lane = threadIdx.x & 63, wid = rfl(threadIdx.x >> 6), wm = wid / GQ::WN, wn = wid % GQ::WN;
    const float c = 0.08838834764831845f * 1.4426950408889634f;
    u16* dst = Qb + ((size_t)h * S + j * 256 + wm * 64 + (lane & 15)) * HD + wn * 64 + 8 * (lane >> 4);
#pragma unroll
    for (int mt = 0; mt < GQ::MT; ++mt)
#pragma unroll
        for (int n2 = 0; n2 < GQ::NT / 2; ++n2) *(u32x4*)(dst + mt * 16 * HD + n2 * 32) = pack32(acc[mt][2 * n2] * c, acc[mt][2 * n2 + 1] * c);
}
using GZ = Gemm<128, 1, 2, 4>;
__global__ __launch_bounds__(512, 2) void k_z(const u16* __restrict__ xn, const u16* __restrict__ win, u16* __restrict__ z1, u16* __restrict__ z2) {
    extern __shared__ __attribute__((aligned(16))) char lds[];
    const int b = blockIdx.x, pm = b & 15, pn = b >> 4;
    f32x4 acc[GZ::MT][GZ::NT] = {};
    const BSrc bs[1] = {{mk_srd(win, DM * FF * 2), (unsigned)(pn * 256)}};
    gemm_run<GZ, 1>(acc, (unsigned)(uintptr_t)lds, mk_srd(xn, S * DM * 2), (unsigned)(pm * 128 * DM * 2), DM * 2, bs, FF * 2, DM / 64);
    const int lane = threadIdx.x & 63, wid = rfl(threadIdx.x >> 6), wm = wid / GZ::WN, wn = wid % GZ::WN;
    const int colw = pn * 128 + wn * 32;
    if (colw < FF) {
        const int col = colw + 8 * (lane >> 4);
        u16* dst = (colw < FH ? z1 + col : z2 + (col - FH)) + (size_t)(pm * 128 + wm * 64 + (lane & 15)) * FP;
#pragma unroll
        for (int mt = 0; mt < GZ::MT; ++mt) {
            f32x4 a = acc[mt][0], c = acc[mt][1];
#pragma unroll
            for (int r = 0; r < 4; ++r) { a[r] = gelu_t(a[r]); c[r] = gelu_t(c[r]); }
            *(u32x4*)(dst + (size_t)mt * 16 * FP) = pack32(a, c);
        }
    }
}
using GS = Gemm<64, 1, 2, 4>;
__global__ __launch_bounds__(512, 2) void k_sp(const u16* __restrict__ wsp, const u16* __restrict__ z2n, const u16* __restrict__ bsp, const u16* __restrict__ z1, u16* __restrict__ cy) {
    extern __shared__ __attribute__((aligned(16))) char lds[];
    const int b = blockIdx.x, pm = b & 31, pn = b >> 5;
    f32x4 acc[GS::MT][GS::NT] = {};
    const BSrc bs[1] = {{mk_srd(z2n, S * FP * 2), (unsigned)(pn * 256)}};
    gemm_run<GS, 1>(acc, (unsigned)(uintptr_t)lds, mk_srd(wsp, S * S * 2), (unsigned)(pm * 64 * S * 2), S * 2, bs, FP * 2, S / 64);
    const int lane = threadIdx.x & 63, wid = rfl(threadIdx.x >> 6), wm = wid / GS::WN, wn = wid % GS::WN;
    const int col = pn * 128 + wn * 32 + 8 * (lane >> 4), colr = pn * 128 + wn * 32 + 4 * (lane >> 4);
#pragma unroll
    for (int mt = 0; mt < GS::MT; ++mt) {
        const int row = pm * 64 + wm * 32 + mt * 16 + (lane & 15);
        const float bb = __uint_as_float((unsigned)bsp[row] << 16);
        f32x4 a = acc[mt][0] + bb, c = acc[mt][1] + bb;
        const u32x2 za = *(const u32x2*)(z1 + (size_t)row * FP + colr), zc = *(const u32x2*)(z1 + (size_t)row * FP + colr + 16);
        a[0] *= bf_lo(za[0]); a[1] *= bf_hi(za[0]); a[2] *= bf_lo(za[1]); a[3] *= bf_hi(za[1]);
        c[0] *= bf_lo(zc[0]); c[1] *= bf_hi(zc[0]); c[2] *= bf_lo(zc[1]); c[3] *= bf_hi(zc[1]);
        u32x4 w = pack32(a, c);
        if (col >= FH) w = (u32x4){0u, 0u, 0u, 0u};
        *(u32x4*)(cy + (size_t)row * CY + H * HD + col) = w;
    }
}
using GO = Gemm<128, 1, 2, 4>;
__global__ __launch_bounds__(512, 2) void k_out(const u16* __restrict__ cy, const u16* __restrict__ wo, const u16* __restrict__ wout, float* __restrict__ part) {
    extern __shared__ __attribute__((aligned(16))) char lds[];
    const int b = blockIdx.x, pm = b & 15, r = b >> 4, pn = r % 5, sl = r / 5;
    f32x4 acc[GO::MT][GO::NT] = {};
    const srd_t srdA = mk_srd(cy, S * CY * 2);
    const unsigned a0 = (unsigned)(pm * 128 * CY * 2);
    const unsigned lds0 = (unsigned)(uintptr_t)lds;
    const int k0 = 26 * sl, nko = sl < 2 ? 26 : 12;
    { const BSrc bs[1] = {{mk_srd(wo, H * HD * DM * 2), (unsigned)(k0 * 64 * DM * 2 + pn * 256)}};
      gemm_run<GO, 1>(acc, lds0, srdA, a0 + k0 * 128, CY * 2, bs, DM * 2, nko); }
    if (sl == 2) { const BSrc bs[1] = {{mk_srd(wout, FH * DM * 2), (unsigned)(pn * 256)}};
      gemm_run<GO, 1>(acc, lds0, srdA, a0 + 64 * 128, CY * 2, bs, DM * 2, 14); }
    const int lane = threadIdx.x & 63, wid = rfl(threadIdx.x >> 6), wm = wid / GO::WN, wn = wid % GO::WN;
    float* dst = part + ((size_t)sl * S + pm * 128 + wm * 64 + (lane & 15)) * DM + pn * 128 + wn * 32 + 4 * (lane >> 4);
#pragma unroll
    for (int mt = 0; mt < GO::MT; ++mt)
#pragma unroll
        for (int nt = 0; nt < GO::NT; ++nt) *(f32x4*)(dst + (size_t)mt * 16 * DM + nt * 16) = acc[mt][nt];
}
__global__ __launch_bounds__(256) void k_red(const u16* __restrict__ x, const float* __restrict__ part, u16* __restrict__ out) {
    const int i = blockIdx.x * 256 + threadIdx.x;
    const f32x4 a = *(const f32x4*)(part + (size_t)i * 4), b = *(const f32x4*)(part + (size_t)S * DM + (size_t)i * 4), c = *(const f32x4*)(part + (size_t)2 * S * DM + (size_t)i * 4);
    const u32x2 xw = *(const u32x2*)(x + (size_t)i * 4);
    const f32x4 s = a + b + c;
    u32x2 o; o[0] = cvtpk(s[0] + bf_lo(xw[0]), s[1] + bf_hi(xw[0])); o[1] = cvtpk(s[2] + bf_lo(xw[1]), s[3] + bf_hi(xw[1]));
    *(u32x2*)(out + (size_t)i * 4) = o;
}

#define MFMA32(a, b, c) __builtin_amdgcn_mfma_f32_32x32x16_bf16(a, b, c, 0, 0, 0)
__global__ __launch_bounds__(256, 2) void k_attn(const u16* __restrict__ Qb, const u16* __restrict__ Kb, const u16* __restrict__ Vb, u16* __restrict__ cy) {
    extern __shared__ __attribute__((aligned(16))) char lds[];
    const int b = blockIdx.x, h = (b & 7) * 4 + ((b >> 3) & 3), x = b >> 5;
    const int tid = threadIdx.x, lane = tid & 63, wid = rfl(tid >> 6), l32 = lane & 31, hi = lane >> 5;
    const unsigned lds0 = (unsigned)(uintptr_t)lds;
    const srd_t srdK = mk_srd(Kb + (size_t)h * S * HD, S * HD * 2), srdV = mk_srd(Vb + (size_t)h * S * HD, S * HD * 2);
    unsigned vk[2], vv;
    {   const int row = (lane >> 2) & 7, sub = lane >> 5, slot = lane & 3;
#pragma unroll
        for (int par = 0; par < 2; ++par) { const int rowabs = 8 * par + row;
            vk[par] = row * 256 + sub * 64 + 16 * (slot ^ ((rowabs >> 2) & 3)); }
        vv = row * 256 + sub * 64 + 16 * slot; }
    auto issue = [&](int t) {
        const unsigned st = lds0 + (t & 1) * 32768;
#pragma unroll
        for (int i = 0; i < 4; ++i) { const int p = 4 * wid + i, kb8 = p >> 1, ch = p & 1;
            dma16(srdK, st + p * 1024, vk[kb8 & 1], t * 16384 + kb8 * 2048 + ch * 128);
            dma16(srdV, st + 16384 + p * 1024, vv, t * 16384 + kb8 * 2048 + ch * 128); }
    };
    unsigned kofs[2];
#pragma unroll
    for (int e = 0; e < 2; ++e) kofs[e] = 2048 * (l32 >> 3) + 64 * (l32 & 7) + 16 * ((2 * e + hi) ^ ((l32 >> 2) & 3));
    const unsigned vofs = 16384 + ((lane & 3) << 3) + (((lane >> 2) & 3) << 6) + (((lane >> 4) & 1) << 5) + (hi << 8);
    for (int pass = 0; pass < 2; ++pass) {
        const int qb = pass ? 15 - x : x, nt = 2 * qb + 2, q0 = qb * 128 + wid * 32;
        bf16x8 qf[8];
        const u16* qp = Qb + ((size_t)h * S + q0 + l32) * HD + 8 * hi;
#pragma unroll
        for (int s = 0; s < 8; ++s) qf[s] = *(const bf16x8*)(qp + 16 * s);
        float m = -1e30f, l = 0.f; f32x16 o[4] = {};
        issue(0);
        for (int t = 0; t < nt; ++t) {
            wait_vm<0>(); bar();
            if (t + 1 < nt) issue(t + 1);
            const unsigned st = lds0 + (t & 1) * 32768;
            f32x16 s0 = {}, s1 = {};
#pragma unroll
            for (int s = 0; s < 8; ++s) {
                const bf16x8 k0 = lds_rd128(st + kofs[s & 1] + 512 * (s >> 1)), k1 = lds_rd128(st + kofs[s & 1] + 512 * (s >> 1) + 8192);
                s0 = MFMA32(k0, qf[s], s0); s1 = MFMA32(k1, qf[s], s1);
            }
            if (t >= nt - 2) {
                const int dq = q0 + l32 - t * 64 - 4 * hi;
#pragma unroll
                for (int r = 0; r < 16; ++r) { const int c = (r & 3) + 8 * (r >> 2); if (c > dq) s0[r] = -__builtin_inff(); if (c + 32 > dq) s1[r] = -__builtin_inff(); }
            }
            float mx = s0[0];
#pragma unroll
            for (int r = 1; r < 16; ++r) mx = fmaxf(mx, s0[r]);
#pragma unroll
            for (int r = 0; r < 16; ++r) mx = fmaxf(mx, s1[r]);
            mx = fmaxf(mx, __shfl_xor(mx, 32));
            const float mn = fmaxf(m, mx), alpha = __builtin_amdgcn_exp2f(m - mn); m = mn;
            float ps = 0.f;
#pragma unroll
            for (int r = 0; r < 16; ++r) { s0[r] = __builtin_amdgcn_exp2f(s0[r] - mn); s1[r] = __builtin_amdgcn_exp2f(s1[r] - mn); ps += s0[r] + s1[r]; }
            ps += __shfl_xor(ps, 32);
            l = l * alpha + ps;
#pragma unroll
            for (int d = 0; d < 4; ++d) o[d] *= alpha;
#pragma unroll
            for (int ks = 0; ks < 4; ++ks) {
                const f32x16& sx = ks < 2 ? s0 : s1; const int r0 = 8 * (ks & 1);
                u32x4 pw = {cvtpk(sx[r0], sx[r0 + 1]), cvtpk(sx[r0 + 2], sx[r0 + 3]), cvtpk(sx[r0 + 4], sx[r0 + 5]), cvtpk(sx[r0 + 6], sx[r0 + 7])};
                const bf16x8 pf = __builtin_bit_cast(bf16x8, pw);
#pragma unroll
                for (int d = 0; d < 4; ++d) {
                    const bf16x8 vf = cat4(lds_tr(st + vofs + 4096 * ks + 512 * d), lds_tr(st + vofs + 4096 * ks + 512 * d + 2048));
                    o[d] = MFMA32(vf, pf, o[d]);
                }
            }
        }
        const float il = 1.f / l;
        u16* op = cy + (size_t)(q0 + l32) * CY + h * HD + 8 * hi;
#pragma unroll
        for (int d = 0; d < 4; ++d)
#pragma unroll
            for (int g2 = 0; g2 < 2; ++g2) {
                const int r = 8 * g2;
                unsigned a0 = cvtpk(o[d][r] * il, o[d][r + 1] * il), a1 = cvtpk(o[d][r + 2] * il, o[d][r + 3] * il);
                unsigned b0 = cvtpk(o[d][r + 4] * il, o[d][r + 5] * il), b1 = cvtpk(o[d][r + 6] * il, o[d][r + 7] * il);
                auto r0 = __builtin_amdgcn_permlane32_swap(a0, b0, false, false); auto r1 = __builtin_amdgcn_permlane32_swap(a1, b1, false, false);
                *(u32x4*)(op + 32 * d + 16 * g2) = (u32x4){r0[0], r1[0], r0[1], r1[1]};
            }
        bar();
    }
}

extern "C" void kernel_launch(void* const* d_in, const int* in_sizes, int n_in, void* d_out, int out_size, void* d_ws, size_t ws_size, hipStream_t stream) {
    const u16* x = (const u16*)d_in[0]; const u16* ckv = (const u16*)d_in[1]; const u16* wq = (const u16*)d_in[2]; const u16* wuk = (const u16*)d_in[3];
    const u16* wuv = (const u16*)d_in[4]; const u16* wo = (const u16*)d_in[5]; const u16* win = (const u16*)d_in[7]; const u16* wsp = (const u16*)d_in[8];
    const u16* bsp = (const u16*)d_in[9]; const u16* wout = (const u16*)d_in[10];
    char* ws = (char*)d_ws;
    u16* xn = (u16*)(ws + WS_XN); u16* Kb = (u16*)(ws + WS_K); u16* Vb = (u16*)(ws + WS_V); u16* Qb = (u16*)(ws + WS_Q);
    u16* z1 = (u16*)(ws + WS_Z1); u16* z2 = (u16*)(ws + WS_Z2); u16* z2n = (u16*)(ws + WS_Z2N); u16* cy = (u16*)(ws + WS_CY); float* part = (float*)(ws + WS_PART);
    (void)hipFuncSetAttribute((const void*)k_kv, hipFuncAttributeMaxDynamicSharedMemorySize, GKV::LDS);
    (void)hipFuncSetAttribute((const void*)k_q, hipFuncAttributeMaxDynamicSharedMemorySize, GQ::LDS);
    (void)hipFuncSetAttribute((const void*)k_z, hipFuncAttributeMaxDynamicSharedMemorySize, GZ::LDS);
    (void)hipFuncSetAttribute((const void*)k_sp, hipFuncAttributeMaxDynamicSharedMemorySize, GS::LDS);
    (void)hipFuncSetAttribute((const void*)k_out, hipFuncAttributeMaxDynamicSharedMemorySize, GO::LDS);
    (void)hipFuncSetAttribute((const void*)k_attn, hipFuncAttributeMaxDynamicSharedMemorySize, 65536);
    k_ln<<<S / 4, 256, 0, stream>>>(x, xn);
    k_kv<<<256, 512, GKV::LDS, stream>>>(ckv, wuk, wuv, Kb, Vb);
    k_q<<<256, 512, GQ::LDS, stream>>>(xn, wq, Qb);
    k_z<<<224, 512, GZ::LDS, stream>>>(xn, win, z1, z2);
    k_ln2<<<S / 4, 256, 0, stream>>>(z2, z2n);
    k_sp<<<224, 512, GS::LDS, stream>>>(wsp, z2n, bsp, z1, cy);
    k_attn<<<256, 256, 65536, stream>>>(Qb, Kb, Vb, cy);
    k_out<<<240, 512, GO::LDS, stream>>>(cy, wo, wout, part);
    k_red<<<S * DM / 4 / 256, 256, 0, stream>>>(x, part, (u16*)d_out);
}
```

```cpp
#include <hip/hip_runtime.h>
#include <stdint.h>
constexpr int S = 2048, DM = 640, H = 32, HD = 128, FP = 896, NKK = 156;
constexpr size_t WS_FLAG = 0;
constexpr size_t WS_XQ = 16384;
constexpr size_t WS_K = WS_XQ + (size_t)S * DM * 2;
constexpr size_t WS_V = WS_K + (size_t)H * S * HD * 2;
constexpr size_t WS_Z1 = WS_V + (size_t)H * S * HD * 2;
constexpr size_t WS_Z2 = WS_Z1 + (size_t)128 * 27 * 1024;
constexpr size_t WS_Z2N = WS_Z2 + (size_t)S * FP * 2;
constexpr size_t WS_CY = WS_Z2N + (size_t)S * FP * 2;
constexpr size_t WS_PART = WS_CY + (size_t)128 * NKK * 1024;
constexpr size_t WS_SPX = WS_PART + (size_t)80 * 3 * 4 * 8192;
constexpr size_t WS_ST = WS_SPX + (size_t)112 * 2 * 4 * 4096;
struct K1Args { const void *ckv, *wuk, *wuv; void *Kf, *Vf; const void* x; void *xq, *flags; const void *wsp, *wo, *wout, *wq, *win; void* st; };
struct K2Args { const void *xq, *wq, *Kf, *Vf; void* cy; const void* win; void *z1, *z2, *z2n, *flags, *st; const void *wsp, *wo, *wout; };
struct K3Args { const void *wsp, *z2n, *bsp, *z1; void* cy; const void *wo, *wout; void *part, *flags; const void* x; void *out, *st, *spx; };
extern "C" __global__ __launch_bounds__(256, 1) void k1(K1Args p) {
  auto ka = __builtin_amdgcn_kernarg_segment_ptr();
  asm volatile(
R"A(.macro MF d,a,b,c
v_mfma_f32_16x16x32_bf16 \d,\a,\b,\c
.endm
.macro AR d,a
v_accvgpr_read_b32 \d,\a
.endm
.macro CP d,a,b
v_cvt_pk_bf16_f32 \d,\a,\b
.endm
s_load_dwordx16 s[8:23],s[4:5],0x0
s_load_dwordx8 s[24:31],s[4:5],0x40
s_load_dwordx4 s[32:35],s[4:5],0x60
v_and_b32 v1,63,v0
v_lshrrev_b32 v2,6,v0
s_nop 0
v_readfirstlane_b32 s52,v2
v_lshlrev_b32 v8,4,v1
s_and_b32 s53,s6,7
s_lshl_b32 s53,s53,2
s_lshr_b32 s54,s6,3
s_and_b32 s54,s54,3
s_add_u32 s53,s53,s54
s_lshr_b32 s54,s6,5
v_lshrrev_b32 v2,3,v1
v_and_b32 v3,7,v1
v_lshrrev_b32 v4,4,v1
s_mul_i32 s100,s52,64
s_mov_b32 s101,1024
v_add_u32 v6,s100,v2
v_mul_lo_u32 v6,v6,s101
v_add_u32 v5,0,v4
v_xor_b32 v5,v5,v3
v_lshl_add_u32 v146,v5,4,v6
v_add_u32 v5,4,v4
v_xor_b32 v5,v5,v3
v_lshl_add_u32 v147,v5,4,v6
v_add_u32 v147,8192,v147
v_add_u32 v148,16384,v146
v_add_u32 v149,16384,v147
v_add_u32 v150,32768,v146
v_add_u32 v151,32768,v147
v_add_u32 v152,49152,v146
v_add_u32 v153,49152,v147
s_mul_i32 s57,s52,8192
v_lshrrev_b32 v2,2,v1
v_and_b32 v2,7,v2
v_lshrrev_b32 v3,5,v1
v_and_b32 v4,3,v1
s_lshl_b32 s100,s52,4
s_mov_b32 s101,8192
v_add_u32 v5,s100,v2
v_mul_lo_u32 v5,v5,s101
v_xor_b32 v6,0,v4
v_lshl_add_u32 v6,v3,2,v6
v_lshl_add_u32 v9,v6,4,v5
v_add_u32 v10,128,v9
v_add_u32 v5,s100,v2
v_add_u32 v5,8,v5
v_mul_lo_u32 v5,v5,s101
v_xor_b32 v6,2,v4
v_lshl_add_u32 v6,v3,2,v6
v_lshl_add_u32 v11,v6,4,v5
v_add_u32 v12,128,v11
s_lshl_b32 s58,s52,12
s_waitcnt lgkmcnt(0)
s_mov_b32 s48,s18
s_and_b32 s49,s19,0xffff
s_mov_b32 s50,2621440
s_mov_b32 s51,0x20000
s_lshl_b32 s100,s6,3
s_lshl_b32 s101,s52,1
s_add_u32 s100,s100,s101
v_lshrrev_b32 v198,5,v1
v_add_u32 v198,s100,v198
s_mul_i32 s100,s100,1280
s_mul_i32 s59,s52,3072
s_add_u32 s59,s59,132096
s_mov_b32 m0,s59
s_nop 0
buffer_load_dwordx4 v8,s[48:51],s100 offen lds
buffer_load_dwordx4 v8,s[48:51],s100 offen offset:1024 lds
buffer_load_dwordx4 v8,s[48:51],s100 offen offset:2048 lds
v_and_b32 v199,31,v1
v_lshrrev_b32 v196,5,v1
v_mul_u32_u24 v196,1280,v196
v_lshl_add_u32 v196,v199,4,v196
v_add_u32 v196,s59,v196
s_mov_b32 s36,s8
s_and_b32 s37,s9,0xffff
s_mov_b32 s38,2097152
s_mov_b32 s39,0x20000
s_mov_b32 s40,s10
s_and_b32 s41,s11,0xffff
s_mov_b32 s42,4194304
s_mov_b32 s43,0x20000
s_mov_b32 s44,s12
s_and_b32 s45,s13,0xffff
s_mov_b32 s46,4194304
s_mov_b32 s47,0x20000
s_mul_i32 s56,s54,262144
s_mul_i32 s55,s53,256
s_add_u32 m0,s57,0
s_nop 0
buffer_load_dwordx4 v146,s[36:39],s56 offen lds
s_add_u32 m0,s57,1024
s_nop 0
buffer_load_dwordx4 v147,s[36:39],s56 offen lds
s_add_u32 m0,s57,2048
s_nop 0
buffer_load_dwordx4 v148,s[36:39],s56 offen lds
s_add_u32 m0,s57,3072
s_nop 0
buffer_load_dwordx4 v149,s[36:39],s56 offen lds
s_add_u32 m0,s57,4096
s_nop 0
buffer_load_dwordx4 v150,s[36:39],s56 offen lds
s_add_u32 m0,s57,5120
s_nop 0
buffer_load_dwordx4 v151,s[36:39],s56 offen lds
s_add_u32 m0,s57,6144
s_nop 0
buffer_load_dwordx4 v152,s[36:39],s56 offen lds
s_add_u32 m0,s57,7168
s_nop 0
buffer_load_dwordx4 v153,s[36:39],s56 offen lds
s_add_u32 m0,s58,32768
s_nop 0
buffer_load_dwordx4 v9,s[40:43],s55 offen lds
s_add_u32 m0,s58,33792
s_nop 0
buffer_load_dwordx4 v10,s[40:43],s55 offen lds
s_add_u32 m0,s58,34816
s_nop 0
buffer_load_dwordx4 v11,s[40:43],s55 offen lds
s_add_u32 m0,s58,35840
s_nop 0
buffer_load_dwordx4 v12,s[40:43],s55 offen lds
s_add_u32 m0,s58,49152
s_nop 0
buffer_load_dwordx4 v9,s[44:47],s55 offen lds
s_add_u32 m0,s58,50176
s_nop 0
buffer_load_dwordx4 v10,s[44:47],s55 offen lds
s_add_u32 m0,s58,51200
s_nop 0
buffer_load_dwordx4 v11,s[44:47],s55 offen lds
s_add_u32 m0,s58,52224
s_nop 0
buffer_load_dwordx4 v12,s[44:47],s55 offen lds
s_add_u32 s56,s56,128
s_add_u32 s55,s55,524288
s_add_u32 m0,s57,65536
s_nop 0
buffer_load_dwordx4 v146,s[36:39],s56 offen lds
s_add_u32 m0,s57,66560
s_nop 0
buffer_load_dwordx4 v147,s[36:39],s56 offen lds
s_add_u32 m0,s57,67584
s_nop 0
buffer_load_dwordx4 v148,s[36:39],s56 offen lds
s_add_u32 m0,s57,68608
s_nop 0
buffer_load_dwordx4 v149,s[36:39],s56 offen lds
s_add_u32 m0,s57,69632
s_nop 0
buffer_load_dwordx4 v150,s[36:39],s56 offen lds
s_add_u32 m0,s57,70656
s_nop 0
buffer_load_dwordx4 v151,s[36:39],s56 offen lds
s_add_u32 m0,s57,71680
s_nop 0
buffer_load_dwordx4 v152,s[36:39],s56 offen lds
s_add_u32 m0,s57,72704
s_nop 0
buffer_load_dwordx4 v153,s[36:39],s56 offen lds
s_add_u32 m0,s58,98304
s_nop 0
buffer_load_dwordx4 v9,s[40:43],s55 offen lds
s_add_u32 m0,s58,99328
s_nop 0
buffer_load_dwordx4 v10,s[40:43],s55 offen lds
s_add_u32 m0,s58,100352
s_nop 0
buffer_load_dwordx4 v11,s[40:43],s55 offen lds
s_add_u32 m0,s58,101376
s_nop 0
buffer_load_dwordx4 v12,s[40:43],s55 offen lds
s_add_u32 m0,s58,114688
s_nop 0
buffer_load_dwordx4 v9,s[44:47],s55 offen lds
s_add_u32 m0,s58,115712
s_nop 0
buffer_load_dwordx4 v10,s[44:47],s55 offen lds
s_add_u32 m0,s58,116736
s_nop 0
buffer_load_dwordx4 v11,s[44:47],s55 offen lds
s_add_u32 m0,s58,117760
s_nop 0
buffer_load_dwordx4 v12,s[44:47],s55 offen lds
s_add_u32 s56,s56,128
s_add_u32 s55,s55,524288
s_lshr_b32 s98,s52,1
s_and_b32 s99,s52,1
v_and_b32 v2,15,v1
v_lshrrev_b32 v3,4,v1
v_lshrrev_b32 v4,1,v2
s_mul_i32 s100,s98,16384
v_add_u32 v5,0,v3
v_xor_b32 v5,v5,v4
v_lshlrev_b32 v5,4,v5
v_lshl_add_u32 v5,v2,7,v5
v_add_u32 v5,s100,v5
v_add_u32 v142,0,v5
v_add_u32 v144,65536,v5
v_add_u32 v5,4,v3
v_xor_b32 v5,v5,v4
v_lshlrev_b32 v5,4,v5
v_lshl_add_u32 v5,v2,7,v5
v_add_u32 v5,s100,v5
v_add_u32 v143,0,v5
v_add_u32 v145,65536,v5
v_lshrrev_b32 v2,4,v1
v_lshrrev_b32 v3,2,v1
v_and_b32 v3,3,v3
v_and_b32 v4,3,v1
v_lshlrev_b32 v5,11,v2
v_lshl_add_u32 v5,v3,6,v5
v_lshl_add_u32 v5,v4,3,v5
v_and_b32 v6,1,v2
s_mul_i32 s100,s99,16384
v_add_u32 v5,s100,v5
v_xor_b32 v7,0,v6
v_lshl_add_u32 v7,v7,5,v5
v_add_u32 v154,32768,v7
v_add_u32 v156,98304,v7
v_xor_b32 v7,1,v6
v_lshl_add_u32 v7,v7,5,v5
v_add_u32 v155,32768,v7
v_add_u32 v157,98304,v7
s_mov_b32 s64,s20
s_and_b32 s65,s21,0xffff
s_mov_b32 s66,2621440
s_mov_b32 s67,0x20000
s_mov_b32 s62,8192
s_mov_b32 s63,16384
s_lshr_b32 s100,s6,3
s_lshl_b32 s100,s100,8
v_add_u32 v204,s100,v0
s_lshl_b32 s72,s52,8
s_add_u32 s72,s72,131072
s_mov_b32 m0,s72
v_lshlrev_b32 v202,8,v204
buffer_load_dword v202,s[36:39],0 offen lds
v_add_u32 v202,128,v202
buffer_load_dword v202,s[36:39],0 offen lds
v_lshrrev_b32 v202,4,v204
v_lshlrev_b32 v202,13,v202
v_and_b32 v209,7,v204
v_lshl_add_u32 v202,v209,7,v202
s_and_b32 s100,s6,7
s_lshl_b32 s100,s100,10
v_and_b32 v209,8,v204
v_cmp_eq_u32 vcc,0,v209
v_mov_b32 v209,0x7ffffff0
s_nop 0
v_cndmask_b32 v207,v209,v202,vcc
v_cndmask_b32 v208,v202,v209,vcc
buffer_load_dword v207,s[40:43],s100 offen lds
buffer_load_dword v208,s[44:47],s100 offen lds
s_cmp_lg_u32 s6,0
s_cbranch_scc1 LNOZ_%=
s_mov_b32 s48,s22
s_and_b32 s49,s23,0xffff
s_mov_b32 s50,16384
s_mov_b32 s51,0x20000
v_mov_b32 v212,0
v_mov_b32 v213,0
v_mov_b32 v214,0
v_mov_b32 v215,0
v_lshlrev_b32 v4,4,v0
s_mov_b32 s100,0
buffer_store_dwordx4 v[212:215],v4,s[48:51],s100 offen
s_mov_b32 s100,4096
buffer_store_dwordx4 v[212:215],v4,s[48:51],s100 offen
s_mov_b32 s100,8192
buffer_store_dwordx4 v[212:215],v4,s[48:51],s100 offen
s_mov_b32 s100,12288
buffer_store_dwordx4 v[212:215],v4,s[48:51],s100 offen
LNOZ_%=:
s_lshr_b32 s73,s52,1
s_and_b32 s74,s52,1
s_lshl_b32 s75,s54,1
s_add_u32 s75,s75,s73
s_lshl_b32 s75,s75,15
s_lshl_b32 s100,s53,19
s_add_u32 s75,s75,s100
s_lshl_b32 s100,s6,15
v_lshlrev_b32 v204,7,v0
v_add_u32 v204,s100,v204
s_cmp_eq_u32 s74,0
s_cbranch_scc0 LV_%=
s_waitcnt vmcnt(20)
s_barrier
ds_read_b64_tr_b16 v[14:15],v154
ds_read_b64_tr_b16 v[16:17],v154 offset:256
ds_read_b128 v[78:81],v142
ds_read_b64_tr_b16 v[18:19],v155
ds_read_b64_tr_b16 v[20:21],v155 offset:256
ds_read_b64_tr_b16 v[22:23],v154 offset:512
ds_read_b64_tr_b16 v[24:25],v154 offset:768
ds_read_b64_tr_b16 v[26:27],v155 offset:512
ds_read_b64_tr_b16 v[28:29],v155 offset:768
ds_read_b64_tr_b16 v[30:31],v154 offset:1024
ds_read_b64_tr_b16 v[32:33],v154 offset:1280
ds_read_b64_tr_b16 v[34:35],v155 offset:1024
ds_read_b64_tr_b16 v[36:37],v155 offset:1280
ds_read_b64_tr_b16 v[38:39],v154 offset:1536
ds_read_b64_tr_b16 v[40:41],v154 offset:1792
ds_read_b64_tr_b16 v[42:43],v155 offset:1536
ds_read_b64_tr_b16 v[44:45],v155 offset:1792
ds_read_b128 v[82:85],v142 offset:2048
ds_read_b128 v[86:89],v142 offset:4096
ds_read_b128 v[90:93],v142 offset:6144
ds_read_b128 v[94:97],v142 offset:8192
ds_read_b128 v[98:101],v142 offset:10240
ds_read_b128 v[102:105],v142 offset:12288
ds_read_b128 v[106:109],v142 offset:14336
ds_read_b128 v[160:163],v196
ds_read_b128 v[164:167],v196 offset:512
ds_read_b128 v[168:171],v196 offset:1024
s_waitcnt lgkmcnt(15)
MF a[0:3],v[14:17],v[78:81],0
ds_read_b64_tr_b16 v[46:47],v154 offset:8192
MF a[4:7],v[18:21],v[78:81],0
MF a[8:11],v[22:25],v[78:81],0
ds_read_b64_tr_b16 v[48:49],v154 offset:8448
MF a[12:15],v[26:29],v[78:81],0
MF a[16:19],v[30:33],v[78:81],0
ds_read_b128 v[110:113],v143
s_waitcnt lgkmcnt(15)
MF a[20:23],v[34:37],v[78:81],0
MF a[24:27],v[38:41],v[78:81],0
ds_read_b64_tr_b16 v[50:51],v155 offset:8192
s_waitcnt lgkmcnt(14)
MF a[28:31],v[42:45],v[78:81],0
s_waitcnt lgkmcnt(13)
MF a[32:35],v[14:17],v[82:85],0
ds_read_b64_tr_b16 v[52:53],v155 offset:8448
MF a[36:39],v[18:21],v[82:85],0
MF a[40:43],v[22:25],v[82:85],0
ds_read_b64_tr_b16 v[54:55],v154 offset:8704
MF a[44:47],v[26:29],v[82:85],0
MF a[48:51],v[30:33],v[82:85],0
ds_read_b64_tr_b16 v[56:57],v154 offset:8960
MF a[52:55],v[34:37],v[82:85],0
MF a[56:59],v[38:41],v[82:85],0
ds_read_b64_tr_b16 v[58:59],v155 offset:8704
MF a[60:63],v[42:45],v[82:85],0
s_waitcnt lgkmcnt(15)
MF a[64:67],v[14:17],v[86:89],0
ds_read_b64_tr_b16 v[60:61],v155 offset:8960
MF a[68:71],v[18:21],v[86:89],0
MF a[72:75],v[22:25],v[86:89],0
ds_read_b64_tr_b16 v[62:63],v154 offset:9216
MF a[76:79],v[26:29],v[86:89],0
MF a[80:83],v[30:33],v[86:89],0
ds_read_b64_tr_b16 v[64:65],v154 offset:9472
MF a[84:87],v[34:37],v[86:89],0
MF a[88:91],v[38:41],v[86:89],0
ds_read_b64_tr_b16 v[66:67],v155 offset:9216
MF a[92:95],v[42:45],v[86:89],0
MF a[96:99],v[14:17],v[90:93],0
ds_read_b64_tr_b16 v[68:69],v155 offset:9472
MF a[100:103],v[18:21],v[90:93],0
MF a[104:107],v[22:25],v[90:93],0
ds_read_b64_tr_b16 v[70:71],v154 offset:9728
MF a[108:111],v[26:29],v[90:93],0
MF a[112:115],v[30:33],v[90:93],0
ds_read_b64_tr_b16 v[72:73],v154 offset:9984
MF a[116:119],v[34:37],v[90:93],0
MF a[120:123],v[38:41],v[90:93],0
ds_read_b64_tr_b16 v[74:75],v155 offset:9728
MF a[124:127],v[42:45],v[90:93],0
s_waitcnt lgkmcnt(15)
MF a[128:131],v[14:17],v[94:97],0
ds_read_b64_tr_b16 v[76:77],v155 offset:9984
MF a[132:135],v[18:21],v[94:97],0
MF a[136:139],v[22:25],v[94:97],0
ds_read_b128 v[114:117],v143 offset:2048
MF a[140:143],v[26:29],v[94:97],0
MF a[144:147],v[30:33],v[94:97],0
ds_read_b128 v[118:121],v143 offset:4096
MF a[148:151],v[34:37],v[94:97],0
MF a[152:155],v[38:41],v[94:97],0
ds_read_b128 v[122:125],v143 offset:6144
MF a[156:159],v[42:45],v[94:97],0
MF a[160:163],v[14:17],v[98:101],0
ds_read_b128 v[126:129],v143 offset:8192
MF a[164:167],v[18:21],v[98:101],0
MF a[168:171],v[22:25],v[98:101],0
ds_read_b128 v[130:133],v143 offset:10240
MF a[172:175],v[26:29],v[98:101],0
MF a[176:179],v[30:33],v[98:101],0
ds_read_b128 v[134:137],v143 offset:12288
MF a[180:183],v[34:37],v[98:101],0
MF a[184:187],v[38:41],v[98:101],0
ds_read_b128 v[138:141],v143 offset:14336
MF a[188:191],v[42:45],v[98:101],0
MF a[192:195],v[14:17],v[102:105],0
MF a[196:199],v[18:21],v[102:105],0
MF a[200:203],v[22:25],v[102:105],0
MF a[204:207],v[26:29],v[102:105],0
MF a[208:211],v[30:33],v[102:105],0
MF a[212:215],v[34:37],v[102:105],0
MF a[216:219],v[38:41],v[102:105],0
MF a[220:223],v[42:45],v[102:105],0
MF a[224:227],v[14:17],v[106:109],0
MF a[228:231],v[18:21],v[106:109],0
MF a[232:235],v[22:25],v[106:109],0
MF a[236:239],v[26:29],v[106:109],0
MF a[240:243],v[30:33],v[106:109],0
MF a[244:247],v[34:37],v[106:109],0
MF a[248:251],v[38:41],v[106:109],0
MF a[252:255],v[42:45],v[106:109],0
s_waitcnt vmcnt(4) lgkmcnt(0)
s_barrier
s_add_u32 m0,s57,0
MF a[0:3],v[46:49],v[110:113],a[0:3]
buffer_load_dwordx4 v146,s[36:39],s56 offen lds
MF a[4:7],v[50:53],v[110:113],a[4:7]
ds_read_b64_tr_b16 v[14:15],v156
MF a[8:11],v[54:57],v[110:113],a[8:11]
ds_read_b64_tr_b16 v[16:17],v156 offset:256
MF a[12:15],v[58:61],v[110:113],a[12:15]
s_add_u32 m0,s57,1024
MF a[16:19],v[62:65],v[110:113],a[16:19]
buffer_load_dwordx4 v147,s[36:39],s56 offen lds
MF a[20:23],v[66:69],v[110:113],a[20:23]
ds_read_b128 v[78:81],v144
MF a[24:27],v[70:73],v[110:113],a[24:27]
s_add_u32 m0,s57,2048
MF a[28:31],v[74:77],v[110:113],a[28:31]
buffer_load_dwordx4 v148,s[36:39],s56 offen lds
MF a[32:35],v[46:49],v[114:117],a[32:35]
ds_read_b64_tr_b16 v[18:19],v157
MF a[36:39],v[50:53],v[114:117],a[36:39]
ds_read_b64_tr_b16 v[20:21],v157 offset:256
MF a[40:43],v[54:57],v[114:117],a[40:43]
s_add_u32 m0,s57,3072
MF a[44:47],v[58:61],v[114:117],a[44:47]
buffer_load_dwordx4 v149,s[36:39],s56 offen lds
MF a[48:51],v[62:65],v[114:117],a[48:51]
ds_read_b64_tr_b16 v[22:23],v156 offset:512
MF a[52:55],v[66:69],v[114:117],a[52:55]
s_add_u32 m0,s57,4096
MF a[56:59],v[70:73],v[114:117],a[56:59]
buffer_load_dwordx4 v150,s[36:39],s56 offen lds
MF a[60:63],v[74:77],v[114:117],a[60:63]
ds_read_b64_tr_b16 v[24:25],v156 offset:768
MF a[64:67],v[46:49],v[118:121],a[64:67]
ds_read_b64_tr_b16 v[26:27],v157 offset:512
MF a[68:71],v[50:53],v[118:121],a[68:71]
s_add_u32 m0,s57,5120
MF a[72:75],v[54:57],v[118:121],a[72:75]
buffer_load_dwordx4 v151,s[36:39],s56 offen lds
MF a[76:79],v[58:61],v[118:121],a[76:79]
ds_read_b64_tr_b16 v[28:29],v157 offset:768
MF a[80:83],v[62:65],v[118:121],a[80:83]
s_add_u32 m0,s57,6144
MF a[84:87],v[66:69],v[118:121],a[84:87]
buffer_load_dwordx4 v152,s[36:39],s56 offen lds
MF a[88:91],v[70:73],v[118:121],a[88:91]
ds_read_b64_tr_b16 v[30:31],v156 offset:1024
MF a[92:95],v[74:77],v[118:121],a[92:95]
ds_read_b64_tr_b16 v[32:33],v156 offset:1280
MF a[96:99],v[46:49],v[122:125],a[96:99]
s_add_u32 m0,s57,7168
MF a[100:103],v[50:53],v[122:125],a[100:103]
buffer_load_dwordx4 v153,s[36:39],s56 offen lds
MF a[104:107],v[54:57],v[122:125],a[104:107]
ds_read_b64_tr_b16 v[34:35],v157 offset:1024
MF a[108:111],v[58:61],v[122:125],a[108:111]
s_add_u32 m0,s58,32768
MF a[112:115],v[62:65],v[122:125],a[112:115]
buffer_load_dwordx4 v9,s[40:43],s55 offen lds
MF a[116:119],v[66:69],v[122:125],a[116:119]
ds_read_b64_tr_b16 v[36:37],v157 offset:1280
MF a[120:123],v[70:73],v[122:125],a[120:123]
ds_read_b64_tr_b16 v[38:39],v156 offset:1536
MF a[124:127],v[74:77],v[122:125],a[124:127]
s_add_u32 m0,s58,33792
MF a[128:131],v[46:49],v[126:129],a[128:131]
buffer_load_dwordx4 v10,s[40:43],s55 offen lds
MF a[132:135],v[50:53],v[126:129],a[132:135]
ds_read_b64_tr_b16 v[40:41],v156 offset:1792
MF a[136:139],v[54:57],v[126:129],a[136:139]
s_add_u32 m0,s58,34816
MF a[140:143],v[58:61],v[126:129],a[140:143]
buffer_load_dwordx4 v11,s[40:43],s55 offen lds
MF a[144:147],v[62:65],v[126:129],a[144:147]
ds_read_b64_tr_b16 v[42:43],v157 offset:1536
MF a[148:151],v[66:69],v[126:129],a[148:151]
ds_read_b64_tr_b16 v[44:45],v157 offset:1792
MF a[152:155],v[70:73],v[126:129],a[152:155]
s_add_u32 m0,s58,35840
MF a[156:159],v[74:77],v[126:129],a[156:159]
buffer_load_dwordx4 v12,s[40:43],s55 offen lds
MF a[160:163],v[46:49],v[130:133],a[160:163]
ds_read_b128 v[82:85],v144 offset:2048
MF a[164:167],v[50:53],v[130:133],a[164:167]
s_add_u32 m0,s58,49152
MF a[168:171],v[54:57],v[130:133],a[168:171]
buffer_load_dwordx4 v9,s[44:47],s55 offen lds
MF a[172:175],v[58:61],v[130:133],a[172:175]
ds_read_b128 v[86:89],v144 offset:4096
MF a[176:179],v[62:65],v[130:133],a[176:179]
ds_read_b128 v[90:93],v144 offset:6144
MF a[180:183],v[66:69],v[130:133],a[180:183]
s_add_u32 m0,s58,50176
MF a[184:187],v[70:73],v[130:133],a[184:187]
buffer_load_dwordx4 v10,s[44:47],s55 offen lds
MF a[188:191],v[74:77],v[130:133],a[188:191]
ds_read_b128 v[94:97],v144 offset:8192
MF a[192:195],v[46:49],v[134:137],a[192:195]
s_add_u32 m0,s58,51200
MF a[196:199],v[50:53],v[134:137],a[196:199]
buffer_load_dwordx4 v11,s[44:47],s55 offen lds
MF a[200:203],v[54:57],v[134:137],a[200:203]
ds_read_b128 v[98:101],v144 offset:10240
MF a[204:207],v[58:61],v[134:137],a[204:207]
ds_read_b128 v[102:105],v144 offset:12288
MF a[208:211],v[62:65],v[134:137],a[208:211]
s_add_u32 m0,s58,52224
MF a[212:215],v[66:69],v[134:137],a[212:215]
buffer_load_dwordx4 v12,s[44:47],s55 offen lds
MF a[216:219],v[70:73],v[134:137],a[216:219]
ds_read_b128 v[106:109],v144 offset:14336
MF a[220:223],v[74:77],v[134:137],a[220:223]
MF a[224:227],v[46:49],v[138:141],a[224:227]
MF a[228:231],v[50:53],v[138:141],a[228:231]
MF a[232:235],v[54:57],v[138:141],a[232:235]
MF a[236:239],v[58:61],v[138:141],a[236:239]
MF a[240:243],v[62:65],v[138:141],a[240:243]
MF a[244:247],v[66:69],v[138:141],a[244:247]
MF a[248:251],v[70:73],v[138:141],a[248:251]
MF a[252:255],v[74:77],v[138:141],a[252:255]
s_add_u32 s56,s56,128
s_add_u32 s55,s55,524288
s_waitcnt lgkmcnt(15)
MF a[0:3],v[14:17],v[78:81],a[0:3]
ds_read_b64_tr_b16 v[46:47],v156 offset:8192
v_cmp_gt_u32_e64 s[60:61],16,v199
MF a[4:7],v[18:21],v[78:81],a[4:7]
MF a[8:11],v[22:25],v[78:81],a[8:11]
ds_read_b64_tr_b16 v[48:49],v156 offset:8448
v_lshlrev_b32 v172,16,v160
MF a[12:15],v[26:29],v[78:81],a[12:15]
v_and_b32 v173,0xffff0000,v160
s_waitcnt lgkmcnt(15)
MF a[16:19],v[30:33],v[78:81],a[16:19]
ds_read_b128 v[110:113],v145
v_lshlrev_b32 v174,16,v161
s_waitcnt lgkmcnt(14)
MF a[20:23],v[34:37],v[78:81],a[20:23]
s_waitcnt lgkmcnt(12)
MF a[24:27],v[38:41],v[78:81],a[24:27]
ds_read_b64_tr_b16 v[50:51],v157 offset:8192
v_and_b32 v175,0xffff0000,v161
s_waitcnt lgkmcnt(11)
MF a[28:31],v[42:45],v[78:81],a[28:31]
v_lshlrev_b32 v176,16,v162
s_waitcnt lgkmcnt(10)
MF a[32:35],v[14:17],v[82:85],a[32:35]
ds_read_b64_tr_b16 v[52:53],v157 offset:8448
MF a[36:39],v[18:21],v[82:85],a[36:39]
v_and_b32 v177,0xffff0000,v162
MF a[40:43],v[22:25],v[82:85],a[40:43]
ds_read_b64_tr_b16 v[54:55],v156 offset:8704
v_lshlrev_b32 v178,16,v163
MF a[44:47],v[26:29],v[82:85],a[44:47]
MF a[48:51],v[30:33],v[82:85],a[48:51]
ds_read_b64_tr_b16 v[56:57],v156 offset:8960
v_and_b32 v179,0xffff0000,v163
MF a[52:55],v[34:37],v[82:85],a[52:55]
v_lshlrev_b32 v180,16,v164
MF a[56:59],v[38:41],v[82:85],a[56:59]
ds_read_b64_tr_b16 v[58:59],v157 offset:8704
v_and_b32 v181,0xffff0000,v164
MF a[60:63],v[42:45],v[82:85],a[60:63]
s_waitcnt lgkmcnt(13)
MF a[64:67],v[14:17],v[86:89],a[64:67]
ds_read_b64_tr_b16 v[60:61],v157 offset:8960
v_lshlrev_b32 v182,16,v165
MF a[68:71],v[18:21],v[86:89],a[68:71]
v_and_b32 v183,0xffff0000,v165
MF a[72:75],v[22:25],v[86:89],a[72:75]
ds_read_b64_tr_b16 v[62:63],v156 offset:9216
MF a[76:79],v[26:29],v[86:89],a[76:79]
v_lshlrev_b32 v184,16,v166
MF a[80:83],v[30:33],v[86:89],a[80:83]
ds_read_b64_tr_b16 v[64:65],v156 offset:9472
v_and_b32 v185,0xffff0000,v166
MF a[84:87],v[34:37],v[86:89],a[84:87]
MF a[88:91],v[38:41],v[86:89],a[88:91]
ds_read_b64_tr_b16 v[66:67],v157 offset:9216
v_lshlrev_b32 v186,16,v167
MF a[92:95],v[42:45],v[86:89],a[92:95]
v_and_b32 v187,0xffff0000,v167
s_waitcnt lgkmcnt(15)
MF a[96:99],v[14:17],v[90:93],a[96:99]
ds_read_b64_tr_b16 v[68:69],v157 offset:9472
v_lshlrev_b32 v188,16,v168
MF a[100:103],v[18:21],v[90:93],a[100:103]
MF a[104:107],v[22:25],v[90:93],a[104:107]
ds_read_b64_tr_b16 v[70:71],v156 offset:9728
v_and_b32 v189,0xffff0000,v168
MF a[108:111],v[26:29],v[90:93],a[108:111]
v_lshlrev_b32 v190,16,v169
MF a[112:115],v[30:33],v[90:93],a[112:115]
ds_read_b64_tr_b16 v[72:73],v156 offset:9984
MF a[116:119],v[34:37],v[90:93],a[116:119]
v_and_b32 v191,0xffff0000,v169
MF a[120:123],v[38:41],v[90:93],a[120:123]
ds_read_b64_tr_b16 v[74:75],v157 offset:9728
v_lshlrev_b32 v192,16,v170
MF a[124:127],v[42:45],v[90:93],a[124:127]
MF a[128:131],v[14:17],v[94:97],a[128:131]
ds_read_b64_tr_b16 v[76:77],v157 offset:9984
v_and_b32 v193,0xffff0000,v170
MF a[132:135],v[18:21],v[94:97],a[132:135]
v_lshlrev_b32 v194,16,v171
MF a[136:139],v[22:25],v[94:97],a[136:139]
ds_read_b128 v[114:117],v145 offset:2048
v_and_b32 v195,0xffff0000,v171
MF a[140:143],v[26:29],v[94:97],a[140:143]
MF a[144:147],v[30:33],v[94:97],a[144:147]
ds_read_b128 v[118:121],v145 offset:4096
v_cndmask_b32_e64 v188,0,v188,s[60:61]
MF a[148:151],v[34:37],v[94:97],a[148:151]
v_cndmask_b32_e64 v189,0,v189,s[60:61]
MF a[152:155],v[38:41],v[94:97],a[152:155]
ds_read_b128 v[122:125],v145 offset:6144
MF a[156:159],v[42:45],v[94:97],a[156:159]
v_cndmask_b32_e64 v190,0,v190,s[60:61]
s_waitcnt lgkmcnt(15)
MF a[160:163],v[14:17],v[98:101],a[160:163]
ds_read_b128 v[126:129],v145 offset:8192
v_cndmask_b32_e64 v191,0,v191,s[60:61]
MF a[164:167],v[18:21],v[98:101],a[164:167]
v_cndmask_b32_e64 v192,0,v192,s[60:61]
MF a[168:171],v[22:25],v[98:101],a[168:171]
ds_read_b128 v[130:133],v145 offset:10240
MF a[172:175],v[26:29],v[98:101],a[172:175]
v_cndmask_b32_e64 v193,0,v193,s[60:61]
MF a[176:179],v[30:33],v[98:101],a[176:179]
ds_read_b128 v[134:137],v145 offset:12288
v_cndmask_b32_e64 v194,0,v194,s[60:61]
MF a[180:183],v[34:37],v[98:101],a[180:183]
MF a[184:187],v[38:41],v[98:101],a[184:187]
ds_read_b128 v[138:141],v145 offset:14336
v_cndmask_b32_e64 v195,0,v195,s[60:61]
MF a[188:191],v[42:45],v[98:101],a[188:191]
v_add_f32 v200,v172,v173
MF a[192:195],v[14:17],v[102:105],a[192:195]
MF a[196:199],v[18:21],v[102:105],a[196:199]
v_add_f32 v201,v174,v175
MF a[200:203],v[22:25],v[102:105],a[200:203]
v_add_f32 v200,v176,v200
MF a[204:207],v[26:29],v[102:105],a[204:207]
v_add_f32 v201,v177,v201
MF a[208:211],v[30:33],v[102:105],a[208:211]
MF a[212:215],v[34:37],v[102:105],a[212:215]
v_add_f32 v200,v178,v200
MF a[216:219],v[38:41],v[102:105],a[216:219]
v_add_f32 v201,v179,v201
MF a[220:223],v[42:45],v[102:105],a[220:223]
MF a[224:227],v[14:17],v[106:109],a[224:227]
v_add_f32 v200,v180,v200
MF a[228:231],v[18:21],v[106:109],a[228:231]
v_add_f32 v201,v181,v201
MF a[232:235],v[22:25],v[106:109],a[232:235]
MF a[236:239],v[26:29],v[106:109],a[236:239]
v_add_f32 v200,v182,v200
MF a[240:243],v[30:33],v[106:109],a[240:243]
v_add_f32 v201,v183,v201
MF a[244:247],v[34:37],v[106:109],a[244:247]
v_add_f32 v200,v184,v200
MF a[248:251],v[38:41],v[106:109],a[248:251]
MF a[252:255],v[42:45],v[106:109],a[252:255]
v_add_f32 v201,v185,v201
s_waitcnt vmcnt(0) lgkmcnt(0)
s_barrier
s_add_u32 m0,s57,65536
MF a[0:3],v[46:49],v[110:113],a[0:3]
buffer_load_dwordx4 v146,s[36:39],s56 offen lds
MF a[4:7],v[50:53],v[110:113],a[4:7]
ds_read_b64_tr_b16 v[14:15],v154
MF a[8:11],v[54:57],v[110:113],a[8:11]
ds_read_b64_tr_b16 v[16:17],v154 offset:256
MF a[12:15],v[58:61],v[110:113],a[12:15]
s_add_u32 m0,s57,66560
MF a[16:19],v[62:65],v[110:113],a[16:19]
buffer_load_dwordx4 v147,s[36:39],s56 offen lds
MF a[20:23],v[66:69],v[110:113],a[20:23]
ds_read_b128 v[78:81],v142
MF a[24:27],v[70:73],v[110:113],a[24:27]
s_add_u32 m0,s57,67584
MF a[28:31],v[74:77],v[110:113],a[28:31]
buffer_load_dwordx4 v148,s[36:39],s56 offen lds
MF a[32:35],v[46:49],v[114:117],a[32:35]
ds_read_b64_tr_b16 v[18:19],v155
MF a[36:39],v[50:53],v[114:117],a[36:39]
ds_read_b64_tr_b16 v[20:21],v155 offset:256
MF a[40:43],v[54:57],v[114:117],a[40:43]
s_add_u32 m0,s57,68608
MF a[44:47],v[58:61],v[114:117],a[44:47]
buffer_load_dwordx4 v149,s[36:39],s56 offen lds
MF a[48:51],v[62:65],v[114:117],a[48:51]
ds_read_b64_tr_b16 v[22:23],v154 offset:512
MF a[52:55],v[66:69],v[114:117],a[52:55]
s_add_u32 m0,s57,69632
MF a[56:59],v[70:73],v[114:117],a[56:59]
buffer_load_dwordx4 v150,s[36:39],s56 offen lds
MF a[60:63],v[74:77],v[114:117],a[60:63]
ds_read_b64_tr_b16 v[24:25],v154 offset:768
MF a[64:67],v[46:49],v[118:121],a[64:67]
ds_read_b64_tr_b16 v[26:27],v155 offset:512
MF a[68:71],v[50:53],v[118:121],a[68:71]
s_add_u32 m0,s57,70656
MF a[72:75],v[54:57],v[118:121],a[72:75]
buffer_load_dwordx4 v151,s[36:39],s56 offen lds
MF a[76:79],v[58:61],v[118:121],a[76:79]
ds_read_b64_tr_b16 v[28:29],v155 offset:768
MF a[80:83],v[62:65],v[118:121],a[80:83]
s_add_u32 m0,s57,71680
MF a[84:87],v[66:69],v[118:121],a[84:87]
buffer_load_dwordx4 v152,s[36:39],s56 offen lds
MF a[88:91],v[70:73],v[118:121],a[88:91]
ds_read_b64_tr_b16 v[30:31],v154 offset:1024
MF a[92:95],v[74:77],v[118:121],a[92:95]
ds_read_b64_tr_b16 v[32:33],v154 offset:1280
MF a[96:99],v[46:49],v[122:125],a[96:99]
s_add_u32 m0,s57,72704
MF a[100:103],v[50:53],v[122:125],a[100:103]
buffer_load_dwordx4 v153,s[36:39],s56 offen lds
MF a[104:107],v[54:57],v[122:125],a[104:107]
ds_read_b64_tr_b16 v[34:35],v155 offset:1024
MF a[108:111],v[58:61],v[122:125],a[108:111]
s_add_u32 m0,s58,98304
MF a[112:115],v[62:65],v[122:125],a[112:115]
buffer_load_dwordx4 v9,s[40:43],s55 offen lds
MF a[116:119],v[66:69],v[122:125],a[116:119]
ds_read_b64_tr_b16 v[36:37],v155 offset:1280
MF a[120:123],v[70:73],v[122:125],a[120:123]
ds_read_b64_tr_b16 v[38:39],v154 offset:1536
MF a[124:127],v[74:77],v[122:125],a[124:127]
s_add_u32 m0,s58,99328
MF a[128:131],v[46:49],v[126:129],a[128:131]
buffer_load_dwordx4 v10,s[40:43],s55 offen lds
MF a[132:135],v[50:53],v[126:129],a[132:135]
ds_read_b64_tr_b16 v[40:41],v154 offset:1792
MF a[136:139],v[54:57],v[126:129],a[136:139]
s_add_u32 m0,s58,100352
MF a[140:143],v[58:61],v[126:129],a[140:143]
buffer_load_dwordx4 v11,s[40:43],s55 offen lds
MF a[144:147],v[62:65],v[126:129],a[144:147]
ds_read_b64_tr_b16 v[42:43],v155 offset:1536
MF a[148:151],v[66:69],v[126:129],a[148:151]
ds_read_b64_tr_b16 v[44:45],v155 offset:1792
MF a[152:155],v[70:73],v[126:129],a[152:155]
s_add_u32 m0,s58,101376
MF a[156:159],v[74:77],v[126:129],a[156:159]
buffer_load_dwordx4 v12,s[40:43],s55 offen lds
MF a[160:163],v[46:49],v[130:133],a[160:163]
ds_read_b128 v[82:85],v142 offset:2048
MF a[164:167],v[50:53],v[130:133],a[164:167]
s_add_u32 m0,s58,114688
MF a[168:171],v[54:57],v[130:133],a[168:171]
buffer_load_dwordx4 v9,s[44:47],s55 offen lds
MF a[172:175],v[58:61],v[130:133],a[172:175]
ds_read_b128 v[86:89],v142 offset:4096
MF a[176:179],v[62:65],v[130:133],a[176:179]
ds_read_b128 v[90:93],v142 offset:6144
MF a[180:183],v[66:69],v[130:133],a[180:183]
s_add_u32 m0,s58,115712
MF a[184:187],v[70:73],v[130:133],a[184:187]
buffer_load_dwordx4 v10,s[44:47],s55 offen lds
MF a[188:191],v[74:77],v[130:133],a[188:191]
ds_read_b128 v[94:97],v142 offset:8192
MF a[192:195],v[46:49],v[134:137],a[192:195]
s_add_u32 m0,s58,116736
MF a[196:199],v[50:53],v[134:137],a[196:199]
buffer_load_dwordx4 v11,s[44:47],s55 offen lds
MF a[200:203],v[54:57],v[134:137],a[200:203]
ds_read_b128 v[98:101],v142 offset:10240
MF a[204:207],v[58:61],v[134:137],a[204:207]
ds_read_b128 v[102:105],v142 offset:12288
MF a[208:211],v[62:65],v[134:137],a[208:211]
s_add_u32 m0,s58,117760
MF a[212:215],v[66:69],v[134:137],a[212:215]
buffer_load_dwordx4 v12,s[44:47],s55 offen lds
MF a[216:219],v[70:73],v[134:137],a[216:219]
ds_read_b128 v[106:109],v142 offset:14336
MF a[220:223],v[74:77],v[134:137],a[220:223]
MF a[224:227],v[46:49],v[138:141],a[224:227]
MF a[228:231],v[50:53],v[138:141],a[228:231]
MF a[232:235],v[54:57],v[138:141],a[232:235]
MF a[236:239],v[58:61],v[138:141],a[236:239]
MF a[240:243],v[62:65],v[138:141],a[240:243]
MF a[244:247],v[66:69],v[138:141],a[244:247]
MF a[248:251],v[70:73],v[138:141],a[248:251]
MF a[252:255],v[74:77],v[138:141],a[252:255]
s_add_u32 s56,s56,128
s_add_u32 s55,s55,524288
s_waitcnt lgkmcnt(15)
MF a[0:3],v[14:17],v[78:81],a[0:3]
ds_read_b64_tr_b16 v[46:47],v154 offset:8192
v_add_f32 v200,v186,v200
MF a[4:7],v[18:21],v[78:81],a[4:7]
MF a[8:11],v[22:25],v[78:81],a[8:11]
ds_read_b64_tr_b16 v[48:49],v154 offset:8448
v_add_f32 v201,v187,v201
MF a[12:15],v[26:29],v[78:81],a[12:15]
v_add_f32 v200,v188,v200
s_waitcnt lgkmcnt(15)
MF a[16:19],v[30:33],v[78:81],a[16:19]
ds_read_b128 v[110:113],v143
v_add_f32 v201,v189,v201
s_waitcnt lgkmcnt(14)
MF a[20:23],v[34:37],v[78:81],a[20:23]
s_waitcnt lgkmcnt(12)
MF a[24:27],v[38:41],v[78:81],a[24:27]
ds_read_b64_tr_b16 v[50:51],v155 offset:8192
v_add_f32 v200,v190,v200
s_waitcnt lgkmcnt(11)
MF a[28:31],v[42:45],v[78:81],a[28:31]
v_add_f32 v201,v191,v201
s_waitcnt lgkmcnt(10)
MF a[32:35],v[14:17],v[82:85],a[32:35]
ds_read_b64_tr_b16 v[52:53],v155 offset:8448
MF a[36:39],v[18:21],v[82:85],a[36:39]
v_add_f32 v200,v192,v200
MF a[40:43],v[22:25],v[82:85],a[40:43]
ds_read_b64_tr_b16 v[54:55],v154 offset:8704
v_add_f32 v201,v193,v201
MF a[44:47],v[26:29],v[82:85],a[44:47]
MF a[48:51],v[30:33],v[82:85],a[48:51]
ds_read_b64_tr_b16 v[56:57],v154 offset:8960
v_add_f32 v200,v194,v200
MF a[52:55],v[34:37],v[82:85],a[52:55]
v_add_f32 v201,v195,v201
MF a[56:59],v[38:41],v[82:85],a[56:59]
ds_read_b64_tr_b16 v[58:59],v155 offset:8704
v_add_f32 v200,v200,v201
MF a[60:63],v[42:45],v[82:85],a[60:63]
s_waitcnt lgkmcnt(13)
MF a[64:67],v[14:17],v[86:89],a[64:67]
ds_read_b64_tr_b16 v[60:61],v155 offset:8960
v_add_f32_dpp v200,v200,v200 row_ror:8 row_mask:0xf bank_mask:0xf
MF a[68:71],v[18:21],v[86:89],a[68:71]
s_nop 0
v_add_f32_dpp v200,v200,v200 row_ror:4 row_mask:0xf bank_mask:0xf
MF a[72:75],v[22:25],v[86:89],a[72:75]
ds_read_b64_tr_b16 v[62:63],v154 offset:9216
MF a[76:79],v[26:29],v[86:89],a[76:79]
v_add_f32_dpp v200,v200,v200 row_ror:2 row_mask:0xf bank_mask:0xf
MF a[80:83],v[30:33],v[86:89],a[80:83]
ds_read_b64_tr_b16 v[64:65],v154 offset:9472
v_add_f32_dpp v200,v200,v200 row_ror:1 row_mask:0xf bank_mask:0xf
MF a[84:87],v[34:37],v[86:89],a[84:87]
MF a[88:91],v[38:41],v[86:89],a[88:91]
ds_read_b64_tr_b16 v[66:67],v155 offset:9216
v_mov_b32 v201,v200
MF a[92:95],v[42:45],v[86:89],a[92:95]
s_nop 0
v_permlane16_swap_b32 v200,v201
s_waitcnt lgkmcnt(15)
MF a[96:99],v[14:17],v[90:93],a[96:99]
ds_read_b64_tr_b16 v[68:69],v155 offset:9472
v_add_f32 v200,v200,v201
MF a[100:103],v[18:21],v[90:93],a[100:103]
MF a[104:107],v[22:25],v[90:93],a[104:107]
ds_read_b64_tr_b16 v[70:71],v154 offset:9728
v_mul_f32 v200,0x3acccccd,v200
MF a[108:111],v[26:29],v[90:93],a[108:111]
v_sub_f32 v172,v172,v200
MF a[112:115],v[30:33],v[90:93],a[112:115]
ds_read_b64_tr_b16 v[72:73],v154 offset:9984
MF a[116:119],v[34:37],v[90:93],a[116:119]
v_sub_f32 v173,v173,v200
MF a[120:123],v[38:41],v[90:93],a[120:123]
ds_read_b64_tr_b16 v[74:75],v155 offset:9728
v_sub_f32 v174,v174,v200
MF a[124:127],v[42:45],v[90:93],a[124:127]
MF a[128:131],v[14:17],v[94:97],a[128:131]
ds_read_b64_tr_b16 v[76:77],v155 offset:9984
v_sub_f32 v175,v175,v200
)A"
R"A(MF a[132:135],v[18:21],v[94:97],a[132:135]
v_sub_f32 v176,v176,v200
MF a[136:139],v[22:25],v[94:97],a[136:139]
ds_read_b128 v[114:117],v143 offset:2048
v_sub_f32 v177,v177,v200
MF a[140:143],v[26:29],v[94:97],a[140:143]
MF a[144:147],v[30:33],v[94:97],a[144:147]
ds_read_b128 v[118:121],v143 offset:4096
v_sub_f32 v178,v178,v200
MF a[148:151],v[34:37],v[94:97],a[148:151]
v_sub_f32 v179,v179,v200
MF a[152:155],v[38:41],v[94:97],a[152:155]
ds_read_b128 v[122:125],v143 offset:6144
MF a[156:159],v[42:45],v[94:97],a[156:159]
v_sub_f32 v180,v180,v200
s_waitcnt lgkmcnt(15)
MF a[160:163],v[14:17],v[98:101],a[160:163]
ds_read_b128 v[126:129],v143 offset:8192
v_sub_f32 v181,v181,v200
MF a[164:167],v[18:21],v[98:101],a[164:167]
v_sub_f32 v182,v182,v200
MF a[168:171],v[22:25],v[98:101],a[168:171]
ds_read_b128 v[130:133],v143 offset:10240
MF a[172:175],v[26:29],v[98:101],a[172:175]
v_sub_f32 v183,v183,v200
MF a[176:179],v[30:33],v[98:101],a[176:179]
ds_read_b128 v[134:137],v143 offset:12288
v_sub_f32 v184,v184,v200
MF a[180:183],v[34:37],v[98:101],a[180:183]
MF a[184:187],v[38:41],v[98:101],a[184:187]
ds_read_b128 v[138:141],v143 offset:14336
v_sub_f32 v185,v185,v200
MF a[188:191],v[42:45],v[98:101],a[188:191]
v_sub_f32 v186,v186,v200
MF a[192:195],v[14:17],v[102:105],a[192:195]
MF a[196:199],v[18:21],v[102:105],a[196:199]
v_sub_f32 v187,v187,v200
MF a[200:203],v[22:25],v[102:105],a[200:203]
v_sub_f32 v188,v188,v200
MF a[204:207],v[26:29],v[102:105],a[204:207]
v_sub_f32 v189,v189,v200
MF a[208:211],v[30:33],v[102:105],a[208:211]
MF a[212:215],v[34:37],v[102:105],a[212:215]
v_sub_f32 v190,v190,v200
MF a[216:219],v[38:41],v[102:105],a[216:219]
v_sub_f32 v191,v191,v200
MF a[220:223],v[42:45],v[102:105],a[220:223]
MF a[224:227],v[14:17],v[106:109],a[224:227]
v_sub_f32 v192,v192,v200
MF a[228:231],v[18:21],v[106:109],a[228:231]
v_sub_f32 v193,v193,v200
MF a[232:235],v[22:25],v[106:109],a[232:235]
MF a[236:239],v[26:29],v[106:109],a[236:239]
v_sub_f32 v194,v194,v200
MF a[240:243],v[30:33],v[106:109],a[240:243]
v_sub_f32 v195,v195,v200
MF a[244:247],v[34:37],v[106:109],a[244:247]
v_cndmask_b32_e64 v188,0,v188,s[60:61]
MF a[248:251],v[38:41],v[106:109],a[248:251]
MF a[252:255],v[42:45],v[106:109],a[252:255]
v_cndmask_b32_e64 v189,0,v189,s[60:61]
s_waitcnt vmcnt(0) lgkmcnt(0)
s_barrier
s_add_u32 m0,s57,0
MF a[0:3],v[46:49],v[110:113],a[0:3]
buffer_load_dwordx4 v146,s[36:39],s56 offen lds
MF a[4:7],v[50:53],v[110:113],a[4:7]
ds_read_b64_tr_b16 v[14:15],v156
MF a[8:11],v[54:57],v[110:113],a[8:11]
ds_read_b64_tr_b16 v[16:17],v156 offset:256
MF a[12:15],v[58:61],v[110:113],a[12:15]
s_add_u32 m0,s57,1024
MF a[16:19],v[62:65],v[110:113],a[16:19]
buffer_load_dwordx4 v147,s[36:39],s56 offen lds
MF a[20:23],v[66:69],v[110:113],a[20:23]
ds_read_b128 v[78:81],v144
MF a[24:27],v[70:73],v[110:113],a[24:27]
s_add_u32 m0,s57,2048
MF a[28:31],v[74:77],v[110:113],a[28:31]
buffer_load_dwordx4 v148,s[36:39],s56 offen lds
MF a[32:35],v[46:49],v[114:117],a[32:35]
ds_read_b64_tr_b16 v[18:19],v157
MF a[36:39],v[50:53],v[114:117],a[36:39]
ds_read_b64_tr_b16 v[20:21],v157 offset:256
MF a[40:43],v[54:57],v[114:117],a[40:43]
s_add_u32 m0,s57,3072
MF a[44:47],v[58:61],v[114:117],a[44:47]
buffer_load_dwordx4 v149,s[36:39],s56 offen lds
MF a[48:51],v[62:65],v[114:117],a[48:51]
ds_read_b64_tr_b16 v[22:23],v156 offset:512
MF a[52:55],v[66:69],v[114:117],a[52:55]
s_add_u32 m0,s57,4096
MF a[56:59],v[70:73],v[114:117],a[56:59]
buffer_load_dwordx4 v150,s[36:39],s56 offen lds
MF a[60:63],v[74:77],v[114:117],a[60:63]
ds_read_b64_tr_b16 v[24:25],v156 offset:768
MF a[64:67],v[46:49],v[118:121],a[64:67]
ds_read_b64_tr_b16 v[26:27],v157 offset:512
MF a[68:71],v[50:53],v[118:121],a[68:71]
s_add_u32 m0,s57,5120
MF a[72:75],v[54:57],v[118:121],a[72:75]
buffer_load_dwordx4 v151,s[36:39],s56 offen lds
MF a[76:79],v[58:61],v[118:121],a[76:79]
ds_read_b64_tr_b16 v[28:29],v157 offset:768
MF a[80:83],v[62:65],v[118:121],a[80:83]
s_add_u32 m0,s57,6144
MF a[84:87],v[66:69],v[118:121],a[84:87]
buffer_load_dwordx4 v152,s[36:39],s56 offen lds
MF a[88:91],v[70:73],v[118:121],a[88:91]
ds_read_b64_tr_b16 v[30:31],v156 offset:1024
MF a[92:95],v[74:77],v[118:121],a[92:95]
ds_read_b64_tr_b16 v[32:33],v156 offset:1280
MF a[96:99],v[46:49],v[122:125],a[96:99]
s_add_u32 m0,s57,7168
MF a[100:103],v[50:53],v[122:125],a[100:103]
buffer_load_dwordx4 v153,s[36:39],s56 offen lds
MF a[104:107],v[54:57],v[122:125],a[104:107]
ds_read_b64_tr_b16 v[34:35],v157 offset:1024
MF a[108:111],v[58:61],v[122:125],a[108:111]
s_add_u32 m0,s58,32768
MF a[112:115],v[62:65],v[122:125],a[112:115]
buffer_load_dwordx4 v9,s[40:43],s55 offen lds
MF a[116:119],v[66:69],v[122:125],a[116:119]
ds_read_b64_tr_b16 v[36:37],v157 offset:1280
MF a[120:123],v[70:73],v[122:125],a[120:123]
ds_read_b64_tr_b16 v[38:39],v156 offset:1536
MF a[124:127],v[74:77],v[122:125],a[124:127]
s_add_u32 m0,s58,33792
MF a[128:131],v[46:49],v[126:129],a[128:131]
buffer_load_dwordx4 v10,s[40:43],s55 offen lds
MF a[132:135],v[50:53],v[126:129],a[132:135]
ds_read_b64_tr_b16 v[40:41],v156 offset:1792
MF a[136:139],v[54:57],v[126:129],a[136:139]
s_add_u32 m0,s58,34816
MF a[140:143],v[58:61],v[126:129],a[140:143]
buffer_load_dwordx4 v11,s[40:43],s55 offen lds
MF a[144:147],v[62:65],v[126:129],a[144:147]
ds_read_b64_tr_b16 v[42:43],v157 offset:1536
MF a[148:151],v[66:69],v[126:129],a[148:151]
ds_read_b64_tr_b16 v[44:45],v157 offset:1792
MF a[152:155],v[70:73],v[126:129],a[152:155]
s_add_u32 m0,s58,35840
MF a[156:159],v[74:77],v[126:129],a[156:159]
buffer_load_dwordx4 v12,s[40:43],s55 offen lds
MF a[160:163],v[46:49],v[130:133],a[160:163]
ds_read_b128 v[82:85],v144 offset:2048
MF a[164:167],v[50:53],v[130:133],a[164:167]
s_add_u32 m0,s58,49152
MF a[168:171],v[54:57],v[130:133],a[168:171]
buffer_load_dwordx4 v9,s[44:47],s55 offen lds
MF a[172:175],v[58:61],v[130:133],a[172:175]
ds_read_b128 v[86:89],v144 offset:4096
MF a[176:179],v[62:65],v[130:133],a[176:179]
ds_read_b128 v[90:93],v144 offset:6144
MF a[180:183],v[66:69],v[130:133],a[180:183]
s_add_u32 m0,s58,50176
MF a[184:187],v[70:73],v[130:133],a[184:187]
buffer_load_dwordx4 v10,s[44:47],s55 offen lds
MF a[188:191],v[74:77],v[130:133],a[188:191]
ds_read_b128 v[94:97],v144 offset:8192
MF a[192:195],v[46:49],v[134:137],a[192:195]
s_add_u32 m0,s58,51200
MF a[196:199],v[50:53],v[134:137],a[196:199]
buffer_load_dwordx4 v11,s[44:47],s55 offen lds
MF a[200:203],v[54:57],v[134:137],a[200:203]
ds_read_b128 v[98:101],v144 offset:10240
MF a[204:207],v[58:61],v[134:137],a[204:207]
ds_read_b128 v[102:105],v144 offset:12288
MF a[208:211],v[62:65],v[134:137],a[208:211]
s_add_u32 m0,s58,52224
MF a[212:215],v[66:69],v[134:137],a[212:215]
buffer_load_dwordx4 v12,s[44:47],s55 offen lds
MF a[216:219],v[70:73],v[134:137],a[216:219]
ds_read_b128 v[106:109],v144 offset:14336
MF a[220:223],v[74:77],v[134:137],a[220:223]
MF a[224:227],v[46:49],v[138:141],a[224:227]
MF a[228:231],v[50:53],v[138:141],a[228:231]
MF a[232:235],v[54:57],v[138:141],a[232:235]
MF a[236:239],v[58:61],v[138:141],a[236:239]
MF a[240:243],v[62:65],v[138:141],a[240:243]
MF a[244:247],v[66:69],v[138:141],a[244:247]
MF a[248:251],v[70:73],v[138:141],a[248:251]
MF a[252:255],v[74:77],v[138:141],a[252:255]
s_add_u32 s56,s56,128
s_add_u32 s55,s55,524288
s_waitcnt lgkmcnt(15)
MF a[0:3],v[14:17],v[78:81],a[0:3]
ds_read_b64_tr_b16 v[46:47],v156 offset:8192
v_cndmask_b32_e64 v190,0,v190,s[60:61]
MF a[4:7],v[18:21],v[78:81],a[4:7]
MF a[8:11],v[22:25],v[78:81],a[8:11]
ds_read_b64_tr_b16 v[48:49],v156 offset:8448
v_cndmask_b32_e64 v191,0,v191,s[60:61]
MF a[12:15],v[26:29],v[78:81],a[12:15]
v_cndmask_b32_e64 v192,0,v192,s[60:61]
s_waitcnt lgkmcnt(15)
MF a[16:19],v[30:33],v[78:81],a[16:19]
ds_read_b128 v[110:113],v145
v_cndmask_b32_e64 v193,0,v193,s[60:61]
s_waitcnt lgkmcnt(14)
MF a[20:23],v[34:37],v[78:81],a[20:23]
s_waitcnt lgkmcnt(12)
MF a[24:27],v[38:41],v[78:81],a[24:27]
ds_read_b64_tr_b16 v[50:51],v157 offset:8192
v_cndmask_b32_e64 v194,0,v194,s[60:61]
s_waitcnt lgkmcnt(11)
MF a[28:31],v[42:45],v[78:81],a[28:31]
v_cndmask_b32_e64 v195,0,v195,s[60:61]
s_waitcnt lgkmcnt(10)
MF a[32:35],v[14:17],v[82:85],a[32:35]
ds_read_b64_tr_b16 v[52:53],v157 offset:8448
MF a[36:39],v[18:21],v[82:85],a[36:39]
v_mul_f32 v200,v172,v172
MF a[40:43],v[22:25],v[82:85],a[40:43]
ds_read_b64_tr_b16 v[54:55],v156 offset:8704
v_mul_f32 v201,v173,v173
MF a[44:47],v[26:29],v[82:85],a[44:47]
MF a[48:51],v[30:33],v[82:85],a[48:51]
ds_read_b64_tr_b16 v[56:57],v156 offset:8960
v_fmac_f32 v200,v174,v174
MF a[52:55],v[34:37],v[82:85],a[52:55]
v_fmac_f32 v201,v175,v175
MF a[56:59],v[38:41],v[82:85],a[56:59]
ds_read_b64_tr_b16 v[58:59],v157 offset:8704
v_fmac_f32 v200,v176,v176
MF a[60:63],v[42:45],v[82:85],a[60:63]
s_waitcnt lgkmcnt(13)
MF a[64:67],v[14:17],v[86:89],a[64:67]
ds_read_b64_tr_b16 v[60:61],v157 offset:8960
v_fmac_f32 v201,v177,v177
MF a[68:71],v[18:21],v[86:89],a[68:71]
v_fmac_f32 v200,v178,v178
MF a[72:75],v[22:25],v[86:89],a[72:75]
ds_read_b64_tr_b16 v[62:63],v156 offset:9216
MF a[76:79],v[26:29],v[86:89],a[76:79]
v_fmac_f32 v201,v179,v179
MF a[80:83],v[30:33],v[86:89],a[80:83]
ds_read_b64_tr_b16 v[64:65],v156 offset:9472
v_fmac_f32 v200,v180,v180
MF a[84:87],v[34:37],v[86:89],a[84:87]
MF a[88:91],v[38:41],v[86:89],a[88:91]
ds_read_b64_tr_b16 v[66:67],v157 offset:9216
v_fmac_f32 v201,v181,v181
MF a[92:95],v[42:45],v[86:89],a[92:95]
v_fmac_f32 v200,v182,v182
s_waitcnt lgkmcnt(15)
MF a[96:99],v[14:17],v[90:93],a[96:99]
ds_read_b64_tr_b16 v[68:69],v157 offset:9472
v_fmac_f32 v201,v183,v183
MF a[100:103],v[18:21],v[90:93],a[100:103]
MF a[104:107],v[22:25],v[90:93],a[104:107]
ds_read_b64_tr_b16 v[70:71],v156 offset:9728
v_fmac_f32 v200,v184,v184
MF a[108:111],v[26:29],v[90:93],a[108:111]
v_fmac_f32 v201,v185,v185
MF a[112:115],v[30:33],v[90:93],a[112:115]
ds_read_b64_tr_b16 v[72:73],v156 offset:9984
MF a[116:119],v[34:37],v[90:93],a[116:119]
v_fmac_f32 v200,v186,v186
MF a[120:123],v[38:41],v[90:93],a[120:123]
ds_read_b64_tr_b16 v[74:75],v157 offset:9728
v_fmac_f32 v201,v187,v187
MF a[124:127],v[42:45],v[90:93],a[124:127]
MF a[128:131],v[14:17],v[94:97],a[128:131]
ds_read_b64_tr_b16 v[76:77],v157 offset:9984
v_fmac_f32 v200,v188,v188
MF a[132:135],v[18:21],v[94:97],a[132:135]
v_fmac_f32 v201,v189,v189
MF a[136:139],v[22:25],v[94:97],a[136:139]
ds_read_b128 v[114:117],v145 offset:2048
v_fmac_f32 v200,v190,v190
MF a[140:143],v[26:29],v[94:97],a[140:143]
MF a[144:147],v[30:33],v[94:97],a[144:147]
ds_read_b128 v[118:121],v145 offset:4096
v_fmac_f32 v201,v191,v191
MF a[148:151],v[34:37],v[94:97],a[148:151]
v_fmac_f32 v200,v192,v192
MF a[152:155],v[38:41],v[94:97],a[152:155]
ds_read_b128 v[122:125],v145 offset:6144
MF a[156:159],v[42:45],v[94:97],a[156:159]
v_fmac_f32 v201,v193,v193
s_waitcnt lgkmcnt(15)
MF a[160:163],v[14:17],v[98:101],a[160:163]
ds_read_b128 v[126:129],v145 offset:8192
v_fmac_f32 v200,v194,v194
MF a[164:167],v[18:21],v[98:101],a[164:167]
v_fmac_f32 v201,v195,v195
MF a[168:171],v[22:25],v[98:101],a[168:171]
ds_read_b128 v[130:133],v145 offset:10240
MF a[172:175],v[26:29],v[98:101],a[172:175]
v_add_f32 v200,v200,v201
MF a[176:179],v[30:33],v[98:101],a[176:179]
ds_read_b128 v[134:137],v145 offset:12288
v_add_f32_dpp v200,v200,v200 row_ror:8 row_mask:0xf bank_mask:0xf
MF a[180:183],v[34:37],v[98:101],a[180:183]
MF a[184:187],v[38:41],v[98:101],a[184:187]
ds_read_b128 v[138:141],v145 offset:14336
v_add_f32_dpp v200,v200,v200 row_ror:4 row_mask:0xf bank_mask:0xf
MF a[188:191],v[42:45],v[98:101],a[188:191]
s_nop 0
v_add_f32_dpp v200,v200,v200 row_ror:2 row_mask:0xf bank_mask:0xf
MF a[192:195],v[14:17],v[102:105],a[192:195]
MF a[196:199],v[18:21],v[102:105],a[196:199]
v_add_f32_dpp v200,v200,v200 row_ror:1 row_mask:0xf bank_mask:0xf
MF a[200:203],v[22:25],v[102:105],a[200:203]
v_mov_b32 v201,v200
MF a[204:207],v[26:29],v[102:105],a[204:207]
s_nop 0
v_permlane16_swap_b32 v200,v201
MF a[208:211],v[30:33],v[102:105],a[208:211]
MF a[212:215],v[34:37],v[102:105],a[212:215]
v_add_f32 v200,v200,v201
MF a[216:219],v[38:41],v[102:105],a[216:219]
v_mov_b32 v201,0x3727c5ac
MF a[220:223],v[42:45],v[102:105],a[220:223]
MF a[224:227],v[14:17],v[106:109],a[224:227]
v_fmac_f32 v201,0x3acccccd,v200
MF a[228:231],v[18:21],v[106:109],a[228:231]
v_rsq_f32 v201,v201
MF a[232:235],v[22:25],v[106:109],a[232:235]
MF a[236:239],v[26:29],v[106:109],a[236:239]
v_mul_f32 v201,0x3e0293ee,v201
MF a[240:243],v[30:33],v[106:109],a[240:243]
v_mul_f32 v172,v172,v201
MF a[244:247],v[34:37],v[106:109],a[244:247]
v_mul_f32 v173,v173,v201
MF a[248:251],v[38:41],v[106:109],a[248:251]
MF a[252:255],v[42:45],v[106:109],a[252:255]
v_mul_f32 v174,v174,v201
s_waitcnt vmcnt(0) lgkmcnt(0)
s_barrier
s_add_u32 m0,s57,65536
MF a[0:3],v[46:49],v[110:113],a[0:3]
buffer_load_dwordx4 v146,s[36:39],s56 offen lds
MF a[4:7],v[50:53],v[110:113],a[4:7]
ds_read_b64_tr_b16 v[14:15],v154
MF a[8:11],v[54:57],v[110:113],a[8:11]
ds_read_b64_tr_b16 v[16:17],v154 offset:256
MF a[12:15],v[58:61],v[110:113],a[12:15]
s_add_u32 m0,s57,66560
MF a[16:19],v[62:65],v[110:113],a[16:19]
buffer_load_dwordx4 v147,s[36:39],s56 offen lds
MF a[20:23],v[66:69],v[110:113],a[20:23]
ds_read_b128 v[78:81],v142
MF a[24:27],v[70:73],v[110:113],a[24:27]
s_add_u32 m0,s57,67584
MF a[28:31],v[74:77],v[110:113],a[28:31]
buffer_load_dwordx4 v148,s[36:39],s56 offen lds
MF a[32:35],v[46:49],v[114:117],a[32:35]
ds_read_b64_tr_b16 v[18:19],v155
MF a[36:39],v[50:53],v[114:117],a[36:39]
ds_read_b64_tr_b16 v[20:21],v155 offset:256
MF a[40:43],v[54:57],v[114:117],a[40:43]
s_add_u32 m0,s57,68608
MF a[44:47],v[58:61],v[114:117],a[44:47]
buffer_load_dwordx4 v149,s[36:39],s56 offen lds
MF a[48:51],v[62:65],v[114:117],a[48:51]
ds_read_b64_tr_b16 v[22:23],v154 offset:512
MF a[52:55],v[66:69],v[114:117],a[52:55]
s_add_u32 m0,s57,69632
MF a[56:59],v[70:73],v[114:117],a[56:59]
buffer_load_dwordx4 v150,s[36:39],s56 offen lds
MF a[60:63],v[74:77],v[114:117],a[60:63]
ds_read_b64_tr_b16 v[24:25],v154 offset:768
MF a[64:67],v[46:49],v[118:121],a[64:67]
ds_read_b64_tr_b16 v[26:27],v155 offset:512
MF a[68:71],v[50:53],v[118:121],a[68:71]
s_add_u32 m0,s57,70656
MF a[72:75],v[54:57],v[118:121],a[72:75]
buffer_load_dwordx4 v151,s[36:39],s56 offen lds
MF a[76:79],v[58:61],v[118:121],a[76:79]
ds_read_b64_tr_b16 v[28:29],v155 offset:768
MF a[80:83],v[62:65],v[118:121],a[80:83]
s_add_u32 m0,s57,71680
MF a[84:87],v[66:69],v[118:121],a[84:87]
buffer_load_dwordx4 v152,s[36:39],s56 offen lds
MF a[88:91],v[70:73],v[118:121],a[88:91]
ds_read_b64_tr_b16 v[30:31],v154 offset:1024
MF a[92:95],v[74:77],v[118:121],a[92:95]
ds_read_b64_tr_b16 v[32:33],v154 offset:1280
MF a[96:99],v[46:49],v[122:125],a[96:99]
s_add_u32 m0,s57,72704
MF a[100:103],v[50:53],v[122:125],a[100:103]
buffer_load_dwordx4 v153,s[36:39],s56 offen lds
MF a[104:107],v[54:57],v[122:125],a[104:107]
ds_read_b64_tr_b16 v[34:35],v155 offset:1024
MF a[108:111],v[58:61],v[122:125],a[108:111]
s_add_u32 m0,s58,98304
MF a[112:115],v[62:65],v[122:125],a[112:115]
buffer_load_dwordx4 v9,s[40:43],s55 offen lds
MF a[116:119],v[66:69],v[122:125],a[116:119]
ds_read_b64_tr_b16 v[36:37],v155 offset:1280
MF a[120:123],v[70:73],v[122:125],a[120:123]
ds_read_b64_tr_b16 v[38:39],v154 offset:1536
MF a[124:127],v[74:77],v[122:125],a[124:127]
s_add_u32 m0,s58,99328
MF a[128:131],v[46:49],v[126:129],a[128:131]
buffer_load_dwordx4 v10,s[40:43],s55 offen lds
MF a[132:135],v[50:53],v[126:129],a[132:135]
ds_read_b64_tr_b16 v[40:41],v154 offset:1792
MF a[136:139],v[54:57],v[126:129],a[136:139]
s_add_u32 m0,s58,100352
MF a[140:143],v[58:61],v[126:129],a[140:143]
buffer_load_dwordx4 v11,s[40:43],s55 offen lds
MF a[144:147],v[62:65],v[126:129],a[144:147]
ds_read_b64_tr_b16 v[42:43],v155 offset:1536
MF a[148:151],v[66:69],v[126:129],a[148:151]
ds_read_b64_tr_b16 v[44:45],v155 offset:1792
MF a[152:155],v[70:73],v[126:129],a[152:155]
s_add_u32 m0,s58,101376
MF a[156:159],v[74:77],v[126:129],a[156:159]
buffer_load_dwordx4 v12,s[40:43],s55 offen lds
MF a[160:163],v[46:49],v[130:133],a[160:163]
ds_read_b128 v[82:85],v142 offset:2048
MF a[164:167],v[50:53],v[130:133],a[164:167]
s_add_u32 m0,s58,114688
MF a[168:171],v[54:57],v[130:133],a[168:171]
buffer_load_dwordx4 v9,s[44:47],s55 offen lds
MF a[172:175],v[58:61],v[130:133],a[172:175]
ds_read_b128 v[86:89],v142 offset:4096
MF a[176:179],v[62:65],v[130:133],a[176:179]
ds_read_b128 v[90:93],v142 offset:6144
MF a[180:183],v[66:69],v[130:133],a[180:183]
s_add_u32 m0,s58,115712
MF a[184:187],v[70:73],v[130:133],a[184:187]
buffer_load_dwordx4 v10,s[44:47],s55 offen lds
MF a[188:191],v[74:77],v[130:133],a[188:191]
ds_read_b128 v[94:97],v142 offset:8192
MF a[192:195],v[46:49],v[134:137],a[192:195]
s_add_u32 m0,s58,116736
MF a[196:199],v[50:53],v[134:137],a[196:199]
buffer_load_dwordx4 v11,s[44:47],s55 offen lds
MF a[200:203],v[54:57],v[134:137],a[200:203]
ds_read_b128 v[98:101],v142 offset:10240
MF a[204:207],v[58:61],v[134:137],a[204:207]
ds_read_b128 v[102:105],v142 offset:12288
MF a[208:211],v[62:65],v[134:137],a[208:211]
s_add_u32 m0,s58,117760
MF a[212:215],v[66:69],v[134:137],a[212:215]
buffer_load_dwordx4 v12,s[44:47],s55 offen lds
MF a[216:219],v[70:73],v[134:137],a[216:219]
ds_read_b128 v[106:109],v142 offset:14336
MF a[220:223],v[74:77],v[134:137],a[220:223]
MF a[224:227],v[46:49],v[138:141],a[224:227]
MF a[228:231],v[50:53],v[138:141],a[228:231]
MF a[232:235],v[54:57],v[138:141],a[232:235]
MF a[236:239],v[58:61],v[138:141],a[236:239]
MF a[240:243],v[62:65],v[138:141],a[240:243]
MF a[244:247],v[66:69],v[138:141],a[244:247]
MF a[248:251],v[70:73],v[138:141],a[248:251]
MF a[252:255],v[74:77],v[138:141],a[252:255]
s_add_u32 s56,s56,128
s_add_u32 s55,s55,524288
s_waitcnt lgkmcnt(15)
MF a[0:3],v[14:17],v[78:81],a[0:3]
ds_read_b64_tr_b16 v[46:47],v154 offset:8192
v_mul_f32 v175,v175,v201
MF a[4:7],v[18:21],v[78:81],a[4:7]
MF a[8:11],v[22:25],v[78:81],a[8:11]
ds_read_b64_tr_b16 v[48:49],v154 offset:8448
v_mul_f32 v176,v176,v201
MF a[12:15],v[26:29],v[78:81],a[12:15]
v_mul_f32 v177,v177,v201
s_waitcnt lgkmcnt(15)
MF a[16:19],v[30:33],v[78:81],a[16:19]
ds_read_b128 v[110:113],v143
s_waitcnt lgkmcnt(14)
MF a[20:23],v[34:37],v[78:81],a[20:23]
v_mul_f32 v178,v178,v201
s_waitcnt lgkmcnt(12)
MF a[24:27],v[38:41],v[78:81],a[24:27]
ds_read_b64_tr_b16 v[50:51],v155 offset:8192
v_mul_f32 v179,v179,v201
s_waitcnt lgkmcnt(11)
MF a[28:31],v[42:45],v[78:81],a[28:31]
s_waitcnt lgkmcnt(10)
MF a[32:35],v[14:17],v[82:85],a[32:35]
ds_read_b64_tr_b16 v[52:53],v155 offset:8448
v_mul_f32 v180,v180,v201
MF a[36:39],v[18:21],v[82:85],a[36:39]
v_mul_f32 v181,v181,v201
MF a[40:43],v[22:25],v[82:85],a[40:43]
ds_read_b64_tr_b16 v[54:55],v154 offset:8704
MF a[44:47],v[26:29],v[82:85],a[44:47]
v_mul_f32 v182,v182,v201
MF a[48:51],v[30:33],v[82:85],a[48:51]
ds_read_b64_tr_b16 v[56:57],v154 offset:8960
v_mul_f32 v183,v183,v201
MF a[52:55],v[34:37],v[82:85],a[52:55]
MF a[56:59],v[38:41],v[82:85],a[56:59]
ds_read_b64_tr_b16 v[58:59],v155 offset:8704
v_mul_f32 v184,v184,v201
MF a[60:63],v[42:45],v[82:85],a[60:63]
v_mul_f32 v185,v185,v201
s_waitcnt lgkmcnt(13)
MF a[64:67],v[14:17],v[86:89],a[64:67]
ds_read_b64_tr_b16 v[60:61],v155 offset:8960
MF a[68:71],v[18:21],v[86:89],a[68:71]
v_mul_f32 v186,v186,v201
MF a[72:75],v[22:25],v[86:89],a[72:75]
ds_read_b64_tr_b16 v[62:63],v154 offset:9216
v_mul_f32 v187,v187,v201
MF a[76:79],v[26:29],v[86:89],a[76:79]
MF a[80:83],v[30:33],v[86:89],a[80:83]
ds_read_b64_tr_b16 v[64:65],v154 offset:9472
v_mul_f32 v188,v188,v201
MF a[84:87],v[34:37],v[86:89],a[84:87]
v_mul_f32 v189,v189,v201
MF a[88:91],v[38:41],v[86:89],a[88:91]
ds_read_b64_tr_b16 v[66:67],v155 offset:9216
MF a[92:95],v[42:45],v[86:89],a[92:95]
v_mul_f32 v190,v190,v201
s_waitcnt lgkmcnt(15)
MF a[96:99],v[14:17],v[90:93],a[96:99]
ds_read_b64_tr_b16 v[68:69],v155 offset:9472
v_mul_f32 v191,v191,v201
MF a[100:103],v[18:21],v[90:93],a[100:103]
MF a[104:107],v[22:25],v[90:93],a[104:107]
ds_read_b64_tr_b16 v[70:71],v154 offset:9728
v_mul_f32 v192,v192,v201
MF a[108:111],v[26:29],v[90:93],a[108:111]
v_mul_f32 v193,v193,v201
MF a[112:115],v[30:33],v[90:93],a[112:115]
ds_read_b64_tr_b16 v[72:73],v154 offset:9984
MF a[116:119],v[34:37],v[90:93],a[116:119]
v_mul_f32 v194,v194,v201
MF a[120:123],v[38:41],v[90:93],a[120:123]
ds_read_b64_tr_b16 v[74:75],v155 offset:9728
v_mul_f32 v195,v195,v201
MF a[124:127],v[42:45],v[90:93],a[124:127]
MF a[128:131],v[14:17],v[94:97],a[128:131]
ds_read_b64_tr_b16 v[76:77],v155 offset:9984
CP v160,v172,v173
MF a[132:135],v[18:21],v[94:97],a[132:135]
CP v161,v174,v175
MF a[136:139],v[22:25],v[94:97],a[136:139]
ds_read_b128 v[114:117],v143 offset:2048
CP v162,v176,v177
MF a[140:143],v[26:29],v[94:97],a[140:143]
MF a[144:147],v[30:33],v[94:97],a[144:147]
ds_read_b128 v[118:121],v143 offset:4096
CP v163,v178,v179
MF a[148:151],v[34:37],v[94:97],a[148:151]
CP v164,v180,v181
MF a[152:155],v[38:41],v[94:97],a[152:155]
ds_read_b128 v[122:125],v143 offset:6144
MF a[156:159],v[42:45],v[94:97],a[156:159]
CP v165,v182,v183
s_waitcnt lgkmcnt(15)
MF a[160:163],v[14:17],v[98:101],a[160:163]
ds_read_b128 v[126:129],v143 offset:8192
CP v166,v184,v185
MF a[164:167],v[18:21],v[98:101],a[164:167]
MF a[168:171],v[22:25],v[98:101],a[168:171]
ds_read_b128 v[130:133],v143 offset:10240
CP v167,v186,v187
MF a[172:175],v[26:29],v[98:101],a[172:175]
CP v168,v188,v189
MF a[176:179],v[30:33],v[98:101],a[176:179]
ds_read_b128 v[134:137],v143 offset:12288
MF a[180:183],v[34:37],v[98:101],a[180:183]
CP v169,v190,v191
MF a[184:187],v[38:41],v[98:101],a[184:187]
ds_read_b128 v[138:141],v143 offset:14336
CP v170,v192,v193
MF a[188:191],v[42:45],v[98:101],a[188:191]
MF a[192:195],v[14:17],v[102:105],a[192:195]
CP v171,v194,v195
MF a[196:199],v[18:21],v[102:105],a[196:199]
v_lshrrev_b32 v202,4,v198
MF a[200:203],v[22:25],v[102:105],a[200:203]
MF a[204:207],v[26:29],v[102:105],a[204:207]
v_mul_u32_u24 v197,20480,v202
MF a[208:211],v[30:33],v[102:105],a[208:211]
v_lshrrev_b32 v202,2,v199
MF a[212:215],v[34:37],v[102:105],a[212:215]
MF a[216:219],v[38:41],v[102:105],a[216:219]
v_lshl_add_u32 v197,v202,10,v197
MF a[220:223],v[42:45],v[102:105],a[220:223]
v_and_b32 v202,3,v199
MF a[224:227],v[14:17],v[106:109],a[224:227]
MF a[228:231],v[18:21],v[106:109],a[228:231]
v_lshl_add_u32 v197,v202,8,v197
MF a[232:235],v[22:25],v[106:109],a[232:235]
v_and_b32 v202,15,v198
MF a[236:239],v[26:29],v[106:109],a[236:239]
MF a[240:243],v[30:33],v[106:109],a[240:243]
v_lshl_add_u32 v197,v202,4,v197
MF a[244:247],v[34:37],v[106:109],a[244:247]
v_mov_b32 v202,0x7ffffff0
MF a[248:251],v[38:41],v[106:109],a[248:251]
MF a[252:255],v[42:45],v[106:109],a[252:255]
v_cndmask_b32_e64 v203,v202,v197,s[60:61]
s_waitcnt vmcnt(0) lgkmcnt(0)
s_barrier
s_add_u32 m0,s57,0
MF a[0:3],v[46:49],v[110:113],a[0:3]
buffer_load_dwordx4 v146,s[36:39],s56 offen lds
MF a[4:7],v[50:53],v[110:113],a[4:7]
ds_read_b64_tr_b16 v[14:15],v156
MF a[8:11],v[54:57],v[110:113],a[8:11]
ds_read_b64_tr_b16 v[16:17],v156 offset:256
MF a[12:15],v[58:61],v[110:113],a[12:15]
s_add_u32 m0,s57,1024
MF a[16:19],v[62:65],v[110:113],a[16:19]
buffer_load_dwordx4 v147,s[36:39],s56 offen lds
MF a[20:23],v[66:69],v[110:113],a[20:23]
ds_read_b128 v[78:81],v144
MF a[24:27],v[70:73],v[110:113],a[24:27]
s_add_u32 m0,s57,2048
MF a[28:31],v[74:77],v[110:113],a[28:31]
buffer_load_dwordx4 v148,s[36:39],s56 offen lds
MF a[32:35],v[46:49],v[114:117],a[32:35]
ds_read_b64_tr_b16 v[18:19],v157
MF a[36:39],v[50:53],v[114:117],a[36:39]
ds_read_b64_tr_b16 v[20:21],v157 offset:256
MF a[40:43],v[54:57],v[114:117],a[40:43]
s_add_u32 m0,s57,3072
MF a[44:47],v[58:61],v[114:117],a[44:47]
buffer_load_dwordx4 v149,s[36:39],s56 offen lds
MF a[48:51],v[62:65],v[114:117],a[48:51]
ds_read_b64_tr_b16 v[22:23],v156 offset:512
MF a[52:55],v[66:69],v[114:117],a[52:55]
s_add_u32 m0,s57,4096
MF a[56:59],v[70:73],v[114:117],a[56:59]
buffer_load_dwordx4 v150,s[36:39],s56 offen lds
MF a[60:63],v[74:77],v[114:117],a[60:63]
ds_read_b64_tr_b16 v[24:25],v156 offset:768
MF a[64:67],v[46:49],v[118:121],a[64:67]
ds_read_b64_tr_b16 v[26:27],v157 offset:512
MF a[68:71],v[50:53],v[118:121],a[68:71]
s_add_u32 m0,s57,5120
MF a[72:75],v[54:57],v[118:121],a[72:75]
buffer_load_dwordx4 v151,s[36:39],s56 offen lds
MF a[76:79],v[58:61],v[118:121],a[76:79]
ds_read_b64_tr_b16 v[28:29],v157 offset:768
MF a[80:83],v[62:65],v[118:121],a[80:83]
s_add_u32 m0,s57,6144
MF a[84:87],v[66:69],v[118:121],a[84:87]
buffer_load_dwordx4 v152,s[36:39],s56 offen lds
MF a[88:91],v[70:73],v[118:121],a[88:91]
ds_read_b64_tr_b16 v[30:31],v156 offset:1024
MF a[92:95],v[74:77],v[118:121],a[92:95]
ds_read_b64_tr_b16 v[32:33],v156 offset:1280
MF a[96:99],v[46:49],v[122:125],a[96:99]
s_add_u32 m0,s57,7168
MF a[100:103],v[50:53],v[122:125],a[100:103]
buffer_load_dwordx4 v153,s[36:39],s56 offen lds
MF a[104:107],v[54:57],v[122:125],a[104:107]
ds_read_b64_tr_b16 v[34:35],v157 offset:1024
MF a[108:111],v[58:61],v[122:125],a[108:111]
s_add_u32 m0,s58,32768
MF a[112:115],v[62:65],v[122:125],a[112:115]
buffer_load_dwordx4 v9,s[40:43],s55 offen lds
MF a[116:119],v[66:69],v[122:125],a[116:119]
ds_read_b64_tr_b16 v[36:37],v157 offset:1280
MF a[120:123],v[70:73],v[122:125],a[120:123]
ds_read_b64_tr_b16 v[38:39],v156 offset:1536
MF a[124:127],v[74:77],v[122:125],a[124:127]
s_add_u32 m0,s58,33792
MF a[128:131],v[46:49],v[126:129],a[128:131]
buffer_load_dwordx4 v10,s[40:43],s55 offen lds
MF a[132:135],v[50:53],v[126:129],a[132:135]
ds_read_b64_tr_b16 v[40:41],v156 offset:1792
MF a[136:139],v[54:57],v[126:129],a[136:139]
s_add_u32 m0,s58,34816
MF a[140:143],v[58:61],v[126:129],a[140:143]
buffer_load_dwordx4 v11,s[40:43],s55 offen lds
MF a[144:147],v[62:65],v[126:129],a[144:147]
ds_read_b64_tr_b16 v[42:43],v157 offset:1536
MF a[148:151],v[66:69],v[126:129],a[148:151]
ds_read_b64_tr_b16 v[44:45],v157 offset:1792
MF a[152:155],v[70:73],v[126:129],a[152:155]
s_add_u32 m0,s58,35840
MF a[156:159],v[74:77],v[126:129],a[156:159]
buffer_load_dwordx4 v12,s[40:43],s55 offen lds
MF a[160:163],v[46:49],v[130:133],a[160:163]
ds_read_b128 v[82:85],v144 offset:2048
MF a[164:167],v[50:53],v[130:133],a[164:167]
s_add_u32 m0,s58,49152
MF a[168:171],v[54:57],v[130:133],a[168:171]
buffer_load_dwordx4 v9,s[44:47],s55 offen lds
MF a[172:175],v[58:61],v[130:133],a[172:175]
ds_read_b128 v[86:89],v144 offset:4096
MF a[176:179],v[62:65],v[130:133],a[176:179]
ds_read_b128 v[90:93],v144 offset:6144
MF a[180:183],v[66:69],v[130:133],a[180:183]
s_add_u32 m0,s58,50176
MF a[184:187],v[70:73],v[130:133],a[184:187]
buffer_load_dwordx4 v10,s[44:47],s55 offen lds
MF a[188:191],v[74:77],v[130:133],a[188:191]
ds_read_b128 v[94:97],v144 offset:8192
MF a[192:195],v[46:49],v[134:137],a[192:195]
s_add_u32 m0,s58,51200
MF a[196:199],v[50:53],v[134:137],a[196:199]
buffer_load_dwordx4 v11,s[44:47],s55 offen lds
MF a[200:203],v[54:57],v[134:137],a[200:203]
ds_read_b128 v[98:101],v144 offset:10240
MF a[204:207],v[58:61],v[134:137],a[204:207]
ds_read_b128 v[102:105],v144 offset:12288
MF a[208:211],v[62:65],v[134:137],a[208:211]
s_add_u32 m0,s58,52224
MF a[212:215],v[66:69],v[134:137],a[212:215]
buffer_load_dwordx4 v12,s[44:47],s55 offen lds
MF a[216:219],v[70:73],v[134:137],a[216:219]
ds_read_b128 v[106:109],v144 offset:14336
MF a[220:223],v[74:77],v[134:137],a[220:223]
MF a[224:227],v[46:49],v[138:141],a[224:227]
MF a[228:231],v[50:53],v[138:141],a[228:231]
MF a[232:235],v[54:57],v[138:141],a[232:235]
MF a[236:239],v[58:61],v[138:141],a[236:239]
MF a[240:243],v[62:65],v[138:141],a[240:243]
MF a[244:247],v[66:69],v[138:141],a[244:247]
MF a[248:251],v[70:73],v[138:141],a[248:251]
MF a[252:255],v[74:77],v[138:141],a[252:255]
s_add_u32 s56,s56,128
s_add_u32 s55,s55,524288
s_waitcnt lgkmcnt(15)
MF a[0:3],v[14:17],v[78:81],a[0:3]
ds_read_b64_tr_b16 v[46:47],v156 offset:8192
MF a[4:7],v[18:21],v[78:81],a[4:7]
MF a[8:11],v[22:25],v[78:81],a[8:11]
ds_read_b64_tr_b16 v[48:49],v156 offset:8448
MF a[12:15],v[26:29],v[78:81],a[12:15]
s_waitcnt lgkmcnt(15)
MF a[16:19],v[30:33],v[78:81],a[16:19]
ds_read_b128 v[110:113],v145
s_waitcnt lgkmcnt(14)
MF a[20:23],v[34:37],v[78:81],a[20:23]
s_waitcnt lgkmcnt(12)
MF a[24:27],v[38:41],v[78:81],a[24:27]
ds_read_b64_tr_b16 v[50:51],v157 offset:8192
s_waitcnt lgkmcnt(11)
MF a[28:31],v[42:45],v[78:81],a[28:31]
s_waitcnt lgkmcnt(10)
MF a[32:35],v[14:17],v[82:85],a[32:35]
ds_read_b64_tr_b16 v[52:53],v157 offset:8448
MF a[36:39],v[18:21],v[82:85],a[36:39]
MF a[40:43],v[22:25],v[82:85],a[40:43]
ds_read_b64_tr_b16 v[54:55],v156 offset:8704
MF a[44:47],v[26:29],v[82:85],a[44:47]
MF a[48:51],v[30:33],v[82:85],a[48:51]
ds_read_b64_tr_b16 v[56:57],v156 offset:8960
MF a[52:55],v[34:37],v[82:85],a[52:55]
MF a[56:59],v[38:41],v[82:85],a[56:59]
ds_read_b64_tr_b16 v[58:59],v157 offset:8704
MF a[60:63],v[42:45],v[82:85],a[60:63]
s_waitcnt lgkmcnt(13)
MF a[64:67],v[14:17],v[86:89],a[64:67]
ds_read_b64_tr_b16 v[60:61],v157 offset:8960
MF a[68:71],v[18:21],v[86:89],a[68:71]
MF a[72:75],v[22:25],v[86:89],a[72:75]
ds_read_b64_tr_b16 v[62:63],v156 offset:9216
MF a[76:79],v[26:29],v[86:89],a[76:79]
MF a[80:83],v[30:33],v[86:89],a[80:83]
ds_read_b64_tr_b16 v[64:65],v156 offset:9472
MF a[84:87],v[34:37],v[86:89],a[84:87]
MF a[88:91],v[38:41],v[86:89],a[88:91]
ds_read_b64_tr_b16 v[66:67],v157 offset:9216
MF a[92:95],v[42:45],v[86:89],a[92:95]
s_waitcnt lgkmcnt(15)
MF a[96:99],v[14:17],v[90:93],a[96:99]
ds_read_b64_tr_b16 v[68:69],v157 offset:9472
MF a[100:103],v[18:21],v[90:93],a[100:103]
MF a[104:107],v[22:25],v[90:93],a[104:107]
ds_read_b64_tr_b16 v[70:71],v156 offset:9728
MF a[108:111],v[26:29],v[90:93],a[108:111]
MF a[112:115],v[30:33],v[90:93],a[112:115]
ds_read_b64_tr_b16 v[72:73],v156 offset:9984
MF a[116:119],v[34:37],v[90:93],a[116:119]
MF a[120:123],v[38:41],v[90:93],a[120:123]
ds_read_b64_tr_b16 v[74:75],v157 offset:9728
MF a[124:127],v[42:45],v[90:93],a[124:127]
MF a[128:131],v[14:17],v[94:97],a[128:131]
ds_read_b64_tr_b16 v[76:77],v157 offset:9984
MF a[132:135],v[18:21],v[94:97],a[132:135]
MF a[136:139],v[22:25],v[94:97],a[136:139]
ds_read_b128 v[114:117],v145 offset:2048
MF a[140:143],v[26:29],v[94:97],a[140:143]
MF a[144:147],v[30:33],v[94:97],a[144:147]
ds_read_b128 v[118:121],v145 offset:4096
MF a[148:151],v[34:37],v[94:97],a[148:151]
)A"
R"A(MF a[152:155],v[38:41],v[94:97],a[152:155]
ds_read_b128 v[122:125],v145 offset:6144
MF a[156:159],v[42:45],v[94:97],a[156:159]
s_waitcnt lgkmcnt(15)
MF a[160:163],v[14:17],v[98:101],a[160:163]
ds_read_b128 v[126:129],v145 offset:8192
MF a[164:167],v[18:21],v[98:101],a[164:167]
MF a[168:171],v[22:25],v[98:101],a[168:171]
ds_read_b128 v[130:133],v145 offset:10240
MF a[172:175],v[26:29],v[98:101],a[172:175]
MF a[176:179],v[30:33],v[98:101],a[176:179]
ds_read_b128 v[134:137],v145 offset:12288
MF a[180:183],v[34:37],v[98:101],a[180:183]
MF a[184:187],v[38:41],v[98:101],a[184:187]
ds_read_b128 v[138:141],v145 offset:14336
MF a[188:191],v[42:45],v[98:101],a[188:191]
MF a[192:195],v[14:17],v[102:105],a[192:195]
MF a[196:199],v[18:21],v[102:105],a[196:199]
MF a[200:203],v[22:25],v[102:105],a[200:203]
MF a[204:207],v[26:29],v[102:105],a[204:207]
MF a[208:211],v[30:33],v[102:105],a[208:211]
MF a[212:215],v[34:37],v[102:105],a[212:215]
MF a[216:219],v[38:41],v[102:105],a[216:219]
MF a[220:223],v[42:45],v[102:105],a[220:223]
MF a[224:227],v[14:17],v[106:109],a[224:227]
MF a[228:231],v[18:21],v[106:109],a[228:231]
MF a[232:235],v[22:25],v[106:109],a[232:235]
MF a[236:239],v[26:29],v[106:109],a[236:239]
MF a[240:243],v[30:33],v[106:109],a[240:243]
MF a[244:247],v[34:37],v[106:109],a[244:247]
MF a[248:251],v[38:41],v[106:109],a[248:251]
MF a[252:255],v[42:45],v[106:109],a[252:255]
s_waitcnt vmcnt(0) lgkmcnt(0)
s_barrier
MF a[0:3],v[46:49],v[110:113],a[0:3]
buffer_store_dwordx4 v[160:163],v197,s[64:67],0 offen
MF a[4:7],v[50:53],v[110:113],a[4:7]
ds_read_b64_tr_b16 v[14:15],v154
MF a[8:11],v[54:57],v[110:113],a[8:11]
ds_read_b64_tr_b16 v[16:17],v154 offset:256
MF a[12:15],v[58:61],v[110:113],a[12:15]
buffer_store_dwordx4 v[164:167],v197,s[64:67],s62 offen
MF a[16:19],v[62:65],v[110:113],a[16:19]
MF a[20:23],v[66:69],v[110:113],a[20:23]
ds_read_b128 v[78:81],v142
MF a[24:27],v[70:73],v[110:113],a[24:27]
buffer_store_dwordx4 v[168:171],v203,s[64:67],s63 offen
MF a[28:31],v[74:77],v[110:113],a[28:31]
ds_read_b64_tr_b16 v[18:19],v155
MF a[32:35],v[46:49],v[114:117],a[32:35]
s_add_u32 m0,s57,65536
MF a[36:39],v[50:53],v[114:117],a[36:39]
buffer_load_dwordx4 v146,s[36:39],s56 offen lds
MF a[40:43],v[54:57],v[114:117],a[40:43]
ds_read_b64_tr_b16 v[20:21],v155 offset:256
MF a[44:47],v[58:61],v[114:117],a[44:47]
ds_read_b64_tr_b16 v[22:23],v154 offset:512
MF a[48:51],v[62:65],v[114:117],a[48:51]
s_add_u32 m0,s57,66560
MF a[52:55],v[66:69],v[114:117],a[52:55]
buffer_load_dwordx4 v147,s[36:39],s56 offen lds
MF a[56:59],v[70:73],v[114:117],a[56:59]
ds_read_b64_tr_b16 v[24:25],v154 offset:768
s_add_u32 m0,s57,67584
MF a[60:63],v[74:77],v[114:117],a[60:63]
buffer_load_dwordx4 v148,s[36:39],s56 offen lds
MF a[64:67],v[46:49],v[118:121],a[64:67]
ds_read_b64_tr_b16 v[26:27],v155 offset:512
MF a[68:71],v[50:53],v[118:121],a[68:71]
s_add_u32 m0,s57,68608
MF a[72:75],v[54:57],v[118:121],a[72:75]
buffer_load_dwordx4 v149,s[36:39],s56 offen lds
MF a[76:79],v[58:61],v[118:121],a[76:79]
ds_read_b64_tr_b16 v[28:29],v155 offset:768
s_add_u32 m0,s57,69632
MF a[80:83],v[62:65],v[118:121],a[80:83]
buffer_load_dwordx4 v150,s[36:39],s56 offen lds
MF a[84:87],v[66:69],v[118:121],a[84:87]
MF a[88:91],v[70:73],v[118:121],a[88:91]
ds_read_b64_tr_b16 v[30:31],v154 offset:1024
MF a[92:95],v[74:77],v[118:121],a[92:95]
ds_read_b64_tr_b16 v[32:33],v154 offset:1280
s_add_u32 m0,s57,70656
MF a[96:99],v[46:49],v[122:125],a[96:99]
buffer_load_dwordx4 v151,s[36:39],s56 offen lds
MF a[100:103],v[50:53],v[122:125],a[100:103]
MF a[104:107],v[54:57],v[122:125],a[104:107]
ds_read_b64_tr_b16 v[34:35],v155 offset:1024
s_add_u32 m0,s57,71680
MF a[108:111],v[58:61],v[122:125],a[108:111]
buffer_load_dwordx4 v152,s[36:39],s56 offen lds
MF a[112:115],v[62:65],v[122:125],a[112:115]
ds_read_b64_tr_b16 v[36:37],v155 offset:1280
s_add_u32 m0,s57,72704
MF a[116:119],v[66:69],v[122:125],a[116:119]
buffer_load_dwordx4 v153,s[36:39],s56 offen lds
MF a[120:123],v[70:73],v[122:125],a[120:123]
MF a[124:127],v[74:77],v[122:125],a[124:127]
ds_read_b64_tr_b16 v[38:39],v154 offset:1536
s_add_u32 m0,s58,98304
MF a[128:131],v[46:49],v[126:129],a[128:131]
buffer_load_dwordx4 v9,s[40:43],s55 offen lds
MF a[132:135],v[50:53],v[126:129],a[132:135]
ds_read_b64_tr_b16 v[40:41],v154 offset:1792
MF a[136:139],v[54:57],v[126:129],a[136:139]
MF a[140:143],v[58:61],v[126:129],a[140:143]
ds_read_b64_tr_b16 v[42:43],v155 offset:1536
s_add_u32 m0,s58,99328
MF a[144:147],v[62:65],v[126:129],a[144:147]
buffer_load_dwordx4 v10,s[40:43],s55 offen lds
MF a[148:151],v[66:69],v[126:129],a[148:151]
ds_read_b64_tr_b16 v[44:45],v155 offset:1792
MF a[152:155],v[70:73],v[126:129],a[152:155]
s_add_u32 m0,s58,100352
MF a[156:159],v[74:77],v[126:129],a[156:159]
buffer_load_dwordx4 v11,s[40:43],s55 offen lds
MF a[160:163],v[46:49],v[130:133],a[160:163]
ds_read_b128 v[82:85],v142 offset:2048
s_add_u32 m0,s58,101376
MF a[164:167],v[50:53],v[130:133],a[164:167]
buffer_load_dwordx4 v12,s[40:43],s55 offen lds
MF a[168:171],v[54:57],v[130:133],a[168:171]
ds_read_b128 v[86:89],v142 offset:4096
MF a[172:175],v[58:61],v[130:133],a[172:175]
s_add_u32 m0,s58,114688
MF a[176:179],v[62:65],v[130:133],a[176:179]
buffer_load_dwordx4 v9,s[44:47],s55 offen lds
MF a[180:183],v[66:69],v[130:133],a[180:183]
ds_read_b128 v[90:93],v142 offset:6144
MF a[184:187],v[70:73],v[130:133],a[184:187]
ds_read_b128 v[94:97],v142 offset:8192
MF a[188:191],v[74:77],v[130:133],a[188:191]
s_add_u32 m0,s58,115712
MF a[192:195],v[46:49],v[134:137],a[192:195]
buffer_load_dwordx4 v10,s[44:47],s55 offen lds
MF a[196:199],v[50:53],v[134:137],a[196:199]
ds_read_b128 v[98:101],v142 offset:10240
s_add_u32 m0,s58,116736
MF a[200:203],v[54:57],v[134:137],a[200:203]
buffer_load_dwordx4 v11,s[44:47],s55 offen lds
MF a[204:207],v[58:61],v[134:137],a[204:207]
MF a[208:211],v[62:65],v[134:137],a[208:211]
ds_read_b128 v[102:105],v142 offset:12288
s_add_u32 m0,s58,117760
MF a[212:215],v[66:69],v[134:137],a[212:215]
buffer_load_dwordx4 v12,s[44:47],s55 offen lds
MF a[216:219],v[70:73],v[134:137],a[216:219]
ds_read_b128 v[106:109],v142 offset:14336
MF a[220:223],v[74:77],v[134:137],a[220:223]
MF a[224:227],v[46:49],v[138:141],a[224:227]
MF a[228:231],v[50:53],v[138:141],a[228:231]
MF a[232:235],v[54:57],v[138:141],a[232:235]
MF a[236:239],v[58:61],v[138:141],a[236:239]
MF a[240:243],v[62:65],v[138:141],a[240:243]
MF a[244:247],v[66:69],v[138:141],a[244:247]
MF a[248:251],v[70:73],v[138:141],a[248:251]
MF a[252:255],v[74:77],v[138:141],a[252:255]
s_add_u32 s56,s56,128
s_add_u32 s55,s55,524288
s_mov_b32 m0,s72
s_mov_b32 s68,s30
s_and_b32 s69,s31,0xffff
s_mov_b32 s70,5242880
s_mov_b32 s71,0x20000
buffer_load_dword v204,s[68:71],0 offen lds
s_mov_b32 s68,s32
s_and_b32 s69,s33,0xffff
s_mov_b32 s70,2211840
s_mov_b32 s71,0x20000
buffer_load_dword v204,s[68:71],0 offen lds
s_getpc_b64 s[68:69]
LPCK:
s_add_u32 s68,s68,k2-LPCK
s_addc_u32 s69,s69,0
s_lshr_b32 s100,s6,3
s_lshl_b32 s100,s100,8
v_add_u32 v207,s100,v0
v_min_u32 v207,(LK3END-k2)/128,v207
v_lshlrev_b32 v207,7,v207
global_load_lds_dword v207,s[68:69]
s_lshl_b32 s100,s6,8
v_add_u32 v208,s100,v0
v_min_u32 v208,20000,v208
v_lshlrev_b32 v208,12,v208
global_load_lds_dword v208,s[22:23]
s_waitcnt lgkmcnt(15)
MF a[0:3],v[14:17],v[78:81],a[0:3]
ds_read_b64_tr_b16 v[46:47],v154 offset:8192
MF a[4:7],v[18:21],v[78:81],a[4:7]
MF a[8:11],v[22:25],v[78:81],a[8:11]
ds_read_b64_tr_b16 v[48:49],v154 offset:8448
MF a[12:15],v[26:29],v[78:81],a[12:15]
s_waitcnt lgkmcnt(15)
MF a[16:19],v[30:33],v[78:81],a[16:19]
ds_read_b128 v[110:113],v143
s_waitcnt lgkmcnt(14)
MF a[20:23],v[34:37],v[78:81],a[20:23]
s_waitcnt lgkmcnt(12)
MF a[24:27],v[38:41],v[78:81],a[24:27]
ds_read_b64_tr_b16 v[50:51],v155 offset:8192
s_waitcnt lgkmcnt(11)
MF a[28:31],v[42:45],v[78:81],a[28:31]
s_waitcnt lgkmcnt(10)
MF a[32:35],v[14:17],v[82:85],a[32:35]
ds_read_b64_tr_b16 v[52:53],v155 offset:8448
MF a[36:39],v[18:21],v[82:85],a[36:39]
MF a[40:43],v[22:25],v[82:85],a[40:43]
ds_read_b64_tr_b16 v[54:55],v154 offset:8704
MF a[44:47],v[26:29],v[82:85],a[44:47]
MF a[48:51],v[30:33],v[82:85],a[48:51]
ds_read_b64_tr_b16 v[56:57],v154 offset:8960
MF a[52:55],v[34:37],v[82:85],a[52:55]
MF a[56:59],v[38:41],v[82:85],a[56:59]
ds_read_b64_tr_b16 v[58:59],v155 offset:8704
MF a[60:63],v[42:45],v[82:85],a[60:63]
s_waitcnt lgkmcnt(13)
MF a[64:67],v[14:17],v[86:89],a[64:67]
ds_read_b64_tr_b16 v[60:61],v155 offset:8960
MF a[68:71],v[18:21],v[86:89],a[68:71]
MF a[72:75],v[22:25],v[86:89],a[72:75]
ds_read_b64_tr_b16 v[62:63],v154 offset:9216
MF a[76:79],v[26:29],v[86:89],a[76:79]
MF a[80:83],v[30:33],v[86:89],a[80:83]
ds_read_b64_tr_b16 v[64:65],v154 offset:9472
MF a[84:87],v[34:37],v[86:89],a[84:87]
MF a[88:91],v[38:41],v[86:89],a[88:91]
ds_read_b64_tr_b16 v[66:67],v155 offset:9216
MF a[92:95],v[42:45],v[86:89],a[92:95]
s_waitcnt lgkmcnt(15)
MF a[96:99],v[14:17],v[90:93],a[96:99]
ds_read_b64_tr_b16 v[68:69],v155 offset:9472
MF a[100:103],v[18:21],v[90:93],a[100:103]
MF a[104:107],v[22:25],v[90:93],a[104:107]
ds_read_b64_tr_b16 v[70:71],v154 offset:9728
MF a[108:111],v[26:29],v[90:93],a[108:111]
MF a[112:115],v[30:33],v[90:93],a[112:115]
ds_read_b64_tr_b16 v[72:73],v154 offset:9984
MF a[116:119],v[34:37],v[90:93],a[116:119]
MF a[120:123],v[38:41],v[90:93],a[120:123]
ds_read_b64_tr_b16 v[74:75],v155 offset:9728
MF a[124:127],v[42:45],v[90:93],a[124:127]
MF a[128:131],v[14:17],v[94:97],a[128:131]
ds_read_b64_tr_b16 v[76:77],v155 offset:9984
MF a[132:135],v[18:21],v[94:97],a[132:135]
MF a[136:139],v[22:25],v[94:97],a[136:139]
ds_read_b128 v[114:117],v143 offset:2048
MF a[140:143],v[26:29],v[94:97],a[140:143]
MF a[144:147],v[30:33],v[94:97],a[144:147]
ds_read_b128 v[118:121],v143 offset:4096
MF a[148:151],v[34:37],v[94:97],a[148:151]
MF a[152:155],v[38:41],v[94:97],a[152:155]
ds_read_b128 v[122:125],v143 offset:6144
MF a[156:159],v[42:45],v[94:97],a[156:159]
s_waitcnt lgkmcnt(15)
MF a[160:163],v[14:17],v[98:101],a[160:163]
ds_read_b128 v[126:129],v143 offset:8192
MF a[164:167],v[18:21],v[98:101],a[164:167]
MF a[168:171],v[22:25],v[98:101],a[168:171]
ds_read_b128 v[130:133],v143 offset:10240
MF a[172:175],v[26:29],v[98:101],a[172:175]
MF a[176:179],v[30:33],v[98:101],a[176:179]
ds_read_b128 v[134:137],v143 offset:12288
MF a[180:183],v[34:37],v[98:101],a[180:183]
MF a[184:187],v[38:41],v[98:101],a[184:187]
ds_read_b128 v[138:141],v143 offset:14336
MF a[188:191],v[42:45],v[98:101],a[188:191]
MF a[192:195],v[14:17],v[102:105],a[192:195]
MF a[196:199],v[18:21],v[102:105],a[196:199]
MF a[200:203],v[22:25],v[102:105],a[200:203]
MF a[204:207],v[26:29],v[102:105],a[204:207]
MF a[208:211],v[30:33],v[102:105],a[208:211]
MF a[212:215],v[34:37],v[102:105],a[212:215]
MF a[216:219],v[38:41],v[102:105],a[216:219]
MF a[220:223],v[42:45],v[102:105],a[220:223]
MF a[224:227],v[14:17],v[106:109],a[224:227]
MF a[228:231],v[18:21],v[106:109],a[228:231]
MF a[232:235],v[22:25],v[106:109],a[232:235]
MF a[236:239],v[26:29],v[106:109],a[236:239]
MF a[240:243],v[30:33],v[106:109],a[240:243]
MF a[244:247],v[34:37],v[106:109],a[244:247]
MF a[248:251],v[38:41],v[106:109],a[248:251]
MF a[252:255],v[42:45],v[106:109],a[252:255]
s_waitcnt vmcnt(4) lgkmcnt(0)
s_barrier
MF a[0:3],v[46:49],v[110:113],a[0:3]
ds_read_b64_tr_b16 v[14:15],v156
MF a[4:7],v[50:53],v[110:113],a[4:7]
MF a[8:11],v[54:57],v[110:113],a[8:11]
ds_read_b64_tr_b16 v[16:17],v156 offset:256
MF a[12:15],v[58:61],v[110:113],a[12:15]
MF a[16:19],v[62:65],v[110:113],a[16:19]
ds_read_b128 v[78:81],v144
MF a[20:23],v[66:69],v[110:113],a[20:23]
MF a[24:27],v[70:73],v[110:113],a[24:27]
MF a[28:31],v[74:77],v[110:113],a[28:31]
ds_read_b64_tr_b16 v[18:19],v157
MF a[32:35],v[46:49],v[114:117],a[32:35]
MF a[36:39],v[50:53],v[114:117],a[36:39]
ds_read_b64_tr_b16 v[20:21],v157 offset:256
MF a[40:43],v[54:57],v[114:117],a[40:43]
MF a[44:47],v[58:61],v[114:117],a[44:47]
ds_read_b64_tr_b16 v[22:23],v156 offset:512
MF a[48:51],v[62:65],v[114:117],a[48:51]
MF a[52:55],v[66:69],v[114:117],a[52:55]
MF a[56:59],v[70:73],v[114:117],a[56:59]
ds_read_b64_tr_b16 v[24:25],v156 offset:768
MF a[60:63],v[74:77],v[114:117],a[60:63]
MF a[64:67],v[46:49],v[118:121],a[64:67]
ds_read_b64_tr_b16 v[26:27],v157 offset:512
MF a[68:71],v[50:53],v[118:121],a[68:71]
MF a[72:75],v[54:57],v[118:121],a[72:75]
ds_read_b64_tr_b16 v[28:29],v157 offset:768
MF a[76:79],v[58:61],v[118:121],a[76:79]
MF a[80:83],v[62:65],v[118:121],a[80:83]
MF a[84:87],v[66:69],v[118:121],a[84:87]
ds_read_b64_tr_b16 v[30:31],v156 offset:1024
MF a[88:91],v[70:73],v[118:121],a[88:91]
MF a[92:95],v[74:77],v[118:121],a[92:95]
ds_read_b64_tr_b16 v[32:33],v156 offset:1280
MF a[96:99],v[46:49],v[122:125],a[96:99]
MF a[100:103],v[50:53],v[122:125],a[100:103]
ds_read_b64_tr_b16 v[34:35],v157 offset:1024
MF a[104:107],v[54:57],v[122:125],a[104:107]
MF a[108:111],v[58:61],v[122:125],a[108:111]
MF a[112:115],v[62:65],v[122:125],a[112:115]
ds_read_b64_tr_b16 v[36:37],v157 offset:1280
MF a[116:119],v[66:69],v[122:125],a[116:119]
MF a[120:123],v[70:73],v[122:125],a[120:123]
ds_read_b64_tr_b16 v[38:39],v156 offset:1536
MF a[124:127],v[74:77],v[122:125],a[124:127]
MF a[128:131],v[46:49],v[126:129],a[128:131]
ds_read_b64_tr_b16 v[40:41],v156 offset:1792
MF a[132:135],v[50:53],v[126:129],a[132:135]
MF a[136:139],v[54:57],v[126:129],a[136:139]
MF a[140:143],v[58:61],v[126:129],a[140:143]
ds_read_b64_tr_b16 v[42:43],v157 offset:1536
MF a[144:147],v[62:65],v[126:129],a[144:147]
MF a[148:151],v[66:69],v[126:129],a[148:151]
ds_read_b64_tr_b16 v[44:45],v157 offset:1792
MF a[152:155],v[70:73],v[126:129],a[152:155]
MF a[156:159],v[74:77],v[126:129],a[156:159]
ds_read_b128 v[82:85],v144 offset:2048
MF a[160:163],v[46:49],v[130:133],a[160:163]
MF a[164:167],v[50:53],v[130:133],a[164:167]
MF a[168:171],v[54:57],v[130:133],a[168:171]
ds_read_b128 v[86:89],v144 offset:4096
MF a[172:175],v[58:61],v[130:133],a[172:175]
MF a[176:179],v[62:65],v[130:133],a[176:179]
ds_read_b128 v[90:93],v144 offset:6144
MF a[180:183],v[66:69],v[130:133],a[180:183]
MF a[184:187],v[70:73],v[130:133],a[184:187]
ds_read_b128 v[94:97],v144 offset:8192
MF a[188:191],v[74:77],v[130:133],a[188:191]
MF a[192:195],v[46:49],v[134:137],a[192:195]
MF a[196:199],v[50:53],v[134:137],a[196:199]
ds_read_b128 v[98:101],v144 offset:10240
MF a[200:203],v[54:57],v[134:137],a[200:203]
MF a[204:207],v[58:61],v[134:137],a[204:207]
ds_read_b128 v[102:105],v144 offset:12288
MF a[208:211],v[62:65],v[134:137],a[208:211]
MF a[212:215],v[66:69],v[134:137],a[212:215]
ds_read_b128 v[106:109],v144 offset:14336
MF a[216:219],v[70:73],v[134:137],a[216:219]
MF a[220:223],v[74:77],v[134:137],a[220:223]
MF a[224:227],v[46:49],v[138:141],a[224:227]
MF a[228:231],v[50:53],v[138:141],a[228:231]
MF a[232:235],v[54:57],v[138:141],a[232:235]
MF a[236:239],v[58:61],v[138:141],a[236:239]
MF a[240:243],v[62:65],v[138:141],a[240:243]
MF a[244:247],v[66:69],v[138:141],a[244:247]
MF a[248:251],v[70:73],v[138:141],a[248:251]
MF a[252:255],v[74:77],v[138:141],a[252:255]
s_mov_b32 s48,s14
s_and_b32 s49,s15,0xffff
s_mov_b32 s50,16777216
s_mov_b32 s51,0x20000
s_waitcnt lgkmcnt(15)
MF a[0:3],v[14:17],v[78:81],a[0:3]
ds_read_b64_tr_b16 v[46:47],v156 offset:8192
MF a[4:7],v[18:21],v[78:81],a[4:7]
MF a[8:11],v[22:25],v[78:81],a[8:11]
ds_read_b64_tr_b16 v[48:49],v156 offset:8448
MF a[12:15],v[26:29],v[78:81],a[12:15]
s_waitcnt lgkmcnt(15)
MF a[16:19],v[30:33],v[78:81],a[16:19]
ds_read_b128 v[110:113],v145
s_waitcnt lgkmcnt(14)
MF a[20:23],v[34:37],v[78:81],a[20:23]
s_waitcnt lgkmcnt(12)
MF a[24:27],v[38:41],v[78:81],a[24:27]
ds_read_b64_tr_b16 v[50:51],v157 offset:8192
s_waitcnt lgkmcnt(11)
MF a[28:31],v[42:45],v[78:81],a[28:31]
s_waitcnt lgkmcnt(10)
MF a[32:35],v[14:17],v[82:85],a[32:35]
ds_read_b64_tr_b16 v[52:53],v157 offset:8448
MF a[36:39],v[18:21],v[82:85],a[36:39]
MF a[40:43],v[22:25],v[82:85],a[40:43]
ds_read_b64_tr_b16 v[54:55],v156 offset:8704
MF a[44:47],v[26:29],v[82:85],a[44:47]
MF a[48:51],v[30:33],v[82:85],a[48:51]
ds_read_b64_tr_b16 v[56:57],v156 offset:8960
MF a[52:55],v[34:37],v[82:85],a[52:55]
MF a[56:59],v[38:41],v[82:85],a[56:59]
ds_read_b64_tr_b16 v[58:59],v157 offset:8704
MF a[60:63],v[42:45],v[82:85],a[60:63]
s_waitcnt lgkmcnt(13)
MF a[64:67],v[14:17],v[86:89],a[64:67]
ds_read_b64_tr_b16 v[60:61],v157 offset:8960
MF a[68:71],v[18:21],v[86:89],a[68:71]
MF a[72:75],v[22:25],v[86:89],a[72:75]
ds_read_b64_tr_b16 v[62:63],v156 offset:9216
MF a[76:79],v[26:29],v[86:89],a[76:79]
MF a[80:83],v[30:33],v[86:89],a[80:83]
ds_read_b64_tr_b16 v[64:65],v156 offset:9472
MF a[84:87],v[34:37],v[86:89],a[84:87]
MF a[88:91],v[38:41],v[86:89],a[88:91]
ds_read_b64_tr_b16 v[66:67],v157 offset:9216
MF a[92:95],v[42:45],v[86:89],a[92:95]
s_waitcnt lgkmcnt(15)
MF a[96:99],v[14:17],v[90:93],a[96:99]
ds_read_b64_tr_b16 v[68:69],v157 offset:9472
MF a[100:103],v[18:21],v[90:93],a[100:103]
MF a[104:107],v[22:25],v[90:93],a[104:107]
ds_read_b64_tr_b16 v[70:71],v156 offset:9728
MF a[108:111],v[26:29],v[90:93],a[108:111]
MF a[112:115],v[30:33],v[90:93],a[112:115]
ds_read_b64_tr_b16 v[72:73],v156 offset:9984
MF a[116:119],v[34:37],v[90:93],a[116:119]
MF a[120:123],v[38:41],v[90:93],a[120:123]
ds_read_b64_tr_b16 v[74:75],v157 offset:9728
MF a[124:127],v[42:45],v[90:93],a[124:127]
MF a[128:131],v[14:17],v[94:97],a[128:131]
ds_read_b64_tr_b16 v[76:77],v157 offset:9984
MF a[132:135],v[18:21],v[94:97],a[132:135]
MF a[136:139],v[22:25],v[94:97],a[136:139]
ds_read_b128 v[114:117],v145 offset:2048
MF a[140:143],v[26:29],v[94:97],a[140:143]
MF a[144:147],v[30:33],v[94:97],a[144:147]
ds_read_b128 v[118:121],v145 offset:4096
MF a[148:151],v[34:37],v[94:97],a[148:151]
MF a[152:155],v[38:41],v[94:97],a[152:155]
ds_read_b128 v[122:125],v145 offset:6144
MF a[156:159],v[42:45],v[94:97],a[156:159]
s_waitcnt lgkmcnt(15)
MF a[160:163],v[14:17],v[98:101],a[160:163]
ds_read_b128 v[126:129],v145 offset:8192
MF a[164:167],v[18:21],v[98:101],a[164:167]
MF a[168:171],v[22:25],v[98:101],a[168:171]
ds_read_b128 v[130:133],v145 offset:10240
MF a[172:175],v[26:29],v[98:101],a[172:175]
MF a[176:179],v[30:33],v[98:101],a[176:179]
ds_read_b128 v[134:137],v145 offset:12288
MF a[180:183],v[34:37],v[98:101],a[180:183]
MF a[184:187],v[38:41],v[98:101],a[184:187]
ds_read_b128 v[138:141],v145 offset:14336
MF a[188:191],v[42:45],v[98:101],a[188:191]
MF a[192:195],v[14:17],v[102:105],a[192:195]
MF a[196:199],v[18:21],v[102:105],a[196:199]
MF a[200:203],v[22:25],v[102:105],a[200:203]
MF a[204:207],v[26:29],v[102:105],a[204:207]
MF a[208:211],v[30:33],v[102:105],a[208:211]
MF a[212:215],v[34:37],v[102:105],a[212:215]
MF a[216:219],v[38:41],v[102:105],a[216:219]
MF a[220:223],v[42:45],v[102:105],a[220:223]
MF a[224:227],v[14:17],v[106:109],a[224:227]
MF a[228:231],v[18:21],v[106:109],a[228:231]
MF a[232:235],v[22:25],v[106:109],a[232:235]
MF a[236:239],v[26:29],v[106:109],a[236:239]
MF a[240:243],v[30:33],v[106:109],a[240:243]
MF a[244:247],v[34:37],v[106:109],a[244:247]
MF a[248:251],v[38:41],v[106:109],a[248:251]
MF a[252:255],v[42:45],v[106:109],a[252:255]
s_waitcnt lgkmcnt(0)
MF a[0:3],v[46:49],v[110:113],a[0:3]
MF a[4:7],v[50:53],v[110:113],a[4:7]
MF a[8:11],v[54:57],v[110:113],a[8:11]
MF a[12:15],v[58:61],v[110:113],a[12:15]
MF a[16:19],v[62:65],v[110:113],a[16:19]
s_nop 3
AR v224,a0
AR v225,a1
AR v226,a2
MF a[20:23],v[66:69],v[110:113],a[20:23]
AR v227,a3
AR v228,a4
AR v229,a5
MF a[24:27],v[70:73],v[110:113],a[24:27]
AR v230,a6
AR v231,a7
CP v216,v224,v225
MF a[28:31],v[74:77],v[110:113],a[28:31]
CP v217,v226,v227
CP v218,v228,v229
CP v219,v230,v231
MF a[32:35],v[46:49],v[114:117],a[32:35]
buffer_store_dwordx4 v[216:219],v8,s[48:51],s75 offen nt
AR v232,a8
MF a[36:39],v[50:53],v[114:117],a[36:39]
AR v233,a9
AR v234,a10
AR v235,a11
MF a[40:43],v[54:57],v[114:117],a[40:43]
AR v236,a12
AR v237,a13
MF a[44:47],v[58:61],v[114:117],a[44:47]
AR v238,a14
AR v239,a15
MF a[48:51],v[62:65],v[114:117],a[48:51]
CP v220,v232,v233
CP v221,v234,v235
MF a[52:55],v[66:69],v[114:117],a[52:55]
CP v222,v236,v237
CP v223,v238,v239
MF a[56:59],v[70:73],v[114:117],a[56:59]
buffer_store_dwordx4 v[220:223],v8,s[48:51],s75 offen offset:1024 nt
MF a[60:63],v[74:77],v[114:117],a[60:63]
AR v224,a16
AR v225,a17
MF a[64:67],v[46:49],v[118:121],a[64:67]
AR v226,a18
AR v227,a19
MF a[68:71],v[50:53],v[118:121],a[68:71]
AR v228,a20
AR v229,a21
MF a[72:75],v[54:57],v[118:121],a[72:75]
AR v230,a22
AR v231,a23
MF a[76:79],v[58:61],v[118:121],a[76:79]
CP v216,v224,v225
CP v217,v226,v227
MF a[80:83],v[62:65],v[118:121],a[80:83]
CP v218,v228,v229
CP v219,v230,v231
MF a[84:87],v[66:69],v[118:121],a[84:87]
buffer_store_dwordx4 v[216:219],v8,s[48:51],s75 offen offset:2048 nt
MF a[88:91],v[70:73],v[118:121],a[88:91]
AR v232,a24
AR v233,a25
MF a[92:95],v[74:77],v[118:121],a[92:95]
AR v234,a26
AR v235,a27
MF a[96:99],v[46:49],v[122:125],a[96:99]
AR v236,a28
AR v237,a29
MF a[100:103],v[50:53],v[122:125],a[100:103]
AR v238,a30
AR v239,a31
MF a[104:107],v[54:57],v[122:125],a[104:107]
CP v220,v232,v233
CP v221,v234,v235
MF a[108:111],v[58:61],v[122:125],a[108:111]
CP v222,v236,v237
CP v223,v238,v239
MF a[112:115],v[62:65],v[122:125],a[112:115]
buffer_store_dwordx4 v[220:223],v8,s[48:51],s75 offen offset:3072 nt
s_add_u32 s75,s75,4096
MF a[116:119],v[66:69],v[122:125],a[116:119]
AR v224,a32
AR v225,a33
MF a[120:123],v[70:73],v[122:125],a[120:123]
AR v226,a34
AR v227,a35
MF a[124:127],v[74:77],v[122:125],a[124:127]
AR v228,a36
AR v229,a37
MF a[128:131],v[46:49],v[126:129],a[128:131]
AR v230,a38
AR v231,a39
MF a[132:135],v[50:53],v[126:129],a[132:135]
CP v216,v224,v225
CP v217,v226,v227
MF a[136:139],v[54:57],v[126:129],a[136:139]
CP v218,v228,v229
CP v219,v230,v231
MF a[140:143],v[58:61],v[126:129],a[140:143]
buffer_store_dwordx4 v[216:219],v8,s[48:51],s75 offen nt
MF a[144:147],v[62:65],v[126:129],a[144:147]
AR v232,a40
AR v233,a41
MF a[148:151],v[66:69],v[126:129],a[148:151]
AR v234,a42
AR v235,a43
MF a[152:155],v[70:73],v[126:129],a[152:155]
AR v236,a44
AR v237,a45
MF a[156:159],v[74:77],v[126:129],a[156:159]
AR v238,a46
AR v239,a47
MF a[160:163],v[46:49],v[130:133],a[160:163]
CP v220,v232,v233
CP v221,v234,v235
MF a[164:167],v[50:53],v[130:133],a[164:167]
CP v222,v236,v237
CP v223,v238,v239
MF a[168:171],v[54:57],v[130:133],a[168:171]
buffer_store_dwordx4 v[220:223],v8,s[48:51],s75 offen offset:1024 nt
MF a[172:175],v[58:61],v[130:133],a[172:175]
AR v224,a48
AR v225,a49
MF a[176:179],v[62:65],v[130:133],a[176:179]
AR v226,a50
AR v227,a51
MF a[180:183],v[66:69],v[130:133],a[180:183]
AR v228,a52
AR v229,a53
MF a[184:187],v[70:73],v[130:133],a[184:187]
AR v230,a54
AR v231,a55
MF a[188:191],v[74:77],v[130:133],a[188:191]
CP v216,v224,v225
CP v217,v226,v227
MF a[192:195],v[46:49],v[134:137],a[192:195]
CP v218,v228,v229
CP v219,v230,v231
MF a[196:199],v[50:53],v[134:137],a[196:199]
buffer_store_dwordx4 v[216:219],v8,s[48:51],s75 offen offset:2048 nt
MF a[200:203],v[54:57],v[134:137],a[200:203]
AR v232,a56
AR v233,a57
MF a[204:207],v[58:61],v[134:137],a[204:207]
AR v234,a58
AR v235,a59
MF a[208:211],v[62:65],v[134:137],a[208:211]
AR v236,a60
AR v237,a61
MF a[212:215],v[66:69],v[134:137],a[212:215]
AR v238,a62
AR v239,a63
MF a[216:219],v[70:73],v[134:137],a[216:219]
CP v220,v232,v233
CP v221,v234,v235
MF a[220:223],v[74:77],v[134:137],a[220:223]
CP v222,v236,v237
CP v223,v238,v239
MF a[224:227],v[46:49],v[138:141],a[224:227]
buffer_store_dwordx4 v[220:223],v8,s[48:51],s75 offen offset:3072 nt
s_add_u32 s75,s75,4096
MF a[228:231],v[50:53],v[138:141],a[228:231]
AR v224,a64
AR v225,a65
MF a[232:235],v[54:57],v[138:141],a[232:235]
AR v226,a66
AR v227,a67
MF a[236:239],v[58:61],v[138:141],a[236:239]
AR v228,a68
AR v229,a69
MF a[240:243],v[62:65],v[138:141],a[240:243]
AR v230,a70
AR v231,a71
MF a[244:247],v[66:69],v[138:141],a[244:247]
CP v216,v224,v225
CP v217,v226,v227
MF a[248:251],v[70:73],v[138:141],a[248:251]
CP v218,v228,v229
CP v219,v230,v231
MF a[252:255],v[74:77],v[138:141],a[252:255]
buffer_store_dwordx4 v[216:219],v8,s[48:51],s75 offen nt
AR v232,a72
AR v233,a73
AR v234,a74
AR v235,a75
AR v236,a76
AR v237,a77
AR v238,a78
AR v239,a79
CP v220,v232,v233
CP v221,v234,v235
CP v222,v236,v237
CP v223,v238,v239
buffer_store_dwordx4 v[220:223],v8,s[48:51],s75 offen offset:1024 nt
AR v224,a80
AR v225,a81
AR v226,a82
AR v227,a83
AR v228,a84
AR v229,a85
AR v230,a86
AR v231,a87
CP v216,v224,v225
CP v217,v226,v227
CP v218,v228,v229
CP v219,v230,v231
buffer_store_dwordx4 v[216:219],v8,s[48:51],s75 offen offset:2048 nt
AR v232,a88
AR v233,a89
AR v234,a90
AR v235,a91
AR v236,a92
AR v237,a93
AR v238,a94
AR v239,a95
CP v220,v232,v233
CP v221,v234,v235
CP v222,v236,v237
CP v223,v238,v239
buffer_store_dwordx4 v[220:223],v8,s[48:51],s75 offen offset:3072 nt
s_add_u32 s75,s75,4096
AR v224,a96
AR v225,a97
AR v226,a98
AR v227,a99
AR v228,a100
AR v229,a101
AR v230,a102
AR v231,a103
CP v216,v224,v225
CP v217,v226,v227
CP v218,v228,v229
CP v219,v230,v231
buffer_store_dwordx4 v[216:219],v8,s[48:51],s75 offen nt
AR v232,a104
AR v233,a105
AR v234,a106
AR v235,a107
AR v236,a108
AR v237,a109
AR v238,a110
AR v239,a111
CP v220,v232,v233
CP v221,v234,v235
CP v222,v236,v237
CP v223,v238,v239
buffer_store_dwordx4 v[220:223],v8,s[48:51],s75 offen offset:1024 nt
AR v224,a112
AR v225,a113
AR v226,a114
AR v227,a115
AR v228,a116
AR v229,a117
AR v230,a118
AR v231,a119
CP v216,v224,v225
CP v217,v226,v227
CP v218,v228,v229
CP v219,v230,v231
buffer_store_dwordx4 v[216:219],v8,s[48:51],s75 offen offset:2048 nt
AR v232,a120
AR v233,a121
AR v234,a122
AR v235,a123
AR v236,a124
AR v237,a125
AR v238,a126
AR v239,a127
CP v220,v232,v233
CP v221,v234,v235
CP v222,v236,v237
CP v223,v238,v239
buffer_store_dwordx4 v[220:223],v8,s[48:51],s75 offen offset:3072 nt
s_add_u32 s75,s75,4096
AR v224,a128
AR v225,a129
AR v226,a130
AR v227,a131
AR v228,a132
AR v229,a133
AR v230,a134
AR v231,a135
CP v216,v224,v225
CP v217,v226,v227
CP v218,v228,v229
CP v219,v230,v231
buffer_store_dwordx4 v[216:219],v8,s[48:51],s75 offen nt
AR v232,a136
AR v233,a137
AR v234,a138
AR v235,a139
AR v236,a140
AR v237,a141
AR v238,a142
AR v239,a143
CP v220,v232,v233
CP v221,v234,v235
CP v222,v236,v237
CP v223,v238,v239
buffer_store_dwordx4 v[220:223],v8,s[48:51],s75 offen offset:1024 nt
AR v224,a144
AR v225,a145
AR v226,a146
AR v227,a147
AR v228,a148
AR v229,a149
AR v230,a150
AR v231,a151
CP v216,v224,v225
CP v217,v226,v227
CP v218,v228,v229
CP v219,v230,v231
buffer_store_dwordx4 v[216:219],v8,s[48:51],s75 offen offset:2048 nt
AR v232,a152
AR v233,a153
AR v234,a154
AR v235,a155
AR v236,a156
AR v237,a157
AR v238,a158
AR v239,a159
CP v220,v232,v233
CP v221,v234,v235
CP v222,v236,v237
CP v223,v238,v239
buffer_store_dwordx4 v[220:223],v8,s[48:51],s75 offen offset:3072 nt
s_add_u32 s75,s75,4096
AR v224,a160
AR v225,a161
AR v226,a162
AR v227,a163
AR v228,a164
AR v229,a165
AR v230,a166
AR v231,a167
CP v216,v224,v225
CP v217,v226,v227
CP v218,v228,v229
CP v219,v230,v231
buffer_store_dwordx4 v[216:219],v8,s[48:51],s75 offen nt
AR v232,a168
AR v233,a169
AR v234,a170
AR v235,a171
AR v236,a172
AR v237,a173
AR v238,a174
AR v239,a175
CP v220,v232,v233
CP v221,v234,v235
CP v222,v236,v237
CP v223,v238,v239
buffer_store_dwordx4 v[220:223],v8,s[48:51],s75 offen offset:1024 nt
AR v224,a176
AR v225,a177
AR v226,a178
AR v227,a179
AR v228,a180
AR v229,a181
AR v230,a182
AR v231,a183
CP v216,v224,v225
CP v217,v226,v227
CP v218,v228,v229
CP v219,v230,v231
buffer_store_dwordx4 v[216:219],v8,s[48:51],s75 offen offset:2048 nt
AR v232,a184
AR v233,a185
AR v234,a186
AR v235,a187
AR v236,a188
AR v237,a189
AR v238,a190
AR v239,a191
CP v220,v232,v233
CP v221,v234,v235
CP v222,v236,v237
CP v223,v238,v239
buffer_store_dwordx4 v[220:223],v8,s[48:51],s75 offen offset:3072 nt
s_add_u32 s75,s75,4096
AR v224,a192
AR v225,a193
AR v226,a194
AR v227,a195
AR v228,a196
AR v229,a197
AR v230,a198
AR v231,a199
CP v216,v224,v225
CP v217,v226,v227
CP v218,v228,v229
CP v219,v230,v231
buffer_store_dwordx4 v[216:219],v8,s[48:51],s75 offen nt
AR v232,a200
AR v233,a201
AR v234,a202
AR v235,a203
AR v236,a204
AR v237,a205
AR v238,a206
AR v239,a207
CP v220,v232,v233
CP v221,v234,v235
CP v222,v236,v237
CP v223,v238,v239
buffer_store_dwordx4 v[220:223],v8,s[48:51],s75 offen offset:1024 nt
AR v224,a208
AR v225,a209
AR v226,a210
AR v227,a211
AR v228,a212
AR v229,a213
AR v230,a214
AR v231,a215
CP v216,v224,v225
CP v217,v226,v227
CP v218,v228,v229
CP v219,v230,v231
buffer_store_dwordx4 v[216:219],v8,s[48:51],s75 offen offset:2048 nt
AR v232,a216
AR v233,a217
AR v234,a218
AR v235,a219
AR v236,a220
AR v237,a221
AR v238,a222
AR v239,a223
CP v220,v232,v233
CP v221,v234,v235
CP v222,v236,v237
CP v223,v238,v239
buffer_store_dwordx4 v[220:223],v8,s[48:51],s75 offen offset:3072 nt
s_add_u32 s75,s75,4096
AR v224,a224
AR v225,a225
AR v226,a226
AR v227,a227
AR v228,a228
AR v229,a229
AR v230,a230
AR v231,a231
CP v216,v224,v225
CP v217,v226,v227
CP v218,v228,v229
CP v219,v230,v231
buffer_store_dwordx4 v[216:219],v8,s[48:51],s75 offen nt
AR v232,a232
AR v233,a233
AR v234,a234
AR v235,a235
AR v236,a236
AR v237,a237
AR v238,a238
AR v239,a239
CP v220,v232,v233
CP v221,v234,v235
CP v222,v236,v237
CP v223,v238,v239
buffer_store_dwordx4 v[220:223],v8,s[48:51],s75 offen offset:1024 nt
AR v224,a240
AR v225,a241
AR v226,a242
AR v227,a243
AR v228,a244
AR v229,a245
AR v230,a246
AR v231,a247
CP v216,v224,v225
CP v217,v226,v227
CP v218,v228,v229
CP v219,v230,v231
buffer_store_dwordx4 v[216:219],v8,s[48:51],s75 offen offset:2048 nt
AR v232,a248
AR v233,a249
AR v234,a250
AR v235,a251
AR v236,a252
AR v237,a253
AR v238,a254
AR v239,a255
CP v220,v232,v233
CP v221,v234,v235
CP v222,v236,v237
CP v223,v238,v239
buffer_store_dwordx4 v[220:223],v8,s[48:51],s75 offen offset:3072 nt
s_add_u32 s75,s75,4096
s_branch LE_%=
LV_%=:
s_waitcnt vmcnt(20)
s_barrier
ds_read_b64_tr_b16 v[14:15],v154
ds_read_b64_tr_b16 v[16:17],v154 offset:256
ds_read_b128 v[78:81],v142
ds_read_b64_tr_b16 v[18:19],v155
ds_read_b64_tr_b16 v[20:21],v155 offset:256
ds_read_b64_tr_b16 v[22:23],v154 offset:512
ds_read_b64_tr_b16 v[24:25],v154 offset:768
ds_read_b64_tr_b16 v[26:27],v155 offset:512
ds_read_b64_tr_b16 v[28:29],v155 offset:768
ds_read_b64_tr_b16 v[30:31],v154 offset:1024
ds_read_b64_tr_b16 v[32:33],v154 offset:1280
)A"
R"A(ds_read_b64_tr_b16 v[34:35],v155 offset:1024
ds_read_b64_tr_b16 v[36:37],v155 offset:1280
ds_read_b64_tr_b16 v[38:39],v154 offset:1536
ds_read_b64_tr_b16 v[40:41],v154 offset:1792
ds_read_b64_tr_b16 v[42:43],v155 offset:1536
ds_read_b64_tr_b16 v[44:45],v155 offset:1792
ds_read_b128 v[82:85],v142 offset:2048
ds_read_b128 v[86:89],v142 offset:4096
ds_read_b128 v[90:93],v142 offset:6144
ds_read_b128 v[94:97],v142 offset:8192
ds_read_b128 v[98:101],v142 offset:10240
ds_read_b128 v[102:105],v142 offset:12288
ds_read_b128 v[106:109],v142 offset:14336
ds_read_b128 v[160:163],v196
ds_read_b128 v[164:167],v196 offset:512
ds_read_b128 v[168:171],v196 offset:1024
s_waitcnt lgkmcnt(15)
MF a[0:3],v[78:81],v[14:17],0
ds_read_b64_tr_b16 v[46:47],v154 offset:8192
MF a[4:7],v[78:81],v[18:21],0
MF a[8:11],v[78:81],v[22:25],0
ds_read_b64_tr_b16 v[48:49],v154 offset:8448
MF a[12:15],v[78:81],v[26:29],0
MF a[16:19],v[78:81],v[30:33],0
ds_read_b128 v[110:113],v143
s_waitcnt lgkmcnt(15)
MF a[20:23],v[78:81],v[34:37],0
MF a[24:27],v[78:81],v[38:41],0
ds_read_b64_tr_b16 v[50:51],v155 offset:8192
s_waitcnt lgkmcnt(14)
MF a[28:31],v[78:81],v[42:45],0
s_waitcnt lgkmcnt(13)
MF a[32:35],v[82:85],v[14:17],0
ds_read_b64_tr_b16 v[52:53],v155 offset:8448
MF a[36:39],v[82:85],v[18:21],0
MF a[40:43],v[82:85],v[22:25],0
ds_read_b64_tr_b16 v[54:55],v154 offset:8704
MF a[44:47],v[82:85],v[26:29],0
MF a[48:51],v[82:85],v[30:33],0
ds_read_b64_tr_b16 v[56:57],v154 offset:8960
MF a[52:55],v[82:85],v[34:37],0
MF a[56:59],v[82:85],v[38:41],0
ds_read_b64_tr_b16 v[58:59],v155 offset:8704
MF a[60:63],v[82:85],v[42:45],0
s_waitcnt lgkmcnt(15)
MF a[64:67],v[86:89],v[14:17],0
ds_read_b64_tr_b16 v[60:61],v155 offset:8960
MF a[68:71],v[86:89],v[18:21],0
MF a[72:75],v[86:89],v[22:25],0
ds_read_b64_tr_b16 v[62:63],v154 offset:9216
MF a[76:79],v[86:89],v[26:29],0
MF a[80:83],v[86:89],v[30:33],0
ds_read_b64_tr_b16 v[64:65],v154 offset:9472
MF a[84:87],v[86:89],v[34:37],0
MF a[88:91],v[86:89],v[38:41],0
ds_read_b64_tr_b16 v[66:67],v155 offset:9216
MF a[92:95],v[86:89],v[42:45],0
MF a[96:99],v[90:93],v[14:17],0
ds_read_b64_tr_b16 v[68:69],v155 offset:9472
MF a[100:103],v[90:93],v[18:21],0
MF a[104:107],v[90:93],v[22:25],0
ds_read_b64_tr_b16 v[70:71],v154 offset:9728
MF a[108:111],v[90:93],v[26:29],0
MF a[112:115],v[90:93],v[30:33],0
ds_read_b64_tr_b16 v[72:73],v154 offset:9984
MF a[116:119],v[90:93],v[34:37],0
MF a[120:123],v[90:93],v[38:41],0
ds_read_b64_tr_b16 v[74:75],v155 offset:9728
MF a[124:127],v[90:93],v[42:45],0
s_waitcnt lgkmcnt(15)
MF a[128:131],v[94:97],v[14:17],0
ds_read_b64_tr_b16 v[76:77],v155 offset:9984
MF a[132:135],v[94:97],v[18:21],0
MF a[136:139],v[94:97],v[22:25],0
ds_read_b128 v[114:117],v143 offset:2048
MF a[140:143],v[94:97],v[26:29],0
MF a[144:147],v[94:97],v[30:33],0
ds_read_b128 v[118:121],v143 offset:4096
MF a[148:151],v[94:97],v[34:37],0
MF a[152:155],v[94:97],v[38:41],0
ds_read_b128 v[122:125],v143 offset:6144
MF a[156:159],v[94:97],v[42:45],0
MF a[160:163],v[98:101],v[14:17],0
ds_read_b128 v[126:129],v143 offset:8192
MF a[164:167],v[98:101],v[18:21],0
MF a[168:171],v[98:101],v[22:25],0
ds_read_b128 v[130:133],v143 offset:10240
MF a[172:175],v[98:101],v[26:29],0
MF a[176:179],v[98:101],v[30:33],0
ds_read_b128 v[134:137],v143 offset:12288
MF a[180:183],v[98:101],v[34:37],0
MF a[184:187],v[98:101],v[38:41],0
ds_read_b128 v[138:141],v143 offset:14336
MF a[188:191],v[98:101],v[42:45],0
MF a[192:195],v[102:105],v[14:17],0
MF a[196:199],v[102:105],v[18:21],0
MF a[200:203],v[102:105],v[22:25],0
MF a[204:207],v[102:105],v[26:29],0
MF a[208:211],v[102:105],v[30:33],0
MF a[212:215],v[102:105],v[34:37],0
MF a[216:219],v[102:105],v[38:41],0
MF a[220:223],v[102:105],v[42:45],0
MF a[224:227],v[106:109],v[14:17],0
MF a[228:231],v[106:109],v[18:21],0
MF a[232:235],v[106:109],v[22:25],0
MF a[236:239],v[106:109],v[26:29],0
MF a[240:243],v[106:109],v[30:33],0
MF a[244:247],v[106:109],v[34:37],0
MF a[248:251],v[106:109],v[38:41],0
MF a[252:255],v[106:109],v[42:45],0
s_waitcnt vmcnt(4) lgkmcnt(0)
s_barrier
s_add_u32 m0,s57,0
MF a[0:3],v[110:113],v[46:49],a[0:3]
buffer_load_dwordx4 v146,s[36:39],s56 offen lds
MF a[4:7],v[110:113],v[50:53],a[4:7]
ds_read_b64_tr_b16 v[14:15],v156
MF a[8:11],v[110:113],v[54:57],a[8:11]
ds_read_b64_tr_b16 v[16:17],v156 offset:256
MF a[12:15],v[110:113],v[58:61],a[12:15]
s_add_u32 m0,s57,1024
MF a[16:19],v[110:113],v[62:65],a[16:19]
buffer_load_dwordx4 v147,s[36:39],s56 offen lds
MF a[20:23],v[110:113],v[66:69],a[20:23]
ds_read_b128 v[78:81],v144
MF a[24:27],v[110:113],v[70:73],a[24:27]
s_add_u32 m0,s57,2048
MF a[28:31],v[110:113],v[74:77],a[28:31]
buffer_load_dwordx4 v148,s[36:39],s56 offen lds
MF a[32:35],v[114:117],v[46:49],a[32:35]
ds_read_b64_tr_b16 v[18:19],v157
MF a[36:39],v[114:117],v[50:53],a[36:39]
ds_read_b64_tr_b16 v[20:21],v157 offset:256
MF a[40:43],v[114:117],v[54:57],a[40:43]
s_add_u32 m0,s57,3072
MF a[44:47],v[114:117],v[58:61],a[44:47]
buffer_load_dwordx4 v149,s[36:39],s56 offen lds
MF a[48:51],v[114:117],v[62:65],a[48:51]
ds_read_b64_tr_b16 v[22:23],v156 offset:512
MF a[52:55],v[114:117],v[66:69],a[52:55]
s_add_u32 m0,s57,4096
MF a[56:59],v[114:117],v[70:73],a[56:59]
buffer_load_dwordx4 v150,s[36:39],s56 offen lds
MF a[60:63],v[114:117],v[74:77],a[60:63]
ds_read_b64_tr_b16 v[24:25],v156 offset:768
MF a[64:67],v[118:121],v[46:49],a[64:67]
ds_read_b64_tr_b16 v[26:27],v157 offset:512
MF a[68:71],v[118:121],v[50:53],a[68:71]
s_add_u32 m0,s57,5120
MF a[72:75],v[118:121],v[54:57],a[72:75]
buffer_load_dwordx4 v151,s[36:39],s56 offen lds
MF a[76:79],v[118:121],v[58:61],a[76:79]
ds_read_b64_tr_b16 v[28:29],v157 offset:768
MF a[80:83],v[118:121],v[62:65],a[80:83]
s_add_u32 m0,s57,6144
MF a[84:87],v[118:121],v[66:69],a[84:87]
buffer_load_dwordx4 v152,s[36:39],s56 offen lds
MF a[88:91],v[118:121],v[70:73],a[88:91]
ds_read_b64_tr_b16 v[30:31],v156 offset:1024
MF a[92:95],v[118:121],v[74:77],a[92:95]
ds_read_b64_tr_b16 v[32:33],v156 offset:1280
MF a[96:99],v[122:125],v[46:49],a[96:99]
s_add_u32 m0,s57,7168
MF a[100:103],v[122:125],v[50:53],a[100:103]
buffer_load_dwordx4 v153,s[36:39],s56 offen lds
MF a[104:107],v[122:125],v[54:57],a[104:107]
ds_read_b64_tr_b16 v[34:35],v157 offset:1024
MF a[108:111],v[122:125],v[58:61],a[108:111]
s_add_u32 m0,s58,32768
MF a[112:115],v[122:125],v[62:65],a[112:115]
buffer_load_dwordx4 v9,s[40:43],s55 offen lds
MF a[116:119],v[122:125],v[66:69],a[116:119]
ds_read_b64_tr_b16 v[36:37],v157 offset:1280
MF a[120:123],v[122:125],v[70:73],a[120:123]
ds_read_b64_tr_b16 v[38:39],v156 offset:1536
MF a[124:127],v[122:125],v[74:77],a[124:127]
s_add_u32 m0,s58,33792
MF a[128:131],v[126:129],v[46:49],a[128:131]
buffer_load_dwordx4 v10,s[40:43],s55 offen lds
MF a[132:135],v[126:129],v[50:53],a[132:135]
ds_read_b64_tr_b16 v[40:41],v156 offset:1792
MF a[136:139],v[126:129],v[54:57],a[136:139]
s_add_u32 m0,s58,34816
MF a[140:143],v[126:129],v[58:61],a[140:143]
buffer_load_dwordx4 v11,s[40:43],s55 offen lds
MF a[144:147],v[126:129],v[62:65],a[144:147]
ds_read_b64_tr_b16 v[42:43],v157 offset:1536
MF a[148:151],v[126:129],v[66:69],a[148:151]
ds_read_b64_tr_b16 v[44:45],v157 offset:1792
MF a[152:155],v[126:129],v[70:73],a[152:155]
s_add_u32 m0,s58,35840
MF a[156:159],v[126:129],v[74:77],a[156:159]
buffer_load_dwordx4 v12,s[40:43],s55 offen lds
MF a[160:163],v[130:133],v[46:49],a[160:163]
ds_read_b128 v[82:85],v144 offset:2048
MF a[164:167],v[130:133],v[50:53],a[164:167]
s_add_u32 m0,s58,49152
MF a[168:171],v[130:133],v[54:57],a[168:171]
buffer_load_dwordx4 v9,s[44:47],s55 offen lds
MF a[172:175],v[130:133],v[58:61],a[172:175]
ds_read_b128 v[86:89],v144 offset:4096
MF a[176:179],v[130:133],v[62:65],a[176:179]
ds_read_b128 v[90:93],v144 offset:6144
MF a[180:183],v[130:133],v[66:69],a[180:183]
s_add_u32 m0,s58,50176
MF a[184:187],v[130:133],v[70:73],a[184:187]
buffer_load_dwordx4 v10,s[44:47],s55 offen lds
MF a[188:191],v[130:133],v[74:77],a[188:191]
ds_read_b128 v[94:97],v144 offset:8192
MF a[192:195],v[134:137],v[46:49],a[192:195]
s_add_u32 m0,s58,51200
MF a[196:199],v[134:137],v[50:53],a[196:199]
buffer_load_dwordx4 v11,s[44:47],s55 offen lds
MF a[200:203],v[134:137],v[54:57],a[200:203]
ds_read_b128 v[98:101],v144 offset:10240
MF a[204:207],v[134:137],v[58:61],a[204:207]
ds_read_b128 v[102:105],v144 offset:12288
MF a[208:211],v[134:137],v[62:65],a[208:211]
s_add_u32 m0,s58,52224
MF a[212:215],v[134:137],v[66:69],a[212:215]
buffer_load_dwordx4 v12,s[44:47],s55 offen lds
MF a[216:219],v[134:137],v[70:73],a[216:219]
ds_read_b128 v[106:109],v144 offset:14336
MF a[220:223],v[134:137],v[74:77],a[220:223]
MF a[224:227],v[138:141],v[46:49],a[224:227]
MF a[228:231],v[138:141],v[50:53],a[228:231]
MF a[232:235],v[138:141],v[54:57],a[232:235]
MF a[236:239],v[138:141],v[58:61],a[236:239]
MF a[240:243],v[138:141],v[62:65],a[240:243]
MF a[244:247],v[138:141],v[66:69],a[244:247]
MF a[248:251],v[138:141],v[70:73],a[248:251]
MF a[252:255],v[138:141],v[74:77],a[252:255]
s_add_u32 s56,s56,128
s_add_u32 s55,s55,524288
s_waitcnt lgkmcnt(15)
MF a[0:3],v[78:81],v[14:17],a[0:3]
ds_read_b64_tr_b16 v[46:47],v156 offset:8192
v_cmp_gt_u32_e64 s[60:61],16,v199
MF a[4:7],v[78:81],v[18:21],a[4:7]
MF a[8:11],v[78:81],v[22:25],a[8:11]
ds_read_b64_tr_b16 v[48:49],v156 offset:8448
v_lshlrev_b32 v172,16,v160
MF a[12:15],v[78:81],v[26:29],a[12:15]
v_and_b32 v173,0xffff0000,v160
s_waitcnt lgkmcnt(15)
MF a[16:19],v[78:81],v[30:33],a[16:19]
ds_read_b128 v[110:113],v145
v_lshlrev_b32 v174,16,v161
s_waitcnt lgkmcnt(14)
MF a[20:23],v[78:81],v[34:37],a[20:23]
s_waitcnt lgkmcnt(12)
MF a[24:27],v[78:81],v[38:41],a[24:27]
ds_read_b64_tr_b16 v[50:51],v157 offset:8192
v_and_b32 v175,0xffff0000,v161
s_waitcnt lgkmcnt(11)
MF a[28:31],v[78:81],v[42:45],a[28:31]
v_lshlrev_b32 v176,16,v162
s_waitcnt lgkmcnt(10)
MF a[32:35],v[82:85],v[14:17],a[32:35]
ds_read_b64_tr_b16 v[52:53],v157 offset:8448
MF a[36:39],v[82:85],v[18:21],a[36:39]
v_and_b32 v177,0xffff0000,v162
MF a[40:43],v[82:85],v[22:25],a[40:43]
ds_read_b64_tr_b16 v[54:55],v156 offset:8704
v_lshlrev_b32 v178,16,v163
MF a[44:47],v[82:85],v[26:29],a[44:47]
MF a[48:51],v[82:85],v[30:33],a[48:51]
ds_read_b64_tr_b16 v[56:57],v156 offset:8960
v_and_b32 v179,0xffff0000,v163
MF a[52:55],v[82:85],v[34:37],a[52:55]
v_lshlrev_b32 v180,16,v164
MF a[56:59],v[82:85],v[38:41],a[56:59]
ds_read_b64_tr_b16 v[58:59],v157 offset:8704
v_and_b32 v181,0xffff0000,v164
MF a[60:63],v[82:85],v[42:45],a[60:63]
s_waitcnt lgkmcnt(13)
MF a[64:67],v[86:89],v[14:17],a[64:67]
ds_read_b64_tr_b16 v[60:61],v157 offset:8960
v_lshlrev_b32 v182,16,v165
MF a[68:71],v[86:89],v[18:21],a[68:71]
v_and_b32 v183,0xffff0000,v165
MF a[72:75],v[86:89],v[22:25],a[72:75]
ds_read_b64_tr_b16 v[62:63],v156 offset:9216
MF a[76:79],v[86:89],v[26:29],a[76:79]
v_lshlrev_b32 v184,16,v166
MF a[80:83],v[86:89],v[30:33],a[80:83]
ds_read_b64_tr_b16 v[64:65],v156 offset:9472
v_and_b32 v185,0xffff0000,v166
MF a[84:87],v[86:89],v[34:37],a[84:87]
MF a[88:91],v[86:89],v[38:41],a[88:91]
ds_read_b64_tr_b16 v[66:67],v157 offset:9216
v_lshlrev_b32 v186,16,v167
MF a[92:95],v[86:89],v[42:45],a[92:95]
v_and_b32 v187,0xffff0000,v167
s_waitcnt lgkmcnt(15)
MF a[96:99],v[90:93],v[14:17],a[96:99]
ds_read_b64_tr_b16 v[68:69],v157 offset:9472
v_lshlrev_b32 v188,16,v168
MF a[100:103],v[90:93],v[18:21],a[100:103]
MF a[104:107],v[90:93],v[22:25],a[104:107]
ds_read_b64_tr_b16 v[70:71],v156 offset:9728
v_and_b32 v189,0xffff0000,v168
MF a[108:111],v[90:93],v[26:29],a[108:111]
v_lshlrev_b32 v190,16,v169
MF a[112:115],v[90:93],v[30:33],a[112:115]
ds_read_b64_tr_b16 v[72:73],v156 offset:9984
MF a[116:119],v[90:93],v[34:37],a[116:119]
v_and_b32 v191,0xffff0000,v169
MF a[120:123],v[90:93],v[38:41],a[120:123]
ds_read_b64_tr_b16 v[74:75],v157 offset:9728
v_lshlrev_b32 v192,16,v170
MF a[124:127],v[90:93],v[42:45],a[124:127]
MF a[128:131],v[94:97],v[14:17],a[128:131]
ds_read_b64_tr_b16 v[76:77],v157 offset:9984
v_and_b32 v193,0xffff0000,v170
MF a[132:135],v[94:97],v[18:21],a[132:135]
v_lshlrev_b32 v194,16,v171
MF a[136:139],v[94:97],v[22:25],a[136:139]
ds_read_b128 v[114:117],v145 offset:2048
v_and_b32 v195,0xffff0000,v171
MF a[140:143],v[94:97],v[26:29],a[140:143]
MF a[144:147],v[94:97],v[30:33],a[144:147]
ds_read_b128 v[118:121],v145 offset:4096
v_cndmask_b32_e64 v188,0,v188,s[60:61]
MF a[148:151],v[94:97],v[34:37],a[148:151]
v_cndmask_b32_e64 v189,0,v189,s[60:61]
MF a[152:155],v[94:97],v[38:41],a[152:155]
ds_read_b128 v[122:125],v145 offset:6144
MF a[156:159],v[94:97],v[42:45],a[156:159]
v_cndmask_b32_e64 v190,0,v190,s[60:61]
s_waitcnt lgkmcnt(15)
MF a[160:163],v[98:101],v[14:17],a[160:163]
ds_read_b128 v[126:129],v145 offset:8192
v_cndmask_b32_e64 v191,0,v191,s[60:61]
MF a[164:167],v[98:101],v[18:21],a[164:167]
v_cndmask_b32_e64 v192,0,v192,s[60:61]
MF a[168:171],v[98:101],v[22:25],a[168:171]
ds_read_b128 v[130:133],v145 offset:10240
MF a[172:175],v[98:101],v[26:29],a[172:175]
v_cndmask_b32_e64 v193,0,v193,s[60:61]
MF a[176:179],v[98:101],v[30:33],a[176:179]
ds_read_b128 v[134:137],v145 offset:12288
v_cndmask_b32_e64 v194,0,v194,s[60:61]
MF a[180:183],v[98:101],v[34:37],a[180:183]
MF a[184:187],v[98:101],v[38:41],a[184:187]
ds_read_b128 v[138:141],v145 offset:14336
v_cndmask_b32_e64 v195,0,v195,s[60:61]
MF a[188:191],v[98:101],v[42:45],a[188:191]
v_add_f32 v200,v172,v173
MF a[192:195],v[102:105],v[14:17],a[192:195]
MF a[196:199],v[102:105],v[18:21],a[196:199]
v_add_f32 v201,v174,v175
MF a[200:203],v[102:105],v[22:25],a[200:203]
v_add_f32 v200,v176,v200
MF a[204:207],v[102:105],v[26:29],a[204:207]
v_add_f32 v201,v177,v201
MF a[208:211],v[102:105],v[30:33],a[208:211]
MF a[212:215],v[102:105],v[34:37],a[212:215]
v_add_f32 v200,v178,v200
MF a[216:219],v[102:105],v[38:41],a[216:219]
v_add_f32 v201,v179,v201
MF a[220:223],v[102:105],v[42:45],a[220:223]
MF a[224:227],v[106:109],v[14:17],a[224:227]
v_add_f32 v200,v180,v200
MF a[228:231],v[106:109],v[18:21],a[228:231]
v_add_f32 v201,v181,v201
MF a[232:235],v[106:109],v[22:25],a[232:235]
MF a[236:239],v[106:109],v[26:29],a[236:239]
v_add_f32 v200,v182,v200
MF a[240:243],v[106:109],v[30:33],a[240:243]
v_add_f32 v201,v183,v201
MF a[244:247],v[106:109],v[34:37],a[244:247]
v_add_f32 v200,v184,v200
MF a[248:251],v[106:109],v[38:41],a[248:251]
MF a[252:255],v[106:109],v[42:45],a[252:255]
v_add_f32 v201,v185,v201
s_waitcnt vmcnt(0) lgkmcnt(0)
s_barrier
s_add_u32 m0,s57,65536
MF a[0:3],v[110:113],v[46:49],a[0:3]
buffer_load_dwordx4 v146,s[36:39],s56 offen lds
MF a[4:7],v[110:113],v[50:53],a[4:7]
ds_read_b64_tr_b16 v[14:15],v154
MF a[8:11],v[110:113],v[54:57],a[8:11]
ds_read_b64_tr_b16 v[16:17],v154 offset:256
MF a[12:15],v[110:113],v[58:61],a[12:15]
s_add_u32 m0,s57,66560
MF a[16:19],v[110:113],v[62:65],a[16:19]
buffer_load_dwordx4 v147,s[36:39],s56 offen lds
MF a[20:23],v[110:113],v[66:69],a[20:23]
ds_read_b128 v[78:81],v142
MF a[24:27],v[110:113],v[70:73],a[24:27]
s_add_u32 m0,s57,67584
MF a[28:31],v[110:113],v[74:77],a[28:31]
buffer_load_dwordx4 v148,s[36:39],s56 offen lds
MF a[32:35],v[114:117],v[46:49],a[32:35]
ds_read_b64_tr_b16 v[18:19],v155
MF a[36:39],v[114:117],v[50:53],a[36:39]
ds_read_b64_tr_b16 v[20:21],v155 offset:256
MF a[40:43],v[114:117],v[54:57],a[40:43]
s_add_u32 m0,s57,68608
MF a[44:47],v[114:117],v[58:61],a[44:47]
buffer_load_dwordx4 v149,s[36:39],s56 offen lds
MF a[48:51],v[114:117],v[62:65],a[48:51]
ds_read_b64_tr_b16 v[22:23],v154 offset:512
MF a[52:55],v[114:117],v[66:69],a[52:55]
s_add_u32 m0,s57,69632
MF a[56:59],v[114:117],v[70:73],a[56:59]
buffer_load_dwordx4 v150,s[36:39],s56 offen lds
MF a[60:63],v[114:117],v[74:77],a[60:63]
ds_read_b64_tr_b16 v[24:25],v154 offset:768
MF a[64:67],v[118:121],v[46:49],a[64:67]
ds_read_b64_tr_b16 v[26:27],v155 offset:512
MF a[68:71],v[118:121],v[50:53],a[68:71]
s_add_u32 m0,s57,70656
MF a[72:75],v[118:121],v[54:57],a[72:75]
buffer_load_dwordx4 v151,s[36:39],s56 offen lds
MF a[76:79],v[118:121],v[58:61],a[76:79]
ds_read_b64_tr_b16 v[28:29],v155 offset:768
MF a[80:83],v[118:121],v[62:65],a[80:83]
s_add_u32 m0,s57,71680
MF a[84:87],v[118:121],v[66:69],a[84:87]
buffer_load_dwordx4 v152,s[36:39],s56 offen lds
MF a[88:91],v[118:121],v[70:73],a[88:91]
ds_read_b64_tr_b16 v[30:31],v154 offset:1024
MF a[92:95],v[118:121],v[74:77],a[92:95]
ds_read_b64_tr_b16 v[32:33],v154 offset:1280
MF a[96:99],v[122:125],v[46:49],a[96:99]
s_add_u32 m0,s57,72704
MF a[100:103],v[122:125],v[50:53],a[100:103]
buffer_load_dwordx4 v153,s[36:39],s56 offen lds
MF a[104:107],v[122:125],v[54:57],a[104:107]
ds_read_b64_tr_b16 v[34:35],v155 offset:1024
MF a[108:111],v[122:125],v[58:61],a[108:111]
s_add_u32 m0,s58,98304
MF a[112:115],v[122:125],v[62:65],a[112:115]
buffer_load_dwordx4 v9,s[40:43],s55 offen lds
MF a[116:119],v[122:125],v[66:69],a[116:119]
ds_read_b64_tr_b16 v[36:37],v155 offset:1280
MF a[120:123],v[122:125],v[70:73],a[120:123]
ds_read_b64_tr_b16 v[38:39],v154 offset:1536
MF a[124:127],v[122:125],v[74:77],a[124:127]
s_add_u32 m0,s58,99328
MF a[128:131],v[126:129],v[46:49],a[128:131]
buffer_load_dwordx4 v10,s[40:43],s55 offen lds
MF a[132:135],v[126:129],v[50:53],a[132:135]
ds_read_b64_tr_b16 v[40:41],v154 offset:1792
MF a[136:139],v[126:129],v[54:57],a[136:139]
s_add_u32 m0,s58,100352
MF a[140:143],v[126:129],v[58:61],a[140:143]
buffer_load_dwordx4 v11,s[40:43],s55 offen lds
MF a[144:147],v[126:129],v[62:65],a[144:147]
ds_read_b64_tr_b16 v[42:43],v155 offset:1536
MF a[148:151],v[126:129],v[66:69],a[148:151]
ds_read_b64_tr_b16 v[44:45],v155 offset:1792
MF a[152:155],v[126:129],v[70:73],a[152:155]
s_add_u32 m0,s58,101376
MF a[156:159],v[126:129],v[74:77],a[156:159]
buffer_load_dwordx4 v12,s[40:43],s55 offen lds
MF a[160:163],v[130:133],v[46:49],a[160:163]
ds_read_b128 v[82:85],v142 offset:2048
MF a[164:167],v[130:133],v[50:53],a[164:167]
s_add_u32 m0,s58,114688
MF a[168:171],v[130:133],v[54:57],a[168:171]
buffer_load_dwordx4 v9,s[44:47],s55 offen lds
MF a[172:175],v[130:133],v[58:61],a[172:175]
ds_read_b128 v[86:89],v142 offset:4096
MF a[176:179],v[130:133],v[62:65],a[176:179]
ds_read_b128 v[90:93],v142 offset:6144
MF a[180:183],v[130:133],v[66:69],a[180:183]
s_add_u32 m0,s58,115712
MF a[184:187],v[130:133],v[70:73],a[184:187]
buffer_load_dwordx4 v10,s[44:47],s55 offen lds
MF a[188:191],v[130:133],v[74:77],a[188:191]
ds_read_b128 v[94:97],v142 offset:8192
MF a[192:195],v[134:137],v[46:49],a[192:195]
s_add_u32 m0,s58,116736
MF a[196:199],v[134:137],v[50:53],a[196:199]
buffer_load_dwordx4 v11,s[44:47],s55 offen lds
MF a[200:203],v[134:137],v[54:57],a[200:203]
ds_read_b128 v[98:101],v142 offset:10240
MF a[204:207],v[134:137],v[58:61],a[204:207]
ds_read_b128 v[102:105],v142 offset:12288
MF a[208:211],v[134:137],v[62:65],a[208:211]
s_add_u32 m0,s58,117760
MF a[212:215],v[134:137],v[66:69],a[212:215]
buffer_load_dwordx4 v12,s[44:47],s55 offen lds
MF a[216:219],v[134:137],v[70:73],a[216:219]
ds_read_b128 v[106:109],v142 offset:14336
MF a[220:223],v[134:137],v[74:77],a[220:223]
MF a[224:227],v[138:141],v[46:49],a[224:227]
MF a[228:231],v[138:141],v[50:53],a[228:231]
MF a[232:235],v[138:141],v[54:57],a[232:235]
MF a[236:239],v[138:141],v[58:61],a[236:239]
MF a[240:243],v[138:141],v[62:65],a[240:243]
MF a[244:247],v[138:141],v[66:69],a[244:247]
MF a[248:251],v[138:141],v[70:73],a[248:251]
MF a[252:255],v[138:141],v[74:77],a[252:255]
s_add_u32 s56,s56,128
s_add_u32 s55,s55,524288
s_waitcnt lgkmcnt(15)
MF a[0:3],v[78:81],v[14:17],a[0:3]
ds_read_b64_tr_b16 v[46:47],v154 offset:8192
v_add_f32 v200,v186,v200
MF a[4:7],v[78:81],v[18:21],a[4:7]
MF a[8:11],v[78:81],v[22:25],a[8:11]
ds_read_b64_tr_b16 v[48:49],v154 offset:8448
v_add_f32 v201,v187,v201
MF a[12:15],v[78:81],v[26:29],a[12:15]
v_add_f32 v200,v188,v200
s_waitcnt lgkmcnt(15)
MF a[16:19],v[78:81],v[30:33],a[16:19]
ds_read_b128 v[110:113],v143
v_add_f32 v201,v189,v201
s_waitcnt lgkmcnt(14)
MF a[20:23],v[78:81],v[34:37],a[20:23]
s_waitcnt lgkmcnt(12)
MF a[24:27],v[78:81],v[38:41],a[24:27]
ds_read_b64_tr_b16 v[50:51],v155 offset:8192
v_add_f32 v200,v190,v200
s_waitcnt lgkmcnt(11)
MF a[28:31],v[78:81],v[42:45],a[28:31]
v_add_f32 v201,v191,v201
s_waitcnt lgkmcnt(10)
MF a[32:35],v[82:85],v[14:17],a[32:35]
ds_read_b64_tr_b16 v[52:53],v155 offset:8448
MF a[36:39],v[82:85],v[18:21],a[36:39]
v_add_f32 v200,v192,v200
MF a[40:43],v[82:85],v[22:25],a[40:43]
ds_read_b64_tr_b16 v[54:55],v154 offset:8704
v_add_f32 v201,v193,v201
MF a[44:47],v[82:85],v[26:29],a[44:47]
MF a[48:51],v[82:85],v[30:33],a[48:51]
ds_read_b64_tr_b16 v[56:57],v154 offset:8960
v_add_f32 v200,v194,v200
MF a[52:55],v[82:85],v[34:37],a[52:55]
v_add_f32 v201,v195,v201
MF a[56:59],v[82:85],v[38:41],a[56:59]
ds_read_b64_tr_b16 v[58:59],v155 offset:8704
v_add_f32 v200,v200,v201
MF a[60:63],v[82:85],v[42:45],a[60:63]
s_waitcnt lgkmcnt(13)
MF a[64:67],v[86:89],v[14:17],a[64:67]
ds_read_b64_tr_b16 v[60:61],v155 offset:8960
v_add_f32_dpp v200,v200,v200 row_ror:8 row_mask:0xf bank_mask:0xf
MF a[68:71],v[86:89],v[18:21],a[68:71]
s_nop 0
v_add_f32_dpp v200,v200,v200 row_ror:4 row_mask:0xf bank_mask:0xf
MF a[72:75],v[86:89],v[22:25],a[72:75]
ds_read_b64_tr_b16 v[62:63],v154 offset:9216
MF a[76:79],v[86:89],v[26:29],a[76:79]
v_add_f32_dpp v200,v200,v200 row_ror:2 row_mask:0xf bank_mask:0xf
MF a[80:83],v[86:89],v[30:33],a[80:83]
ds_read_b64_tr_b16 v[64:65],v154 offset:9472
v_add_f32_dpp v200,v200,v200 row_ror:1 row_mask:0xf bank_mask:0xf
MF a[84:87],v[86:89],v[34:37],a[84:87]
MF a[88:91],v[86:89],v[38:41],a[88:91]
ds_read_b64_tr_b16 v[66:67],v155 offset:9216
v_mov_b32 v201,v200
MF a[92:95],v[86:89],v[42:45],a[92:95]
s_nop 0
v_permlane16_swap_b32 v200,v201
s_waitcnt lgkmcnt(15)
MF a[96:99],v[90:93],v[14:17],a[96:99]
ds_read_b64_tr_b16 v[68:69],v155 offset:9472
v_add_f32 v200,v200,v201
MF a[100:103],v[90:93],v[18:21],a[100:103]
MF a[104:107],v[90:93],v[22:25],a[104:107]
ds_read_b64_tr_b16 v[70:71],v154 offset:9728
v_mul_f32 v200,0x3acccccd,v200
MF a[108:111],v[90:93],v[26:29],a[108:111]
v_sub_f32 v172,v172,v200
MF a[112:115],v[90:93],v[30:33],a[112:115]
ds_read_b64_tr_b16 v[72:73],v154 offset:9984
MF a[116:119],v[90:93],v[34:37],a[116:119]
v_sub_f32 v173,v173,v200
MF a[120:123],v[90:93],v[38:41],a[120:123]
ds_read_b64_tr_b16 v[74:75],v155 offset:9728
v_sub_f32 v174,v174,v200
MF a[124:127],v[90:93],v[42:45],a[124:127]
MF a[128:131],v[94:97],v[14:17],a[128:131]
ds_read_b64_tr_b16 v[76:77],v155 offset:9984
v_sub_f32 v175,v175,v200
MF a[132:135],v[94:97],v[18:21],a[132:135]
v_sub_f32 v176,v176,v200
MF a[136:139],v[94:97],v[22:25],a[136:139]
ds_read_b128 v[114:117],v143 offset:2048
v_sub_f32 v177,v177,v200
MF a[140:143],v[94:97],v[26:29],a[140:143]
MF a[144:147],v[94:97],v[30:33],a[144:147]
ds_read_b128 v[118:121],v143 offset:4096
v_sub_f32 v178,v178,v200
MF a[148:151],v[94:97],v[34:37],a[148:151]
v_sub_f32 v179,v179,v200
MF a[152:155],v[94:97],v[38:41],a[152:155]
ds_read_b128 v[122:125],v143 offset:6144
MF a[156:159],v[94:97],v[42:45],a[156:159]
v_sub_f32 v180,v180,v200
s_waitcnt lgkmcnt(15)
MF a[160:163],v[98:101],v[14:17],a[160:163]
ds_read_b128 v[126:129],v143 offset:8192
v_sub_f32 v181,v181,v200
MF a[164:167],v[98:101],v[18:21],a[164:167]
v_sub_f32 v182,v182,v200
MF a[168:171],v[98:101],v[22:25],a[168:171]
ds_read_b128 v[130:133],v143 offset:10240
MF a[172:175],v[98:101],v[26:29],a[172:175]
v_sub_f32 v183,v183,v200
MF a[176:179],v[98:101],v[30:33],a[176:179]
ds_read_b128 v[134:137],v143 offset:12288
v_sub_f32 v184,v184,v200
MF a[180:183],v[98:101],v[34:37],a[180:183]
MF a[184:187],v[98:101],v[38:41],a[184:187]
ds_read_b128 v[138:141],v143 offset:14336
v_sub_f32 v185,v185,v200
MF a[188:191],v[98:101],v[42:45],a[188:191]
v_sub_f32 v186,v186,v200
MF a[192:195],v[102:105],v[14:17],a[192:195]
MF a[196:199],v[102:105],v[18:21],a[196:199]
v_sub_f32 v187,v187,v200
MF a[200:203],v[102:105],v[22:25],a[200:203]
v_sub_f32 v188,v188,v200
MF a[204:207],v[102:105],v[26:29],a[204:207]
v_sub_f32 v189,v189,v200
MF a[208:211],v[102:105],v[30:33],a[208:211]
MF a[212:215],v[102:105],v[34:37],a[212:215]
v_sub_f32 v190,v190,v200
MF a[216:219],v[102:105],v[38:41],a[216:219]
v_sub_f32 v191,v191,v200
MF a[220:223],v[102:105],v[42:45],a[220:223]
MF a[224:227],v[106:109],v[14:17],a[224:227]
v_sub_f32 v192,v192,v200
MF a[228:231],v[106:109],v[18:21],a[228:231]
v_sub_f32 v193,v193,v200
MF a[232:235],v[106:109],v[22:25],a[232:235]
MF a[236:239],v[106:109],v[26:29],a[236:239]
v_sub_f32 v194,v194,v200
MF a[240:243],v[106:109],v[30:33],a[240:243]
v_sub_f32 v195,v195,v200
MF a[244:247],v[106:109],v[34:37],a[244:247]
v_cndmask_b32_e64 v188,0,v188,s[60:61]
MF a[248:251],v[106:109],v[38:41],a[248:251]
MF a[252:255],v[106:109],v[42:45],a[252:255]
v_cndmask_b32_e64 v189,0,v189,s[60:61]
s_waitcnt vmcnt(0) lgkmcnt(0)
s_barrier
s_add_u32 m0,s57,0
MF a[0:3],v[110:113],v[46:49],a[0:3]
buffer_load_dwordx4 v146,s[36:39],s56 offen lds
MF a[4:7],v[110:113],v[50:53],a[4:7]
ds_read_b64_tr_b16 v[14:15],v156
MF a[8:11],v[110:113],v[54:57],a[8:11]
ds_read_b64_tr_b16 v[16:17],v156 offset:256
MF a[12:15],v[110:113],v[58:61],a[12:15]
s_add_u32 m0,s57,1024
MF a[16:19],v[110:113],v[62:65],a[16:19]
buffer_load_dwordx4 v147,s[36:39],s56 offen lds
MF a[20:23],v[110:113],v[66:69],a[20:23]
ds_read_b128 v[78:81],v144
MF a[24:27],v[110:113],v[70:73],a[24:27]
s_add_u32 m0,s57,2048
MF a[28:31],v[110:113],v[74:77],a[28:31]
buffer_load_dwordx4 v148,s[36:39],s56 offen lds
MF a[32:35],v[114:117],v[46:49],a[32:35]
ds_read_b64_tr_b16 v[18:19],v157
MF a[36:39],v[114:117],v[50:53],a[36:39]
ds_read_b64_tr_b16 v[20:21],v157 offset:256
MF a[40:43],v[114:117],v[54:57],a[40:43]
s_add_u32 m0,s57,3072
MF a[44:47],v[114:117],v[58:61],a[44:47]
buffer_load_dwordx4 v149,s[36:39],s56 offen lds
MF a[48:51],v[114:117],v[62:65],a[48:51]
ds_read_b64_tr_b16 v[22:23],v156 offset:512
MF a[52:55],v[114:117],v[66:69],a[52:55]
s_add_u32 m0,s57,4096
MF a[56:59],v[114:117],v[70:73],a[56:59]
buffer_load_dwordx4 v150,s[36:39],s56 offen lds
MF a[60:63],v[114:117],v[74:77],a[60:63]
ds_read_b64_tr_b16 v[24:25],v156 offset:768
MF a[64:67],v[118:121],v[46:49],a[64:67]
ds_read_b64_tr_b16 v[26:27],v157 offset:512
MF a[68:71],v[118:121],v[50:53],a[68:71]
s_add_u32 m0,s57,5120
MF a[72:75],v[118:121],v[54:57],a[72:75]
buffer_load_dwordx4 v151,s[36:39],s56 offen lds
MF a[76:79],v[118:121],v[58:61],a[76:79]
ds_read_b64_tr_b16 v[28:29],v157 offset:768
MF a[80:83],v[118:121],v[62:65],a[80:83]
s_add_u32 m0,s57,6144
MF a[84:87],v[118:121],v[66:69],a[84:87]
buffer_load_dwordx4 v152,s[36:39],s56 offen lds
MF a[88:91],v[118:121],v[70:73],a[88:91]
ds_read_b64_tr_b16 v[30:31],v156 offset:1024
MF a[92:95],v[118:121],v[74:77],a[92:95]
ds_read_b64_tr_b16 v[32:33],v156 offset:1280
MF a[96:99],v[122:125],v[46:49],a[96:99]
s_add_u32 m0,s57,7168
MF a[100:103],v[122:125],v[50:53],a[100:103]
buffer_load_dwordx4 v153,s[36:39],s56 offen lds
MF a[104:107],v[122:125],v[54:57],a[104:107]
ds_read_b64_tr_b16 v[34:35],v157 offset:1024
MF a[108:111],v[122:125],v[58:61],a[108:111]
s_add_u32 m0,s58,32768
MF a[112:115],v[122:125],v[62:65],a[112:115]
buffer_load_dwordx4 v9,s[40:43],s55 offen lds
MF a[116:119],v[122:125],v[66:69],a[116:119]
ds_read_b64_tr_b16 v[36:37],v157 offset:1280
MF a[120:123],v[122:125],v[70:73],a[120:123]
ds_read_b64_tr_b16 v[38:39],v156 offset:1536
MF a[124:127],v[122:125],v[74:77],a[124:127]
s_add_u32 m0,s58,33792
MF a[128:131],v[126:129],v[46:49],a[128:131]
buffer_load_dwordx4 v10,s[40:43],s55 offen lds
MF a[132:135],v[126:129],v[50:53],a[132:135]
ds_read_b64_tr_b16 v[40:41],v156 offset:1792
MF a[136:139],v[126:129],v[54:57],a[136:139]
s_add_u32 m0,s58,34816
MF a[140:143],v[126:129],v[58:61],a[140:143]
buffer_load_dwordx4 v11,s[40:43],s55 offen lds
MF a[144:147],v[126:129],v[62:65],a[144:147]
ds_read_b64_tr_b16 v[42:43],v157 offset:1536
MF a[148:151],v[126:129],v[66:69],a[148:151]
ds_read_b64_tr_b16 v[44:45],v157 offset:1792
MF a[152:155],v[126:129],v[70:73],a[152:155]
s_add_u32 m0,s58,35840
MF a[156:159],v[126:129],v[74:77],a[156:159]
buffer_load_dwordx4 v12,s[40:43],s55 offen lds
MF a[160:163],v[130:133],v[46:49],a[160:163]
ds_read_b128 v[82:85],v144 offset:2048
MF a[164:167],v[130:133],v[50:53],a[164:167]
s_add_u32 m0,s58,49152
MF a[168:171],v[130:133],v[54:57],a[168:171]
buffer_load_dwordx4 v9,s[44:47],s55 offen lds
MF a[172:175],v[130:133],v[58:61],a[172:175]
ds_read_b128 v[86:89],v144 offset:4096
MF a[176:179],v[130:133],v[62:65],a[176:179]
ds_read_b128 v[90:93],v144 offset:6144
MF a[180:183],v[130:133],v[66:69],a[180:183]
s_add_u32 m0,s58,50176
MF a[184:187],v[130:133],v[70:73],a[184:187]
buffer_load_dwordx4 v10,s[44:47],s55 offen lds
MF a[188:191],v[130:133],v[74:77],a[188:191]
ds_read_b128 v[94:97],v144 offset:8192
MF a[192:195],v[134:137],v[46:49],a[192:195]
s_add_u32 m0,s58,51200
MF a[196:199],v[134:137],v[50:53],a[196:199]
buffer_load_dwordx4 v11,s[44:47],s55 offen lds
MF a[200:203],v[134:137],v[54:57],a[200:203]
ds_read_b128 v[98:101],v144 offset:10240
MF a[204:207],v[134:137],v[58:61],a[204:207]
ds_read_b128 v[102:105],v144 offset:12288
MF a[208:211],v[134:137],v[62:65],a[208:211]
s_add_u32 m0,s58,52224
MF a[212:215],v[134:137],v[66:69],a[212:215]
buffer_load_dwordx4 v12,s[44:47],s55 offen lds
MF a[216:219],v[134:137],v[70:73],a[216:219]
ds_read_b128 v[106:109],v144 offset:14336
MF a[220:223],v[134:137],v[74:77],a[220:223]
MF a[224:227],v[138:141],v[46:49],a[224:227]
MF a[228:231],v[138:141],v[50:53],a[228:231]
MF a[232:235],v[138:141],v[54:57],a[232:235]
MF a[236:239],v[138:141],v[58:61],a[236:239]
MF a[240:243],v[138:141],v[62:65],a[240:243]
MF a[244:247],v[138:141],v[66:69],a[244:247]
MF a[248:251],v[138:141],v[70:73],a[248:251]
MF a[252:255],v[138:141],v[74:77],a[252:255]
s_add_u32 s56,s56,128
s_add_u32 s55,s55,524288
s_waitcnt lgkmcnt(15)
MF a[0:3],v[78:81],v[14:17],a[0:3]
ds_read_b64_tr_b16 v[46:47],v156 offset:8192
v_cndmask_b32_e64 v190,0,v190,s[60:61]
MF a[4:7],v[78:81],v[18:21],a[4:7]
MF a[8:11],v[78:81],v[22:25],a[8:11]
ds_read_b64_tr_b16 v[48:49],v156 offset:8448
v_cndmask_b32_e64 v191,0,v191,s[60:61]
MF a[12:15],v[78:81],v[26:29],a[12:15]
v_cndmask_b32_e64 v192,0,v192,s[60:61]
s_waitcnt lgkmcnt(15)
MF a[16:19],v[78:81],v[30:33],a[16:19]
ds_read_b128 v[110:113],v145
v_cndmask_b32_e64 v193,0,v193,s[60:61]
s_waitcnt lgkmcnt(14)
MF a[20:23],v[78:81],v[34:37],a[20:23]
)A"
R"A(s_waitcnt lgkmcnt(12)
MF a[24:27],v[78:81],v[38:41],a[24:27]
ds_read_b64_tr_b16 v[50:51],v157 offset:8192
v_cndmask_b32_e64 v194,0,v194,s[60:61]
s_waitcnt lgkmcnt(11)
MF a[28:31],v[78:81],v[42:45],a[28:31]
v_cndmask_b32_e64 v195,0,v195,s[60:61]
s_waitcnt lgkmcnt(10)
MF a[32:35],v[82:85],v[14:17],a[32:35]
ds_read_b64_tr_b16 v[52:53],v157 offset:8448
MF a[36:39],v[82:85],v[18:21],a[36:39]
v_mul_f32 v200,v172,v172
MF a[40:43],v[82:85],v[22:25],a[40:43]
ds_read_b64_tr_b16 v[54:55],v156 offset:8704
v_mul_f32 v201,v173,v173
MF a[44:47],v[82:85],v[26:29],a[44:47]
MF a[48:51],v[82:85],v[30:33],a[48:51]
ds_read_b64_tr_b16 v[56:57],v156 offset:8960
v_fmac_f32 v200,v174,v174
MF a[52:55],v[82:85],v[34:37],a[52:55]
v_fmac_f32 v201,v175,v175
MF a[56:59],v[82:85],v[38:41],a[56:59]
ds_read_b64_tr_b16 v[58:59],v157 offset:8704
v_fmac_f32 v200,v176,v176
MF a[60:63],v[82:85],v[42:45],a[60:63]
s_waitcnt lgkmcnt(13)
MF a[64:67],v[86:89],v[14:17],a[64:67]
ds_read_b64_tr_b16 v[60:61],v157 offset:8960
v_fmac_f32 v201,v177,v177
MF a[68:71],v[86:89],v[18:21],a[68:71]
v_fmac_f32 v200,v178,v178
MF a[72:75],v[86:89],v[22:25],a[72:75]
ds_read_b64_tr_b16 v[62:63],v156 offset:9216
MF a[76:79],v[86:89],v[26:29],a[76:79]
v_fmac_f32 v201,v179,v179
MF a[80:83],v[86:89],v[30:33],a[80:83]
ds_read_b64_tr_b16 v[64:65],v156 offset:9472
v_fmac_f32 v200,v180,v180
MF a[84:87],v[86:89],v[34:37],a[84:87]
MF a[88:91],v[86:89],v[38:41],a[88:91]
ds_read_b64_tr_b16 v[66:67],v157 offset:9216
v_fmac_f32 v201,v181,v181
MF a[92:95],v[86:89],v[42:45],a[92:95]
v_fmac_f32 v200,v182,v182
s_waitcnt lgkmcnt(15)
MF a[96:99],v[90:93],v[14:17],a[96:99]
ds_read_b64_tr_b16 v[68:69],v157 offset:9472
v_fmac_f32 v201,v183,v183
MF a[100:103],v[90:93],v[18:21],a[100:103]
MF a[104:107],v[90:93],v[22:25],a[104:107]
ds_read_b64_tr_b16 v[70:71],v156 offset:9728
v_fmac_f32 v200,v184,v184
MF a[108:111],v[90:93],v[26:29],a[108:111]
v_fmac_f32 v201,v185,v185
MF a[112:115],v[90:93],v[30:33],a[112:115]
ds_read_b64_tr_b16 v[72:73],v156 offset:9984
MF a[116:119],v[90:93],v[34:37],a[116:119]
v_fmac_f32 v200,v186,v186
MF a[120:123],v[90:93],v[38:41],a[120:123]
ds_read_b64_tr_b16 v[74:75],v157 offset:9728
v_fmac_f32 v201,v187,v187
MF a[124:127],v[90:93],v[42:45],a[124:127]
MF a[128:131],v[94:97],v[14:17],a[128:131]
ds_read_b64_tr_b16 v[76:77],v157 offset:9984
v_fmac_f32 v200,v188,v188
MF a[132:135],v[94:97],v[18:21],a[132:135]
v_fmac_f32 v201,v189,v189
MF a[136:139],v[94:97],v[22:25],a[136:139]
ds_read_b128 v[114:117],v145 offset:2048
v_fmac_f32 v200,v190,v190
MF a[140:143],v[94:97],v[26:29],a[140:143]
MF a[144:147],v[94:97],v[30:33],a[144:147]
ds_read_b128 v[118:121],v145 offset:4096
v_fmac_f32 v201,v191,v191
MF a[148:151],v[94:97],v[34:37],a[148:151]
v_fmac_f32 v200,v192,v192
MF a[152:155],v[94:97],v[38:41],a[152:155]
ds_read_b128 v[122:125],v145 offset:6144
MF a[156:159],v[94:97],v[42:45],a[156:159]
v_fmac_f32 v201,v193,v193
s_waitcnt lgkmcnt(15)
MF a[160:163],v[98:101],v[14:17],a[160:163]
ds_read_b128 v[126:129],v145 offset:8192
v_fmac_f32 v200,v194,v194
MF a[164:167],v[98:101],v[18:21],a[164:167]
v_fmac_f32 v201,v195,v195
MF a[168:171],v[98:101],v[22:25],a[168:171]
ds_read_b128 v[130:133],v145 offset:10240
MF a[172:175],v[98:101],v[26:29],a[172:175]
v_add_f32 v200,v200,v201
MF a[176:179],v[98:101],v[30:33],a[176:179]
ds_read_b128 v[134:137],v145 offset:12288
v_add_f32_dpp v200,v200,v200 row_ror:8 row_mask:0xf bank_mask:0xf
MF a[180:183],v[98:101],v[34:37],a[180:183]
MF a[184:187],v[98:101],v[38:41],a[184:187]
ds_read_b128 v[138:141],v145 offset:14336
v_add_f32_dpp v200,v200,v200 row_ror:4 row_mask:0xf bank_mask:0xf
MF a[188:191],v[98:101],v[42:45],a[188:191]
s_nop 0
v_add_f32_dpp v200,v200,v200 row_ror:2 row_mask:0xf bank_mask:0xf
MF a[192:195],v[102:105],v[14:17],a[192:195]
MF a[196:199],v[102:105],v[18:21],a[196:199]
v_add_f32_dpp v200,v200,v200 row_ror:1 row_mask:0xf bank_mask:0xf
MF a[200:203],v[102:105],v[22:25],a[200:203]
v_mov_b32 v201,v200
MF a[204:207],v[102:105],v[26:29],a[204:207]
s_nop 0
v_permlane16_swap_b32 v200,v201
MF a[208:211],v[102:105],v[30:33],a[208:211]
MF a[212:215],v[102:105],v[34:37],a[212:215]
v_add_f32 v200,v200,v201
MF a[216:219],v[102:105],v[38:41],a[216:219]
v_mov_b32 v201,0x3727c5ac
MF a[220:223],v[102:105],v[42:45],a[220:223]
MF a[224:227],v[106:109],v[14:17],a[224:227]
v_fmac_f32 v201,0x3acccccd,v200
MF a[228:231],v[106:109],v[18:21],a[228:231]
v_rsq_f32 v201,v201
MF a[232:235],v[106:109],v[22:25],a[232:235]
MF a[236:239],v[106:109],v[26:29],a[236:239]
v_mul_f32 v201,0x3e0293ee,v201
MF a[240:243],v[106:109],v[30:33],a[240:243]
v_mul_f32 v172,v172,v201
MF a[244:247],v[106:109],v[34:37],a[244:247]
v_mul_f32 v173,v173,v201
MF a[248:251],v[106:109],v[38:41],a[248:251]
MF a[252:255],v[106:109],v[42:45],a[252:255]
v_mul_f32 v174,v174,v201
s_waitcnt vmcnt(0) lgkmcnt(0)
s_barrier
s_add_u32 m0,s57,65536
MF a[0:3],v[110:113],v[46:49],a[0:3]
buffer_load_dwordx4 v146,s[36:39],s56 offen lds
MF a[4:7],v[110:113],v[50:53],a[4:7]
ds_read_b64_tr_b16 v[14:15],v154
MF a[8:11],v[110:113],v[54:57],a[8:11]
ds_read_b64_tr_b16 v[16:17],v154 offset:256
MF a[12:15],v[110:113],v[58:61],a[12:15]
s_add_u32 m0,s57,66560
MF a[16:19],v[110:113],v[62:65],a[16:19]
buffer_load_dwordx4 v147,s[36:39],s56 offen lds
MF a[20:23],v[110:113],v[66:69],a[20:23]
ds_read_b128 v[78:81],v142
MF a[24:27],v[110:113],v[70:73],a[24:27]
s_add_u32 m0,s57,67584
MF a[28:31],v[110:113],v[74:77],a[28:31]
buffer_load_dwordx4 v148,s[36:39],s56 offen lds
MF a[32:35],v[114:117],v[46:49],a[32:35]
ds_read_b64_tr_b16 v[18:19],v155
MF a[36:39],v[114:117],v[50:53],a[36:39]
ds_read_b64_tr_b16 v[20:21],v155 offset:256
MF a[40:43],v[114:117],v[54:57],a[40:43]
s_add_u32 m0,s57,68608
MF a[44:47],v[114:117],v[58:61],a[44:47]
buffer_load_dwordx4 v149,s[36:39],s56 offen lds
MF a[48:51],v[114:117],v[62:65],a[48:51]
ds_read_b64_tr_b16 v[22:23],v154 offset:512
MF a[52:55],v[114:117],v[66:69],a[52:55]
s_add_u32 m0,s57,69632
MF a[56:59],v[114:117],v[70:73],a[56:59]
buffer_load_dwordx4 v150,s[36:39],s56 offen lds
MF a[60:63],v[114:117],v[74:77],a[60:63]
ds_read_b64_tr_b16 v[24:25],v154 offset:768
MF a[64:67],v[118:121],v[46:49],a[64:67]
ds_read_b64_tr_b16 v[26:27],v155 offset:512
MF a[68:71],v[118:121],v[50:53],a[68:71]
s_add_u32 m0,s57,70656
MF a[72:75],v[118:121],v[54:57],a[72:75]
buffer_load_dwordx4 v151,s[36:39],s56 offen lds
MF a[76:79],v[118:121],v[58:61],a[76:79]
ds_read_b64_tr_b16 v[28:29],v155 offset:768
MF a[80:83],v[118:121],v[62:65],a[80:83]
s_add_u32 m0,s57,71680
MF a[84:87],v[118:121],v[66:69],a[84:87]
buffer_load_dwordx4 v152,s[36:39],s56 offen lds
MF a[88:91],v[118:121],v[70:73],a[88:91]
ds_read_b64_tr_b16 v[30:31],v154 offset:1024
MF a[92:95],v[118:121],v[74:77],a[92:95]
ds_read_b64_tr_b16 v[32:33],v154 offset:1280
MF a[96:99],v[122:125],v[46:49],a[96:99]
s_add_u32 m0,s57,72704
MF a[100:103],v[122:125],v[50:53],a[100:103]
buffer_load_dwordx4 v153,s[36:39],s56 offen lds
MF a[104:107],v[122:125],v[54:57],a[104:107]
ds_read_b64_tr_b16 v[34:35],v155 offset:1024
MF a[108:111],v[122:125],v[58:61],a[108:111]
s_add_u32 m0,s58,98304
MF a[112:115],v[122:125],v[62:65],a[112:115]
buffer_load_dwordx4 v9,s[40:43],s55 offen lds
MF a[116:119],v[122:125],v[66:69],a[116:119]
ds_read_b64_tr_b16 v[36:37],v155 offset:1280
MF a[120:123],v[122:125],v[70:73],a[120:123]
ds_read_b64_tr_b16 v[38:39],v154 offset:1536
MF a[124:127],v[122:125],v[74:77],a[124:127]
s_add_u32 m0,s58,99328
MF a[128:131],v[126:129],v[46:49],a[128:131]
buffer_load_dwordx4 v10,s[40:43],s55 offen lds
MF a[132:135],v[126:129],v[50:53],a[132:135]
ds_read_b64_tr_b16 v[40:41],v154 offset:1792
MF a[136:139],v[126:129],v[54:57],a[136:139]
s_add_u32 m0,s58,100352
MF a[140:143],v[126:129],v[58:61],a[140:143]
buffer_load_dwordx4 v11,s[40:43],s55 offen lds
MF a[144:147],v[126:129],v[62:65],a[144:147]
ds_read_b64_tr_b16 v[42:43],v155 offset:1536
MF a[148:151],v[126:129],v[66:69],a[148:151]
ds_read_b64_tr_b16 v[44:45],v155 offset:1792
MF a[152:155],v[126:129],v[70:73],a[152:155]
s_add_u32 m0,s58,101376
MF a[156:159],v[126:129],v[74:77],a[156:159]
buffer_load_dwordx4 v12,s[40:43],s55 offen lds
MF a[160:163],v[130:133],v[46:49],a[160:163]
ds_read_b128 v[82:85],v142 offset:2048
MF a[164:167],v[130:133],v[50:53],a[164:167]
s_add_u32 m0,s58,114688
MF a[168:171],v[130:133],v[54:57],a[168:171]
buffer_load_dwordx4 v9,s[44:47],s55 offen lds
MF a[172:175],v[130:133],v[58:61],a[172:175]
ds_read_b128 v[86:89],v142 offset:4096
MF a[176:179],v[130:133],v[62:65],a[176:179]
ds_read_b128 v[90:93],v142 offset:6144
MF a[180:183],v[130:133],v[66:69],a[180:183]
s_add_u32 m0,s58,115712
MF a[184:187],v[130:133],v[70:73],a[184:187]
buffer_load_dwordx4 v10,s[44:47],s55 offen lds
MF a[188:191],v[130:133],v[74:77],a[188:191]
ds_read_b128 v[94:97],v142 offset:8192
MF a[192:195],v[134:137],v[46:49],a[192:195]
s_add_u32 m0,s58,116736
MF a[196:199],v[134:137],v[50:53],a[196:199]
buffer_load_dwordx4 v11,s[44:47],s55 offen lds
MF a[200:203],v[134:137],v[54:57],a[200:203]
ds_read_b128 v[98:101],v142 offset:10240
MF a[204:207],v[134:137],v[58:61],a[204:207]
ds_read_b128 v[102:105],v142 offset:12288
MF a[208:211],v[134:137],v[62:65],a[208:211]
s_add_u32 m0,s58,117760
MF a[212:215],v[134:137],v[66:69],a[212:215]
buffer_load_dwordx4 v12,s[44:47],s55 offen lds
MF a[216:219],v[134:137],v[70:73],a[216:219]
ds_read_b128 v[106:109],v142 offset:14336
MF a[220:223],v[134:137],v[74:77],a[220:223]
MF a[224:227],v[138:141],v[46:49],a[224:227]
MF a[228:231],v[138:141],v[50:53],a[228:231]
MF a[232:235],v[138:141],v[54:57],a[232:235]
MF a[236:239],v[138:141],v[58:61],a[236:239]
MF a[240:243],v[138:141],v[62:65],a[240:243]
MF a[244:247],v[138:141],v[66:69],a[244:247]
MF a[248:251],v[138:141],v[70:73],a[248:251]
MF a[252:255],v[138:141],v[74:77],a[252:255]
s_add_u32 s56,s56,128
s_add_u32 s55,s55,524288
s_waitcnt lgkmcnt(15)
MF a[0:3],v[78:81],v[14:17],a[0:3]
ds_read_b64_tr_b16 v[46:47],v154 offset:8192
v_mul_f32 v175,v175,v201
MF a[4:7],v[78:81],v[18:21],a[4:7]
MF a[8:11],v[78:81],v[22:25],a[8:11]
ds_read_b64_tr_b16 v[48:49],v154 offset:8448
v_mul_f32 v176,v176,v201
MF a[12:15],v[78:81],v[26:29],a[12:15]
v_mul_f32 v177,v177,v201
s_waitcnt lgkmcnt(15)
MF a[16:19],v[78:81],v[30:33],a[16:19]
ds_read_b128 v[110:113],v143
s_waitcnt lgkmcnt(14)
MF a[20:23],v[78:81],v[34:37],a[20:23]
v_mul_f32 v178,v178,v201
s_waitcnt lgkmcnt(12)
MF a[24:27],v[78:81],v[38:41],a[24:27]
ds_read_b64_tr_b16 v[50:51],v155 offset:8192
v_mul_f32 v179,v179,v201
s_waitcnt lgkmcnt(11)
MF a[28:31],v[78:81],v[42:45],a[28:31]
s_waitcnt lgkmcnt(10)
MF a[32:35],v[82:85],v[14:17],a[32:35]
ds_read_b64_tr_b16 v[52:53],v155 offset:8448
v_mul_f32 v180,v180,v201
MF a[36:39],v[82:85],v[18:21],a[36:39]
v_mul_f32 v181,v181,v201
MF a[40:43],v[82:85],v[22:25],a[40:43]
ds_read_b64_tr_b16 v[54:55],v154 offset:8704
MF a[44:47],v[82:85],v[26:29],a[44:47]
v_mul_f32 v182,v182,v201
MF a[48:51],v[82:85],v[30:33],a[48:51]
ds_read_b64_tr_b16 v[56:57],v154 offset:8960
v_mul_f32 v183,v183,v201
MF a[52:55],v[82:85],v[34:37],a[52:55]
MF a[56:59],v[82:85],v[38:41],a[56:59]
ds_read_b64_tr_b16 v[58:59],v155 offset:8704
v_mul_f32 v184,v184,v201
MF a[60:63],v[82:85],v[42:45],a[60:63]
v_mul_f32 v185,v185,v201
s_waitcnt lgkmcnt(13)
MF a[64:67],v[86:89],v[14:17],a[64:67]
ds_read_b64_tr_b16 v[60:61],v155 offset:8960
MF a[68:71],v[86:89],v[18:21],a[68:71]
v_mul_f32 v186,v186,v201
MF a[72:75],v[86:89],v[22:25],a[72:75]
ds_read_b64_tr_b16 v[62:63],v154 offset:9216
v_mul_f32 v187,v187,v201
MF a[76:79],v[86:89],v[26:29],a[76:79]
MF a[80:83],v[86:89],v[30:33],a[80:83]
ds_read_b64_tr_b16 v[64:65],v154 offset:9472
v_mul_f32 v188,v188,v201
MF a[84:87],v[86:89],v[34:37],a[84:87]
v_mul_f32 v189,v189,v201
MF a[88:91],v[86:89],v[38:41],a[88:91]
ds_read_b64_tr_b16 v[66:67],v155 offset:9216
MF a[92:95],v[86:89],v[42:45],a[92:95]
v_mul_f32 v190,v190,v201
s_waitcnt lgkmcnt(15)
MF a[96:99],v[90:93],v[14:17],a[96:99]
ds_read_b64_tr_b16 v[68:69],v155 offset:9472
v_mul_f32 v191,v191,v201
MF a[100:103],v[90:93],v[18:21],a[100:103]
MF a[104:107],v[90:93],v[22:25],a[104:107]
ds_read_b64_tr_b16 v[70:71],v154 offset:9728
v_mul_f32 v192,v192,v201
MF a[108:111],v[90:93],v[26:29],a[108:111]
v_mul_f32 v193,v193,v201
MF a[112:115],v[90:93],v[30:33],a[112:115]
ds_read_b64_tr_b16 v[72:73],v154 offset:9984
MF a[116:119],v[90:93],v[34:37],a[116:119]
v_mul_f32 v194,v194,v201
MF a[120:123],v[90:93],v[38:41],a[120:123]
ds_read_b64_tr_b16 v[74:75],v155 offset:9728
v_mul_f32 v195,v195,v201
MF a[124:127],v[90:93],v[42:45],a[124:127]
MF a[128:131],v[94:97],v[14:17],a[128:131]
ds_read_b64_tr_b16 v[76:77],v155 offset:9984
CP v160,v172,v173
MF a[132:135],v[94:97],v[18:21],a[132:135]
CP v161,v174,v175
MF a[136:139],v[94:97],v[22:25],a[136:139]
ds_read_b128 v[114:117],v143 offset:2048
CP v162,v176,v177
MF a[140:143],v[94:97],v[26:29],a[140:143]
MF a[144:147],v[94:97],v[30:33],a[144:147]
ds_read_b128 v[118:121],v143 offset:4096
CP v163,v178,v179
MF a[148:151],v[94:97],v[34:37],a[148:151]
CP v164,v180,v181
MF a[152:155],v[94:97],v[38:41],a[152:155]
ds_read_b128 v[122:125],v143 offset:6144
MF a[156:159],v[94:97],v[42:45],a[156:159]
CP v165,v182,v183
s_waitcnt lgkmcnt(15)
MF a[160:163],v[98:101],v[14:17],a[160:163]
ds_read_b128 v[126:129],v143 offset:8192
CP v166,v184,v185
MF a[164:167],v[98:101],v[18:21],a[164:167]
MF a[168:171],v[98:101],v[22:25],a[168:171]
ds_read_b128 v[130:133],v143 offset:10240
CP v167,v186,v187
MF a[172:175],v[98:101],v[26:29],a[172:175]
CP v168,v188,v189
MF a[176:179],v[98:101],v[30:33],a[176:179]
ds_read_b128 v[134:137],v143 offset:12288
MF a[180:183],v[98:101],v[34:37],a[180:183]
CP v169,v190,v191
MF a[184:187],v[98:101],v[38:41],a[184:187]
ds_read_b128 v[138:141],v143 offset:14336
CP v170,v192,v193
MF a[188:191],v[98:101],v[42:45],a[188:191]
MF a[192:195],v[102:105],v[14:17],a[192:195]
CP v171,v194,v195
MF a[196:199],v[102:105],v[18:21],a[196:199]
v_lshrrev_b32 v202,4,v198
MF a[200:203],v[102:105],v[22:25],a[200:203]
MF a[204:207],v[102:105],v[26:29],a[204:207]
v_mul_u32_u24 v197,20480,v202
MF a[208:211],v[102:105],v[30:33],a[208:211]
v_lshrrev_b32 v202,2,v199
MF a[212:215],v[102:105],v[34:37],a[212:215]
MF a[216:219],v[102:105],v[38:41],a[216:219]
v_lshl_add_u32 v197,v202,10,v197
MF a[220:223],v[102:105],v[42:45],a[220:223]
v_and_b32 v202,3,v199
MF a[224:227],v[106:109],v[14:17],a[224:227]
MF a[228:231],v[106:109],v[18:21],a[228:231]
v_lshl_add_u32 v197,v202,8,v197
MF a[232:235],v[106:109],v[22:25],a[232:235]
v_and_b32 v202,15,v198
MF a[236:239],v[106:109],v[26:29],a[236:239]
MF a[240:243],v[106:109],v[30:33],a[240:243]
v_lshl_add_u32 v197,v202,4,v197
MF a[244:247],v[106:109],v[34:37],a[244:247]
v_mov_b32 v202,0x7ffffff0
MF a[248:251],v[106:109],v[38:41],a[248:251]
MF a[252:255],v[106:109],v[42:45],a[252:255]
v_cndmask_b32_e64 v203,v202,v197,s[60:61]
s_waitcnt vmcnt(0) lgkmcnt(0)
s_barrier
s_add_u32 m0,s57,0
MF a[0:3],v[110:113],v[46:49],a[0:3]
buffer_load_dwordx4 v146,s[36:39],s56 offen lds
MF a[4:7],v[110:113],v[50:53],a[4:7]
ds_read_b64_tr_b16 v[14:15],v156
MF a[8:11],v[110:113],v[54:57],a[8:11]
ds_read_b64_tr_b16 v[16:17],v156 offset:256
MF a[12:15],v[110:113],v[58:61],a[12:15]
s_add_u32 m0,s57,1024
MF a[16:19],v[110:113],v[62:65],a[16:19]
buffer_load_dwordx4 v147,s[36:39],s56 offen lds
MF a[20:23],v[110:113],v[66:69],a[20:23]
ds_read_b128 v[78:81],v144
MF a[24:27],v[110:113],v[70:73],a[24:27]
s_add_u32 m0,s57,2048
MF a[28:31],v[110:113],v[74:77],a[28:31]
buffer_load_dwordx4 v148,s[36:39],s56 offen lds
MF a[32:35],v[114:117],v[46:49],a[32:35]
ds_read_b64_tr_b16 v[18:19],v157
MF a[36:39],v[114:117],v[50:53],a[36:39]
ds_read_b64_tr_b16 v[20:21],v157 offset:256
MF a[40:43],v[114:117],v[54:57],a[40:43]
s_add_u32 m0,s57,3072
MF a[44:47],v[114:117],v[58:61],a[44:47]
buffer_load_dwordx4 v149,s[36:39],s56 offen lds
MF a[48:51],v[114:117],v[62:65],a[48:51]
ds_read_b64_tr_b16 v[22:23],v156 offset:512
MF a[52:55],v[114:117],v[66:69],a[52:55]
s_add_u32 m0,s57,4096
MF a[56:59],v[114:117],v[70:73],a[56:59]
buffer_load_dwordx4 v150,s[36:39],s56 offen lds
MF a[60:63],v[114:117],v[74:77],a[60:63]
ds_read_b64_tr_b16 v[24:25],v156 offset:768
MF a[64:67],v[118:121],v[46:49],a[64:67]
ds_read_b64_tr_b16 v[26:27],v157 offset:512
MF a[68:71],v[118:121],v[50:53],a[68:71]
s_add_u32 m0,s57,5120
MF a[72:75],v[118:121],v[54:57],a[72:75]
buffer_load_dwordx4 v151,s[36:39],s56 offen lds
MF a[76:79],v[118:121],v[58:61],a[76:79]
ds_read_b64_tr_b16 v[28:29],v157 offset:768
MF a[80:83],v[118:121],v[62:65],a[80:83]
s_add_u32 m0,s57,6144
MF a[84:87],v[118:121],v[66:69],a[84:87]
buffer_load_dwordx4 v152,s[36:39],s56 offen lds
MF a[88:91],v[118:121],v[70:73],a[88:91]
ds_read_b64_tr_b16 v[30:31],v156 offset:1024
MF a[92:95],v[118:121],v[74:77],a[92:95]
ds_read_b64_tr_b16 v[32:33],v156 offset:1280
MF a[96:99],v[122:125],v[46:49],a[96:99]
s_add_u32 m0,s57,7168
MF a[100:103],v[122:125],v[50:53],a[100:103]
buffer_load_dwordx4 v153,s[36:39],s56 offen lds
MF a[104:107],v[122:125],v[54:57],a[104:107]
ds_read_b64_tr_b16 v[34:35],v157 offset:1024
MF a[108:111],v[122:125],v[58:61],a[108:111]
s_add_u32 m0,s58,32768
MF a[112:115],v[122:125],v[62:65],a[112:115]
buffer_load_dwordx4 v9,s[40:43],s55 offen lds
MF a[116:119],v[122:125],v[66:69],a[116:119]
ds_read_b64_tr_b16 v[36:37],v157 offset:1280
MF a[120:123],v[122:125],v[70:73],a[120:123]
ds_read_b64_tr_b16 v[38:39],v156 offset:1536
MF a[124:127],v[122:125],v[74:77],a[124:127]
s_add_u32 m0,s58,33792
MF a[128:131],v[126:129],v[46:49],a[128:131]
buffer_load_dwordx4 v10,s[40:43],s55 offen lds
MF a[132:135],v[126:129],v[50:53],a[132:135]
ds_read_b64_tr_b16 v[40:41],v156 offset:1792
MF a[136:139],v[126:129],v[54:57],a[136:139]
s_add_u32 m0,s58,34816
MF a[140:143],v[126:129],v[58:61],a[140:143]
buffer_load_dwordx4 v11,s[40:43],s55 offen lds
MF a[144:147],v[126:129],v[62:65],a[144:147]
ds_read_b64_tr_b16 v[42:43],v157 offset:1536
MF a[148:151],v[126:129],v[66:69],a[148:151]
ds_read_b64_tr_b16 v[44:45],v157 offset:1792
MF a[152:155],v[126:129],v[70:73],a[152:155]
s_add_u32 m0,s58,35840
MF a[156:159],v[126:129],v[74:77],a[156:159]
buffer_load_dwordx4 v12,s[40:43],s55 offen lds
MF a[160:163],v[130:133],v[46:49],a[160:163]
ds_read_b128 v[82:85],v144 offset:2048
MF a[164:167],v[130:133],v[50:53],a[164:167]
s_add_u32 m0,s58,49152
MF a[168:171],v[130:133],v[54:57],a[168:171]
buffer_load_dwordx4 v9,s[44:47],s55 offen lds
MF a[172:175],v[130:133],v[58:61],a[172:175]
ds_read_b128 v[86:89],v144 offset:4096
MF a[176:179],v[130:133],v[62:65],a[176:179]
ds_read_b128 v[90:93],v144 offset:6144
MF a[180:183],v[130:133],v[66:69],a[180:183]
s_add_u32 m0,s58,50176
MF a[184:187],v[130:133],v[70:73],a[184:187]
buffer_load_dwordx4 v10,s[44:47],s55 offen lds
MF a[188:191],v[130:133],v[74:77],a[188:191]
ds_read_b128 v[94:97],v144 offset:8192
MF a[192:195],v[134:137],v[46:49],a[192:195]
s_add_u32 m0,s58,51200
MF a[196:199],v[134:137],v[50:53],a[196:199]
buffer_load_dwordx4 v11,s[44:47],s55 offen lds
MF a[200:203],v[134:137],v[54:57],a[200:203]
ds_read_b128 v[98:101],v144 offset:10240
MF a[204:207],v[134:137],v[58:61],a[204:207]
ds_read_b128 v[102:105],v144 offset:12288
MF a[208:211],v[134:137],v[62:65],a[208:211]
s_add_u32 m0,s58,52224
MF a[212:215],v[134:137],v[66:69],a[212:215]
buffer_load_dwordx4 v12,s[44:47],s55 offen lds
MF a[216:219],v[134:137],v[70:73],a[216:219]
ds_read_b128 v[106:109],v144 offset:14336
MF a[220:223],v[134:137],v[74:77],a[220:223]
MF a[224:227],v[138:141],v[46:49],a[224:227]
MF a[228:231],v[138:141],v[50:53],a[228:231]
MF a[232:235],v[138:141],v[54:57],a[232:235]
MF a[236:239],v[138:141],v[58:61],a[236:239]
MF a[240:243],v[138:141],v[62:65],a[240:243]
MF a[244:247],v[138:141],v[66:69],a[244:247]
MF a[248:251],v[138:141],v[70:73],a[248:251]
MF a[252:255],v[138:141],v[74:77],a[252:255]
s_add_u32 s56,s56,128
s_add_u32 s55,s55,524288
s_waitcnt lgkmcnt(15)
MF a[0:3],v[78:81],v[14:17],a[0:3]
ds_read_b64_tr_b16 v[46:47],v156 offset:8192
MF a[4:7],v[78:81],v[18:21],a[4:7]
MF a[8:11],v[78:81],v[22:25],a[8:11]
ds_read_b64_tr_b16 v[48:49],v156 offset:8448
MF a[12:15],v[78:81],v[26:29],a[12:15]
s_waitcnt lgkmcnt(15)
MF a[16:19],v[78:81],v[30:33],a[16:19]
ds_read_b128 v[110:113],v145
s_waitcnt lgkmcnt(14)
MF a[20:23],v[78:81],v[34:37],a[20:23]
s_waitcnt lgkmcnt(12)
MF a[24:27],v[78:81],v[38:41],a[24:27]
ds_read_b64_tr_b16 v[50:51],v157 offset:8192
s_waitcnt lgkmcnt(11)
MF a[28:31],v[78:81],v[42:45],a[28:31]
s_waitcnt lgkmcnt(10)
MF a[32:35],v[82:85],v[14:17],a[32:35]
ds_read_b64_tr_b16 v[52:53],v157 offset:8448
MF a[36:39],v[82:85],v[18:21],a[36:39]
MF a[40:43],v[82:85],v[22:25],a[40:43]
ds_read_b64_tr_b16 v[54:55],v156 offset:8704
MF a[44:47],v[82:85],v[26:29],a[44:47]
MF a[48:51],v[82:85],v[30:33],a[48:51]
ds_read_b64_tr_b16 v[56:57],v156 offset:8960
MF a[52:55],v[82:85],v[34:37],a[52:55]
MF a[56:59],v[82:85],v[38:41],a[56:59]
ds_read_b64_tr_b16 v[58:59],v157 offset:8704
MF a[60:63],v[82:85],v[42:45],a[60:63]
s_waitcnt lgkmcnt(13)
MF a[64:67],v[86:89],v[14:17],a[64:67]
ds_read_b64_tr_b16 v[60:61],v157 offset:8960
MF a[68:71],v[86:89],v[18:21],a[68:71]
MF a[72:75],v[86:89],v[22:25],a[72:75]
ds_read_b64_tr_b16 v[62:63],v156 offset:9216
MF a[76:79],v[86:89],v[26:29],a[76:79]
MF a[80:83],v[86:89],v[30:33],a[80:83]
ds_read_b64_tr_b16 v[64:65],v156 offset:9472
MF a[84:87],v[86:89],v[34:37],a[84:87]
MF a[88:91],v[86:89],v[38:41],a[88:91]
ds_read_b64_tr_b16 v[66:67],v157 offset:9216
MF a[92:95],v[86:89],v[42:45],a[92:95]
s_waitcnt lgkmcnt(15)
MF a[96:99],v[90:93],v[14:17],a[96:99]
ds_read_b64_tr_b16 v[68:69],v157 offset:9472
MF a[100:103],v[90:93],v[18:21],a[100:103]
MF a[104:107],v[90:93],v[22:25],a[104:107]
ds_read_b64_tr_b16 v[70:71],v156 offset:9728
MF a[108:111],v[90:93],v[26:29],a[108:111]
MF a[112:115],v[90:93],v[30:33],a[112:115]
ds_read_b64_tr_b16 v[72:73],v156 offset:9984
MF a[116:119],v[90:93],v[34:37],a[116:119]
MF a[120:123],v[90:93],v[38:41],a[120:123]
ds_read_b64_tr_b16 v[74:75],v157 offset:9728
MF a[124:127],v[90:93],v[42:45],a[124:127]
MF a[128:131],v[94:97],v[14:17],a[128:131]
ds_read_b64_tr_b16 v[76:77],v157 offset:9984
MF a[132:135],v[94:97],v[18:21],a[132:135]
MF a[136:139],v[94:97],v[22:25],a[136:139]
ds_read_b128 v[114:117],v145 offset:2048
MF a[140:143],v[94:97],v[26:29],a[140:143]
MF a[144:147],v[94:97],v[30:33],a[144:147]
ds_read_b128 v[118:121],v145 offset:4096
MF a[148:151],v[94:97],v[34:37],a[148:151]
MF a[152:155],v[94:97],v[38:41],a[152:155]
ds_read_b128 v[122:125],v145 offset:6144
MF a[156:159],v[94:97],v[42:45],a[156:159]
s_waitcnt lgkmcnt(15)
MF a[160:163],v[98:101],v[14:17],a[160:163]
ds_read_b128 v[126:129],v145 offset:8192
MF a[164:167],v[98:101],v[18:21],a[164:167]
MF a[168:171],v[98:101],v[22:25],a[168:171]
ds_read_b128 v[130:133],v145 offset:10240
MF a[172:175],v[98:101],v[26:29],a[172:175]
MF a[176:179],v[98:101],v[30:33],a[176:179]
ds_read_b128 v[134:137],v145 offset:12288
MF a[180:183],v[98:101],v[34:37],a[180:183]
MF a[184:187],v[98:101],v[38:41],a[184:187]
ds_read_b128 v[138:141],v145 offset:14336
MF a[188:191],v[98:101],v[42:45],a[188:191]
MF a[192:195],v[102:105],v[14:17],a[192:195]
MF a[196:199],v[102:105],v[18:21],a[196:199]
MF a[200:203],v[102:105],v[22:25],a[200:203]
MF a[204:207],v[102:105],v[26:29],a[204:207]
MF a[208:211],v[102:105],v[30:33],a[208:211]
MF a[212:215],v[102:105],v[34:37],a[212:215]
MF a[216:219],v[102:105],v[38:41],a[216:219]
MF a[220:223],v[102:105],v[42:45],a[220:223]
MF a[224:227],v[106:109],v[14:17],a[224:227]
MF a[228:231],v[106:109],v[18:21],a[228:231]
MF a[232:235],v[106:109],v[22:25],a[232:235]
MF a[236:239],v[106:109],v[26:29],a[236:239]
MF a[240:243],v[106:109],v[30:33],a[240:243]
MF a[244:247],v[106:109],v[34:37],a[244:247]
MF a[248:251],v[106:109],v[38:41],a[248:251]
MF a[252:255],v[106:109],v[42:45],a[252:255]
s_waitcnt vmcnt(0) lgkmcnt(0)
s_barrier
MF a[0:3],v[110:113],v[46:49],a[0:3]
buffer_store_dwordx4 v[160:163],v197,s[64:67],0 offen
MF a[4:7],v[110:113],v[50:53],a[4:7]
ds_read_b64_tr_b16 v[14:15],v154
MF a[8:11],v[110:113],v[54:57],a[8:11]
ds_read_b64_tr_b16 v[16:17],v154 offset:256
MF a[12:15],v[110:113],v[58:61],a[12:15]
buffer_store_dwordx4 v[164:167],v197,s[64:67],s62 offen
MF a[16:19],v[110:113],v[62:65],a[16:19]
MF a[20:23],v[110:113],v[66:69],a[20:23]
ds_read_b128 v[78:81],v142
MF a[24:27],v[110:113],v[70:73],a[24:27]
buffer_store_dwordx4 v[168:171],v203,s[64:67],s63 offen
MF a[28:31],v[110:113],v[74:77],a[28:31]
ds_read_b64_tr_b16 v[18:19],v155
MF a[32:35],v[114:117],v[46:49],a[32:35]
s_add_u32 m0,s57,65536
MF a[36:39],v[114:117],v[50:53],a[36:39]
buffer_load_dwordx4 v146,s[36:39],s56 offen lds
MF a[40:43],v[114:117],v[54:57],a[40:43]
ds_read_b64_tr_b16 v[20:21],v155 offset:256
MF a[44:47],v[114:117],v[58:61],a[44:47]
ds_read_b64_tr_b16 v[22:23],v154 offset:512
MF a[48:51],v[114:117],v[62:65],a[48:51]
s_add_u32 m0,s57,66560
MF a[52:55],v[114:117],v[66:69],a[52:55]
buffer_load_dwordx4 v147,s[36:39],s56 offen lds
MF a[56:59],v[114:117],v[70:73],a[56:59]
ds_read_b64_tr_b16 v[24:25],v154 offset:768
s_add_u32 m0,s57,67584
MF a[60:63],v[114:117],v[74:77],a[60:63]
buffer_load_dwordx4 v148,s[36:39],s56 offen lds
MF a[64:67],v[118:121],v[46:49],a[64:67]
ds_read_b64_tr_b16 v[26:27],v155 offset:512
MF a[68:71],v[118:121],v[50:53],a[68:71]
s_add_u32 m0,s57,68608
MF a[72:75],v[118:121],v[54:57],a[72:75]
buffer_load_dwordx4 v149,s[36:39],s56 offen lds
MF a[76:79],v[118:121],v[58:61],a[76:79]
ds_read_b64_tr_b16 v[28:29],v155 offset:768
s_add_u32 m0,s57,69632
MF a[80:83],v[118:121],v[62:65],a[80:83]
buffer_load_dwordx4 v150,s[36:39],s56 offen lds
MF a[84:87],v[118:121],v[66:69],a[84:87]
MF a[88:91],v[118:121],v[70:73],a[88:91]
ds_read_b64_tr_b16 v[30:31],v154 offset:1024
MF a[92:95],v[118:121],v[74:77],a[92:95]
ds_read_b64_tr_b16 v[32:33],v154 offset:1280
s_add_u32 m0,s57,70656
MF a[96:99],v[122:125],v[46:49],a[96:99]
buffer_load_dwordx4 v151,s[36:39],s56 offen lds
MF a[100:103],v[122:125],v[50:53],a[100:103]
MF a[104:107],v[122:125],v[54:57],a[104:107]
ds_read_b64_tr_b16 v[34:35],v155 offset:1024
s_add_u32 m0,s57,71680
MF a[108:111],v[122:125],v[58:61],a[108:111]
buffer_load_dwordx4 v152,s[36:39],s56 offen lds
MF a[112:115],v[122:125],v[62:65],a[112:115]
ds_read_b64_tr_b16 v[36:37],v155 offset:1280
s_add_u32 m0,s57,72704
MF a[116:119],v[122:125],v[66:69],a[116:119]
buffer_load_dwordx4 v153,s[36:39],s56 offen lds
MF a[120:123],v[122:125],v[70:73],a[120:123]
MF a[124:127],v[122:125],v[74:77],a[124:127]
ds_read_b64_tr_b16 v[38:39],v154 offset:1536
s_add_u32 m0,s58,98304
MF a[128:131],v[126:129],v[46:49],a[128:131]
buffer_load_dwordx4 v9,s[40:43],s55 offen lds
MF a[132:135],v[126:129],v[50:53],a[132:135]
ds_read_b64_tr_b16 v[40:41],v154 offset:1792
MF a[136:139],v[126:129],v[54:57],a[136:139]
MF a[140:143],v[126:129],v[58:61],a[140:143]
ds_read_b64_tr_b16 v[42:43],v155 offset:1536
s_add_u32 m0,s58,99328
MF a[144:147],v[126:129],v[62:65],a[144:147]
buffer_load_dwordx4 v10,s[40:43],s55 offen lds
MF a[148:151],v[126:129],v[66:69],a[148:151]
ds_read_b64_tr_b16 v[44:45],v155 offset:1792
MF a[152:155],v[126:129],v[70:73],a[152:155]
s_add_u32 m0,s58,100352
MF a[156:159],v[126:129],v[74:77],a[156:159]
buffer_load_dwordx4 v11,s[40:43],s55 offen lds
MF a[160:163],v[130:133],v[46:49],a[160:163]
ds_read_b128 v[82:85],v142 offset:2048
s_add_u32 m0,s58,101376
MF a[164:167],v[130:133],v[50:53],a[164:167]
buffer_load_dwordx4 v12,s[40:43],s55 offen lds
MF a[168:171],v[130:133],v[54:57],a[168:171]
ds_read_b128 v[86:89],v142 offset:4096
MF a[172:175],v[130:133],v[58:61],a[172:175]
s_add_u32 m0,s58,114688
MF a[176:179],v[130:133],v[62:65],a[176:179]
buffer_load_dwordx4 v9,s[44:47],s55 offen lds
MF a[180:183],v[130:133],v[66:69],a[180:183]
ds_read_b128 v[90:93],v142 offset:6144
MF a[184:187],v[130:133],v[70:73],a[184:187]
ds_read_b128 v[94:97],v142 offset:8192
MF a[188:191],v[130:133],v[74:77],a[188:191]
s_add_u32 m0,s58,115712
MF a[192:195],v[134:137],v[46:49],a[192:195]
buffer_load_dwordx4 v10,s[44:47],s55 offen lds
MF a[196:199],v[134:137],v[50:53],a[196:199]
ds_read_b128 v[98:101],v142 offset:10240
s_add_u32 m0,s58,116736
MF a[200:203],v[134:137],v[54:57],a[200:203]
buffer_load_dwordx4 v11,s[44:47],s55 offen lds
MF a[204:207],v[134:137],v[58:61],a[204:207]
MF a[208:211],v[134:137],v[62:65],a[208:211]
ds_read_b128 v[102:105],v142 offset:12288
s_add_u32 m0,s58,117760
MF a[212:215],v[134:137],v[66:69],a[212:215]
buffer_load_dwordx4 v12,s[44:47],s55 offen lds
MF a[216:219],v[134:137],v[70:73],a[216:219]
ds_read_b128 v[106:109],v142 offset:14336
MF a[220:223],v[134:137],v[74:77],a[220:223]
MF a[224:227],v[138:141],v[46:49],a[224:227]
MF a[228:231],v[138:141],v[50:53],a[228:231]
MF a[232:235],v[138:141],v[54:57],a[232:235]
MF a[236:239],v[138:141],v[58:61],a[236:239]
MF a[240:243],v[138:141],v[62:65],a[240:243]
MF a[244:247],v[138:141],v[66:69],a[244:247]
MF a[248:251],v[138:141],v[70:73],a[248:251]
MF a[252:255],v[138:141],v[74:77],a[252:255]
s_add_u32 s56,s56,128
s_add_u32 s55,s55,524288
s_mov_b32 m0,s72
s_mov_b32 s68,s30
s_and_b32 s69,s31,0xffff
s_mov_b32 s70,5242880
s_mov_b32 s71,0x20000
buffer_load_dword v204,s[68:71],0 offen lds
s_mov_b32 s68,s32
s_and_b32 s69,s33,0xffff
s_mov_b32 s70,2211840
s_mov_b32 s71,0x20000
buffer_load_dword v204,s[68:71],0 offen lds
s_getpc_b64 s[68:69]
LPCV:
s_add_u32 s68,s68,k2-LPCV
s_addc_u32 s69,s69,0
s_lshr_b32 s100,s6,3
s_lshl_b32 s100,s100,8
v_add_u32 v207,s100,v0
v_min_u32 v207,(LK3END-k2)/128,v207
v_lshlrev_b32 v207,7,v207
global_load_lds_dword v207,s[68:69]
s_lshl_b32 s100,s6,8
v_add_u32 v208,s100,v0
v_min_u32 v208,20000,v208
v_lshlrev_b32 v208,12,v208
global_load_lds_dword v208,s[22:23]
s_waitcnt lgkmcnt(15)
MF a[0:3],v[78:81],v[14:17],a[0:3]
ds_read_b64_tr_b16 v[46:47],v154 offset:8192
MF a[4:7],v[78:81],v[18:21],a[4:7]
MF a[8:11],v[78:81],v[22:25],a[8:11]
ds_read_b64_tr_b16 v[48:49],v154 offset:8448
MF a[12:15],v[78:81],v[26:29],a[12:15]
s_waitcnt lgkmcnt(15)
MF a[16:19],v[78:81],v[30:33],a[16:19]
ds_read_b128 v[110:113],v143
s_waitcnt lgkmcnt(14)
MF a[20:23],v[78:81],v[34:37],a[20:23]
s_waitcnt lgkmcnt(12)
MF a[24:27],v[78:81],v[38:41],a[24:27]
ds_read_b64_tr_b16 v[50:51],v155 offset:8192
s_waitcnt lgkmcnt(11)
MF a[28:31],v[78:81],v[42:45],a[28:31]
s_waitcnt lgkmcnt(10)
)A"
R"A(MF a[32:35],v[82:85],v[14:17],a[32:35]
ds_read_b64_tr_b16 v[52:53],v155 offset:8448
MF a[36:39],v[82:85],v[18:21],a[36:39]
MF a[40:43],v[82:85],v[22:25],a[40:43]
ds_read_b64_tr_b16 v[54:55],v154 offset:8704
MF a[44:47],v[82:85],v[26:29],a[44:47]
MF a[48:51],v[82:85],v[30:33],a[48:51]
ds_read_b64_tr_b16 v[56:57],v154 offset:8960
MF a[52:55],v[82:85],v[34:37],a[52:55]
MF a[56:59],v[82:85],v[38:41],a[56:59]
ds_read_b64_tr_b16 v[58:59],v155 offset:8704
MF a[60:63],v[82:85],v[42:45],a[60:63]
s_waitcnt lgkmcnt(13)
MF a[64:67],v[86:89],v[14:17],a[64:67]
ds_read_b64_tr_b16 v[60:61],v155 offset:8960
MF a[68:71],v[86:89],v[18:21],a[68:71]
MF a[72:75],v[86:89],v[22:25],a[72:75]
ds_read_b64_tr_b16 v[62:63],v154 offset:9216
MF a[76:79],v[86:89],v[26:29],a[76:79]
MF a[80:83],v[86:89],v[30:33],a[80:83]
ds_read_b64_tr_b16 v[64:65],v154 offset:9472
MF a[84:87],v[86:89],v[34:37],a[84:87]
MF a[88:91],v[86:89],v[38:41],a[88:91]
ds_read_b64_tr_b16 v[66:67],v155 offset:9216
MF a[92:95],v[86:89],v[42:45],a[92:95]
s_waitcnt lgkmcnt(15)
MF a[96:99],v[90:93],v[14:17],a[96:99]
ds_read_b64_tr_b16 v[68:69],v155 offset:9472
MF a[100:103],v[90:93],v[18:21],a[100:103]
MF a[104:107],v[90:93],v[22:25],a[104:107]
ds_read_b64_tr_b16 v[70:71],v154 offset:9728
MF a[108:111],v[90:93],v[26:29],a[108:111]
MF a[112:115],v[90:93],v[30:33],a[112:115]
ds_read_b64_tr_b16 v[72:73],v154 offset:9984
MF a[116:119],v[90:93],v[34:37],a[116:119]
MF a[120:123],v[90:93],v[38:41],a[120:123]
ds_read_b64_tr_b16 v[74:75],v155 offset:9728
MF a[124:127],v[90:93],v[42:45],a[124:127]
MF a[128:131],v[94:97],v[14:17],a[128:131]
ds_read_b64_tr_b16 v[76:77],v155 offset:9984
MF a[132:135],v[94:97],v[18:21],a[132:135]
MF a[136:139],v[94:97],v[22:25],a[136:139]
ds_read_b128 v[114:117],v143 offset:2048
MF a[140:143],v[94:97],v[26:29],a[140:143]
MF a[144:147],v[94:97],v[30:33],a[144:147]
ds_read_b128 v[118:121],v143 offset:4096
MF a[148:151],v[94:97],v[34:37],a[148:151]
MF a[152:155],v[94:97],v[38:41],a[152:155]
ds_read_b128 v[122:125],v143 offset:6144
MF a[156:159],v[94:97],v[42:45],a[156:159]
s_waitcnt lgkmcnt(15)
MF a[160:163],v[98:101],v[14:17],a[160:163]
ds_read_b128 v[126:129],v143 offset:8192
MF a[164:167],v[98:101],v[18:21],a[164:167]
MF a[168:171],v[98:101],v[22:25],a[168:171]
ds_read_b128 v[130:133],v143 offset:10240
MF a[172:175],v[98:101],v[26:29],a[172:175]
MF a[176:179],v[98:101],v[30:33],a[176:179]
ds_read_b128 v[134:137],v143 offset:12288
MF a[180:183],v[98:101],v[34:37],a[180:183]
MF a[184:187],v[98:101],v[38:41],a[184:187]
ds_read_b128 v[138:141],v143 offset:14336
MF a[188:191],v[98:101],v[42:45],a[188:191]
MF a[192:195],v[102:105],v[14:17],a[192:195]
MF a[196:199],v[102:105],v[18:21],a[196:199]
MF a[200:203],v[102:105],v[22:25],a[200:203]
MF a[204:207],v[102:105],v[26:29],a[204:207]
MF a[208:211],v[102:105],v[30:33],a[208:211]
MF a[212:215],v[102:105],v[34:37],a[212:215]
MF a[216:219],v[102:105],v[38:41],a[216:219]
MF a[220:223],v[102:105],v[42:45],a[220:223]
MF a[224:227],v[106:109],v[14:17],a[224:227]
MF a[228:231],v[106:109],v[18:21],a[228:231]
MF a[232:235],v[106:109],v[22:25],a[232:235]
MF a[236:239],v[106:109],v[26:29],a[236:239]
MF a[240:243],v[106:109],v[30:33],a[240:243]
MF a[244:247],v[106:109],v[34:37],a[244:247]
MF a[248:251],v[106:109],v[38:41],a[248:251]
MF a[252:255],v[106:109],v[42:45],a[252:255]
s_waitcnt vmcnt(4) lgkmcnt(0)
s_barrier
MF a[0:3],v[110:113],v[46:49],a[0:3]
ds_read_b64_tr_b16 v[14:15],v156
MF a[4:7],v[110:113],v[50:53],a[4:7]
MF a[8:11],v[110:113],v[54:57],a[8:11]
ds_read_b64_tr_b16 v[16:17],v156 offset:256
MF a[12:15],v[110:113],v[58:61],a[12:15]
MF a[16:19],v[110:113],v[62:65],a[16:19]
ds_read_b128 v[78:81],v144
MF a[20:23],v[110:113],v[66:69],a[20:23]
MF a[24:27],v[110:113],v[70:73],a[24:27]
MF a[28:31],v[110:113],v[74:77],a[28:31]
ds_read_b64_tr_b16 v[18:19],v157
MF a[32:35],v[114:117],v[46:49],a[32:35]
MF a[36:39],v[114:117],v[50:53],a[36:39]
ds_read_b64_tr_b16 v[20:21],v157 offset:256
MF a[40:43],v[114:117],v[54:57],a[40:43]
MF a[44:47],v[114:117],v[58:61],a[44:47]
ds_read_b64_tr_b16 v[22:23],v156 offset:512
MF a[48:51],v[114:117],v[62:65],a[48:51]
MF a[52:55],v[114:117],v[66:69],a[52:55]
MF a[56:59],v[114:117],v[70:73],a[56:59]
ds_read_b64_tr_b16 v[24:25],v156 offset:768
MF a[60:63],v[114:117],v[74:77],a[60:63]
MF a[64:67],v[118:121],v[46:49],a[64:67]
ds_read_b64_tr_b16 v[26:27],v157 offset:512
MF a[68:71],v[118:121],v[50:53],a[68:71]
MF a[72:75],v[118:121],v[54:57],a[72:75]
ds_read_b64_tr_b16 v[28:29],v157 offset:768
MF a[76:79],v[118:121],v[58:61],a[76:79]
MF a[80:83],v[118:121],v[62:65],a[80:83]
MF a[84:87],v[118:121],v[66:69],a[84:87]
ds_read_b64_tr_b16 v[30:31],v156 offset:1024
MF a[88:91],v[118:121],v[70:73],a[88:91]
MF a[92:95],v[118:121],v[74:77],a[92:95]
ds_read_b64_tr_b16 v[32:33],v156 offset:1280
MF a[96:99],v[122:125],v[46:49],a[96:99]
MF a[100:103],v[122:125],v[50:53],a[100:103]
ds_read_b64_tr_b16 v[34:35],v157 offset:1024
MF a[104:107],v[122:125],v[54:57],a[104:107]
MF a[108:111],v[122:125],v[58:61],a[108:111]
MF a[112:115],v[122:125],v[62:65],a[112:115]
ds_read_b64_tr_b16 v[36:37],v157 offset:1280
MF a[116:119],v[122:125],v[66:69],a[116:119]
MF a[120:123],v[122:125],v[70:73],a[120:123]
ds_read_b64_tr_b16 v[38:39],v156 offset:1536
MF a[124:127],v[122:125],v[74:77],a[124:127]
MF a[128:131],v[126:129],v[46:49],a[128:131]
ds_read_b64_tr_b16 v[40:41],v156 offset:1792
MF a[132:135],v[126:129],v[50:53],a[132:135]
MF a[136:139],v[126:129],v[54:57],a[136:139]
MF a[140:143],v[126:129],v[58:61],a[140:143]
ds_read_b64_tr_b16 v[42:43],v157 offset:1536
MF a[144:147],v[126:129],v[62:65],a[144:147]
MF a[148:151],v[126:129],v[66:69],a[148:151]
ds_read_b64_tr_b16 v[44:45],v157 offset:1792
MF a[152:155],v[126:129],v[70:73],a[152:155]
MF a[156:159],v[126:129],v[74:77],a[156:159]
ds_read_b128 v[82:85],v144 offset:2048
MF a[160:163],v[130:133],v[46:49],a[160:163]
MF a[164:167],v[130:133],v[50:53],a[164:167]
MF a[168:171],v[130:133],v[54:57],a[168:171]
ds_read_b128 v[86:89],v144 offset:4096
MF a[172:175],v[130:133],v[58:61],a[172:175]
MF a[176:179],v[130:133],v[62:65],a[176:179]
ds_read_b128 v[90:93],v144 offset:6144
MF a[180:183],v[130:133],v[66:69],a[180:183]
MF a[184:187],v[130:133],v[70:73],a[184:187]
ds_read_b128 v[94:97],v144 offset:8192
MF a[188:191],v[130:133],v[74:77],a[188:191]
MF a[192:195],v[134:137],v[46:49],a[192:195]
MF a[196:199],v[134:137],v[50:53],a[196:199]
ds_read_b128 v[98:101],v144 offset:10240
MF a[200:203],v[134:137],v[54:57],a[200:203]
MF a[204:207],v[134:137],v[58:61],a[204:207]
ds_read_b128 v[102:105],v144 offset:12288
MF a[208:211],v[134:137],v[62:65],a[208:211]
MF a[212:215],v[134:137],v[66:69],a[212:215]
ds_read_b128 v[106:109],v144 offset:14336
MF a[216:219],v[134:137],v[70:73],a[216:219]
MF a[220:223],v[134:137],v[74:77],a[220:223]
MF a[224:227],v[138:141],v[46:49],a[224:227]
MF a[228:231],v[138:141],v[50:53],a[228:231]
MF a[232:235],v[138:141],v[54:57],a[232:235]
MF a[236:239],v[138:141],v[58:61],a[236:239]
MF a[240:243],v[138:141],v[62:65],a[240:243]
MF a[244:247],v[138:141],v[66:69],a[244:247]
MF a[248:251],v[138:141],v[70:73],a[248:251]
MF a[252:255],v[138:141],v[74:77],a[252:255]
s_mov_b32 s48,s16
s_and_b32 s49,s17,0xffff
s_mov_b32 s50,16777216
s_mov_b32 s51,0x20000
s_waitcnt lgkmcnt(15)
MF a[0:3],v[78:81],v[14:17],a[0:3]
ds_read_b64_tr_b16 v[46:47],v156 offset:8192
MF a[4:7],v[78:81],v[18:21],a[4:7]
MF a[8:11],v[78:81],v[22:25],a[8:11]
ds_read_b64_tr_b16 v[48:49],v156 offset:8448
MF a[12:15],v[78:81],v[26:29],a[12:15]
s_waitcnt lgkmcnt(15)
MF a[16:19],v[78:81],v[30:33],a[16:19]
ds_read_b128 v[110:113],v145
s_waitcnt lgkmcnt(14)
MF a[20:23],v[78:81],v[34:37],a[20:23]
s_waitcnt lgkmcnt(12)
MF a[24:27],v[78:81],v[38:41],a[24:27]
ds_read_b64_tr_b16 v[50:51],v157 offset:8192
s_waitcnt lgkmcnt(11)
MF a[28:31],v[78:81],v[42:45],a[28:31]
s_waitcnt lgkmcnt(10)
MF a[32:35],v[82:85],v[14:17],a[32:35]
ds_read_b64_tr_b16 v[52:53],v157 offset:8448
MF a[36:39],v[82:85],v[18:21],a[36:39]
MF a[40:43],v[82:85],v[22:25],a[40:43]
ds_read_b64_tr_b16 v[54:55],v156 offset:8704
MF a[44:47],v[82:85],v[26:29],a[44:47]
MF a[48:51],v[82:85],v[30:33],a[48:51]
ds_read_b64_tr_b16 v[56:57],v156 offset:8960
MF a[52:55],v[82:85],v[34:37],a[52:55]
MF a[56:59],v[82:85],v[38:41],a[56:59]
ds_read_b64_tr_b16 v[58:59],v157 offset:8704
MF a[60:63],v[82:85],v[42:45],a[60:63]
s_waitcnt lgkmcnt(13)
MF a[64:67],v[86:89],v[14:17],a[64:67]
ds_read_b64_tr_b16 v[60:61],v157 offset:8960
MF a[68:71],v[86:89],v[18:21],a[68:71]
MF a[72:75],v[86:89],v[22:25],a[72:75]
ds_read_b64_tr_b16 v[62:63],v156 offset:9216
MF a[76:79],v[86:89],v[26:29],a[76:79]
MF a[80:83],v[86:89],v[30:33],a[80:83]
ds_read_b64_tr_b16 v[64:65],v156 offset:9472
MF a[84:87],v[86:89],v[34:37],a[84:87]
MF a[88:91],v[86:89],v[38:41],a[88:91]
ds_read_b64_tr_b16 v[66:67],v157 offset:9216
MF a[92:95],v[86:89],v[42:45],a[92:95]
s_waitcnt lgkmcnt(15)
MF a[96:99],v[90:93],v[14:17],a[96:99]
ds_read_b64_tr_b16 v[68:69],v157 offset:9472
MF a[100:103],v[90:93],v[18:21],a[100:103]
MF a[104:107],v[90:93],v[22:25],a[104:107]
ds_read_b64_tr_b16 v[70:71],v156 offset:9728
MF a[108:111],v[90:93],v[26:29],a[108:111]
MF a[112:115],v[90:93],v[30:33],a[112:115]
ds_read_b64_tr_b16 v[72:73],v156 offset:9984
MF a[116:119],v[90:93],v[34:37],a[116:119]
MF a[120:123],v[90:93],v[38:41],a[120:123]
ds_read_b64_tr_b16 v[74:75],v157 offset:9728
MF a[124:127],v[90:93],v[42:45],a[124:127]
MF a[128:131],v[94:97],v[14:17],a[128:131]
ds_read_b64_tr_b16 v[76:77],v157 offset:9984
MF a[132:135],v[94:97],v[18:21],a[132:135]
MF a[136:139],v[94:97],v[22:25],a[136:139]
ds_read_b128 v[114:117],v145 offset:2048
MF a[140:143],v[94:97],v[26:29],a[140:143]
MF a[144:147],v[94:97],v[30:33],a[144:147]
ds_read_b128 v[118:121],v145 offset:4096
MF a[148:151],v[94:97],v[34:37],a[148:151]
MF a[152:155],v[94:97],v[38:41],a[152:155]
ds_read_b128 v[122:125],v145 offset:6144
MF a[156:159],v[94:97],v[42:45],a[156:159]
s_waitcnt lgkmcnt(15)
MF a[160:163],v[98:101],v[14:17],a[160:163]
ds_read_b128 v[126:129],v145 offset:8192
MF a[164:167],v[98:101],v[18:21],a[164:167]
MF a[168:171],v[98:101],v[22:25],a[168:171]
ds_read_b128 v[130:133],v145 offset:10240
MF a[172:175],v[98:101],v[26:29],a[172:175]
MF a[176:179],v[98:101],v[30:33],a[176:179]
ds_read_b128 v[134:137],v145 offset:12288
MF a[180:183],v[98:101],v[34:37],a[180:183]
MF a[184:187],v[98:101],v[38:41],a[184:187]
ds_read_b128 v[138:141],v145 offset:14336
MF a[188:191],v[98:101],v[42:45],a[188:191]
MF a[192:195],v[102:105],v[14:17],a[192:195]
MF a[196:199],v[102:105],v[18:21],a[196:199]
MF a[200:203],v[102:105],v[22:25],a[200:203]
MF a[204:207],v[102:105],v[26:29],a[204:207]
MF a[208:211],v[102:105],v[30:33],a[208:211]
MF a[212:215],v[102:105],v[34:37],a[212:215]
MF a[216:219],v[102:105],v[38:41],a[216:219]
MF a[220:223],v[102:105],v[42:45],a[220:223]
MF a[224:227],v[106:109],v[14:17],a[224:227]
MF a[228:231],v[106:109],v[18:21],a[228:231]
MF a[232:235],v[106:109],v[22:25],a[232:235]
MF a[236:239],v[106:109],v[26:29],a[236:239]
MF a[240:243],v[106:109],v[30:33],a[240:243]
MF a[244:247],v[106:109],v[34:37],a[244:247]
MF a[248:251],v[106:109],v[38:41],a[248:251]
MF a[252:255],v[106:109],v[42:45],a[252:255]
s_waitcnt lgkmcnt(0)
MF a[0:3],v[110:113],v[46:49],a[0:3]
MF a[4:7],v[110:113],v[50:53],a[4:7]
MF a[8:11],v[110:113],v[54:57],a[8:11]
MF a[12:15],v[110:113],v[58:61],a[12:15]
MF a[16:19],v[110:113],v[62:65],a[16:19]
MF a[20:23],v[110:113],v[66:69],a[20:23]
MF a[24:27],v[110:113],v[70:73],a[24:27]
MF a[28:31],v[110:113],v[74:77],a[28:31]
MF a[32:35],v[114:117],v[46:49],a[32:35]
MF a[36:39],v[114:117],v[50:53],a[36:39]
MF a[40:43],v[114:117],v[54:57],a[40:43]
MF a[44:47],v[114:117],v[58:61],a[44:47]
AR v224,a0
AR v225,a1
AR v226,a2
MF a[48:51],v[114:117],v[62:65],a[48:51]
AR v227,a3
AR v228,a32
AR v229,a33
MF a[52:55],v[114:117],v[66:69],a[52:55]
AR v230,a34
AR v231,a35
CP v216,v224,v225
MF a[56:59],v[114:117],v[70:73],a[56:59]
CP v217,v226,v227
CP v218,v228,v229
CP v219,v230,v231
MF a[60:63],v[114:117],v[74:77],a[60:63]
buffer_store_dwordx4 v[216:219],v8,s[48:51],s75 offen nt
AR v232,a4
MF a[64:67],v[118:121],v[46:49],a[64:67]
AR v233,a5
AR v234,a6
AR v235,a7
MF a[68:71],v[118:121],v[50:53],a[68:71]
AR v236,a36
AR v237,a37
AR v238,a38
MF a[72:75],v[118:121],v[54:57],a[72:75]
AR v239,a39
CP v220,v232,v233
CP v221,v234,v235
MF a[76:79],v[118:121],v[58:61],a[76:79]
CP v222,v236,v237
CP v223,v238,v239
MF a[80:83],v[118:121],v[62:65],a[80:83]
buffer_store_dwordx4 v[220:223],v8,s[48:51],s75 offen offset:1024 nt
AR v224,a8
MF a[84:87],v[118:121],v[66:69],a[84:87]
AR v225,a9
AR v226,a10
AR v227,a11
MF a[88:91],v[118:121],v[70:73],a[88:91]
AR v228,a40
AR v229,a41
AR v230,a42
MF a[92:95],v[118:121],v[74:77],a[92:95]
AR v231,a43
CP v216,v224,v225
CP v217,v226,v227
MF a[96:99],v[122:125],v[46:49],a[96:99]
CP v218,v228,v229
CP v219,v230,v231
MF a[100:103],v[122:125],v[50:53],a[100:103]
buffer_store_dwordx4 v[216:219],v8,s[48:51],s75 offen offset:2048 nt
AR v232,a12
MF a[104:107],v[122:125],v[54:57],a[104:107]
AR v233,a13
AR v234,a14
AR v235,a15
MF a[108:111],v[122:125],v[58:61],a[108:111]
AR v236,a44
AR v237,a45
AR v238,a46
MF a[112:115],v[122:125],v[62:65],a[112:115]
AR v239,a47
CP v220,v232,v233
CP v221,v234,v235
MF a[116:119],v[122:125],v[66:69],a[116:119]
CP v222,v236,v237
CP v223,v238,v239
MF a[120:123],v[122:125],v[70:73],a[120:123]
buffer_store_dwordx4 v[220:223],v8,s[48:51],s75 offen offset:3072 nt
s_add_u32 s75,s75,4096
MF a[124:127],v[122:125],v[74:77],a[124:127]
AR v224,a16
AR v225,a17
AR v226,a18
MF a[128:131],v[126:129],v[46:49],a[128:131]
AR v227,a19
AR v228,a48
AR v229,a49
MF a[132:135],v[126:129],v[50:53],a[132:135]
AR v230,a50
AR v231,a51
CP v216,v224,v225
MF a[136:139],v[126:129],v[54:57],a[136:139]
CP v217,v226,v227
CP v218,v228,v229
CP v219,v230,v231
MF a[140:143],v[126:129],v[58:61],a[140:143]
buffer_store_dwordx4 v[216:219],v8,s[48:51],s75 offen nt
MF a[144:147],v[126:129],v[62:65],a[144:147]
AR v232,a20
AR v233,a21
MF a[148:151],v[126:129],v[66:69],a[148:151]
AR v234,a22
AR v235,a23
MF a[152:155],v[126:129],v[70:73],a[152:155]
AR v236,a52
AR v237,a53
MF a[156:159],v[126:129],v[74:77],a[156:159]
AR v238,a54
AR v239,a55
MF a[160:163],v[130:133],v[46:49],a[160:163]
CP v220,v232,v233
CP v221,v234,v235
MF a[164:167],v[130:133],v[50:53],a[164:167]
CP v222,v236,v237
CP v223,v238,v239
MF a[168:171],v[130:133],v[54:57],a[168:171]
buffer_store_dwordx4 v[220:223],v8,s[48:51],s75 offen offset:1024 nt
MF a[172:175],v[130:133],v[58:61],a[172:175]
AR v224,a24
AR v225,a25
MF a[176:179],v[130:133],v[62:65],a[176:179]
AR v226,a26
AR v227,a27
MF a[180:183],v[130:133],v[66:69],a[180:183]
AR v228,a56
AR v229,a57
MF a[184:187],v[130:133],v[70:73],a[184:187]
AR v230,a58
AR v231,a59
MF a[188:191],v[130:133],v[74:77],a[188:191]
CP v216,v224,v225
CP v217,v226,v227
MF a[192:195],v[134:137],v[46:49],a[192:195]
CP v218,v228,v229
CP v219,v230,v231
MF a[196:199],v[134:137],v[50:53],a[196:199]
buffer_store_dwordx4 v[216:219],v8,s[48:51],s75 offen offset:2048 nt
MF a[200:203],v[134:137],v[54:57],a[200:203]
AR v232,a28
AR v233,a29
MF a[204:207],v[134:137],v[58:61],a[204:207]
AR v234,a30
AR v235,a31
MF a[208:211],v[134:137],v[62:65],a[208:211]
AR v236,a60
AR v237,a61
MF a[212:215],v[134:137],v[66:69],a[212:215]
AR v238,a62
AR v239,a63
MF a[216:219],v[134:137],v[70:73],a[216:219]
CP v220,v232,v233
CP v221,v234,v235
MF a[220:223],v[134:137],v[74:77],a[220:223]
CP v222,v236,v237
CP v223,v238,v239
MF a[224:227],v[138:141],v[46:49],a[224:227]
buffer_store_dwordx4 v[220:223],v8,s[48:51],s75 offen offset:3072 nt
s_add_u32 s75,s75,4096
MF a[228:231],v[138:141],v[50:53],a[228:231]
AR v224,a64
AR v225,a65
MF a[232:235],v[138:141],v[54:57],a[232:235]
AR v226,a66
AR v227,a67
MF a[236:239],v[138:141],v[58:61],a[236:239]
AR v228,a96
AR v229,a97
MF a[240:243],v[138:141],v[62:65],a[240:243]
AR v230,a98
AR v231,a99
MF a[244:247],v[138:141],v[66:69],a[244:247]
CP v216,v224,v225
CP v217,v226,v227
MF a[248:251],v[138:141],v[70:73],a[248:251]
CP v218,v228,v229
CP v219,v230,v231
MF a[252:255],v[138:141],v[74:77],a[252:255]
buffer_store_dwordx4 v[216:219],v8,s[48:51],s75 offen nt
AR v232,a68
AR v233,a69
AR v234,a70
AR v235,a71
AR v236,a100
AR v237,a101
AR v238,a102
AR v239,a103
CP v220,v232,v233
CP v221,v234,v235
CP v222,v236,v237
CP v223,v238,v239
buffer_store_dwordx4 v[220:223],v8,s[48:51],s75 offen offset:1024 nt
AR v224,a72
AR v225,a73
AR v226,a74
AR v227,a75
AR v228,a104
AR v229,a105
AR v230,a106
AR v231,a107
CP v216,v224,v225
CP v217,v226,v227
CP v218,v228,v229
CP v219,v230,v231
buffer_store_dwordx4 v[216:219],v8,s[48:51],s75 offen offset:2048 nt
AR v232,a76
AR v233,a77
AR v234,a78
AR v235,a79
AR v236,a108
AR v237,a109
AR v238,a110
AR v239,a111
CP v220,v232,v233
CP v221,v234,v235
CP v222,v236,v237
CP v223,v238,v239
buffer_store_dwordx4 v[220:223],v8,s[48:51],s75 offen offset:3072 nt
s_add_u32 s75,s75,4096
AR v224,a80
AR v225,a81
AR v226,a82
AR v227,a83
AR v228,a112
AR v229,a113
AR v230,a114
AR v231,a115
CP v216,v224,v225
CP v217,v226,v227
CP v218,v228,v229
CP v219,v230,v231
buffer_store_dwordx4 v[216:219],v8,s[48:51],s75 offen nt
AR v232,a84
AR v233,a85
AR v234,a86
AR v235,a87
AR v236,a116
AR v237,a117
AR v238,a118
AR v239,a119
CP v220,v232,v233
CP v221,v234,v235
CP v222,v236,v237
CP v223,v238,v239
buffer_store_dwordx4 v[220:223],v8,s[48:51],s75 offen offset:1024 nt
AR v224,a88
AR v225,a89
AR v226,a90
AR v227,a91
AR v228,a120
AR v229,a121
AR v230,a122
AR v231,a123
CP v216,v224,v225
CP v217,v226,v227
CP v218,v228,v229
CP v219,v230,v231
buffer_store_dwordx4 v[216:219],v8,s[48:51],s75 offen offset:2048 nt
AR v232,a92
AR v233,a93
AR v234,a94
AR v235,a95
AR v236,a124
AR v237,a125
AR v238,a126
AR v239,a127
CP v220,v232,v233
CP v221,v234,v235
CP v222,v236,v237
CP v223,v238,v239
buffer_store_dwordx4 v[220:223],v8,s[48:51],s75 offen offset:3072 nt
s_add_u32 s75,s75,4096
AR v224,a128
AR v225,a129
AR v226,a130
AR v227,a131
AR v228,a160
AR v229,a161
AR v230,a162
AR v231,a163
CP v216,v224,v225
CP v217,v226,v227
CP v218,v228,v229
CP v219,v230,v231
buffer_store_dwordx4 v[216:219],v8,s[48:51],s75 offen nt
AR v232,a132
AR v233,a133
AR v234,a134
AR v235,a135
AR v236,a164
AR v237,a165
AR v238,a166
AR v239,a167
CP v220,v232,v233
CP v221,v234,v235
CP v222,v236,v237
CP v223,v238,v239
buffer_store_dwordx4 v[220:223],v8,s[48:51],s75 offen offset:1024 nt
AR v224,a136
AR v225,a137
AR v226,a138
AR v227,a139
AR v228,a168
AR v229,a169
AR v230,a170
AR v231,a171
CP v216,v224,v225
CP v217,v226,v227
CP v218,v228,v229
CP v219,v230,v231
buffer_store_dwordx4 v[216:219],v8,s[48:51],s75 offen offset:2048 nt
AR v232,a140
AR v233,a141
AR v234,a142
AR v235,a143
AR v236,a172
AR v237,a173
AR v238,a174
AR v239,a175
CP v220,v232,v233
CP v221,v234,v235
CP v222,v236,v237
CP v223,v238,v239
buffer_store_dwordx4 v[220:223],v8,s[48:51],s75 offen offset:3072 nt
s_add_u32 s75,s75,4096
AR v224,a144
AR v225,a145
AR v226,a146
AR v227,a147
AR v228,a176
AR v229,a177
AR v230,a178
AR v231,a179
CP v216,v224,v225
CP v217,v226,v227
CP v218,v228,v229
CP v219,v230,v231
buffer_store_dwordx4 v[216:219],v8,s[48:51],s75 offen nt
AR v232,a148
AR v233,a149
AR v234,a150
AR v235,a151
AR v236,a180
AR v237,a181
AR v238,a182
AR v239,a183
CP v220,v232,v233
CP v221,v234,v235
CP v222,v236,v237
CP v223,v238,v239
buffer_store_dwordx4 v[220:223],v8,s[48:51],s75 offen offset:1024 nt
AR v224,a152
AR v225,a153
AR v226,a154
AR v227,a155
AR v228,a184
AR v229,a185
AR v230,a186
AR v231,a187
CP v216,v224,v225
CP v217,v226,v227
CP v218,v228,v229
CP v219,v230,v231
buffer_store_dwordx4 v[216:219],v8,s[48:51],s75 offen offset:2048 nt
AR v232,a156
AR v233,a157
AR v234,a158
AR v235,a159
AR v236,a188
AR v237,a189
AR v238,a190
AR v239,a191
CP v220,v232,v233
CP v221,v234,v235
CP v222,v236,v237
CP v223,v238,v239
buffer_store_dwordx4 v[220:223],v8,s[48:51],s75 offen offset:3072 nt
s_add_u32 s75,s75,4096
AR v224,a192
AR v225,a193
AR v226,a194
AR v227,a195
AR v228,a224
AR v229,a225
AR v230,a226
AR v231,a227
CP v216,v224,v225
CP v217,v226,v227
CP v218,v228,v229
CP v219,v230,v231
buffer_store_dwordx4 v[216:219],v8,s[48:51],s75 offen nt
AR v232,a196
AR v233,a197
AR v234,a198
AR v235,a199
AR v236,a228
AR v237,a229
AR v238,a230
AR v239,a231
CP v220,v232,v233
CP v221,v234,v235
CP v222,v236,v237
CP v223,v238,v239
buffer_store_dwordx4 v[220:223],v8,s[48:51],s75 offen offset:1024 nt
AR v224,a200
AR v225,a201
AR v226,a202
AR v227,a203
AR v228,a232
AR v229,a233
AR v230,a234
AR v231,a235
CP v216,v224,v225
CP v217,v226,v227
CP v218,v228,v229
CP v219,v230,v231
buffer_store_dwordx4 v[216:219],v8,s[48:51],s75 offen offset:2048 nt
AR v232,a204
AR v233,a205
AR v234,a206
AR v235,a207
AR v236,a236
AR v237,a237
AR v238,a238
AR v239,a239
CP v220,v232,v233
CP v221,v234,v235
CP v222,v236,v237
CP v223,v238,v239
buffer_store_dwordx4 v[220:223],v8,s[48:51],s75 offen offset:3072 nt
s_add_u32 s75,s75,4096
AR v224,a208
AR v225,a209
AR v226,a210
AR v227,a211
AR v228,a240
AR v229,a241
AR v230,a242
AR v231,a243
CP v216,v224,v225
CP v217,v226,v227
CP v218,v228,v229
CP v219,v230,v231
buffer_store_dwordx4 v[216:219],v8,s[48:51],s75 offen nt
AR v232,a212
AR v233,a213
AR v234,a214
AR v235,a215
AR v236,a244
AR v237,a245
AR v238,a246
AR v239,a247
CP v220,v232,v233
CP v221,v234,v235
CP v222,v236,v237
CP v223,v238,v239
buffer_store_dwordx4 v[220:223],v8,s[48:51],s75 offen offset:1024 nt
AR v224,a216
AR v225,a217
AR v226,a218
AR v227,a219
AR v228,a248
AR v229,a249
AR v230,a250
AR v231,a251
CP v216,v224,v225
CP v217,v226,v227
CP v218,v228,v229
CP v219,v230,v231
buffer_store_dwordx4 v[216:219],v8,s[48:51],s75 offen offset:2048 nt
AR v232,a220
AR v233,a221
AR v234,a222
AR v235,a223
AR v236,a252
AR v237,a253
AR v238,a254
AR v239,a255
CP v220,v232,v233
CP v221,v234,v235
CP v222,v236,v237
CP v223,v238,v239
buffer_store_dwordx4 v[220:223],v8,s[48:51],s75 offen offset:3072 nt
s_add_u32 s75,s75,4096
LE_%=:
.purgem MF
.purgem AR
.purgem CP
)A"
    :: "{s[4:5]}"(ka), "{s6}"(blockIdx.x), "{v0}"(threadIdx.x) : "memory", "vcc", "s8", "s9", "s10", "s11", "s12", "s13", "s14", "s15", "s16", "s17", "s18", "s19", "s20", "s21", "s22", "s23", "s24", "s25", "s26", "s27", "s28", "s29", "s30", "s31", "s32", "s33", "s34", "s35", "s36", "s37", "s38", "s39", "s40", "s41", "s42", "s43", "s44", "s45", "s46", "s47", "s48", "s49", "s50", "s51", "s52", "s53", "s54", "s55", "s56", "s57", "s58", "s59", "s60", "s61", "s62", "s63", "s64", "s65", "s66", "s67", "s68", "s69", "s70", "s71", "s72", "s73", "s74", "s75", "s76", "s77", "s78", "s79", "s80", "s81", "s82", "s83", "s84", "s85", "s86", "s87", "s88", "s89", "s90", "s91", "s92", "s93", "s94", "s95", "s96", "s97", "s98", "s99", "s100", "s101", "v1", "v2", "v3", "v4", "v5", "v6", "v7", "v8", "v9", "v10", "v11", "v12", "v13", "v14", "v15", "v16", "v17", "v18", "v19", "v20", "v21", "v22", "v23", "v24", "v25", "v26", "v27", "v28", "v29", "v30", "v31", "v32", "v33", "v34", "v35", "v36", "v37", "v38", "v39", "v40", "v41", "v42", "v43", "v44", "v45", "v46", "v47", "v48", "v49", "v50", "v51", "v52", "v53", "v54", "v55", "v56", "v57", "v58", "v59", "v60", "v61", "v62", "v63", "v64", "v65", "v66", "v67", "v68", "v69", "v70", "v71", "v72", "v73", "v74", "v75", "v76", "v77", "v78", "v79", "v80", "v81", "v82", "v83", "v84", "v85", "v86", "v87", "v88", "v89", "v90", "v91", "v92", "v93", "v94", "v95", "v96", "v97", "v98", "v99", "v100", "v101", "v102", "v103", "v104", "v105", "v106", "v107", "v108", "v109", "v110", "v111", "v112", "v113", "v114", "v115", "v116", "v117", "v118", "v119", "v120", "v121", "v122", "v123", "v124", "v125", "v126", "v127", "v128", "v129", "v130", "v131", "v132", "v133", "v134", "v135", "v136", "v137", "v138", "v139", "v140", "v141", "v142", "v143", "v144", "v145", "v146", "v147", "v148", "v149", "v150", "v151", "v152", "v153", "v154", "v155", "v156", "v157", "v158", "v159", "v160", "v161", "v162", "v163", "v164", "v165", "v166", "v167", "v168", "v169", "v170", "v171", "v172", "v173", "v174", "v175", "v176", "v177", "v178", "v179", "v180", "v181", "v182", "v183", "v184", "v185", "v186", "v187", "v188", "v189", "v190", "v191", "v192", "v193", "v194", "v195", "v196", "v197", "v198", "v199", "v200", "v201", "v202", "v203", "v204", "v205", "v206", "v207", "v208", "v209", "v210", "v211", "v212", "v213", "v214", "v215", "v216", "v217", "v218", "v219", "v220", "v221", "v222", "v223", "v224", "v225", "v226", "v227", "v228", "v229", "v230", "v231", "v232", "v233", "v234", "v235", "v236", "v237", "v238", "v239", "v240", "v241", "v242", "v243", "v244", "v245", "v246", "v247", "v248", "v249", "v250", "v251", "v252", "v253", "v254", "v255", "a0", "a1", "a2", "a3", "a4", "a5", "a6", "a7", "a8", "a9", "a10", "a11", "a12", "a13", "a14", "a15", "a16", "a17", "a18", "a19", "a20", "a21", "a22", "a23", "a24", "a25", "a26", "a27", "a28", "a29", "a30", "a31", "a32", "a33", "a34", "a35", "a36", "a37", "a38", "a39", "a40", "a41", "a42", "a43", "a44", "a45", "a46", "a47", "a48", "a49", "a50", "a51", "a52", "a53", "a54", "a55", "a56", "a57", "a58", "a59", "a60", "a61", "a62", "a63", "a64", "a65", "a66", "a67", "a68", "a69", "a70", "a71", "a72", "a73", "a74", "a75", "a76", "a77", "a78", "a79", "a80", "a81", "a82", "a83", "a84", "a85", "a86", "a87", "a88", "a89", "a90", "a91", "a92", "a93", "a94", "a95", "a96", "a97", "a98", "a99", "a100", "a101", "a102", "a103", "a104", "a105", "a106", "a107", "a108", "a109", "a110", "a111", "a112", "a113", "a114", "a115", "a116", "a117", "a118", "a119", "a120", "a121", "a122", "a123", "a124", "a125", "a126", "a127", "a128", "a129", "a130", "a131", "a132", "a133", "a134", "a135", "a136", "a137", "a138", "a139", "a140", "a141", "a142", "a143", "a144", "a145", "a146", "a147", "a148", "a149", "a150", "a151", "a152", "a153", "a154", "a155", "a156", "a157", "a158", "a159", "a160", "a161", "a162", "a163", "a164", "a165", "a166", "a167", "a168", "a169", "a170", "a171", "a172", "a173", "a174", "a175", "a176", "a177", "a178", "a179", "a180", "a181", "a182", "a183", "a184", "a185", "a186", "a187", "a188", "a189", "a190", "a191", "a192", "a193", "a194", "a195", "a196", "a197", "a198", "a199", "a200", "a201", "a202", "a203", "a204", "a205", "a206", "a207", "a208", "a209", "a210", "a211", "a212", "a213", "a214", "a215", "a216", "a217", "a218", "a219", "a220", "a221", "a222", "a223", "a224", "a225", "a226", "a227", "a228", "a229", "a230", "a231", "a232", "a233", "a234", "a235", "a236", "a237", "a238", "a239", "a240", "a241", "a242", "a243", "a244", "a245", "a246", "a247", "a248", "a249", "a250", "a251", "a252", "a253", "a254", "a255");
}
extern "C" __global__ __launch_bounds__(256, 1) void k2(K2Args p) {
  auto ka = __builtin_amdgcn_kernarg_segment_ptr();
  asm volatile(
R"A(.macro MF d,a,b,c
v_mfma_f32_16x16x32_bf16 \d,\a,\b,\c
.endm
.macro AR d,a
v_accvgpr_read_b32 \d,\a
.endm
.macro CP d,a,b
v_cvt_pk_bf16_f32 \d,\a,\b
.endm
s_load_dwordx16 s[8:23],s[4:5],0x0
s_load_dwordx4 s[24:27],s[4:5],0x40
s_load_dwordx2 s[28:29],s[4:5],0x50
v_and_b32 v1,63,v0
v_lshrrev_b32 v2,6,v0
s_nop 0
v_readfirstlane_b32 s56,v2
v_lshlrev_b32 v231,4,v1
s_and_b32 s57,s6,7
s_lshl_b32 s57,s57,2
s_lshr_b32 s58,s6,3
s_and_b32 s58,s58,3
s_add_u32 s57,s57,s58
s_lshr_b32 s58,s6,5
v_lshrrev_b32 v2,2,v1
v_and_b32 v2,7,v2
v_lshrrev_b32 v3,5,v1
v_and_b32 v4,3,v1
s_lshl_b32 s100,s56,4
s_mov_b32 s101,8192
v_add_u32 v5,s100,v2
v_mul_lo_u32 v5,v5,s101
v_xor_b32 v6,0,v4
v_lshl_add_u32 v6,v3,2,v6
v_lshl_add_u32 v237,v6,4,v5
v_add_u32 v238,128,v237
v_add_u32 v5,s100,v2
v_add_u32 v5,8,v5
v_mul_lo_u32 v5,v5,s101
v_xor_b32 v6,2,v4
v_lshl_add_u32 v6,v3,2,v6
v_lshl_add_u32 v239,v6,4,v5
v_add_u32 v240,128,v239
s_mov_b32 s101,3456
v_add_u32 v5,s100,v2
v_mul_lo_u32 v5,v5,s101
v_xor_b32 v6,0,v4
v_lshl_add_u32 v6,v3,2,v6
v_lshl_add_u32 v241,v6,4,v5
v_add_u32 v242,128,v241
v_add_u32 v5,s100,v2
v_add_u32 v5,8,v5
v_mul_lo_u32 v5,v5,s101
v_xor_b32 v6,2,v4
v_lshl_add_u32 v6,v3,2,v6
v_lshl_add_u32 v243,v6,4,v5
v_add_u32 v244,128,v243
s_lshl_b32 s69,s56,12
s_cmp_ge_u32 s57,14
s_cselect_b32 s70,1,0
s_mul_i32 s100,s70,14
s_sub_u32 s71,s57,s100
s_min_u32 s71,s71,13
s_cmp_ge_u32 s57,28
s_cselect_b32 s70,0,s70
s_lshl_b32 s101,s56,1
s_lshl_b32 s86,s58,3
s_add_u32 s86,s86,s101
s_sub_u32 s87,15,s58
s_lshl_b32 s87,s87,3
s_add_u32 s87,s87,s101
s_cmp_eq_u32 s70,1
s_cselect_b32 s88,s87,s86
s_cselect_b32 s100,s86,s87
s_mul_i32 s63,s88,20480
s_add_u32 s64,s63,20480
s_mul_i32 s65,s100,20480
s_add_u32 s66,s65,20480
s_waitcnt lgkmcnt(0)
s_mov_b32 s32,s8
s_and_b32 s33,s9,0xffff
s_mov_b32 s34,2621440
s_mov_b32 s35,0x20000
s_mov_b32 s36,s10
s_and_b32 s37,s11,0xffff
s_mov_b32 s38,5242880
s_mov_b32 s39,0x20000
s_mov_b32 s52,s18
s_and_b32 s53,s19,0xffff
s_mov_b32 s54,2211840
s_mov_b32 s55,0x20000
s_lshl_b32 s67,s57,8
s_lshl_b32 s68,s71,8
s_cmp_ge_u32 s57,28
s_cselect_b32 s68,0x7ffff000,s68
buffer_load_dwordx4 v[8:11],v231,s[32:35],s63 offen
buffer_load_dwordx4 v[12:15],v231,s[32:35],s63 offen offset:1024
buffer_load_dwordx4 v[16:19],v231,s[32:35],s64 offen
buffer_load_dwordx4 v[20:23],v231,s[32:35],s64 offen offset:1024
buffer_load_dwordx4 v[24:27],v231,s[32:35],s65 offen
buffer_load_dwordx4 v[28:31],v231,s[32:35],s65 offen offset:1024
buffer_load_dwordx4 v[32:35],v231,s[32:35],s66 offen
buffer_load_dwordx4 v[36:39],v231,s[32:35],s66 offen offset:1024
s_add_u32 s63,s63,2048
s_add_u32 s64,s64,2048
s_add_u32 s65,s65,2048
s_add_u32 s66,s66,2048
s_add_u32 m0,s69,32768
s_nop 0
buffer_load_dwordx4 v237,s[36:39],s67 offen lds
s_add_u32 m0,s69,33792
s_nop 0
buffer_load_dwordx4 v238,s[36:39],s67 offen lds
s_add_u32 m0,s69,34816
s_nop 0
buffer_load_dwordx4 v239,s[36:39],s67 offen lds
s_add_u32 m0,s69,35840
s_nop 0
buffer_load_dwordx4 v240,s[36:39],s67 offen lds
s_add_u32 m0,s69,49152
s_nop 0
buffer_load_dwordx4 v241,s[52:55],s68 offen lds
s_add_u32 m0,s69,50176
s_nop 0
buffer_load_dwordx4 v242,s[52:55],s68 offen lds
s_add_u32 m0,s69,51200
s_nop 0
buffer_load_dwordx4 v243,s[52:55],s68 offen lds
s_add_u32 m0,s69,52224
s_nop 0
buffer_load_dwordx4 v244,s[52:55],s68 offen lds
s_add_u32 s67,s67,524288
s_add_u32 s68,s68,221184
buffer_load_dwordx4 v[40:43],v231,s[32:35],s63 offen
buffer_load_dwordx4 v[44:47],v231,s[32:35],s63 offen offset:1024
buffer_load_dwordx4 v[48:51],v231,s[32:35],s64 offen
buffer_load_dwordx4 v[52:55],v231,s[32:35],s64 offen offset:1024
buffer_load_dwordx4 v[56:59],v231,s[32:35],s65 offen
buffer_load_dwordx4 v[60:63],v231,s[32:35],s65 offen offset:1024
buffer_load_dwordx4 v[64:67],v231,s[32:35],s66 offen
buffer_load_dwordx4 v[68:71],v231,s[32:35],s66 offen offset:1024
s_add_u32 s63,s63,2048
s_add_u32 s64,s64,2048
s_add_u32 s65,s65,2048
s_add_u32 s66,s66,2048
s_add_u32 m0,s69,81920
s_nop 0
buffer_load_dwordx4 v237,s[36:39],s67 offen lds
s_add_u32 m0,s69,82944
s_nop 0
buffer_load_dwordx4 v238,s[36:39],s67 offen lds
s_add_u32 m0,s69,83968
s_nop 0
buffer_load_dwordx4 v239,s[36:39],s67 offen lds
s_add_u32 m0,s69,84992
s_nop 0
buffer_load_dwordx4 v240,s[36:39],s67 offen lds
s_add_u32 m0,s69,98304
s_nop 0
buffer_load_dwordx4 v241,s[52:55],s68 offen lds
s_add_u32 m0,s69,99328
s_nop 0
buffer_load_dwordx4 v242,s[52:55],s68 offen lds
s_add_u32 m0,s69,100352
s_nop 0
buffer_load_dwordx4 v243,s[52:55],s68 offen lds
s_add_u32 m0,s69,101376
s_nop 0
buffer_load_dwordx4 v244,s[52:55],s68 offen lds
s_add_u32 s67,s67,524288
s_add_u32 s68,s68,221184
buffer_load_dwordx4 v[72:75],v231,s[32:35],s63 offen
buffer_load_dwordx4 v[76:79],v231,s[32:35],s63 offen offset:1024
buffer_load_dwordx4 v[80:83],v231,s[32:35],s64 offen
buffer_load_dwordx4 v[84:87],v231,s[32:35],s64 offen offset:1024
buffer_load_dwordx4 v[88:91],v231,s[32:35],s65 offen
buffer_load_dwordx4 v[92:95],v231,s[32:35],s65 offen offset:1024
buffer_load_dwordx4 v[96:99],v231,s[32:35],s66 offen
buffer_load_dwordx4 v[100:103],v231,s[32:35],s66 offen offset:1024
s_add_u32 s63,s63,2048
s_add_u32 s64,s64,2048
s_add_u32 s65,s65,2048
s_add_u32 s66,s66,2048
s_add_u32 m0,s69,114688
s_nop 0
buffer_load_dwordx4 v237,s[36:39],s67 offen lds
s_add_u32 m0,s69,115712
s_nop 0
buffer_load_dwordx4 v238,s[36:39],s67 offen lds
s_add_u32 m0,s69,116736
s_nop 0
buffer_load_dwordx4 v239,s[36:39],s67 offen lds
s_add_u32 m0,s69,117760
s_nop 0
buffer_load_dwordx4 v240,s[36:39],s67 offen lds
s_add_u32 m0,s69,131072
s_nop 0
buffer_load_dwordx4 v241,s[52:55],s68 offen lds
s_add_u32 m0,s69,132096
s_nop 0
buffer_load_dwordx4 v242,s[52:55],s68 offen lds
s_add_u32 m0,s69,133120
s_nop 0
buffer_load_dwordx4 v243,s[52:55],s68 offen lds
s_add_u32 m0,s69,134144
s_nop 0
buffer_load_dwordx4 v244,s[52:55],s68 offen lds
s_add_u32 s67,s67,524288
s_add_u32 s68,s68,221184
s_lshr_b32 s98,s56,0
s_and_b32 s99,s56,0
v_lshrrev_b32 v2,4,v1
v_lshrrev_b32 v3,2,v1
v_and_b32 v3,3,v3
v_and_b32 v4,3,v1
v_lshlrev_b32 v5,11,v2
v_lshl_add_u32 v5,v3,6,v5
v_lshl_add_u32 v5,v4,3,v5
v_and_b32 v6,1,v2
v_xor_b32 v7,0,v6
v_lshl_add_u32 v7,v7,5,v5
v_add_u32 v245,32768,v7
v_add_u32 v247,81920,v7
v_add_u32 v249,114688,v7
v_xor_b32 v7,1,v6
v_lshl_add_u32 v7,v7,5,v5
v_add_u32 v246,32768,v7
v_add_u32 v248,81920,v7
v_add_u32 v250,114688,v7
s_mov_b32 s40,s12
s_and_b32 s41,s13,0xffff
s_mov_b32 s42,16777216
s_mov_b32 s43,0x20000
s_mov_b32 s44,s14
s_and_b32 s45,s15,0xffff
s_mov_b32 s46,16777216
s_mov_b32 s47,0x20000
s_lshl_b32 s72,s57,19
s_mov_b32 s73,s72
s_lshl_b32 s74,s56,12
s_mov_b32 s75,0
v_add_u32 v230,s74,v231
v_add_u32 v228,0,v231
s_mov_b32 s59,16384
s_mov_b32 s61,49152
s_mov_b32 s60,114688
s_mov_b32 s62,98304
v_and_b32 v2,15,v1
v_lshrrev_b32 v3,4,v1
v_lshlrev_b32 v3,2,v3
v_sub_u32 v2,v2,v3
s_lshl_b32 s100,s56,5
v_add_u32 v232,s100,v2
v_add_u32 v233,16,v232
v_mov_b32 v236,0xff800000
v_mov_b32 v224,0
v_mov_b32 v225,0
v_mov_b32 v226,0
v_mov_b32 v227,0
s_waitcnt vmcnt(16)
s_barrier
ds_read_b64_tr_b16 v[136:137],v245
ds_read_b64_tr_b16 v[138:139],v245 offset:256
ds_read_b64_tr_b16 v[140:141],v246
ds_read_b64_tr_b16 v[142:143],v246 offset:256
ds_read_b64_tr_b16 v[144:145],v245 offset:512
ds_read_b64_tr_b16 v[146:147],v245 offset:768
ds_read_b64_tr_b16 v[148:149],v246 offset:512
ds_read_b64_tr_b16 v[150:151],v246 offset:768
ds_read_b64_tr_b16 v[152:153],v245 offset:1024
ds_read_b64_tr_b16 v[154:155],v245 offset:1280
ds_read_b64_tr_b16 v[156:157],v246 offset:1024
ds_read_b64_tr_b16 v[158:159],v246 offset:1280
ds_read_b64_tr_b16 v[160:161],v245 offset:1536
ds_read_b64_tr_b16 v[162:163],v245 offset:1792
ds_read_b64_tr_b16 v[164:165],v246 offset:1536
ds_read_b64_tr_b16 v[166:167],v246 offset:1792
ds_read_b64_tr_b16 v[168:169],v245 offset:16384
ds_read_b64_tr_b16 v[170:171],v245 offset:16640
ds_read_b64_tr_b16 v[172:173],v246 offset:16384
ds_read_b64_tr_b16 v[174:175],v246 offset:16640
ds_read_b64_tr_b16 v[176:177],v245 offset:16896
ds_read_b64_tr_b16 v[178:179],v245 offset:17152
ds_read_b64_tr_b16 v[180:181],v246 offset:16896
ds_read_b64_tr_b16 v[182:183],v246 offset:17152
ds_read_b64_tr_b16 v[184:185],v245 offset:17408
ds_read_b64_tr_b16 v[186:187],v245 offset:17664
ds_read_b64_tr_b16 v[188:189],v246 offset:17408
ds_read_b64_tr_b16 v[190:191],v246 offset:17664
ds_read_b64_tr_b16 v[192:193],v245 offset:17920
ds_read_b64_tr_b16 v[194:195],v245 offset:18176
ds_read_b64_tr_b16 v[196:197],v246 offset:17920
ds_read_b64_tr_b16 v[198:199],v246 offset:18176
s_waitcnt lgkmcnt(15)
MF a[0:3],v[136:139],v[8:11],0
ds_read_b64_tr_b16 a[192:193],v245 offset:8192
MF a[32:35],v[136:139],v[16:19],0
ds_read_b64_tr_b16 a[194:195],v245 offset:8448
MF a[64:67],v[136:139],v[24:27],0
ds_read_b64_tr_b16 a[196:197],v246 offset:8192
MF a[96:99],v[136:139],v[32:35],0
ds_read_b64_tr_b16 a[198:199],v246 offset:8448
MF a[4:7],v[140:143],v[8:11],0
ds_read_b64_tr_b16 a[200:201],v245 offset:8704
MF a[36:39],v[140:143],v[16:19],0
ds_read_b64_tr_b16 a[202:203],v245 offset:8960
MF a[68:71],v[140:143],v[24:27],0
ds_read_b64_tr_b16 a[204:205],v246 offset:8704
MF a[100:103],v[140:143],v[32:35],0
ds_read_b64_tr_b16 a[206:207],v246 offset:8960
MF a[8:11],v[144:147],v[8:11],0
MF a[40:43],v[144:147],v[16:19],0
ds_read_b64_tr_b16 a[208:209],v245 offset:9216
MF a[72:75],v[144:147],v[24:27],0
ds_read_b64_tr_b16 a[210:211],v245 offset:9472
MF a[104:107],v[144:147],v[32:35],0
ds_read_b64_tr_b16 a[212:213],v246 offset:9216
MF a[12:15],v[148:151],v[8:11],0
ds_read_b64_tr_b16 a[214:215],v246 offset:9472
MF a[44:47],v[148:151],v[16:19],0
ds_read_b64_tr_b16 a[216:217],v245 offset:9728
MF a[76:79],v[148:151],v[24:27],0
ds_read_b64_tr_b16 a[218:219],v245 offset:9984
MF a[108:111],v[148:151],v[32:35],0
ds_read_b64_tr_b16 a[220:221],v246 offset:9728
MF a[16:19],v[152:155],v[8:11],0
ds_read_b64_tr_b16 a[222:223],v246 offset:9984
MF a[48:51],v[152:155],v[16:19],0
MF a[80:83],v[152:155],v[24:27],0
ds_read_b64_tr_b16 a[224:225],v245 offset:24576
MF a[112:115],v[152:155],v[32:35],0
ds_read_b64_tr_b16 a[226:227],v245 offset:24832
MF a[20:23],v[156:159],v[8:11],0
ds_read_b64_tr_b16 a[228:229],v246 offset:24576
MF a[52:55],v[156:159],v[16:19],0
ds_read_b64_tr_b16 a[230:231],v246 offset:24832
MF a[84:87],v[156:159],v[24:27],0
ds_read_b64_tr_b16 a[232:233],v245 offset:25088
MF a[116:119],v[156:159],v[32:35],0
ds_read_b64_tr_b16 a[234:235],v245 offset:25344
MF a[24:27],v[160:163],v[8:11],0
ds_read_b64_tr_b16 a[236:237],v246 offset:25088
MF a[56:59],v[160:163],v[16:19],0
ds_read_b64_tr_b16 a[238:239],v246 offset:25344
MF a[88:91],v[160:163],v[24:27],0
MF a[120:123],v[160:163],v[32:35],0
ds_read_b64_tr_b16 a[240:241],v245 offset:25600
MF a[28:31],v[164:167],v[8:11],0
ds_read_b64_tr_b16 a[242:243],v245 offset:25856
MF a[60:63],v[164:167],v[16:19],0
ds_read_b64_tr_b16 a[244:245],v246 offset:25600
MF a[92:95],v[164:167],v[24:27],0
ds_read_b64_tr_b16 a[246:247],v246 offset:25856
MF a[124:127],v[164:167],v[32:35],0
ds_read_b64_tr_b16 a[248:249],v245 offset:26112
s_waitcnt lgkmcnt(15)
MF a[128:131],v[168:171],v[8:11],0
ds_read_b64_tr_b16 a[250:251],v245 offset:26368
MF a[160:163],v[168:171],v[16:19],0
ds_read_b64_tr_b16 a[252:253],v246 offset:26112
MF a[132:135],v[172:175],v[8:11],0
ds_read_b64_tr_b16 a[254:255],v246 offset:26368
MF a[164:167],v[172:175],v[16:19],0
MF a[136:139],v[176:179],v[8:11],0
MF a[168:171],v[176:179],v[16:19],0
MF a[140:143],v[180:183],v[8:11],0
MF a[172:175],v[180:183],v[16:19],0
MF a[144:147],v[184:187],v[8:11],0
MF a[176:179],v[184:187],v[16:19],0
MF a[148:151],v[188:191],v[8:11],0
MF a[180:183],v[188:191],v[16:19],0
MF a[152:155],v[192:195],v[8:11],0
MF a[184:187],v[192:195],v[16:19],0
MF a[156:159],v[196:199],v[8:11],0
MF a[188:191],v[196:199],v[16:19],0
s_waitcnt vmcnt(8) lgkmcnt(0)
s_barrier
MF a[0:3],a[192:195],v[12:15],a[0:3]
buffer_load_dwordx4 v[104:107],v231,s[32:35],s63 offen
MF a[32:35],a[192:195],v[20:23],a[32:35]
ds_read_b64_tr_b16 v[136:137],v247
MF a[64:67],a[192:195],v[28:31],a[64:67]
ds_read_b64_tr_b16 v[138:139],v247 offset:256
MF a[96:99],a[192:195],v[36:39],a[96:99]
buffer_load_dwordx4 v[108:111],v231,s[32:35],s63 offen offset:1024
MF a[4:7],a[196:199],v[12:15],a[4:7]
ds_read_b64_tr_b16 v[140:141],v248
MF a[36:39],a[196:199],v[20:23],a[36:39]
ds_read_b64_tr_b16 v[142:143],v248 offset:256
MF a[68:71],a[196:199],v[28:31],a[68:71]
buffer_load_dwordx4 v[112:115],v231,s[32:35],s64 offen
MF a[100:103],a[196:199],v[36:39],a[100:103]
ds_read_b64_tr_b16 v[144:145],v247 offset:512
MF a[8:11],a[200:203],v[12:15],a[8:11]
ds_read_b64_tr_b16 v[146:147],v247 offset:768
MF a[40:43],a[200:203],v[20:23],a[40:43]
buffer_load_dwordx4 v[116:119],v231,s[32:35],s64 offen offset:1024
MF a[72:75],a[200:203],v[28:31],a[72:75]
ds_read_b64_tr_b16 v[148:149],v248 offset:512
MF a[104:107],a[200:203],v[36:39],a[104:107]
ds_read_b64_tr_b16 v[150:151],v248 offset:768
MF a[12:15],a[204:207],v[12:15],a[12:15]
buffer_load_dwordx4 v[120:123],v231,s[32:35],s65 offen
MF a[44:47],a[204:207],v[20:23],a[44:47]
ds_read_b64_tr_b16 v[152:153],v247 offset:1024
MF a[76:79],a[204:207],v[28:31],a[76:79]
ds_read_b64_tr_b16 v[154:155],v247 offset:1280
MF a[108:111],a[204:207],v[36:39],a[108:111]
buffer_load_dwordx4 v[124:127],v231,s[32:35],s65 offen offset:1024
MF a[16:19],a[208:211],v[12:15],a[16:19]
ds_read_b64_tr_b16 v[156:157],v248 offset:1024
MF a[48:51],a[208:211],v[20:23],a[48:51]
ds_read_b64_tr_b16 v[158:159],v248 offset:1280
MF a[80:83],a[208:211],v[28:31],a[80:83]
buffer_load_dwordx4 v[128:131],v231,s[32:35],s66 offen
MF a[112:115],a[208:211],v[36:39],a[112:115]
ds_read_b64_tr_b16 v[160:161],v247 offset:1536
MF a[20:23],a[212:215],v[12:15],a[20:23]
ds_read_b64_tr_b16 v[162:163],v247 offset:1792
MF a[52:55],a[212:215],v[20:23],a[52:55]
buffer_load_dwordx4 v[132:135],v231,s[32:35],s66 offen offset:1024
MF a[84:87],a[212:215],v[28:31],a[84:87]
ds_read_b64_tr_b16 v[164:165],v248 offset:1536
MF a[116:119],a[212:215],v[36:39],a[116:119]
ds_read_b64_tr_b16 v[166:167],v248 offset:1792
s_add_u32 m0,s69,32768
MF a[24:27],a[216:219],v[12:15],a[24:27]
buffer_load_dwordx4 v237,s[36:39],s67 offen lds
MF a[56:59],a[216:219],v[20:23],a[56:59]
ds_read_b64_tr_b16 v[168:169],v247 offset:16384
MF a[88:91],a[216:219],v[28:31],a[88:91]
ds_read_b64_tr_b16 v[170:171],v247 offset:16640
s_add_u32 m0,s69,33792
MF a[120:123],a[216:219],v[36:39],a[120:123]
buffer_load_dwordx4 v238,s[36:39],s67 offen lds
MF a[28:31],a[220:223],v[12:15],a[28:31]
ds_read_b64_tr_b16 v[172:173],v248 offset:16384
MF a[60:63],a[220:223],v[20:23],a[60:63]
ds_read_b64_tr_b16 v[174:175],v248 offset:16640
s_add_u32 m0,s69,34816
MF a[92:95],a[220:223],v[28:31],a[92:95]
buffer_load_dwordx4 v239,s[36:39],s67 offen lds
MF a[124:127],a[220:223],v[36:39],a[124:127]
ds_read_b64_tr_b16 v[176:177],v247 offset:16896
MF a[128:131],a[224:227],v[12:15],a[128:131]
ds_read_b64_tr_b16 v[178:179],v247 offset:17152
s_add_u32 m0,s69,35840
MF a[160:163],a[224:227],v[20:23],a[160:163]
buffer_load_dwordx4 v240,s[36:39],s67 offen lds
MF a[132:135],a[228:231],v[12:15],a[132:135]
ds_read_b64_tr_b16 v[180:181],v248 offset:16896
MF a[164:167],a[228:231],v[20:23],a[164:167]
ds_read_b64_tr_b16 v[182:183],v248 offset:17152
s_add_u32 m0,s69,49152
MF a[136:139],a[232:235],v[12:15],a[136:139]
buffer_load_dwordx4 v241,s[52:55],s68 offen lds
MF a[168:171],a[232:235],v[20:23],a[168:171]
ds_read_b64_tr_b16 v[184:185],v247 offset:17408
MF a[140:143],a[236:239],v[12:15],a[140:143]
ds_read_b64_tr_b16 v[186:187],v247 offset:17664
s_add_u32 m0,s69,50176
MF a[172:175],a[236:239],v[20:23],a[172:175]
buffer_load_dwordx4 v242,s[52:55],s68 offen lds
MF a[144:147],a[240:243],v[12:15],a[144:147]
ds_read_b64_tr_b16 v[188:189],v248 offset:17408
MF a[176:179],a[240:243],v[20:23],a[176:179]
ds_read_b64_tr_b16 v[190:191],v248 offset:17664
s_add_u32 m0,s69,51200
MF a[148:151],a[244:247],v[12:15],a[148:151]
buffer_load_dwordx4 v243,s[52:55],s68 offen lds
MF a[180:183],a[244:247],v[20:23],a[180:183]
ds_read_b64_tr_b16 v[192:193],v247 offset:17920
MF a[152:155],a[248:251],v[12:15],a[152:155]
ds_read_b64_tr_b16 v[194:195],v247 offset:18176
s_add_u32 m0,s69,52224
MF a[184:187],a[248:251],v[20:23],a[184:187]
buffer_load_dwordx4 v244,s[52:55],s68 offen lds
MF a[156:159],a[252:255],v[12:15],a[156:159]
ds_read_b64_tr_b16 v[196:197],v248 offset:17920
MF a[188:191],a[252:255],v[20:23],a[188:191]
ds_read_b64_tr_b16 v[198:199],v248 offset:18176
s_add_u32 s67,s67,524288
s_add_u32 s68,s68,221184
s_add_u32 s63,s63,2048
s_add_u32 s64,s64,2048
s_add_u32 s65,s65,2048
s_add_u32 s66,s66,2048
s_waitcnt lgkmcnt(15)
MF a[0:3],v[136:139],v[40:43],a[0:3]
ds_read_b64_tr_b16 a[192:193],v247 offset:8192
MF a[32:35],v[136:139],v[48:51],a[32:35]
ds_read_b64_tr_b16 a[194:195],v247 offset:8448
MF a[64:67],v[136:139],v[56:59],a[64:67]
ds_read_b64_tr_b16 a[196:197],v248 offset:8192
MF a[96:99],v[136:139],v[64:67],a[96:99]
ds_read_b64_tr_b16 a[198:199],v248 offset:8448
MF a[4:7],v[140:143],v[40:43],a[4:7]
ds_read_b64_tr_b16 a[200:201],v247 offset:8704
MF a[36:39],v[140:143],v[48:51],a[36:39]
ds_read_b64_tr_b16 a[202:203],v247 offset:8960
MF a[68:71],v[140:143],v[56:59],a[68:71]
ds_read_b64_tr_b16 a[204:205],v248 offset:8704
MF a[100:103],v[140:143],v[64:67],a[100:103]
ds_read_b64_tr_b16 a[206:207],v248 offset:8960
MF a[8:11],v[144:147],v[40:43],a[8:11]
MF a[40:43],v[144:147],v[48:51],a[40:43]
ds_read_b64_tr_b16 a[208:209],v247 offset:9216
MF a[72:75],v[144:147],v[56:59],a[72:75]
ds_read_b64_tr_b16 a[210:211],v247 offset:9472
MF a[104:107],v[144:147],v[64:67],a[104:107]
ds_read_b64_tr_b16 a[212:213],v248 offset:9216
MF a[12:15],v[148:151],v[40:43],a[12:15]
ds_read_b64_tr_b16 a[214:215],v248 offset:9472
MF a[44:47],v[148:151],v[48:51],a[44:47]
ds_read_b64_tr_b16 a[216:217],v247 offset:9728
MF a[76:79],v[148:151],v[56:59],a[76:79]
ds_read_b64_tr_b16 a[218:219],v247 offset:9984
MF a[108:111],v[148:151],v[64:67],a[108:111]
ds_read_b64_tr_b16 a[220:221],v248 offset:9728
MF a[16:19],v[152:155],v[40:43],a[16:19]
ds_read_b64_tr_b16 a[222:223],v248 offset:9984
MF a[48:51],v[152:155],v[48:51],a[48:51]
MF a[80:83],v[152:155],v[56:59],a[80:83]
ds_read_b64_tr_b16 a[224:225],v247 offset:24576
MF a[112:115],v[152:155],v[64:67],a[112:115]
ds_read_b64_tr_b16 a[226:227],v247 offset:24832
MF a[20:23],v[156:159],v[40:43],a[20:23]
ds_read_b64_tr_b16 a[228:229],v248 offset:24576
MF a[52:55],v[156:159],v[48:51],a[52:55]
ds_read_b64_tr_b16 a[230:231],v248 offset:24832
MF a[84:87],v[156:159],v[56:59],a[84:87]
ds_read_b64_tr_b16 a[232:233],v247 offset:25088
MF a[116:119],v[156:159],v[64:67],a[116:119]
ds_read_b64_tr_b16 a[234:235],v247 offset:25344
MF a[24:27],v[160:163],v[40:43],a[24:27]
ds_read_b64_tr_b16 a[236:237],v248 offset:25088
MF a[56:59],v[160:163],v[48:51],a[56:59]
ds_read_b64_tr_b16 a[238:239],v248 offset:25344
MF a[88:91],v[160:163],v[56:59],a[88:91]
MF a[120:123],v[160:163],v[64:67],a[120:123]
ds_read_b64_tr_b16 a[240:241],v247 offset:25600
MF a[28:31],v[164:167],v[40:43],a[28:31]
ds_read_b64_tr_b16 a[242:243],v247 offset:25856
MF a[60:63],v[164:167],v[48:51],a[60:63]
ds_read_b64_tr_b16 a[244:245],v248 offset:25600
MF a[92:95],v[164:167],v[56:59],a[92:95]
ds_read_b64_tr_b16 a[246:247],v248 offset:25856
MF a[124:127],v[164:167],v[64:67],a[124:127]
ds_read_b64_tr_b16 a[248:249],v247 offset:26112
s_waitcnt lgkmcnt(15)
MF a[128:131],v[168:171],v[40:43],a[128:131]
ds_read_b64_tr_b16 a[250:251],v247 offset:26368
MF a[160:163],v[168:171],v[48:51],a[160:163]
ds_read_b64_tr_b16 a[252:253],v248 offset:26112
MF a[132:135],v[172:175],v[40:43],a[132:135]
ds_read_b64_tr_b16 a[254:255],v248 offset:26368
MF a[164:167],v[172:175],v[48:51],a[164:167]
MF a[136:139],v[176:179],v[40:43],a[136:139]
MF a[168:171],v[176:179],v[48:51],a[168:171]
MF a[140:143],v[180:183],v[40:43],a[140:143]
MF a[172:175],v[180:183],v[48:51],a[172:175]
MF a[144:147],v[184:187],v[40:43],a[144:147]
MF a[176:179],v[184:187],v[48:51],a[176:179]
MF a[148:151],v[188:191],v[40:43],a[148:151]
MF a[180:183],v[188:191],v[48:51],a[180:183]
MF a[152:155],v[192:195],v[40:43],a[152:155]
MF a[184:187],v[192:195],v[48:51],a[184:187]
MF a[156:159],v[196:199],v[40:43],a[156:159]
MF a[188:191],v[196:199],v[48:51],a[188:191]
s_waitcnt vmcnt(8) lgkmcnt(0)
s_barrier
MF a[0:3],a[192:195],v[44:47],a[0:3]
buffer_load_dwordx4 v[8:11],v231,s[32:35],s63 offen
MF a[32:35],a[192:195],v[52:55],a[32:35]
ds_read_b64_tr_b16 v[136:137],v249
MF a[64:67],a[192:195],v[60:63],a[64:67]
ds_read_b64_tr_b16 v[138:139],v249 offset:256
MF a[96:99],a[192:195],v[68:71],a[96:99]
buffer_load_dwordx4 v[12:15],v231,s[32:35],s63 offen offset:1024
MF a[4:7],a[196:199],v[44:47],a[4:7]
ds_read_b64_tr_b16 v[140:141],v250
MF a[36:39],a[196:199],v[52:55],a[36:39]
ds_read_b64_tr_b16 v[142:143],v250 offset:256
MF a[68:71],a[196:199],v[60:63],a[68:71]
buffer_load_dwordx4 v[16:19],v231,s[32:35],s64 offen
MF a[100:103],a[196:199],v[68:71],a[100:103]
ds_read_b64_tr_b16 v[144:145],v249 offset:512
MF a[8:11],a[200:203],v[44:47],a[8:11]
ds_read_b64_tr_b16 v[146:147],v249 offset:768
MF a[40:43],a[200:203],v[52:55],a[40:43]
buffer_load_dwordx4 v[20:23],v231,s[32:35],s64 offen offset:1024
MF a[72:75],a[200:203],v[60:63],a[72:75]
ds_read_b64_tr_b16 v[148:149],v250 offset:512
MF a[104:107],a[200:203],v[68:71],a[104:107]
ds_read_b64_tr_b16 v[150:151],v250 offset:768
MF a[12:15],a[204:207],v[44:47],a[12:15]
buffer_load_dwordx4 v[24:27],v231,s[32:35],s65 offen
MF a[44:47],a[204:207],v[52:55],a[44:47]
ds_read_b64_tr_b16 v[152:153],v249 offset:1024
MF a[76:79],a[204:207],v[60:63],a[76:79]
ds_read_b64_tr_b16 v[154:155],v249 offset:1280
MF a[108:111],a[204:207],v[68:71],a[108:111]
buffer_load_dwordx4 v[28:31],v231,s[32:35],s65 offen offset:1024
MF a[16:19],a[208:211],v[44:47],a[16:19]
ds_read_b64_tr_b16 v[156:157],v250 offset:1024
MF a[48:51],a[208:211],v[52:55],a[48:51]
ds_read_b64_tr_b16 v[158:159],v250 offset:1280
MF a[80:83],a[208:211],v[60:63],a[80:83]
buffer_load_dwordx4 v[32:35],v231,s[32:35],s66 offen
MF a[112:115],a[208:211],v[68:71],a[112:115]
ds_read_b64_tr_b16 v[160:161],v249 offset:1536
MF a[20:23],a[212:215],v[44:47],a[20:23]
ds_read_b64_tr_b16 v[162:163],v249 offset:1792
MF a[52:55],a[212:215],v[52:55],a[52:55]
buffer_load_dwordx4 v[36:39],v231,s[32:35],s66 offen offset:1024
MF a[84:87],a[212:215],v[60:63],a[84:87]
ds_read_b64_tr_b16 v[164:165],v250 offset:1536
MF a[116:119],a[212:215],v[68:71],a[116:119]
ds_read_b64_tr_b16 v[166:167],v250 offset:1792
s_add_u32 m0,s69,81920
MF a[24:27],a[216:219],v[44:47],a[24:27]
buffer_load_dwordx4 v237,s[36:39],s67 offen lds
MF a[56:59],a[216:219],v[52:55],a[56:59]
ds_read_b64_tr_b16 v[168:169],v249 offset:16384
MF a[88:91],a[216:219],v[60:63],a[88:91]
ds_read_b64_tr_b16 v[170:171],v249 offset:16640
s_add_u32 m0,s69,82944
MF a[120:123],a[216:219],v[68:71],a[120:123]
buffer_load_dwordx4 v238,s[36:39],s67 offen lds
MF a[28:31],a[220:223],v[44:47],a[28:31]
ds_read_b64_tr_b16 v[172:173],v250 offset:16384
MF a[60:63],a[220:223],v[52:55],a[60:63]
ds_read_b64_tr_b16 v[174:175],v250 offset:16640
s_add_u32 m0,s69,83968
MF a[92:95],a[220:223],v[60:63],a[92:95]
buffer_load_dwordx4 v239,s[36:39],s67 offen lds
MF a[124:127],a[220:223],v[68:71],a[124:127]
ds_read_b64_tr_b16 v[176:177],v249 offset:16896
MF a[128:131],a[224:227],v[44:47],a[128:131]
ds_read_b64_tr_b16 v[178:179],v249 offset:17152
s_add_u32 m0,s69,84992
MF a[160:163],a[224:227],v[52:55],a[160:163]
buffer_load_dwordx4 v240,s[36:39],s67 offen lds
MF a[132:135],a[228:231],v[44:47],a[132:135]
ds_read_b64_tr_b16 v[180:181],v250 offset:16896
MF a[164:167],a[228:231],v[52:55],a[164:167]
ds_read_b64_tr_b16 v[182:183],v250 offset:17152
s_add_u32 m0,s69,98304
MF a[136:139],a[232:235],v[44:47],a[136:139]
buffer_load_dwordx4 v241,s[52:55],s68 offen lds
MF a[168:171],a[232:235],v[52:55],a[168:171]
ds_read_b64_tr_b16 v[184:185],v249 offset:17408
MF a[140:143],a[236:239],v[44:47],a[140:143]
ds_read_b64_tr_b16 v[186:187],v249 offset:17664
s_add_u32 m0,s69,99328
MF a[172:175],a[236:239],v[52:55],a[172:175]
buffer_load_dwordx4 v242,s[52:55],s68 offen lds
MF a[144:147],a[240:243],v[44:47],a[144:147]
ds_read_b64_tr_b16 v[188:189],v250 offset:17408
MF a[176:179],a[240:243],v[52:55],a[176:179]
ds_read_b64_tr_b16 v[190:191],v250 offset:17664
s_add_u32 m0,s69,100352
MF a[148:151],a[244:247],v[44:47],a[148:151]
buffer_load_dwordx4 v243,s[52:55],s68 offen lds
MF a[180:183],a[244:247],v[52:55],a[180:183]
ds_read_b64_tr_b16 v[192:193],v249 offset:17920
MF a[152:155],a[248:251],v[44:47],a[152:155]
ds_read_b64_tr_b16 v[194:195],v249 offset:18176
s_add_u32 m0,s69,101376
MF a[184:187],a[248:251],v[52:55],a[184:187]
buffer_load_dwordx4 v244,s[52:55],s68 offen lds
MF a[156:159],a[252:255],v[44:47],a[156:159]
ds_read_b64_tr_b16 v[196:197],v250 offset:17920
MF a[188:191],a[252:255],v[52:55],a[188:191]
ds_read_b64_tr_b16 v[198:199],v250 offset:18176
s_add_u32 s67,s67,524288
s_add_u32 s68,s68,221184
s_add_u32 s63,s63,2048
s_add_u32 s64,s64,2048
s_add_u32 s65,s65,2048
s_add_u32 s66,s66,2048
s_waitcnt lgkmcnt(15)
MF a[0:3],v[136:139],v[72:75],a[0:3]
ds_read_b64_tr_b16 a[192:193],v249 offset:8192
MF a[32:35],v[136:139],v[80:83],a[32:35]
ds_read_b64_tr_b16 a[194:195],v249 offset:8448
MF a[64:67],v[136:139],v[88:91],a[64:67]
ds_read_b64_tr_b16 a[196:197],v250 offset:8192
MF a[96:99],v[136:139],v[96:99],a[96:99]
ds_read_b64_tr_b16 a[198:199],v250 offset:8448
MF a[4:7],v[140:143],v[72:75],a[4:7]
ds_read_b64_tr_b16 a[200:201],v249 offset:8704
MF a[36:39],v[140:143],v[80:83],a[36:39]
ds_read_b64_tr_b16 a[202:203],v249 offset:8960
MF a[68:71],v[140:143],v[88:91],a[68:71]
ds_read_b64_tr_b16 a[204:205],v250 offset:8704
MF a[100:103],v[140:143],v[96:99],a[100:103]
ds_read_b64_tr_b16 a[206:207],v250 offset:8960
MF a[8:11],v[144:147],v[72:75],a[8:11]
MF a[40:43],v[144:147],v[80:83],a[40:43]
ds_read_b64_tr_b16 a[208:209],v249 offset:9216
MF a[72:75],v[144:147],v[88:91],a[72:75]
ds_read_b64_tr_b16 a[210:211],v249 offset:9472
MF a[104:107],v[144:147],v[96:99],a[104:107]
ds_read_b64_tr_b16 a[212:213],v250 offset:9216
MF a[12:15],v[148:151],v[72:75],a[12:15]
ds_read_b64_tr_b16 a[214:215],v250 offset:9472
MF a[44:47],v[148:151],v[80:83],a[44:47]
ds_read_b64_tr_b16 a[216:217],v249 offset:9728
MF a[76:79],v[148:151],v[88:91],a[76:79]
ds_read_b64_tr_b16 a[218:219],v249 offset:9984
MF a[108:111],v[148:151],v[96:99],a[108:111]
ds_read_b64_tr_b16 a[220:221],v250 offset:9728
MF a[16:19],v[152:155],v[72:75],a[16:19]
ds_read_b64_tr_b16 a[222:223],v250 offset:9984
MF a[48:51],v[152:155],v[80:83],a[48:51]
MF a[80:83],v[152:155],v[88:91],a[80:83]
ds_read_b64_tr_b16 a[224:225],v249 offset:24576
MF a[112:115],v[152:155],v[96:99],a[112:115]
ds_read_b64_tr_b16 a[226:227],v249 offset:24832
MF a[20:23],v[156:159],v[72:75],a[20:23]
ds_read_b64_tr_b16 a[228:229],v250 offset:24576
MF a[52:55],v[156:159],v[80:83],a[52:55]
ds_read_b64_tr_b16 a[230:231],v250 offset:24832
MF a[84:87],v[156:159],v[88:91],a[84:87]
ds_read_b64_tr_b16 a[232:233],v249 offset:25088
MF a[116:119],v[156:159],v[96:99],a[116:119]
ds_read_b64_tr_b16 a[234:235],v249 offset:25344
MF a[24:27],v[160:163],v[72:75],a[24:27]
ds_read_b64_tr_b16 a[236:237],v250 offset:25088
MF a[56:59],v[160:163],v[80:83],a[56:59]
ds_read_b64_tr_b16 a[238:239],v250 offset:25344
MF a[88:91],v[160:163],v[88:91],a[88:91]
MF a[120:123],v[160:163],v[96:99],a[120:123]
ds_read_b64_tr_b16 a[240:241],v249 offset:25600
MF a[28:31],v[164:167],v[72:75],a[28:31]
ds_read_b64_tr_b16 a[242:243],v249 offset:25856
MF a[60:63],v[164:167],v[80:83],a[60:63]
ds_read_b64_tr_b16 a[244:245],v250 offset:25600
MF a[92:95],v[164:167],v[88:91],a[92:95]
ds_read_b64_tr_b16 a[246:247],v250 offset:25856
MF a[124:127],v[164:167],v[96:99],a[124:127]
ds_read_b64_tr_b16 a[248:249],v249 offset:26112
s_waitcnt lgkmcnt(15)
MF a[128:131],v[168:171],v[72:75],a[128:131]
ds_read_b64_tr_b16 a[250:251],v249 offset:26368
MF a[160:163],v[168:171],v[80:83],a[160:163]
ds_read_b64_tr_b16 a[252:253],v250 offset:26112
MF a[132:135],v[172:175],v[72:75],a[132:135]
ds_read_b64_tr_b16 a[254:255],v250 offset:26368
MF a[164:167],v[172:175],v[80:83],a[164:167]
MF a[136:139],v[176:179],v[72:75],a[136:139]
MF a[168:171],v[176:179],v[80:83],a[168:171]
MF a[140:143],v[180:183],v[72:75],a[140:143]
MF a[172:175],v[180:183],v[80:83],a[172:175]
MF a[144:147],v[184:187],v[72:75],a[144:147]
MF a[176:179],v[184:187],v[80:83],a[176:179]
MF a[148:151],v[188:191],v[72:75],a[148:151]
MF a[180:183],v[188:191],v[80:83],a[180:183]
MF a[152:155],v[192:195],v[72:75],a[152:155]
MF a[184:187],v[192:195],v[80:83],a[184:187]
MF a[156:159],v[196:199],v[72:75],a[156:159]
MF a[188:191],v[196:199],v[80:83],a[188:191]
s_waitcnt vmcnt(8) lgkmcnt(0)
s_barrier
MF a[0:3],a[192:195],v[76:79],a[0:3]
buffer_load_dwordx4 v[40:43],v231,s[32:35],s63 offen
MF a[32:35],a[192:195],v[84:87],a[32:35]
ds_read_b64_tr_b16 v[136:137],v245
MF a[64:67],a[192:195],v[92:95],a[64:67]
ds_read_b64_tr_b16 v[138:139],v245 offset:256
MF a[96:99],a[192:195],v[100:103],a[96:99]
buffer_load_dwordx4 v[44:47],v231,s[32:35],s63 offen offset:1024
MF a[4:7],a[196:199],v[76:79],a[4:7]
ds_read_b64_tr_b16 v[140:141],v246
MF a[36:39],a[196:199],v[84:87],a[36:39]
ds_read_b64_tr_b16 v[142:143],v246 offset:256
MF a[68:71],a[196:199],v[92:95],a[68:71]
buffer_load_dwordx4 v[48:51],v231,s[32:35],s64 offen
MF a[100:103],a[196:199],v[100:103],a[100:103]
ds_read_b64_tr_b16 v[144:145],v245 offset:512
MF a[8:11],a[200:203],v[76:79],a[8:11]
ds_read_b64_tr_b16 v[146:147],v245 offset:768
MF a[40:43],a[200:203],v[84:87],a[40:43]
buffer_load_dwordx4 v[52:55],v231,s[32:35],s64 offen offset:1024
MF a[72:75],a[200:203],v[92:95],a[72:75]
ds_read_b64_tr_b16 v[148:149],v246 offset:512
MF a[104:107],a[200:203],v[100:103],a[104:107]
ds_read_b64_tr_b16 v[150:151],v246 offset:768
MF a[12:15],a[204:207],v[76:79],a[12:15]
buffer_load_dwordx4 v[56:59],v231,s[32:35],s65 offen
MF a[44:47],a[204:207],v[84:87],a[44:47]
ds_read_b64_tr_b16 v[152:153],v245 offset:1024
MF a[76:79],a[204:207],v[92:95],a[76:79]
ds_read_b64_tr_b16 v[154:155],v245 offset:1280
MF a[108:111],a[204:207],v[100:103],a[108:111]
buffer_load_dwordx4 v[60:63],v231,s[32:35],s65 offen offset:1024
MF a[16:19],a[208:211],v[76:79],a[16:19]
ds_read_b64_tr_b16 v[156:157],v246 offset:1024
MF a[48:51],a[208:211],v[84:87],a[48:51]
)A"
R"A(ds_read_b64_tr_b16 v[158:159],v246 offset:1280
MF a[80:83],a[208:211],v[92:95],a[80:83]
buffer_load_dwordx4 v[64:67],v231,s[32:35],s66 offen
MF a[112:115],a[208:211],v[100:103],a[112:115]
ds_read_b64_tr_b16 v[160:161],v245 offset:1536
MF a[20:23],a[212:215],v[76:79],a[20:23]
ds_read_b64_tr_b16 v[162:163],v245 offset:1792
MF a[52:55],a[212:215],v[84:87],a[52:55]
buffer_load_dwordx4 v[68:71],v231,s[32:35],s66 offen offset:1024
MF a[84:87],a[212:215],v[92:95],a[84:87]
ds_read_b64_tr_b16 v[164:165],v246 offset:1536
MF a[116:119],a[212:215],v[100:103],a[116:119]
ds_read_b64_tr_b16 v[166:167],v246 offset:1792
s_add_u32 m0,s69,114688
MF a[24:27],a[216:219],v[76:79],a[24:27]
buffer_load_dwordx4 v237,s[36:39],s67 offen lds
MF a[56:59],a[216:219],v[84:87],a[56:59]
ds_read_b64_tr_b16 v[168:169],v245 offset:16384
MF a[88:91],a[216:219],v[92:95],a[88:91]
ds_read_b64_tr_b16 v[170:171],v245 offset:16640
s_add_u32 m0,s69,115712
MF a[120:123],a[216:219],v[100:103],a[120:123]
buffer_load_dwordx4 v238,s[36:39],s67 offen lds
MF a[28:31],a[220:223],v[76:79],a[28:31]
ds_read_b64_tr_b16 v[172:173],v246 offset:16384
MF a[60:63],a[220:223],v[84:87],a[60:63]
ds_read_b64_tr_b16 v[174:175],v246 offset:16640
s_add_u32 m0,s69,116736
MF a[92:95],a[220:223],v[92:95],a[92:95]
buffer_load_dwordx4 v239,s[36:39],s67 offen lds
MF a[124:127],a[220:223],v[100:103],a[124:127]
ds_read_b64_tr_b16 v[176:177],v245 offset:16896
MF a[128:131],a[224:227],v[76:79],a[128:131]
ds_read_b64_tr_b16 v[178:179],v245 offset:17152
s_add_u32 m0,s69,117760
MF a[160:163],a[224:227],v[84:87],a[160:163]
buffer_load_dwordx4 v240,s[36:39],s67 offen lds
MF a[132:135],a[228:231],v[76:79],a[132:135]
ds_read_b64_tr_b16 v[180:181],v246 offset:16896
MF a[164:167],a[228:231],v[84:87],a[164:167]
ds_read_b64_tr_b16 v[182:183],v246 offset:17152
s_add_u32 m0,s69,131072
MF a[136:139],a[232:235],v[76:79],a[136:139]
buffer_load_dwordx4 v241,s[52:55],s68 offen lds
MF a[168:171],a[232:235],v[84:87],a[168:171]
ds_read_b64_tr_b16 v[184:185],v245 offset:17408
MF a[140:143],a[236:239],v[76:79],a[140:143]
ds_read_b64_tr_b16 v[186:187],v245 offset:17664
s_add_u32 m0,s69,132096
MF a[172:175],a[236:239],v[84:87],a[172:175]
buffer_load_dwordx4 v242,s[52:55],s68 offen lds
MF a[144:147],a[240:243],v[76:79],a[144:147]
ds_read_b64_tr_b16 v[188:189],v246 offset:17408
MF a[176:179],a[240:243],v[84:87],a[176:179]
ds_read_b64_tr_b16 v[190:191],v246 offset:17664
s_add_u32 m0,s69,133120
MF a[148:151],a[244:247],v[76:79],a[148:151]
buffer_load_dwordx4 v243,s[52:55],s68 offen lds
MF a[180:183],a[244:247],v[84:87],a[180:183]
ds_read_b64_tr_b16 v[192:193],v245 offset:17920
MF a[152:155],a[248:251],v[76:79],a[152:155]
ds_read_b64_tr_b16 v[194:195],v245 offset:18176
s_add_u32 m0,s69,134144
MF a[184:187],a[248:251],v[84:87],a[184:187]
buffer_load_dwordx4 v244,s[52:55],s68 offen lds
MF a[156:159],a[252:255],v[76:79],a[156:159]
ds_read_b64_tr_b16 v[196:197],v246 offset:17920
MF a[188:191],a[252:255],v[84:87],a[188:191]
ds_read_b64_tr_b16 v[198:199],v246 offset:18176
s_add_u32 s67,s67,524288
s_add_u32 s68,s68,221184
s_add_u32 s63,s63,2048
s_add_u32 s64,s64,2048
s_add_u32 s65,s65,2048
s_add_u32 s66,s66,2048
s_waitcnt lgkmcnt(15)
MF a[0:3],v[136:139],v[104:107],a[0:3]
ds_read_b64_tr_b16 a[192:193],v245 offset:8192
MF a[32:35],v[136:139],v[112:115],a[32:35]
ds_read_b64_tr_b16 a[194:195],v245 offset:8448
MF a[64:67],v[136:139],v[120:123],a[64:67]
ds_read_b64_tr_b16 a[196:197],v246 offset:8192
MF a[96:99],v[136:139],v[128:131],a[96:99]
ds_read_b64_tr_b16 a[198:199],v246 offset:8448
MF a[4:7],v[140:143],v[104:107],a[4:7]
ds_read_b64_tr_b16 a[200:201],v245 offset:8704
MF a[36:39],v[140:143],v[112:115],a[36:39]
ds_read_b64_tr_b16 a[202:203],v245 offset:8960
MF a[68:71],v[140:143],v[120:123],a[68:71]
ds_read_b64_tr_b16 a[204:205],v246 offset:8704
MF a[100:103],v[140:143],v[128:131],a[100:103]
ds_read_b64_tr_b16 a[206:207],v246 offset:8960
MF a[8:11],v[144:147],v[104:107],a[8:11]
MF a[40:43],v[144:147],v[112:115],a[40:43]
ds_read_b64_tr_b16 a[208:209],v245 offset:9216
MF a[72:75],v[144:147],v[120:123],a[72:75]
ds_read_b64_tr_b16 a[210:211],v245 offset:9472
MF a[104:107],v[144:147],v[128:131],a[104:107]
ds_read_b64_tr_b16 a[212:213],v246 offset:9216
MF a[12:15],v[148:151],v[104:107],a[12:15]
ds_read_b64_tr_b16 a[214:215],v246 offset:9472
MF a[44:47],v[148:151],v[112:115],a[44:47]
ds_read_b64_tr_b16 a[216:217],v245 offset:9728
MF a[76:79],v[148:151],v[120:123],a[76:79]
ds_read_b64_tr_b16 a[218:219],v245 offset:9984
MF a[108:111],v[148:151],v[128:131],a[108:111]
ds_read_b64_tr_b16 a[220:221],v246 offset:9728
MF a[16:19],v[152:155],v[104:107],a[16:19]
ds_read_b64_tr_b16 a[222:223],v246 offset:9984
MF a[48:51],v[152:155],v[112:115],a[48:51]
MF a[80:83],v[152:155],v[120:123],a[80:83]
ds_read_b64_tr_b16 a[224:225],v245 offset:24576
MF a[112:115],v[152:155],v[128:131],a[112:115]
ds_read_b64_tr_b16 a[226:227],v245 offset:24832
MF a[20:23],v[156:159],v[104:107],a[20:23]
ds_read_b64_tr_b16 a[228:229],v246 offset:24576
MF a[52:55],v[156:159],v[112:115],a[52:55]
ds_read_b64_tr_b16 a[230:231],v246 offset:24832
MF a[84:87],v[156:159],v[120:123],a[84:87]
ds_read_b64_tr_b16 a[232:233],v245 offset:25088
MF a[116:119],v[156:159],v[128:131],a[116:119]
ds_read_b64_tr_b16 a[234:235],v245 offset:25344
MF a[24:27],v[160:163],v[104:107],a[24:27]
ds_read_b64_tr_b16 a[236:237],v246 offset:25088
MF a[56:59],v[160:163],v[112:115],a[56:59]
ds_read_b64_tr_b16 a[238:239],v246 offset:25344
MF a[88:91],v[160:163],v[120:123],a[88:91]
MF a[120:123],v[160:163],v[128:131],a[120:123]
ds_read_b64_tr_b16 a[240:241],v245 offset:25600
MF a[28:31],v[164:167],v[104:107],a[28:31]
ds_read_b64_tr_b16 a[242:243],v245 offset:25856
MF a[60:63],v[164:167],v[112:115],a[60:63]
ds_read_b64_tr_b16 a[244:245],v246 offset:25600
MF a[92:95],v[164:167],v[120:123],a[92:95]
ds_read_b64_tr_b16 a[246:247],v246 offset:25856
MF a[124:127],v[164:167],v[128:131],a[124:127]
ds_read_b64_tr_b16 a[248:249],v245 offset:26112
s_waitcnt lgkmcnt(15)
MF a[128:131],v[168:171],v[104:107],a[128:131]
ds_read_b64_tr_b16 a[250:251],v245 offset:26368
MF a[160:163],v[168:171],v[112:115],a[160:163]
ds_read_b64_tr_b16 a[252:253],v246 offset:26112
MF a[132:135],v[172:175],v[104:107],a[132:135]
ds_read_b64_tr_b16 a[254:255],v246 offset:26368
MF a[164:167],v[172:175],v[112:115],a[164:167]
MF a[136:139],v[176:179],v[104:107],a[136:139]
MF a[168:171],v[176:179],v[112:115],a[168:171]
MF a[140:143],v[180:183],v[104:107],a[140:143]
MF a[172:175],v[180:183],v[112:115],a[172:175]
MF a[144:147],v[184:187],v[104:107],a[144:147]
MF a[176:179],v[184:187],v[112:115],a[176:179]
MF a[148:151],v[188:191],v[104:107],a[148:151]
MF a[180:183],v[188:191],v[112:115],a[180:183]
MF a[152:155],v[192:195],v[104:107],a[152:155]
MF a[184:187],v[192:195],v[112:115],a[184:187]
MF a[156:159],v[196:199],v[104:107],a[156:159]
MF a[188:191],v[196:199],v[112:115],a[188:191]
s_waitcnt vmcnt(8) lgkmcnt(0)
s_barrier
MF a[0:3],a[192:195],v[108:111],a[0:3]
buffer_load_dwordx4 v[72:75],v231,s[32:35],s63 offen
MF a[32:35],a[192:195],v[116:119],a[32:35]
ds_read_b64_tr_b16 v[136:137],v247
MF a[64:67],a[192:195],v[124:127],a[64:67]
ds_read_b64_tr_b16 v[138:139],v247 offset:256
MF a[96:99],a[192:195],v[132:135],a[96:99]
buffer_load_dwordx4 v[76:79],v231,s[32:35],s63 offen offset:1024
MF a[4:7],a[196:199],v[108:111],a[4:7]
ds_read_b64_tr_b16 v[140:141],v248
MF a[36:39],a[196:199],v[116:119],a[36:39]
ds_read_b64_tr_b16 v[142:143],v248 offset:256
MF a[68:71],a[196:199],v[124:127],a[68:71]
buffer_load_dwordx4 v[80:83],v231,s[32:35],s64 offen
MF a[100:103],a[196:199],v[132:135],a[100:103]
ds_read_b64_tr_b16 v[144:145],v247 offset:512
MF a[8:11],a[200:203],v[108:111],a[8:11]
ds_read_b64_tr_b16 v[146:147],v247 offset:768
MF a[40:43],a[200:203],v[116:119],a[40:43]
buffer_load_dwordx4 v[84:87],v231,s[32:35],s64 offen offset:1024
MF a[72:75],a[200:203],v[124:127],a[72:75]
ds_read_b64_tr_b16 v[148:149],v248 offset:512
MF a[104:107],a[200:203],v[132:135],a[104:107]
ds_read_b64_tr_b16 v[150:151],v248 offset:768
MF a[12:15],a[204:207],v[108:111],a[12:15]
buffer_load_dwordx4 v[88:91],v231,s[32:35],s65 offen
MF a[44:47],a[204:207],v[116:119],a[44:47]
ds_read_b64_tr_b16 v[152:153],v247 offset:1024
MF a[76:79],a[204:207],v[124:127],a[76:79]
ds_read_b64_tr_b16 v[154:155],v247 offset:1280
MF a[108:111],a[204:207],v[132:135],a[108:111]
buffer_load_dwordx4 v[92:95],v231,s[32:35],s65 offen offset:1024
MF a[16:19],a[208:211],v[108:111],a[16:19]
ds_read_b64_tr_b16 v[156:157],v248 offset:1024
MF a[48:51],a[208:211],v[116:119],a[48:51]
ds_read_b64_tr_b16 v[158:159],v248 offset:1280
MF a[80:83],a[208:211],v[124:127],a[80:83]
buffer_load_dwordx4 v[96:99],v231,s[32:35],s66 offen
MF a[112:115],a[208:211],v[132:135],a[112:115]
ds_read_b64_tr_b16 v[160:161],v247 offset:1536
MF a[20:23],a[212:215],v[108:111],a[20:23]
ds_read_b64_tr_b16 v[162:163],v247 offset:1792
MF a[52:55],a[212:215],v[116:119],a[52:55]
buffer_load_dwordx4 v[100:103],v231,s[32:35],s66 offen offset:1024
MF a[84:87],a[212:215],v[124:127],a[84:87]
ds_read_b64_tr_b16 v[164:165],v248 offset:1536
MF a[116:119],a[212:215],v[132:135],a[116:119]
ds_read_b64_tr_b16 v[166:167],v248 offset:1792
s_add_u32 m0,s69,32768
MF a[24:27],a[216:219],v[108:111],a[24:27]
buffer_load_dwordx4 v237,s[36:39],s67 offen lds
MF a[56:59],a[216:219],v[116:119],a[56:59]
ds_read_b64_tr_b16 v[168:169],v247 offset:16384
MF a[88:91],a[216:219],v[124:127],a[88:91]
ds_read_b64_tr_b16 v[170:171],v247 offset:16640
s_add_u32 m0,s69,33792
MF a[120:123],a[216:219],v[132:135],a[120:123]
buffer_load_dwordx4 v238,s[36:39],s67 offen lds
MF a[28:31],a[220:223],v[108:111],a[28:31]
ds_read_b64_tr_b16 v[172:173],v248 offset:16384
MF a[60:63],a[220:223],v[116:119],a[60:63]
ds_read_b64_tr_b16 v[174:175],v248 offset:16640
s_add_u32 m0,s69,34816
MF a[92:95],a[220:223],v[124:127],a[92:95]
buffer_load_dwordx4 v239,s[36:39],s67 offen lds
MF a[124:127],a[220:223],v[132:135],a[124:127]
ds_read_b64_tr_b16 v[176:177],v247 offset:16896
MF a[128:131],a[224:227],v[108:111],a[128:131]
ds_read_b64_tr_b16 v[178:179],v247 offset:17152
s_add_u32 m0,s69,35840
MF a[160:163],a[224:227],v[116:119],a[160:163]
buffer_load_dwordx4 v240,s[36:39],s67 offen lds
MF a[132:135],a[228:231],v[108:111],a[132:135]
ds_read_b64_tr_b16 v[180:181],v248 offset:16896
MF a[164:167],a[228:231],v[116:119],a[164:167]
ds_read_b64_tr_b16 v[182:183],v248 offset:17152
s_add_u32 m0,s69,49152
MF a[136:139],a[232:235],v[108:111],a[136:139]
buffer_load_dwordx4 v241,s[52:55],s68 offen lds
MF a[168:171],a[232:235],v[116:119],a[168:171]
ds_read_b64_tr_b16 v[184:185],v247 offset:17408
MF a[140:143],a[236:239],v[108:111],a[140:143]
ds_read_b64_tr_b16 v[186:187],v247 offset:17664
s_add_u32 m0,s69,50176
MF a[172:175],a[236:239],v[116:119],a[172:175]
buffer_load_dwordx4 v242,s[52:55],s68 offen lds
MF a[144:147],a[240:243],v[108:111],a[144:147]
ds_read_b64_tr_b16 v[188:189],v248 offset:17408
MF a[176:179],a[240:243],v[116:119],a[176:179]
ds_read_b64_tr_b16 v[190:191],v248 offset:17664
s_add_u32 m0,s69,51200
MF a[148:151],a[244:247],v[108:111],a[148:151]
buffer_load_dwordx4 v243,s[52:55],s68 offen lds
MF a[180:183],a[244:247],v[116:119],a[180:183]
ds_read_b64_tr_b16 v[192:193],v247 offset:17920
MF a[152:155],a[248:251],v[108:111],a[152:155]
ds_read_b64_tr_b16 v[194:195],v247 offset:18176
s_add_u32 m0,s69,52224
MF a[184:187],a[248:251],v[116:119],a[184:187]
buffer_load_dwordx4 v244,s[52:55],s68 offen lds
MF a[156:159],a[252:255],v[108:111],a[156:159]
ds_read_b64_tr_b16 v[196:197],v248 offset:17920
MF a[188:191],a[252:255],v[116:119],a[188:191]
ds_read_b64_tr_b16 v[198:199],v248 offset:18176
s_add_u32 s67,s67,524288
s_add_u32 s68,s68,221184
s_add_u32 s63,s63,2048
s_add_u32 s64,s64,2048
s_add_u32 s65,s65,2048
s_add_u32 s66,s66,2048
s_waitcnt lgkmcnt(15)
MF a[0:3],v[136:139],v[8:11],a[0:3]
ds_read_b64_tr_b16 a[192:193],v247 offset:8192
MF a[32:35],v[136:139],v[16:19],a[32:35]
ds_read_b64_tr_b16 a[194:195],v247 offset:8448
MF a[64:67],v[136:139],v[24:27],a[64:67]
ds_read_b64_tr_b16 a[196:197],v248 offset:8192
MF a[96:99],v[136:139],v[32:35],a[96:99]
ds_read_b64_tr_b16 a[198:199],v248 offset:8448
MF a[4:7],v[140:143],v[8:11],a[4:7]
ds_read_b64_tr_b16 a[200:201],v247 offset:8704
MF a[36:39],v[140:143],v[16:19],a[36:39]
ds_read_b64_tr_b16 a[202:203],v247 offset:8960
MF a[68:71],v[140:143],v[24:27],a[68:71]
ds_read_b64_tr_b16 a[204:205],v248 offset:8704
MF a[100:103],v[140:143],v[32:35],a[100:103]
ds_read_b64_tr_b16 a[206:207],v248 offset:8960
MF a[8:11],v[144:147],v[8:11],a[8:11]
MF a[40:43],v[144:147],v[16:19],a[40:43]
ds_read_b64_tr_b16 a[208:209],v247 offset:9216
MF a[72:75],v[144:147],v[24:27],a[72:75]
ds_read_b64_tr_b16 a[210:211],v247 offset:9472
MF a[104:107],v[144:147],v[32:35],a[104:107]
ds_read_b64_tr_b16 a[212:213],v248 offset:9216
MF a[12:15],v[148:151],v[8:11],a[12:15]
ds_read_b64_tr_b16 a[214:215],v248 offset:9472
MF a[44:47],v[148:151],v[16:19],a[44:47]
ds_read_b64_tr_b16 a[216:217],v247 offset:9728
MF a[76:79],v[148:151],v[24:27],a[76:79]
ds_read_b64_tr_b16 a[218:219],v247 offset:9984
MF a[108:111],v[148:151],v[32:35],a[108:111]
ds_read_b64_tr_b16 a[220:221],v248 offset:9728
MF a[16:19],v[152:155],v[8:11],a[16:19]
ds_read_b64_tr_b16 a[222:223],v248 offset:9984
MF a[48:51],v[152:155],v[16:19],a[48:51]
MF a[80:83],v[152:155],v[24:27],a[80:83]
ds_read_b64_tr_b16 a[224:225],v247 offset:24576
MF a[112:115],v[152:155],v[32:35],a[112:115]
ds_read_b64_tr_b16 a[226:227],v247 offset:24832
MF a[20:23],v[156:159],v[8:11],a[20:23]
ds_read_b64_tr_b16 a[228:229],v248 offset:24576
MF a[52:55],v[156:159],v[16:19],a[52:55]
ds_read_b64_tr_b16 a[230:231],v248 offset:24832
MF a[84:87],v[156:159],v[24:27],a[84:87]
ds_read_b64_tr_b16 a[232:233],v247 offset:25088
MF a[116:119],v[156:159],v[32:35],a[116:119]
ds_read_b64_tr_b16 a[234:235],v247 offset:25344
MF a[24:27],v[160:163],v[8:11],a[24:27]
ds_read_b64_tr_b16 a[236:237],v248 offset:25088
MF a[56:59],v[160:163],v[16:19],a[56:59]
ds_read_b64_tr_b16 a[238:239],v248 offset:25344
MF a[88:91],v[160:163],v[24:27],a[88:91]
MF a[120:123],v[160:163],v[32:35],a[120:123]
ds_read_b64_tr_b16 a[240:241],v247 offset:25600
MF a[28:31],v[164:167],v[8:11],a[28:31]
ds_read_b64_tr_b16 a[242:243],v247 offset:25856
MF a[60:63],v[164:167],v[16:19],a[60:63]
ds_read_b64_tr_b16 a[244:245],v248 offset:25600
MF a[92:95],v[164:167],v[24:27],a[92:95]
ds_read_b64_tr_b16 a[246:247],v248 offset:25856
MF a[124:127],v[164:167],v[32:35],a[124:127]
ds_read_b64_tr_b16 a[248:249],v247 offset:26112
s_waitcnt lgkmcnt(15)
MF a[128:131],v[168:171],v[8:11],a[128:131]
ds_read_b64_tr_b16 a[250:251],v247 offset:26368
MF a[160:163],v[168:171],v[16:19],a[160:163]
ds_read_b64_tr_b16 a[252:253],v248 offset:26112
MF a[132:135],v[172:175],v[8:11],a[132:135]
ds_read_b64_tr_b16 a[254:255],v248 offset:26368
MF a[164:167],v[172:175],v[16:19],a[164:167]
MF a[136:139],v[176:179],v[8:11],a[136:139]
MF a[168:171],v[176:179],v[16:19],a[168:171]
MF a[140:143],v[180:183],v[8:11],a[140:143]
MF a[172:175],v[180:183],v[16:19],a[172:175]
MF a[144:147],v[184:187],v[8:11],a[144:147]
MF a[176:179],v[184:187],v[16:19],a[176:179]
MF a[148:151],v[188:191],v[8:11],a[148:151]
MF a[180:183],v[188:191],v[16:19],a[180:183]
MF a[152:155],v[192:195],v[8:11],a[152:155]
MF a[184:187],v[192:195],v[16:19],a[184:187]
MF a[156:159],v[196:199],v[8:11],a[156:159]
MF a[188:191],v[196:199],v[16:19],a[188:191]
s_waitcnt vmcnt(8) lgkmcnt(0)
s_barrier
MF a[0:3],a[192:195],v[12:15],a[0:3]
buffer_load_dwordx4 v[104:107],v231,s[32:35],s63 offen
MF a[32:35],a[192:195],v[20:23],a[32:35]
ds_read_b64_tr_b16 v[136:137],v249
MF a[64:67],a[192:195],v[28:31],a[64:67]
ds_read_b64_tr_b16 v[138:139],v249 offset:256
MF a[96:99],a[192:195],v[36:39],a[96:99]
buffer_load_dwordx4 v[108:111],v231,s[32:35],s63 offen offset:1024
MF a[4:7],a[196:199],v[12:15],a[4:7]
ds_read_b64_tr_b16 v[140:141],v250
MF a[36:39],a[196:199],v[20:23],a[36:39]
ds_read_b64_tr_b16 v[142:143],v250 offset:256
MF a[68:71],a[196:199],v[28:31],a[68:71]
buffer_load_dwordx4 v[112:115],v231,s[32:35],s64 offen
MF a[100:103],a[196:199],v[36:39],a[100:103]
ds_read_b64_tr_b16 v[144:145],v249 offset:512
MF a[8:11],a[200:203],v[12:15],a[8:11]
ds_read_b64_tr_b16 v[146:147],v249 offset:768
MF a[40:43],a[200:203],v[20:23],a[40:43]
buffer_load_dwordx4 v[116:119],v231,s[32:35],s64 offen offset:1024
MF a[72:75],a[200:203],v[28:31],a[72:75]
ds_read_b64_tr_b16 v[148:149],v250 offset:512
MF a[104:107],a[200:203],v[36:39],a[104:107]
ds_read_b64_tr_b16 v[150:151],v250 offset:768
MF a[12:15],a[204:207],v[12:15],a[12:15]
buffer_load_dwordx4 v[120:123],v231,s[32:35],s65 offen
MF a[44:47],a[204:207],v[20:23],a[44:47]
ds_read_b64_tr_b16 v[152:153],v249 offset:1024
MF a[76:79],a[204:207],v[28:31],a[76:79]
ds_read_b64_tr_b16 v[154:155],v249 offset:1280
MF a[108:111],a[204:207],v[36:39],a[108:111]
buffer_load_dwordx4 v[124:127],v231,s[32:35],s65 offen offset:1024
MF a[16:19],a[208:211],v[12:15],a[16:19]
ds_read_b64_tr_b16 v[156:157],v250 offset:1024
MF a[48:51],a[208:211],v[20:23],a[48:51]
ds_read_b64_tr_b16 v[158:159],v250 offset:1280
MF a[80:83],a[208:211],v[28:31],a[80:83]
buffer_load_dwordx4 v[128:131],v231,s[32:35],s66 offen
MF a[112:115],a[208:211],v[36:39],a[112:115]
ds_read_b64_tr_b16 v[160:161],v249 offset:1536
MF a[20:23],a[212:215],v[12:15],a[20:23]
ds_read_b64_tr_b16 v[162:163],v249 offset:1792
MF a[52:55],a[212:215],v[20:23],a[52:55]
buffer_load_dwordx4 v[132:135],v231,s[32:35],s66 offen offset:1024
MF a[84:87],a[212:215],v[28:31],a[84:87]
ds_read_b64_tr_b16 v[164:165],v250 offset:1536
MF a[116:119],a[212:215],v[36:39],a[116:119]
ds_read_b64_tr_b16 v[166:167],v250 offset:1792
s_add_u32 m0,s69,81920
MF a[24:27],a[216:219],v[12:15],a[24:27]
buffer_load_dwordx4 v237,s[36:39],s67 offen lds
MF a[56:59],a[216:219],v[20:23],a[56:59]
ds_read_b64_tr_b16 v[168:169],v249 offset:16384
MF a[88:91],a[216:219],v[28:31],a[88:91]
ds_read_b64_tr_b16 v[170:171],v249 offset:16640
s_add_u32 m0,s69,82944
MF a[120:123],a[216:219],v[36:39],a[120:123]
buffer_load_dwordx4 v238,s[36:39],s67 offen lds
MF a[28:31],a[220:223],v[12:15],a[28:31]
ds_read_b64_tr_b16 v[172:173],v250 offset:16384
MF a[60:63],a[220:223],v[20:23],a[60:63]
ds_read_b64_tr_b16 v[174:175],v250 offset:16640
s_add_u32 m0,s69,83968
MF a[92:95],a[220:223],v[28:31],a[92:95]
buffer_load_dwordx4 v239,s[36:39],s67 offen lds
MF a[124:127],a[220:223],v[36:39],a[124:127]
ds_read_b64_tr_b16 v[176:177],v249 offset:16896
MF a[128:131],a[224:227],v[12:15],a[128:131]
ds_read_b64_tr_b16 v[178:179],v249 offset:17152
s_add_u32 m0,s69,84992
MF a[160:163],a[224:227],v[20:23],a[160:163]
buffer_load_dwordx4 v240,s[36:39],s67 offen lds
MF a[132:135],a[228:231],v[12:15],a[132:135]
ds_read_b64_tr_b16 v[180:181],v250 offset:16896
MF a[164:167],a[228:231],v[20:23],a[164:167]
ds_read_b64_tr_b16 v[182:183],v250 offset:17152
s_add_u32 m0,s69,98304
MF a[136:139],a[232:235],v[12:15],a[136:139]
buffer_load_dwordx4 v241,s[52:55],s68 offen lds
MF a[168:171],a[232:235],v[20:23],a[168:171]
ds_read_b64_tr_b16 v[184:185],v249 offset:17408
MF a[140:143],a[236:239],v[12:15],a[140:143]
ds_read_b64_tr_b16 v[186:187],v249 offset:17664
s_add_u32 m0,s69,99328
MF a[172:175],a[236:239],v[20:23],a[172:175]
buffer_load_dwordx4 v242,s[52:55],s68 offen lds
MF a[144:147],a[240:243],v[12:15],a[144:147]
ds_read_b64_tr_b16 v[188:189],v250 offset:17408
MF a[176:179],a[240:243],v[20:23],a[176:179]
ds_read_b64_tr_b16 v[190:191],v250 offset:17664
s_add_u32 m0,s69,100352
MF a[148:151],a[244:247],v[12:15],a[148:151]
buffer_load_dwordx4 v243,s[52:55],s68 offen lds
MF a[180:183],a[244:247],v[20:23],a[180:183]
ds_read_b64_tr_b16 v[192:193],v249 offset:17920
MF a[152:155],a[248:251],v[12:15],a[152:155]
ds_read_b64_tr_b16 v[194:195],v249 offset:18176
s_add_u32 m0,s69,101376
MF a[184:187],a[248:251],v[20:23],a[184:187]
buffer_load_dwordx4 v244,s[52:55],s68 offen lds
MF a[156:159],a[252:255],v[12:15],a[156:159]
ds_read_b64_tr_b16 v[196:197],v250 offset:17920
MF a[188:191],a[252:255],v[20:23],a[188:191]
ds_read_b64_tr_b16 v[198:199],v250 offset:18176
s_add_u32 s67,s67,524288
s_add_u32 s68,s68,221184
s_add_u32 s63,s63,2048
s_add_u32 s64,s64,2048
s_add_u32 s65,s65,2048
s_add_u32 s66,s66,2048
s_waitcnt lgkmcnt(15)
MF a[0:3],v[136:139],v[40:43],a[0:3]
ds_read_b64_tr_b16 a[192:193],v249 offset:8192
MF a[32:35],v[136:139],v[48:51],a[32:35]
ds_read_b64_tr_b16 a[194:195],v249 offset:8448
MF a[64:67],v[136:139],v[56:59],a[64:67]
ds_read_b64_tr_b16 a[196:197],v250 offset:8192
MF a[96:99],v[136:139],v[64:67],a[96:99]
ds_read_b64_tr_b16 a[198:199],v250 offset:8448
MF a[4:7],v[140:143],v[40:43],a[4:7]
ds_read_b64_tr_b16 a[200:201],v249 offset:8704
MF a[36:39],v[140:143],v[48:51],a[36:39]
ds_read_b64_tr_b16 a[202:203],v249 offset:8960
MF a[68:71],v[140:143],v[56:59],a[68:71]
ds_read_b64_tr_b16 a[204:205],v250 offset:8704
MF a[100:103],v[140:143],v[64:67],a[100:103]
ds_read_b64_tr_b16 a[206:207],v250 offset:8960
MF a[8:11],v[144:147],v[40:43],a[8:11]
MF a[40:43],v[144:147],v[48:51],a[40:43]
ds_read_b64_tr_b16 a[208:209],v249 offset:9216
MF a[72:75],v[144:147],v[56:59],a[72:75]
ds_read_b64_tr_b16 a[210:211],v249 offset:9472
MF a[104:107],v[144:147],v[64:67],a[104:107]
ds_read_b64_tr_b16 a[212:213],v250 offset:9216
MF a[12:15],v[148:151],v[40:43],a[12:15]
ds_read_b64_tr_b16 a[214:215],v250 offset:9472
MF a[44:47],v[148:151],v[48:51],a[44:47]
ds_read_b64_tr_b16 a[216:217],v249 offset:9728
MF a[76:79],v[148:151],v[56:59],a[76:79]
ds_read_b64_tr_b16 a[218:219],v249 offset:9984
MF a[108:111],v[148:151],v[64:67],a[108:111]
ds_read_b64_tr_b16 a[220:221],v250 offset:9728
MF a[16:19],v[152:155],v[40:43],a[16:19]
ds_read_b64_tr_b16 a[222:223],v250 offset:9984
MF a[48:51],v[152:155],v[48:51],a[48:51]
MF a[80:83],v[152:155],v[56:59],a[80:83]
ds_read_b64_tr_b16 a[224:225],v249 offset:24576
MF a[112:115],v[152:155],v[64:67],a[112:115]
ds_read_b64_tr_b16 a[226:227],v249 offset:24832
MF a[20:23],v[156:159],v[40:43],a[20:23]
ds_read_b64_tr_b16 a[228:229],v250 offset:24576
MF a[52:55],v[156:159],v[48:51],a[52:55]
ds_read_b64_tr_b16 a[230:231],v250 offset:24832
MF a[84:87],v[156:159],v[56:59],a[84:87]
ds_read_b64_tr_b16 a[232:233],v249 offset:25088
MF a[116:119],v[156:159],v[64:67],a[116:119]
ds_read_b64_tr_b16 a[234:235],v249 offset:25344
MF a[24:27],v[160:163],v[40:43],a[24:27]
ds_read_b64_tr_b16 a[236:237],v250 offset:25088
MF a[56:59],v[160:163],v[48:51],a[56:59]
ds_read_b64_tr_b16 a[238:239],v250 offset:25344
MF a[88:91],v[160:163],v[56:59],a[88:91]
MF a[120:123],v[160:163],v[64:67],a[120:123]
ds_read_b64_tr_b16 a[240:241],v249 offset:25600
MF a[28:31],v[164:167],v[40:43],a[28:31]
ds_read_b64_tr_b16 a[242:243],v249 offset:25856
MF a[60:63],v[164:167],v[48:51],a[60:63]
ds_read_b64_tr_b16 a[244:245],v250 offset:25600
MF a[92:95],v[164:167],v[56:59],a[92:95]
ds_read_b64_tr_b16 a[246:247],v250 offset:25856
MF a[124:127],v[164:167],v[64:67],a[124:127]
ds_read_b64_tr_b16 a[248:249],v249 offset:26112
s_waitcnt lgkmcnt(15)
MF a[128:131],v[168:171],v[40:43],a[128:131]
ds_read_b64_tr_b16 a[250:251],v249 offset:26368
MF a[160:163],v[168:171],v[48:51],a[160:163]
ds_read_b64_tr_b16 a[252:253],v250 offset:26112
MF a[132:135],v[172:175],v[40:43],a[132:135]
ds_read_b64_tr_b16 a[254:255],v250 offset:26368
MF a[164:167],v[172:175],v[48:51],a[164:167]
MF a[136:139],v[176:179],v[40:43],a[136:139]
MF a[168:171],v[176:179],v[48:51],a[168:171]
MF a[140:143],v[180:183],v[40:43],a[140:143]
MF a[172:175],v[180:183],v[48:51],a[172:175]
MF a[144:147],v[184:187],v[40:43],a[144:147]
MF a[176:179],v[184:187],v[48:51],a[176:179]
MF a[148:151],v[188:191],v[40:43],a[148:151]
MF a[180:183],v[188:191],v[48:51],a[180:183]
MF a[152:155],v[192:195],v[40:43],a[152:155]
MF a[184:187],v[192:195],v[48:51],a[184:187]
MF a[156:159],v[196:199],v[40:43],a[156:159]
MF a[188:191],v[196:199],v[48:51],a[188:191]
s_waitcnt vmcnt(8) lgkmcnt(0)
s_barrier
MF a[0:3],a[192:195],v[44:47],a[0:3]
buffer_load_dwordx4 v[8:11],v231,s[32:35],s63 offen
MF a[32:35],a[192:195],v[52:55],a[32:35]
ds_read_b64_tr_b16 v[136:137],v245
MF a[64:67],a[192:195],v[60:63],a[64:67]
ds_read_b64_tr_b16 v[138:139],v245 offset:256
MF a[96:99],a[192:195],v[68:71],a[96:99]
buffer_load_dwordx4 v[12:15],v231,s[32:35],s63 offen offset:1024
MF a[4:7],a[196:199],v[44:47],a[4:7]
ds_read_b64_tr_b16 v[140:141],v246
MF a[36:39],a[196:199],v[52:55],a[36:39]
ds_read_b64_tr_b16 v[142:143],v246 offset:256
MF a[68:71],a[196:199],v[60:63],a[68:71]
buffer_load_dwordx4 v[16:19],v231,s[32:35],s64 offen
MF a[100:103],a[196:199],v[68:71],a[100:103]
ds_read_b64_tr_b16 v[144:145],v245 offset:512
MF a[8:11],a[200:203],v[44:47],a[8:11]
ds_read_b64_tr_b16 v[146:147],v245 offset:768
MF a[40:43],a[200:203],v[52:55],a[40:43]
buffer_load_dwordx4 v[20:23],v231,s[32:35],s64 offen offset:1024
MF a[72:75],a[200:203],v[60:63],a[72:75]
ds_read_b64_tr_b16 v[148:149],v246 offset:512
MF a[104:107],a[200:203],v[68:71],a[104:107]
ds_read_b64_tr_b16 v[150:151],v246 offset:768
MF a[12:15],a[204:207],v[44:47],a[12:15]
buffer_load_dwordx4 v[24:27],v231,s[32:35],s65 offen
MF a[44:47],a[204:207],v[52:55],a[44:47]
ds_read_b64_tr_b16 v[152:153],v245 offset:1024
MF a[76:79],a[204:207],v[60:63],a[76:79]
ds_read_b64_tr_b16 v[154:155],v245 offset:1280
MF a[108:111],a[204:207],v[68:71],a[108:111]
buffer_load_dwordx4 v[28:31],v231,s[32:35],s65 offen offset:1024
MF a[16:19],a[208:211],v[44:47],a[16:19]
ds_read_b64_tr_b16 v[156:157],v246 offset:1024
MF a[48:51],a[208:211],v[52:55],a[48:51]
ds_read_b64_tr_b16 v[158:159],v246 offset:1280
MF a[80:83],a[208:211],v[60:63],a[80:83]
buffer_load_dwordx4 v[32:35],v231,s[32:35],s66 offen
MF a[112:115],a[208:211],v[68:71],a[112:115]
ds_read_b64_tr_b16 v[160:161],v245 offset:1536
MF a[20:23],a[212:215],v[44:47],a[20:23]
ds_read_b64_tr_b16 v[162:163],v245 offset:1792
MF a[52:55],a[212:215],v[52:55],a[52:55]
buffer_load_dwordx4 v[36:39],v231,s[32:35],s66 offen offset:1024
MF a[84:87],a[212:215],v[60:63],a[84:87]
ds_read_b64_tr_b16 v[164:165],v246 offset:1536
MF a[116:119],a[212:215],v[68:71],a[116:119]
ds_read_b64_tr_b16 v[166:167],v246 offset:1792
s_add_u32 m0,s69,114688
MF a[24:27],a[216:219],v[44:47],a[24:27]
buffer_load_dwordx4 v237,s[36:39],s67 offen lds
MF a[56:59],a[216:219],v[52:55],a[56:59]
ds_read_b64_tr_b16 v[168:169],v245 offset:16384
MF a[88:91],a[216:219],v[60:63],a[88:91]
ds_read_b64_tr_b16 v[170:171],v245 offset:16640
s_add_u32 m0,s69,115712
MF a[120:123],a[216:219],v[68:71],a[120:123]
buffer_load_dwordx4 v238,s[36:39],s67 offen lds
MF a[28:31],a[220:223],v[44:47],a[28:31]
ds_read_b64_tr_b16 v[172:173],v246 offset:16384
MF a[60:63],a[220:223],v[52:55],a[60:63]
ds_read_b64_tr_b16 v[174:175],v246 offset:16640
s_add_u32 m0,s69,116736
MF a[92:95],a[220:223],v[60:63],a[92:95]
buffer_load_dwordx4 v239,s[36:39],s67 offen lds
MF a[124:127],a[220:223],v[68:71],a[124:127]
ds_read_b64_tr_b16 v[176:177],v245 offset:16896
MF a[128:131],a[224:227],v[44:47],a[128:131]
ds_read_b64_tr_b16 v[178:179],v245 offset:17152
s_add_u32 m0,s69,117760
MF a[160:163],a[224:227],v[52:55],a[160:163]
buffer_load_dwordx4 v240,s[36:39],s67 offen lds
MF a[132:135],a[228:231],v[44:47],a[132:135]
ds_read_b64_tr_b16 v[180:181],v246 offset:16896
MF a[164:167],a[228:231],v[52:55],a[164:167]
ds_read_b64_tr_b16 v[182:183],v246 offset:17152
s_add_u32 m0,s69,131072
MF a[136:139],a[232:235],v[44:47],a[136:139]
buffer_load_dwordx4 v241,s[52:55],s68 offen lds
MF a[168:171],a[232:235],v[52:55],a[168:171]
ds_read_b64_tr_b16 v[184:185],v245 offset:17408
MF a[140:143],a[236:239],v[44:47],a[140:143]
ds_read_b64_tr_b16 v[186:187],v245 offset:17664
s_add_u32 m0,s69,132096
MF a[172:175],a[236:239],v[52:55],a[172:175]
buffer_load_dwordx4 v242,s[52:55],s68 offen lds
MF a[144:147],a[240:243],v[44:47],a[144:147]
ds_read_b64_tr_b16 v[188:189],v246 offset:17408
MF a[176:179],a[240:243],v[52:55],a[176:179]
ds_read_b64_tr_b16 v[190:191],v246 offset:17664
s_add_u32 m0,s69,133120
MF a[148:151],a[244:247],v[44:47],a[148:151]
buffer_load_dwordx4 v243,s[52:55],s68 offen lds
MF a[180:183],a[244:247],v[52:55],a[180:183]
ds_read_b64_tr_b16 v[192:193],v245 offset:17920
MF a[152:155],a[248:251],v[44:47],a[152:155]
ds_read_b64_tr_b16 v[194:195],v245 offset:18176
s_add_u32 m0,s69,134144
MF a[184:187],a[248:251],v[52:55],a[184:187]
buffer_load_dwordx4 v244,s[52:55],s68 offen lds
MF a[156:159],a[252:255],v[44:47],a[156:159]
ds_read_b64_tr_b16 v[196:197],v246 offset:17920
MF a[188:191],a[252:255],v[52:55],a[188:191]
ds_read_b64_tr_b16 v[198:199],v246 offset:18176
s_add_u32 s67,s67,524288
s_add_u32 s68,s68,221184
s_add_u32 s63,s63,2048
s_add_u32 s64,s64,2048
s_add_u32 s65,s65,2048
s_add_u32 s66,s66,2048
s_waitcnt lgkmcnt(15)
MF a[0:3],v[136:139],v[72:75],a[0:3]
ds_read_b64_tr_b16 a[192:193],v245 offset:8192
MF a[32:35],v[136:139],v[80:83],a[32:35]
ds_read_b64_tr_b16 a[194:195],v245 offset:8448
MF a[64:67],v[136:139],v[88:91],a[64:67]
ds_read_b64_tr_b16 a[196:197],v246 offset:8192
MF a[96:99],v[136:139],v[96:99],a[96:99]
ds_read_b64_tr_b16 a[198:199],v246 offset:8448
MF a[4:7],v[140:143],v[72:75],a[4:7]
ds_read_b64_tr_b16 a[200:201],v245 offset:8704
MF a[36:39],v[140:143],v[80:83],a[36:39]
ds_read_b64_tr_b16 a[202:203],v245 offset:8960
MF a[68:71],v[140:143],v[88:91],a[68:71]
ds_read_b64_tr_b16 a[204:205],v246 offset:8704
MF a[100:103],v[140:143],v[96:99],a[100:103]
ds_read_b64_tr_b16 a[206:207],v246 offset:8960
MF a[8:11],v[144:147],v[72:75],a[8:11]
MF a[40:43],v[144:147],v[80:83],a[40:43]
ds_read_b64_tr_b16 a[208:209],v245 offset:9216
MF a[72:75],v[144:147],v[88:91],a[72:75]
ds_read_b64_tr_b16 a[210:211],v245 offset:9472
MF a[104:107],v[144:147],v[96:99],a[104:107]
ds_read_b64_tr_b16 a[212:213],v246 offset:9216
MF a[12:15],v[148:151],v[72:75],a[12:15]
ds_read_b64_tr_b16 a[214:215],v246 offset:9472
MF a[44:47],v[148:151],v[80:83],a[44:47]
ds_read_b64_tr_b16 a[216:217],v245 offset:9728
MF a[76:79],v[148:151],v[88:91],a[76:79]
ds_read_b64_tr_b16 a[218:219],v245 offset:9984
MF a[108:111],v[148:151],v[96:99],a[108:111]
ds_read_b64_tr_b16 a[220:221],v246 offset:9728
MF a[16:19],v[152:155],v[72:75],a[16:19]
ds_read_b64_tr_b16 a[222:223],v246 offset:9984
MF a[48:51],v[152:155],v[80:83],a[48:51]
)A"
R"A(MF a[80:83],v[152:155],v[88:91],a[80:83]
ds_read_b64_tr_b16 a[224:225],v245 offset:24576
MF a[112:115],v[152:155],v[96:99],a[112:115]
ds_read_b64_tr_b16 a[226:227],v245 offset:24832
MF a[20:23],v[156:159],v[72:75],a[20:23]
ds_read_b64_tr_b16 a[228:229],v246 offset:24576
MF a[52:55],v[156:159],v[80:83],a[52:55]
ds_read_b64_tr_b16 a[230:231],v246 offset:24832
MF a[84:87],v[156:159],v[88:91],a[84:87]
ds_read_b64_tr_b16 a[232:233],v245 offset:25088
MF a[116:119],v[156:159],v[96:99],a[116:119]
ds_read_b64_tr_b16 a[234:235],v245 offset:25344
MF a[24:27],v[160:163],v[72:75],a[24:27]
ds_read_b64_tr_b16 a[236:237],v246 offset:25088
MF a[56:59],v[160:163],v[80:83],a[56:59]
ds_read_b64_tr_b16 a[238:239],v246 offset:25344
MF a[88:91],v[160:163],v[88:91],a[88:91]
MF a[120:123],v[160:163],v[96:99],a[120:123]
ds_read_b64_tr_b16 a[240:241],v245 offset:25600
MF a[28:31],v[164:167],v[72:75],a[28:31]
ds_read_b64_tr_b16 a[242:243],v245 offset:25856
MF a[60:63],v[164:167],v[80:83],a[60:63]
ds_read_b64_tr_b16 a[244:245],v246 offset:25600
MF a[92:95],v[164:167],v[88:91],a[92:95]
ds_read_b64_tr_b16 a[246:247],v246 offset:25856
MF a[124:127],v[164:167],v[96:99],a[124:127]
ds_read_b64_tr_b16 a[248:249],v245 offset:26112
s_waitcnt lgkmcnt(15)
MF a[128:131],v[168:171],v[72:75],a[128:131]
ds_read_b64_tr_b16 a[250:251],v245 offset:26368
MF a[160:163],v[168:171],v[80:83],a[160:163]
ds_read_b64_tr_b16 a[252:253],v246 offset:26112
MF a[132:135],v[172:175],v[72:75],a[132:135]
ds_read_b64_tr_b16 a[254:255],v246 offset:26368
MF a[164:167],v[172:175],v[80:83],a[164:167]
MF a[136:139],v[176:179],v[72:75],a[136:139]
MF a[168:171],v[176:179],v[80:83],a[168:171]
MF a[140:143],v[180:183],v[72:75],a[140:143]
MF a[172:175],v[180:183],v[80:83],a[172:175]
MF a[144:147],v[184:187],v[72:75],a[144:147]
MF a[176:179],v[184:187],v[80:83],a[176:179]
MF a[148:151],v[188:191],v[72:75],a[148:151]
MF a[180:183],v[188:191],v[80:83],a[180:183]
MF a[152:155],v[192:195],v[72:75],a[152:155]
MF a[184:187],v[192:195],v[80:83],a[184:187]
MF a[156:159],v[196:199],v[72:75],a[156:159]
MF a[188:191],v[196:199],v[80:83],a[188:191]
s_waitcnt vmcnt(8) lgkmcnt(0)
s_barrier
MF a[0:3],a[192:195],v[76:79],a[0:3]
buffer_load_dwordx4 v[40:43],v231,s[32:35],s63 offen
MF a[32:35],a[192:195],v[84:87],a[32:35]
ds_read_b64_tr_b16 v[136:137],v247
MF a[64:67],a[192:195],v[92:95],a[64:67]
ds_read_b64_tr_b16 v[138:139],v247 offset:256
MF a[96:99],a[192:195],v[100:103],a[96:99]
buffer_load_dwordx4 v[44:47],v231,s[32:35],s63 offen offset:1024
MF a[4:7],a[196:199],v[76:79],a[4:7]
ds_read_b64_tr_b16 v[140:141],v248
MF a[36:39],a[196:199],v[84:87],a[36:39]
ds_read_b64_tr_b16 v[142:143],v248 offset:256
MF a[68:71],a[196:199],v[92:95],a[68:71]
buffer_load_dwordx4 v[48:51],v231,s[32:35],s64 offen
MF a[100:103],a[196:199],v[100:103],a[100:103]
ds_read_b64_tr_b16 v[144:145],v247 offset:512
MF a[8:11],a[200:203],v[76:79],a[8:11]
ds_read_b64_tr_b16 v[146:147],v247 offset:768
MF a[40:43],a[200:203],v[84:87],a[40:43]
buffer_load_dwordx4 v[52:55],v231,s[32:35],s64 offen offset:1024
MF a[72:75],a[200:203],v[92:95],a[72:75]
ds_read_b64_tr_b16 v[148:149],v248 offset:512
MF a[104:107],a[200:203],v[100:103],a[104:107]
ds_read_b64_tr_b16 v[150:151],v248 offset:768
MF a[12:15],a[204:207],v[76:79],a[12:15]
buffer_load_dwordx4 v[56:59],v231,s[32:35],s65 offen
MF a[44:47],a[204:207],v[84:87],a[44:47]
ds_read_b64_tr_b16 v[152:153],v247 offset:1024
MF a[76:79],a[204:207],v[92:95],a[76:79]
ds_read_b64_tr_b16 v[154:155],v247 offset:1280
MF a[108:111],a[204:207],v[100:103],a[108:111]
buffer_load_dwordx4 v[60:63],v231,s[32:35],s65 offen offset:1024
MF a[16:19],a[208:211],v[76:79],a[16:19]
ds_read_b64_tr_b16 v[156:157],v248 offset:1024
MF a[48:51],a[208:211],v[84:87],a[48:51]
ds_read_b64_tr_b16 v[158:159],v248 offset:1280
MF a[80:83],a[208:211],v[92:95],a[80:83]
buffer_load_dwordx4 v[64:67],v231,s[32:35],s66 offen
MF a[112:115],a[208:211],v[100:103],a[112:115]
ds_read_b64_tr_b16 v[160:161],v247 offset:1536
MF a[20:23],a[212:215],v[76:79],a[20:23]
ds_read_b64_tr_b16 v[162:163],v247 offset:1792
MF a[52:55],a[212:215],v[84:87],a[52:55]
buffer_load_dwordx4 v[68:71],v231,s[32:35],s66 offen offset:1024
MF a[84:87],a[212:215],v[92:95],a[84:87]
ds_read_b64_tr_b16 v[164:165],v248 offset:1536
MF a[116:119],a[212:215],v[100:103],a[116:119]
ds_read_b64_tr_b16 v[166:167],v248 offset:1792
s_add_u32 m0,s69,32768
MF a[24:27],a[216:219],v[76:79],a[24:27]
buffer_load_dwordx4 v237,s[36:39],s67 offen lds
MF a[56:59],a[216:219],v[84:87],a[56:59]
ds_read_b64_tr_b16 v[168:169],v247 offset:16384
MF a[88:91],a[216:219],v[92:95],a[88:91]
ds_read_b64_tr_b16 v[170:171],v247 offset:16640
s_add_u32 m0,s69,33792
MF a[120:123],a[216:219],v[100:103],a[120:123]
buffer_load_dwordx4 v238,s[36:39],s67 offen lds
MF a[28:31],a[220:223],v[76:79],a[28:31]
ds_read_b64_tr_b16 v[172:173],v248 offset:16384
MF a[60:63],a[220:223],v[84:87],a[60:63]
ds_read_b64_tr_b16 v[174:175],v248 offset:16640
s_add_u32 m0,s69,34816
MF a[92:95],a[220:223],v[92:95],a[92:95]
buffer_load_dwordx4 v239,s[36:39],s67 offen lds
MF a[124:127],a[220:223],v[100:103],a[124:127]
ds_read_b64_tr_b16 v[176:177],v247 offset:16896
MF a[128:131],a[224:227],v[76:79],a[128:131]
ds_read_b64_tr_b16 v[178:179],v247 offset:17152
s_add_u32 m0,s69,35840
MF a[160:163],a[224:227],v[84:87],a[160:163]
buffer_load_dwordx4 v240,s[36:39],s67 offen lds
MF a[132:135],a[228:231],v[76:79],a[132:135]
ds_read_b64_tr_b16 v[180:181],v248 offset:16896
MF a[164:167],a[228:231],v[84:87],a[164:167]
ds_read_b64_tr_b16 v[182:183],v248 offset:17152
s_add_u32 m0,s69,49152
MF a[136:139],a[232:235],v[76:79],a[136:139]
buffer_load_dwordx4 v241,s[52:55],s68 offen lds
MF a[168:171],a[232:235],v[84:87],a[168:171]
ds_read_b64_tr_b16 v[184:185],v247 offset:17408
MF a[140:143],a[236:239],v[76:79],a[140:143]
ds_read_b64_tr_b16 v[186:187],v247 offset:17664
s_add_u32 m0,s69,50176
MF a[172:175],a[236:239],v[84:87],a[172:175]
buffer_load_dwordx4 v242,s[52:55],s68 offen lds
MF a[144:147],a[240:243],v[76:79],a[144:147]
ds_read_b64_tr_b16 v[188:189],v248 offset:17408
MF a[176:179],a[240:243],v[84:87],a[176:179]
ds_read_b64_tr_b16 v[190:191],v248 offset:17664
s_add_u32 m0,s69,51200
MF a[148:151],a[244:247],v[76:79],a[148:151]
buffer_load_dwordx4 v243,s[52:55],s68 offen lds
MF a[180:183],a[244:247],v[84:87],a[180:183]
ds_read_b64_tr_b16 v[192:193],v247 offset:17920
MF a[152:155],a[248:251],v[76:79],a[152:155]
ds_read_b64_tr_b16 v[194:195],v247 offset:18176
s_add_u32 m0,s69,52224
MF a[184:187],a[248:251],v[84:87],a[184:187]
buffer_load_dwordx4 v244,s[52:55],s68 offen lds
MF a[156:159],a[252:255],v[76:79],a[156:159]
ds_read_b64_tr_b16 v[196:197],v248 offset:17920
MF a[188:191],a[252:255],v[84:87],a[188:191]
ds_read_b64_tr_b16 v[198:199],v248 offset:18176
s_add_u32 s67,s67,524288
s_add_u32 s68,s68,221184
s_add_u32 s63,s63,2048
s_add_u32 s64,s64,2048
s_add_u32 s65,s65,2048
s_add_u32 s66,s66,2048
s_waitcnt lgkmcnt(15)
MF a[0:3],v[136:139],v[104:107],a[0:3]
ds_read_b64_tr_b16 a[192:193],v247 offset:8192
MF a[32:35],v[136:139],v[112:115],a[32:35]
ds_read_b64_tr_b16 a[194:195],v247 offset:8448
MF a[64:67],v[136:139],v[120:123],a[64:67]
ds_read_b64_tr_b16 a[196:197],v248 offset:8192
MF a[96:99],v[136:139],v[128:131],a[96:99]
ds_read_b64_tr_b16 a[198:199],v248 offset:8448
MF a[4:7],v[140:143],v[104:107],a[4:7]
ds_read_b64_tr_b16 a[200:201],v247 offset:8704
MF a[36:39],v[140:143],v[112:115],a[36:39]
ds_read_b64_tr_b16 a[202:203],v247 offset:8960
MF a[68:71],v[140:143],v[120:123],a[68:71]
ds_read_b64_tr_b16 a[204:205],v248 offset:8704
MF a[100:103],v[140:143],v[128:131],a[100:103]
ds_read_b64_tr_b16 a[206:207],v248 offset:8960
MF a[8:11],v[144:147],v[104:107],a[8:11]
MF a[40:43],v[144:147],v[112:115],a[40:43]
ds_read_b64_tr_b16 a[208:209],v247 offset:9216
MF a[72:75],v[144:147],v[120:123],a[72:75]
ds_read_b64_tr_b16 a[210:211],v247 offset:9472
MF a[104:107],v[144:147],v[128:131],a[104:107]
ds_read_b64_tr_b16 a[212:213],v248 offset:9216
MF a[12:15],v[148:151],v[104:107],a[12:15]
ds_read_b64_tr_b16 a[214:215],v248 offset:9472
MF a[44:47],v[148:151],v[112:115],a[44:47]
ds_read_b64_tr_b16 a[216:217],v247 offset:9728
MF a[76:79],v[148:151],v[120:123],a[76:79]
ds_read_b64_tr_b16 a[218:219],v247 offset:9984
MF a[108:111],v[148:151],v[128:131],a[108:111]
ds_read_b64_tr_b16 a[220:221],v248 offset:9728
MF a[16:19],v[152:155],v[104:107],a[16:19]
ds_read_b64_tr_b16 a[222:223],v248 offset:9984
MF a[48:51],v[152:155],v[112:115],a[48:51]
MF a[80:83],v[152:155],v[120:123],a[80:83]
ds_read_b64_tr_b16 a[224:225],v247 offset:24576
MF a[112:115],v[152:155],v[128:131],a[112:115]
ds_read_b64_tr_b16 a[226:227],v247 offset:24832
MF a[20:23],v[156:159],v[104:107],a[20:23]
ds_read_b64_tr_b16 a[228:229],v248 offset:24576
MF a[52:55],v[156:159],v[112:115],a[52:55]
ds_read_b64_tr_b16 a[230:231],v248 offset:24832
MF a[84:87],v[156:159],v[120:123],a[84:87]
ds_read_b64_tr_b16 a[232:233],v247 offset:25088
MF a[116:119],v[156:159],v[128:131],a[116:119]
ds_read_b64_tr_b16 a[234:235],v247 offset:25344
MF a[24:27],v[160:163],v[104:107],a[24:27]
ds_read_b64_tr_b16 a[236:237],v248 offset:25088
MF a[56:59],v[160:163],v[112:115],a[56:59]
ds_read_b64_tr_b16 a[238:239],v248 offset:25344
MF a[88:91],v[160:163],v[120:123],a[88:91]
MF a[120:123],v[160:163],v[128:131],a[120:123]
ds_read_b64_tr_b16 a[240:241],v247 offset:25600
MF a[28:31],v[164:167],v[104:107],a[28:31]
ds_read_b64_tr_b16 a[242:243],v247 offset:25856
MF a[60:63],v[164:167],v[112:115],a[60:63]
ds_read_b64_tr_b16 a[244:245],v248 offset:25600
MF a[92:95],v[164:167],v[120:123],a[92:95]
ds_read_b64_tr_b16 a[246:247],v248 offset:25856
MF a[124:127],v[164:167],v[128:131],a[124:127]
ds_read_b64_tr_b16 a[248:249],v247 offset:26112
s_waitcnt lgkmcnt(15)
MF a[128:131],v[168:171],v[104:107],a[128:131]
ds_read_b64_tr_b16 a[250:251],v247 offset:26368
MF a[160:163],v[168:171],v[112:115],a[160:163]
ds_read_b64_tr_b16 a[252:253],v248 offset:26112
MF a[132:135],v[172:175],v[104:107],a[132:135]
ds_read_b64_tr_b16 a[254:255],v248 offset:26368
MF a[164:167],v[172:175],v[112:115],a[164:167]
MF a[136:139],v[176:179],v[104:107],a[136:139]
MF a[168:171],v[176:179],v[112:115],a[168:171]
MF a[140:143],v[180:183],v[104:107],a[140:143]
MF a[172:175],v[180:183],v[112:115],a[172:175]
MF a[144:147],v[184:187],v[104:107],a[144:147]
MF a[176:179],v[184:187],v[112:115],a[176:179]
MF a[148:151],v[188:191],v[104:107],a[148:151]
MF a[180:183],v[188:191],v[112:115],a[180:183]
MF a[152:155],v[192:195],v[104:107],a[152:155]
MF a[184:187],v[192:195],v[112:115],a[184:187]
MF a[156:159],v[196:199],v[104:107],a[156:159]
MF a[188:191],v[196:199],v[112:115],a[188:191]
s_waitcnt vmcnt(8) lgkmcnt(0)
s_barrier
s_add_u32 m0,s74,0
MF a[0:3],a[192:195],v[108:111],a[0:3]
buffer_load_dwordx4 v230,s[40:43],s72 offen lds
MF a[32:35],a[192:195],v[116:119],a[32:35]
ds_read_b64_tr_b16 v[136:137],v249
MF a[64:67],a[192:195],v[124:127],a[64:67]
ds_read_b64_tr_b16 v[138:139],v249 offset:256
MF a[96:99],a[192:195],v[132:135],a[96:99]
ds_read_b64_tr_b16 v[140:141],v250
MF a[4:7],a[196:199],v[108:111],a[4:7]
ds_read_b64_tr_b16 v[142:143],v250 offset:256
MF a[36:39],a[196:199],v[116:119],a[36:39]
buffer_load_dwordx4 v230,s[40:43],s72 offen offset:1024 lds
MF a[68:71],a[196:199],v[124:127],a[68:71]
ds_read_b64_tr_b16 v[144:145],v249 offset:512
MF a[100:103],a[196:199],v[132:135],a[100:103]
ds_read_b64_tr_b16 v[146:147],v249 offset:768
MF a[8:11],a[200:203],v[108:111],a[8:11]
ds_read_b64_tr_b16 v[148:149],v250 offset:512
MF a[40:43],a[200:203],v[116:119],a[40:43]
ds_read_b64_tr_b16 v[150:151],v250 offset:768
MF a[72:75],a[200:203],v[124:127],a[72:75]
buffer_load_dwordx4 v230,s[40:43],s72 offen offset:2048 lds
MF a[104:107],a[200:203],v[132:135],a[104:107]
ds_read_b64_tr_b16 v[152:153],v249 offset:1024
MF a[12:15],a[204:207],v[108:111],a[12:15]
ds_read_b64_tr_b16 v[154:155],v249 offset:1280
MF a[44:47],a[204:207],v[116:119],a[44:47]
ds_read_b64_tr_b16 v[156:157],v250 offset:1024
MF a[76:79],a[204:207],v[124:127],a[76:79]
ds_read_b64_tr_b16 v[158:159],v250 offset:1280
MF a[108:111],a[204:207],v[132:135],a[108:111]
buffer_load_dwordx4 v230,s[40:43],s72 offen offset:3072 lds
MF a[16:19],a[208:211],v[108:111],a[16:19]
ds_read_b64_tr_b16 v[160:161],v249 offset:1536
MF a[48:51],a[208:211],v[116:119],a[48:51]
ds_read_b64_tr_b16 v[162:163],v249 offset:1792
MF a[80:83],a[208:211],v[124:127],a[80:83]
ds_read_b64_tr_b16 v[164:165],v250 offset:1536
MF a[112:115],a[208:211],v[132:135],a[112:115]
ds_read_b64_tr_b16 v[166:167],v250 offset:1792
MF a[20:23],a[212:215],v[108:111],a[20:23]
s_add_u32 m0,s74,65536
MF a[52:55],a[212:215],v[116:119],a[52:55]
buffer_load_dwordx4 v230,s[44:47],s73 offen lds
MF a[84:87],a[212:215],v[124:127],a[84:87]
ds_read_b64_tr_b16 v[168:169],v249 offset:16384
MF a[116:119],a[212:215],v[132:135],a[116:119]
ds_read_b64_tr_b16 v[170:171],v249 offset:16640
MF a[24:27],a[216:219],v[108:111],a[24:27]
ds_read_b64_tr_b16 v[172:173],v250 offset:16384
MF a[56:59],a[216:219],v[116:119],a[56:59]
ds_read_b64_tr_b16 v[174:175],v250 offset:16640
MF a[88:91],a[216:219],v[124:127],a[88:91]
buffer_load_dwordx4 v230,s[44:47],s73 offen offset:1024 lds
MF a[120:123],a[216:219],v[132:135],a[120:123]
ds_read_b64_tr_b16 v[176:177],v249 offset:16896
MF a[28:31],a[220:223],v[108:111],a[28:31]
ds_read_b64_tr_b16 v[178:179],v249 offset:17152
MF a[60:63],a[220:223],v[116:119],a[60:63]
ds_read_b64_tr_b16 v[180:181],v250 offset:16896
MF a[92:95],a[220:223],v[124:127],a[92:95]
ds_read_b64_tr_b16 v[182:183],v250 offset:17152
MF a[124:127],a[220:223],v[132:135],a[124:127]
buffer_load_dwordx4 v230,s[44:47],s73 offen offset:2048 lds
MF a[128:131],a[224:227],v[108:111],a[128:131]
ds_read_b64_tr_b16 v[184:185],v249 offset:17408
MF a[160:163],a[224:227],v[116:119],a[160:163]
ds_read_b64_tr_b16 v[186:187],v249 offset:17664
MF a[132:135],a[228:231],v[108:111],a[132:135]
ds_read_b64_tr_b16 v[188:189],v250 offset:17408
MF a[164:167],a[228:231],v[116:119],a[164:167]
ds_read_b64_tr_b16 v[190:191],v250 offset:17664
MF a[136:139],a[232:235],v[108:111],a[136:139]
buffer_load_dwordx4 v230,s[44:47],s73 offen offset:3072 lds
MF a[168:171],a[232:235],v[116:119],a[168:171]
ds_read_b64_tr_b16 v[192:193],v249 offset:17920
MF a[140:143],a[236:239],v[108:111],a[140:143]
ds_read_b64_tr_b16 v[194:195],v249 offset:18176
MF a[172:175],a[236:239],v[116:119],a[172:175]
ds_read_b64_tr_b16 v[196:197],v250 offset:17920
MF a[144:147],a[240:243],v[108:111],a[144:147]
ds_read_b64_tr_b16 v[198:199],v250 offset:18176
MF a[176:179],a[240:243],v[116:119],a[176:179]
MF a[148:151],a[244:247],v[108:111],a[148:151]
MF a[180:183],a[244:247],v[116:119],a[180:183]
MF a[152:155],a[248:251],v[108:111],a[152:155]
MF a[184:187],a[248:251],v[116:119],a[184:187]
MF a[156:159],a[252:255],v[108:111],a[156:159]
MF a[188:191],a[252:255],v[116:119],a[188:191]
s_add_u32 s72,s72,16384
s_add_u32 s73,s73,16384
s_waitcnt lgkmcnt(15)
MF a[0:3],v[136:139],v[8:11],a[0:3]
ds_read_b64_tr_b16 a[192:193],v249 offset:8192
MF a[32:35],v[136:139],v[16:19],a[32:35]
ds_read_b64_tr_b16 a[194:195],v249 offset:8448
MF a[64:67],v[136:139],v[24:27],a[64:67]
ds_read_b64_tr_b16 a[196:197],v250 offset:8192
MF a[96:99],v[136:139],v[32:35],a[96:99]
ds_read_b64_tr_b16 a[198:199],v250 offset:8448
MF a[4:7],v[140:143],v[8:11],a[4:7]
ds_read_b64_tr_b16 a[200:201],v249 offset:8704
MF a[36:39],v[140:143],v[16:19],a[36:39]
ds_read_b64_tr_b16 a[202:203],v249 offset:8960
MF a[68:71],v[140:143],v[24:27],a[68:71]
ds_read_b64_tr_b16 a[204:205],v250 offset:8704
MF a[100:103],v[140:143],v[32:35],a[100:103]
ds_read_b64_tr_b16 a[206:207],v250 offset:8960
MF a[8:11],v[144:147],v[8:11],a[8:11]
MF a[40:43],v[144:147],v[16:19],a[40:43]
ds_read_b64_tr_b16 a[208:209],v249 offset:9216
MF a[72:75],v[144:147],v[24:27],a[72:75]
ds_read_b64_tr_b16 a[210:211],v249 offset:9472
MF a[104:107],v[144:147],v[32:35],a[104:107]
ds_read_b64_tr_b16 a[212:213],v250 offset:9216
MF a[12:15],v[148:151],v[8:11],a[12:15]
ds_read_b64_tr_b16 a[214:215],v250 offset:9472
MF a[44:47],v[148:151],v[16:19],a[44:47]
ds_read_b64_tr_b16 a[216:217],v249 offset:9728
MF a[76:79],v[148:151],v[24:27],a[76:79]
ds_read_b64_tr_b16 a[218:219],v249 offset:9984
MF a[108:111],v[148:151],v[32:35],a[108:111]
ds_read_b64_tr_b16 a[220:221],v250 offset:9728
MF a[16:19],v[152:155],v[8:11],a[16:19]
ds_read_b64_tr_b16 a[222:223],v250 offset:9984
MF a[48:51],v[152:155],v[16:19],a[48:51]
MF a[80:83],v[152:155],v[24:27],a[80:83]
ds_read_b64_tr_b16 a[224:225],v249 offset:24576
MF a[112:115],v[152:155],v[32:35],a[112:115]
ds_read_b64_tr_b16 a[226:227],v249 offset:24832
MF a[20:23],v[156:159],v[8:11],a[20:23]
ds_read_b64_tr_b16 a[228:229],v250 offset:24576
MF a[52:55],v[156:159],v[16:19],a[52:55]
ds_read_b64_tr_b16 a[230:231],v250 offset:24832
MF a[84:87],v[156:159],v[24:27],a[84:87]
ds_read_b64_tr_b16 a[232:233],v249 offset:25088
MF a[116:119],v[156:159],v[32:35],a[116:119]
ds_read_b64_tr_b16 a[234:235],v249 offset:25344
MF a[24:27],v[160:163],v[8:11],a[24:27]
ds_read_b64_tr_b16 a[236:237],v250 offset:25088
MF a[56:59],v[160:163],v[16:19],a[56:59]
ds_read_b64_tr_b16 a[238:239],v250 offset:25344
MF a[88:91],v[160:163],v[24:27],a[88:91]
MF a[120:123],v[160:163],v[32:35],a[120:123]
ds_read_b64_tr_b16 a[240:241],v249 offset:25600
MF a[28:31],v[164:167],v[8:11],a[28:31]
ds_read_b64_tr_b16 a[242:243],v249 offset:25856
MF a[60:63],v[164:167],v[16:19],a[60:63]
ds_read_b64_tr_b16 a[244:245],v250 offset:25600
MF a[92:95],v[164:167],v[24:27],a[92:95]
ds_read_b64_tr_b16 a[246:247],v250 offset:25856
MF a[124:127],v[164:167],v[32:35],a[124:127]
ds_read_b64_tr_b16 a[248:249],v249 offset:26112
s_waitcnt lgkmcnt(15)
MF a[128:131],v[168:171],v[8:11],a[128:131]
ds_read_b64_tr_b16 a[250:251],v249 offset:26368
MF a[160:163],v[168:171],v[16:19],a[160:163]
ds_read_b64_tr_b16 a[252:253],v250 offset:26112
MF a[132:135],v[172:175],v[8:11],a[132:135]
ds_read_b64_tr_b16 a[254:255],v250 offset:26368
MF a[164:167],v[172:175],v[16:19],a[164:167]
MF a[136:139],v[176:179],v[8:11],a[136:139]
MF a[168:171],v[176:179],v[16:19],a[168:171]
MF a[140:143],v[180:183],v[8:11],a[140:143]
MF a[172:175],v[180:183],v[16:19],a[172:175]
MF a[144:147],v[184:187],v[8:11],a[144:147]
MF a[176:179],v[184:187],v[16:19],a[176:179]
MF a[148:151],v[188:191],v[8:11],a[148:151]
MF a[180:183],v[188:191],v[16:19],a[180:183]
MF a[152:155],v[192:195],v[8:11],a[152:155]
MF a[184:187],v[192:195],v[16:19],a[184:187]
MF a[156:159],v[196:199],v[8:11],a[156:159]
MF a[188:191],v[196:199],v[16:19],a[188:191]
s_waitcnt vmcnt(8) lgkmcnt(0)
s_barrier
s_add_u32 m0,s74,16384
MF a[0:3],a[192:195],v[12:15],a[0:3]
buffer_load_dwordx4 v230,s[40:43],s72 offen lds
MF a[32:35],a[192:195],v[20:23],a[32:35]
ds_read_b64_tr_b16 v[136:137],v245
MF a[64:67],a[192:195],v[28:31],a[64:67]
ds_read_b64_tr_b16 v[138:139],v245 offset:256
MF a[96:99],a[192:195],v[36:39],a[96:99]
ds_read_b64_tr_b16 v[140:141],v246
MF a[4:7],a[196:199],v[12:15],a[4:7]
ds_read_b64_tr_b16 v[142:143],v246 offset:256
MF a[36:39],a[196:199],v[20:23],a[36:39]
ds_read_b64_tr_b16 v[144:145],v245 offset:512
MF a[68:71],a[196:199],v[28:31],a[68:71]
MF a[100:103],a[196:199],v[36:39],a[100:103]
ds_read_b64_tr_b16 v[146:147],v245 offset:768
MF a[8:11],a[200:203],v[12:15],a[8:11]
ds_read_b64_tr_b16 v[148:149],v246 offset:512
MF a[40:43],a[200:203],v[20:23],a[40:43]
ds_read_b64_tr_b16 v[150:151],v246 offset:768
MF a[72:75],a[200:203],v[28:31],a[72:75]
buffer_load_dwordx4 v230,s[40:43],s72 offen offset:1024 lds
MF a[104:107],a[200:203],v[36:39],a[104:107]
ds_read_b64_tr_b16 v[152:153],v245 offset:1024
MF a[12:15],a[204:207],v[12:15],a[12:15]
ds_read_b64_tr_b16 v[154:155],v245 offset:1280
MF a[44:47],a[204:207],v[20:23],a[44:47]
MF a[76:79],a[204:207],v[28:31],a[76:79]
ds_read_b64_tr_b16 v[156:157],v246 offset:1024
MF a[108:111],a[204:207],v[36:39],a[108:111]
ds_read_b64_tr_b16 v[158:159],v246 offset:1280
MF a[16:19],a[208:211],v[12:15],a[16:19]
ds_read_b64_tr_b16 v[160:161],v245 offset:1536
MF a[48:51],a[208:211],v[20:23],a[48:51]
ds_read_b64_tr_b16 v[162:163],v245 offset:1792
MF a[80:83],a[208:211],v[28:31],a[80:83]
ds_read_b64_tr_b16 v[164:165],v246 offset:1536
MF a[112:115],a[208:211],v[36:39],a[112:115]
ds_read_b64_tr_b16 v[166:167],v246 offset:1792
MF a[20:23],a[212:215],v[12:15],a[20:23]
MF a[52:55],a[212:215],v[20:23],a[52:55]
buffer_load_dwordx4 v230,s[40:43],s72 offen offset:2048 lds
MF a[84:87],a[212:215],v[28:31],a[84:87]
ds_read_b64_tr_b16 v[168:169],v245 offset:16384
MF a[116:119],a[212:215],v[36:39],a[116:119]
ds_read_b64_tr_b16 v[170:171],v245 offset:16640
MF a[24:27],a[216:219],v[12:15],a[24:27]
ds_read_b64_tr_b16 v[172:173],v246 offset:16384
MF a[56:59],a[216:219],v[20:23],a[56:59]
ds_read_b64_tr_b16 v[174:175],v246 offset:16640
MF a[88:91],a[216:219],v[28:31],a[88:91]
ds_read_b64_tr_b16 v[176:177],v245 offset:16896
MF a[120:123],a[216:219],v[36:39],a[120:123]
MF a[28:31],a[220:223],v[12:15],a[28:31]
ds_read_b64_tr_b16 v[178:179],v245 offset:17152
MF a[60:63],a[220:223],v[20:23],a[60:63]
ds_read_b64_tr_b16 v[180:181],v246 offset:16896
MF a[92:95],a[220:223],v[28:31],a[92:95]
ds_read_b64_tr_b16 v[182:183],v246 offset:17152
MF a[124:127],a[220:223],v[36:39],a[124:127]
buffer_load_dwordx4 v230,s[40:43],s72 offen offset:3072 lds
MF a[128:131],a[224:227],v[12:15],a[128:131]
ds_read_b64_tr_b16 v[184:185],v245 offset:17408
MF a[160:163],a[224:227],v[20:23],a[160:163]
ds_read_b64_tr_b16 v[186:187],v245 offset:17664
MF a[132:135],a[228:231],v[12:15],a[132:135]
MF a[164:167],a[228:231],v[20:23],a[164:167]
ds_read_b64_tr_b16 v[188:189],v246 offset:17408
MF a[136:139],a[232:235],v[12:15],a[136:139]
ds_read_b64_tr_b16 v[190:191],v246 offset:17664
MF a[168:171],a[232:235],v[20:23],a[168:171]
ds_read_b64_tr_b16 v[192:193],v245 offset:17920
MF a[140:143],a[236:239],v[12:15],a[140:143]
ds_read_b64_tr_b16 v[194:195],v245 offset:18176
MF a[172:175],a[236:239],v[20:23],a[172:175]
ds_read_b64_tr_b16 v[196:197],v246 offset:17920
MF a[144:147],a[240:243],v[12:15],a[144:147]
ds_read_b64_tr_b16 v[198:199],v246 offset:18176
MF a[176:179],a[240:243],v[20:23],a[176:179]
MF a[148:151],a[244:247],v[12:15],a[148:151]
MF a[180:183],a[244:247],v[20:23],a[180:183]
MF a[152:155],a[248:251],v[12:15],a[152:155]
MF a[184:187],a[248:251],v[20:23],a[184:187]
MF a[156:159],a[252:255],v[12:15],a[156:159]
MF a[188:191],a[252:255],v[20:23],a[188:191]
s_add_u32 s72,s72,16384
s_waitcnt vmcnt(7) lgkmcnt(15)
MF a[0:3],v[136:139],v[40:43],a[0:3]
ds_read_b64_tr_b16 a[192:193],v245 offset:8192
s_waitcnt vmcnt(5)
MF a[32:35],v[136:139],v[48:51],a[32:35]
ds_read_b64_tr_b16 a[194:195],v245 offset:8448
s_waitcnt vmcnt(3)
MF a[64:67],v[136:139],v[56:59],a[64:67]
ds_read_b64_tr_b16 a[196:197],v246 offset:8192
s_waitcnt vmcnt(1)
MF a[96:99],v[136:139],v[64:67],a[96:99]
ds_read_b64_tr_b16 a[198:199],v246 offset:8448
MF a[4:7],v[140:143],v[40:43],a[4:7]
ds_read_b64_tr_b16 a[200:201],v245 offset:8704
MF a[36:39],v[140:143],v[48:51],a[36:39]
ds_read_b64_tr_b16 a[202:203],v245 offset:8960
MF a[68:71],v[140:143],v[56:59],a[68:71]
ds_read_b64_tr_b16 a[204:205],v246 offset:8704
MF a[100:103],v[140:143],v[64:67],a[100:103]
ds_read_b64_tr_b16 a[206:207],v246 offset:8960
MF a[8:11],v[144:147],v[40:43],a[8:11]
MF a[40:43],v[144:147],v[48:51],a[40:43]
ds_read_b64_tr_b16 a[208:209],v245 offset:9216
MF a[72:75],v[144:147],v[56:59],a[72:75]
ds_read_b64_tr_b16 a[210:211],v245 offset:9472
MF a[104:107],v[144:147],v[64:67],a[104:107]
ds_read_b64_tr_b16 a[212:213],v246 offset:9216
MF a[12:15],v[148:151],v[40:43],a[12:15]
ds_read_b64_tr_b16 a[214:215],v246 offset:9472
MF a[44:47],v[148:151],v[48:51],a[44:47]
ds_read_b64_tr_b16 a[216:217],v245 offset:9728
MF a[76:79],v[148:151],v[56:59],a[76:79]
ds_read_b64_tr_b16 a[218:219],v245 offset:9984
MF a[108:111],v[148:151],v[64:67],a[108:111]
ds_read_b64_tr_b16 a[220:221],v246 offset:9728
MF a[16:19],v[152:155],v[40:43],a[16:19]
ds_read_b64_tr_b16 a[222:223],v246 offset:9984
MF a[48:51],v[152:155],v[48:51],a[48:51]
MF a[80:83],v[152:155],v[56:59],a[80:83]
ds_read_b64_tr_b16 a[224:225],v245 offset:24576
MF a[112:115],v[152:155],v[64:67],a[112:115]
ds_read_b64_tr_b16 a[226:227],v245 offset:24832
MF a[20:23],v[156:159],v[40:43],a[20:23]
ds_read_b64_tr_b16 a[228:229],v246 offset:24576
MF a[52:55],v[156:159],v[48:51],a[52:55]
ds_read_b64_tr_b16 a[230:231],v246 offset:24832
MF a[84:87],v[156:159],v[56:59],a[84:87]
ds_read_b64_tr_b16 a[232:233],v245 offset:25088
MF a[116:119],v[156:159],v[64:67],a[116:119]
ds_read_b64_tr_b16 a[234:235],v245 offset:25344
MF a[24:27],v[160:163],v[40:43],a[24:27]
ds_read_b64_tr_b16 a[236:237],v246 offset:25088
MF a[56:59],v[160:163],v[48:51],a[56:59]
ds_read_b64_tr_b16 a[238:239],v246 offset:25344
MF a[88:91],v[160:163],v[56:59],a[88:91]
MF a[120:123],v[160:163],v[64:67],a[120:123]
ds_read_b64_tr_b16 a[240:241],v245 offset:25600
MF a[28:31],v[164:167],v[40:43],a[28:31]
ds_read_b64_tr_b16 a[242:243],v245 offset:25856
MF a[60:63],v[164:167],v[48:51],a[60:63]
ds_read_b64_tr_b16 a[244:245],v246 offset:25600
MF a[92:95],v[164:167],v[56:59],a[92:95]
ds_read_b64_tr_b16 a[246:247],v246 offset:25856
MF a[124:127],v[164:167],v[64:67],a[124:127]
ds_read_b64_tr_b16 a[248:249],v245 offset:26112
s_waitcnt lgkmcnt(15)
MF a[128:131],v[168:171],v[40:43],a[128:131]
ds_read_b64_tr_b16 a[250:251],v245 offset:26368
MF a[160:163],v[168:171],v[48:51],a[160:163]
ds_read_b64_tr_b16 a[252:253],v246 offset:26112
MF a[132:135],v[172:175],v[40:43],a[132:135]
ds_read_b64_tr_b16 a[254:255],v246 offset:26368
MF a[164:167],v[172:175],v[48:51],a[164:167]
MF a[136:139],v[176:179],v[40:43],a[136:139]
MF a[168:171],v[176:179],v[48:51],a[168:171]
MF a[140:143],v[180:183],v[40:43],a[140:143]
MF a[172:175],v[180:183],v[48:51],a[172:175]
MF a[144:147],v[184:187],v[40:43],a[144:147]
MF a[176:179],v[184:187],v[48:51],a[176:179]
MF a[148:151],v[188:191],v[40:43],a[148:151]
MF a[180:183],v[188:191],v[48:51],a[180:183]
MF a[152:155],v[192:195],v[40:43],a[152:155]
MF a[184:187],v[192:195],v[48:51],a[184:187]
MF a[156:159],v[196:199],v[40:43],a[156:159]
MF a[188:191],v[196:199],v[48:51],a[188:191]
s_waitcnt lgkmcnt(0)
MF a[0:3],a[192:195],v[44:47],a[0:3]
MF a[32:35],a[192:195],v[52:55],a[32:35]
MF a[64:67],a[192:195],v[60:63],a[64:67]
s_waitcnt vmcnt(0)
MF a[96:99],a[192:195],v[68:71],a[96:99]
MF a[4:7],a[196:199],v[44:47],a[4:7]
MF a[36:39],a[196:199],v[52:55],a[36:39]
MF a[68:71],a[196:199],v[60:63],a[68:71]
MF a[100:103],a[196:199],v[68:71],a[100:103]
MF a[8:11],a[200:203],v[44:47],a[8:11]
MF a[40:43],a[200:203],v[52:55],a[40:43]
MF a[72:75],a[200:203],v[60:63],a[72:75]
MF a[104:107],a[200:203],v[68:71],a[104:107]
MF a[12:15],a[204:207],v[44:47],a[12:15]
MF a[44:47],a[204:207],v[52:55],a[44:47]
MF a[76:79],a[204:207],v[60:63],a[76:79]
MF a[108:111],a[204:207],v[68:71],a[108:111]
MF a[16:19],a[208:211],v[44:47],a[16:19]
MF a[48:51],a[208:211],v[52:55],a[48:51]
MF a[80:83],a[208:211],v[60:63],a[80:83]
MF a[112:115],a[208:211],v[68:71],a[112:115]
MF a[20:23],a[212:215],v[44:47],a[20:23]
MF a[52:55],a[212:215],v[52:55],a[52:55]
MF a[84:87],a[212:215],v[60:63],a[84:87]
MF a[116:119],a[212:215],v[68:71],a[116:119]
MF a[24:27],a[216:219],v[44:47],a[24:27]
MF a[56:59],a[216:219],v[52:55],a[56:59]
MF a[88:91],a[216:219],v[60:63],a[88:91]
MF a[120:123],a[216:219],v[68:71],a[120:123]
MF a[28:31],a[220:223],v[44:47],a[28:31]
MF a[60:63],a[220:223],v[52:55],a[60:63]
MF a[92:95],a[220:223],v[60:63],a[92:95]
MF a[124:127],a[220:223],v[68:71],a[124:127]
MF a[128:131],a[224:227],v[44:47],a[128:131]
MF a[160:163],a[224:227],v[52:55],a[160:163]
MF a[132:135],a[228:231],v[44:47],a[132:135]
MF a[164:167],a[228:231],v[52:55],a[164:167]
MF a[136:139],a[232:235],v[44:47],a[136:139]
MF a[168:171],a[232:235],v[52:55],a[168:171]
MF a[140:143],a[236:239],v[44:47],a[140:143]
MF a[172:175],a[236:239],v[52:55],a[172:175]
MF a[144:147],a[240:243],v[44:47],a[144:147]
MF a[176:179],a[240:243],v[52:55],a[176:179]
MF a[148:151],a[244:247],v[44:47],a[148:151]
MF a[180:183],a[244:247],v[52:55],a[180:183]
MF a[152:155],a[248:251],v[44:47],a[152:155]
MF a[184:187],a[248:251],v[52:55],a[184:187]
MF a[156:159],a[252:255],v[44:47],a[156:159]
MF a[188:191],a[252:255],v[52:55],a[188:191]
s_load_dwordx2 s[8:9],s[4:5],0x58
s_load_dwordx2 s[10:11],s[4:5],0x60
s_load_dwordx2 s[18:19],s[4:5],0x68
s_lshl_b32 s100,s6,8
v_add_u32 v251,s100,v0
s_waitcnt lgkmcnt(0)
s_cmp_ge_u32 s57,28
s_cbranch_scc1 LZEND_%=
s_mov_b32 s48,s20
s_and_b32 s49,s21,0xffff
s_mov_b32 s50,3538944
s_mov_b32 s51,0x20000
s_mov_b32 s36,s22
s_and_b32 s37,s23,0xffff
s_mov_b32 s38,3670016
s_mov_b32 s39,0x20000
s_mul_i32 s78,s88,27648
s_mul_i32 s77,s88,28672
v_and_b32 v2,15,v1
v_mul_u32_u24 v131,1792,v2
v_lshrrev_b32 v2,4,v1
v_lshl_add_u32 v131,v2,4,v131
v_mov_b32 v128,0xc1906ebb
v_mov_b32 v129,0xc2469614
v_mov_b32 v130,0x3e0293ee
s_lshl_b32 s96,s71,2
s_add_u32 s96,s96,0
s_cmp_ge_u32 s96,54
s_cbranch_scc1 LZEND_%=
AR v8,a128
AR v9,a129
AR v10,a130
AR v11,a131
AR v12,a132
AR v13,a133
AR v14,a134
AR v15,a135
v_mul_f32 v120,v8,v8
v_mul_f32 v121,v9,v9
v_mul_f32 v122,v10,v10
v_mul_f32 v123,v11,v11
v_mul_f32 v124,v12,v12
v_mul_f32 v125,v13,v13
v_mul_f32 v126,v14,v14
v_mul_f32 v127,v15,v15
v_fma_f32 v120,v120,v129,v128
v_fma_f32 v121,v121,v129,v128
v_fma_f32 v122,v122,v129,v128
v_fma_f32 v123,v123,v129,v128
v_fma_f32 v124,v124,v129,v128
v_fma_f32 v125,v125,v129,v128
v_fma_f32 v126,v126,v129,v128
v_fma_f32 v127,v127,v129,v128
v_mul_f32 v120,v120,v8
v_mul_f32 v121,v121,v9
v_mul_f32 v122,v122,v10
v_mul_f32 v123,v123,v11
v_mul_f32 v124,v124,v12
v_mul_f32 v125,v125,v13
v_mul_f32 v126,v126,v14
v_mul_f32 v127,v127,v15
v_exp_f32 v120,v120
v_exp_f32 v121,v121
v_exp_f32 v122,v122
v_exp_f32 v123,v123
v_exp_f32 v124,v124
v_exp_f32 v125,v125
v_exp_f32 v126,v126
v_exp_f32 v127,v127
v_fma_f32 v120,v120,v130,v130
v_fma_f32 v121,v121,v130,v130
v_fma_f32 v122,v122,v130,v130
v_fma_f32 v123,v123,v130,v130
v_fma_f32 v124,v124,v130,v130
v_fma_f32 v125,v125,v130,v130
v_fma_f32 v126,v126,v130,v130
v_fma_f32 v127,v127,v130,v130
v_rcp_f32 v120,v120
v_rcp_f32 v121,v121
v_rcp_f32 v122,v122
v_rcp_f32 v123,v123
v_rcp_f32 v124,v124
v_rcp_f32 v125,v125
v_rcp_f32 v126,v126
v_rcp_f32 v127,v127
v_mul_f32 v8,v8,v120
)A"
R"A(v_mul_f32 v9,v9,v121
v_mul_f32 v10,v10,v122
v_mul_f32 v11,v11,v123
v_mul_f32 v12,v12,v124
v_mul_f32 v13,v13,v125
v_mul_f32 v14,v14,v126
v_mul_f32 v15,v15,v127
AR v16,a160
AR v17,a161
AR v18,a162
AR v19,a163
AR v20,a164
AR v21,a165
AR v22,a166
AR v23,a167
v_mul_f32 v120,v16,v16
v_mul_f32 v121,v17,v17
v_mul_f32 v122,v18,v18
v_mul_f32 v123,v19,v19
v_mul_f32 v124,v20,v20
v_mul_f32 v125,v21,v21
v_mul_f32 v126,v22,v22
v_mul_f32 v127,v23,v23
v_fma_f32 v120,v120,v129,v128
v_fma_f32 v121,v121,v129,v128
v_fma_f32 v122,v122,v129,v128
v_fma_f32 v123,v123,v129,v128
v_fma_f32 v124,v124,v129,v128
v_fma_f32 v125,v125,v129,v128
v_fma_f32 v126,v126,v129,v128
v_fma_f32 v127,v127,v129,v128
v_mul_f32 v120,v120,v16
v_mul_f32 v121,v121,v17
v_mul_f32 v122,v122,v18
v_mul_f32 v123,v123,v19
v_mul_f32 v124,v124,v20
v_mul_f32 v125,v125,v21
v_mul_f32 v126,v126,v22
v_mul_f32 v127,v127,v23
v_exp_f32 v120,v120
v_exp_f32 v121,v121
v_exp_f32 v122,v122
v_exp_f32 v123,v123
v_exp_f32 v124,v124
v_exp_f32 v125,v125
v_exp_f32 v126,v126
v_exp_f32 v127,v127
v_fma_f32 v120,v120,v130,v130
v_fma_f32 v121,v121,v130,v130
v_fma_f32 v122,v122,v130,v130
v_fma_f32 v123,v123,v130,v130
v_fma_f32 v124,v124,v130,v130
v_fma_f32 v125,v125,v130,v130
v_fma_f32 v126,v126,v130,v130
v_fma_f32 v127,v127,v130,v130
v_rcp_f32 v120,v120
v_rcp_f32 v121,v121
v_rcp_f32 v122,v122
v_rcp_f32 v123,v123
v_rcp_f32 v124,v124
v_rcp_f32 v125,v125
v_rcp_f32 v126,v126
v_rcp_f32 v127,v127
v_mul_f32 v16,v16,v120
v_mul_f32 v17,v17,v121
v_mul_f32 v18,v18,v122
v_mul_f32 v19,v19,v123
v_mul_f32 v20,v20,v124
v_mul_f32 v21,v21,v125
v_mul_f32 v22,v22,v126
v_mul_f32 v23,v23,v127
s_cmp_ge_u32 s96,27
s_cbranch_scc1 LZ20_%=
CP v72,v8,v9
CP v73,v10,v11
CP v74,v12,v13
CP v75,v14,v15
s_lshl_b32 s100,s96,10
s_add_u32 s100,s100,s78
buffer_store_dwordx4 v[72:75],v231,s[48:51],s100 offen
CP v76,v16,v17
CP v77,v18,v19
CP v78,v20,v21
CP v79,v22,v23
s_lshl_b32 s100,s96,10
s_add_u32 s100,s100,s78
s_add_u32 s100,s100,27648
buffer_store_dwordx4 v[76:79],v231,s[48:51],s100 offen
s_branch LN0_%=
LZ20_%=:
CP v72,v8,v9
CP v73,v10,v11
CP v74,v12,v13
CP v75,v14,v15
s_nop 0
v_permlane32_swap_b32 v72,v74
v_permlane32_swap_b32 v73,v75
s_nop 0
v_permlane16_swap_b32 v72,v74
v_permlane16_swap_b32 v73,v75
s_sub_u32 s100,s96,27
s_lshl_b32 s100,s100,6
s_add_u32 s100,s100,s77
buffer_store_dwordx4 v[72:75],v131,s[36:39],s100 offen sc1
CP v76,v16,v17
CP v77,v18,v19
CP v78,v20,v21
CP v79,v22,v23
s_nop 0
v_permlane32_swap_b32 v76,v78
v_permlane32_swap_b32 v77,v79
s_nop 0
v_permlane16_swap_b32 v76,v78
v_permlane16_swap_b32 v77,v79
s_sub_u32 s100,s96,27
s_lshl_b32 s100,s100,6
s_add_u32 s100,s100,s77
s_add_u32 s100,s100,28672
buffer_store_dwordx4 v[76:79],v131,s[36:39],s100 offen sc1
LN0_%=:
s_lshl_b32 s96,s71,2
s_add_u32 s96,s96,1
s_cmp_ge_u32 s96,54
s_cbranch_scc1 LZEND_%=
AR v8,a136
AR v9,a137
AR v10,a138
AR v11,a139
AR v12,a140
AR v13,a141
AR v14,a142
AR v15,a143
v_mul_f32 v120,v8,v8
v_mul_f32 v121,v9,v9
v_mul_f32 v122,v10,v10
v_mul_f32 v123,v11,v11
v_mul_f32 v124,v12,v12
v_mul_f32 v125,v13,v13
v_mul_f32 v126,v14,v14
v_mul_f32 v127,v15,v15
v_fma_f32 v120,v120,v129,v128
v_fma_f32 v121,v121,v129,v128
v_fma_f32 v122,v122,v129,v128
v_fma_f32 v123,v123,v129,v128
v_fma_f32 v124,v124,v129,v128
v_fma_f32 v125,v125,v129,v128
v_fma_f32 v126,v126,v129,v128
v_fma_f32 v127,v127,v129,v128
v_mul_f32 v120,v120,v8
v_mul_f32 v121,v121,v9
v_mul_f32 v122,v122,v10
v_mul_f32 v123,v123,v11
v_mul_f32 v124,v124,v12
v_mul_f32 v125,v125,v13
v_mul_f32 v126,v126,v14
v_mul_f32 v127,v127,v15
v_exp_f32 v120,v120
v_exp_f32 v121,v121
v_exp_f32 v122,v122
v_exp_f32 v123,v123
v_exp_f32 v124,v124
v_exp_f32 v125,v125
v_exp_f32 v126,v126
v_exp_f32 v127,v127
v_fma_f32 v120,v120,v130,v130
v_fma_f32 v121,v121,v130,v130
v_fma_f32 v122,v122,v130,v130
v_fma_f32 v123,v123,v130,v130
v_fma_f32 v124,v124,v130,v130
v_fma_f32 v125,v125,v130,v130
v_fma_f32 v126,v126,v130,v130
v_fma_f32 v127,v127,v130,v130
v_rcp_f32 v120,v120
v_rcp_f32 v121,v121
v_rcp_f32 v122,v122
v_rcp_f32 v123,v123
v_rcp_f32 v124,v124
v_rcp_f32 v125,v125
v_rcp_f32 v126,v126
v_rcp_f32 v127,v127
v_mul_f32 v8,v8,v120
v_mul_f32 v9,v9,v121
v_mul_f32 v10,v10,v122
v_mul_f32 v11,v11,v123
v_mul_f32 v12,v12,v124
v_mul_f32 v13,v13,v125
v_mul_f32 v14,v14,v126
v_mul_f32 v15,v15,v127
AR v16,a168
AR v17,a169
AR v18,a170
AR v19,a171
AR v20,a172
AR v21,a173
AR v22,a174
AR v23,a175
v_mul_f32 v120,v16,v16
v_mul_f32 v121,v17,v17
v_mul_f32 v122,v18,v18
v_mul_f32 v123,v19,v19
v_mul_f32 v124,v20,v20
v_mul_f32 v125,v21,v21
v_mul_f32 v126,v22,v22
v_mul_f32 v127,v23,v23
v_fma_f32 v120,v120,v129,v128
v_fma_f32 v121,v121,v129,v128
v_fma_f32 v122,v122,v129,v128
v_fma_f32 v123,v123,v129,v128
v_fma_f32 v124,v124,v129,v128
v_fma_f32 v125,v125,v129,v128
v_fma_f32 v126,v126,v129,v128
v_fma_f32 v127,v127,v129,v128
v_mul_f32 v120,v120,v16
v_mul_f32 v121,v121,v17
v_mul_f32 v122,v122,v18
v_mul_f32 v123,v123,v19
v_mul_f32 v124,v124,v20
v_mul_f32 v125,v125,v21
v_mul_f32 v126,v126,v22
v_mul_f32 v127,v127,v23
v_exp_f32 v120,v120
v_exp_f32 v121,v121
v_exp_f32 v122,v122
v_exp_f32 v123,v123
v_exp_f32 v124,v124
v_exp_f32 v125,v125
v_exp_f32 v126,v126
v_exp_f32 v127,v127
v_fma_f32 v120,v120,v130,v130
v_fma_f32 v121,v121,v130,v130
v_fma_f32 v122,v122,v130,v130
v_fma_f32 v123,v123,v130,v130
v_fma_f32 v124,v124,v130,v130
v_fma_f32 v125,v125,v130,v130
v_fma_f32 v126,v126,v130,v130
v_fma_f32 v127,v127,v130,v130
v_rcp_f32 v120,v120
v_rcp_f32 v121,v121
v_rcp_f32 v122,v122
v_rcp_f32 v123,v123
v_rcp_f32 v124,v124
v_rcp_f32 v125,v125
v_rcp_f32 v126,v126
v_rcp_f32 v127,v127
v_mul_f32 v16,v16,v120
v_mul_f32 v17,v17,v121
v_mul_f32 v18,v18,v122
v_mul_f32 v19,v19,v123
v_mul_f32 v20,v20,v124
v_mul_f32 v21,v21,v125
v_mul_f32 v22,v22,v126
v_mul_f32 v23,v23,v127
s_cmp_ge_u32 s96,27
s_cbranch_scc1 LZ21_%=
CP v72,v8,v9
CP v73,v10,v11
CP v74,v12,v13
CP v75,v14,v15
s_lshl_b32 s100,s96,10
s_add_u32 s100,s100,s78
buffer_store_dwordx4 v[72:75],v231,s[48:51],s100 offen
CP v76,v16,v17
CP v77,v18,v19
CP v78,v20,v21
CP v79,v22,v23
s_lshl_b32 s100,s96,10
s_add_u32 s100,s100,s78
s_add_u32 s100,s100,27648
buffer_store_dwordx4 v[76:79],v231,s[48:51],s100 offen
s_branch LN1_%=
LZ21_%=:
CP v72,v8,v9
CP v73,v10,v11
CP v74,v12,v13
CP v75,v14,v15
s_nop 0
v_permlane32_swap_b32 v72,v74
v_permlane32_swap_b32 v73,v75
s_nop 0
v_permlane16_swap_b32 v72,v74
v_permlane16_swap_b32 v73,v75
s_sub_u32 s100,s96,27
s_lshl_b32 s100,s100,6
s_add_u32 s100,s100,s77
buffer_store_dwordx4 v[72:75],v131,s[36:39],s100 offen sc1
CP v76,v16,v17
CP v77,v18,v19
CP v78,v20,v21
CP v79,v22,v23
s_nop 0
v_permlane32_swap_b32 v76,v78
v_permlane32_swap_b32 v77,v79
s_nop 0
v_permlane16_swap_b32 v76,v78
v_permlane16_swap_b32 v77,v79
s_sub_u32 s100,s96,27
s_lshl_b32 s100,s100,6
s_add_u32 s100,s100,s77
s_add_u32 s100,s100,28672
buffer_store_dwordx4 v[76:79],v131,s[36:39],s100 offen sc1
LN1_%=:
s_lshl_b32 s96,s71,2
s_add_u32 s96,s96,2
s_cmp_ge_u32 s96,54
s_cbranch_scc1 LZEND_%=
AR v8,a144
AR v9,a145
AR v10,a146
AR v11,a147
AR v12,a148
AR v13,a149
AR v14,a150
AR v15,a151
v_mul_f32 v120,v8,v8
v_mul_f32 v121,v9,v9
v_mul_f32 v122,v10,v10
v_mul_f32 v123,v11,v11
v_mul_f32 v124,v12,v12
v_mul_f32 v125,v13,v13
v_mul_f32 v126,v14,v14
v_mul_f32 v127,v15,v15
v_fma_f32 v120,v120,v129,v128
v_fma_f32 v121,v121,v129,v128
v_fma_f32 v122,v122,v129,v128
v_fma_f32 v123,v123,v129,v128
v_fma_f32 v124,v124,v129,v128
v_fma_f32 v125,v125,v129,v128
v_fma_f32 v126,v126,v129,v128
v_fma_f32 v127,v127,v129,v128
v_mul_f32 v120,v120,v8
v_mul_f32 v121,v121,v9
v_mul_f32 v122,v122,v10
v_mul_f32 v123,v123,v11
v_mul_f32 v124,v124,v12
v_mul_f32 v125,v125,v13
v_mul_f32 v126,v126,v14
v_mul_f32 v127,v127,v15
v_exp_f32 v120,v120
v_exp_f32 v121,v121
v_exp_f32 v122,v122
v_exp_f32 v123,v123
v_exp_f32 v124,v124
v_exp_f32 v125,v125
v_exp_f32 v126,v126
v_exp_f32 v127,v127
v_fma_f32 v120,v120,v130,v130
v_fma_f32 v121,v121,v130,v130
v_fma_f32 v122,v122,v130,v130
v_fma_f32 v123,v123,v130,v130
v_fma_f32 v124,v124,v130,v130
v_fma_f32 v125,v125,v130,v130
v_fma_f32 v126,v126,v130,v130
v_fma_f32 v127,v127,v130,v130
v_rcp_f32 v120,v120
v_rcp_f32 v121,v121
v_rcp_f32 v122,v122
v_rcp_f32 v123,v123
v_rcp_f32 v124,v124
v_rcp_f32 v125,v125
v_rcp_f32 v126,v126
v_rcp_f32 v127,v127
v_mul_f32 v8,v8,v120
v_mul_f32 v9,v9,v121
v_mul_f32 v10,v10,v122
v_mul_f32 v11,v11,v123
v_mul_f32 v12,v12,v124
v_mul_f32 v13,v13,v125
v_mul_f32 v14,v14,v126
v_mul_f32 v15,v15,v127
AR v16,a176
AR v17,a177
AR v18,a178
AR v19,a179
AR v20,a180
AR v21,a181
AR v22,a182
AR v23,a183
v_mul_f32 v120,v16,v16
v_mul_f32 v121,v17,v17
v_mul_f32 v122,v18,v18
v_mul_f32 v123,v19,v19
v_mul_f32 v124,v20,v20
v_mul_f32 v125,v21,v21
v_mul_f32 v126,v22,v22
v_mul_f32 v127,v23,v23
v_fma_f32 v120,v120,v129,v128
v_fma_f32 v121,v121,v129,v128
v_fma_f32 v122,v122,v129,v128
v_fma_f32 v123,v123,v129,v128
v_fma_f32 v124,v124,v129,v128
v_fma_f32 v125,v125,v129,v128
v_fma_f32 v126,v126,v129,v128
v_fma_f32 v127,v127,v129,v128
v_mul_f32 v120,v120,v16
v_mul_f32 v121,v121,v17
v_mul_f32 v122,v122,v18
v_mul_f32 v123,v123,v19
v_mul_f32 v124,v124,v20
v_mul_f32 v125,v125,v21
v_mul_f32 v126,v126,v22
v_mul_f32 v127,v127,v23
v_exp_f32 v120,v120
v_exp_f32 v121,v121
v_exp_f32 v122,v122
v_exp_f32 v123,v123
v_exp_f32 v124,v124
v_exp_f32 v125,v125
v_exp_f32 v126,v126
v_exp_f32 v127,v127
v_fma_f32 v120,v120,v130,v130
v_fma_f32 v121,v121,v130,v130
v_fma_f32 v122,v122,v130,v130
v_fma_f32 v123,v123,v130,v130
v_fma_f32 v124,v124,v130,v130
v_fma_f32 v125,v125,v130,v130
v_fma_f32 v126,v126,v130,v130
v_fma_f32 v127,v127,v130,v130
v_rcp_f32 v120,v120
v_rcp_f32 v121,v121
v_rcp_f32 v122,v122
v_rcp_f32 v123,v123
v_rcp_f32 v124,v124
v_rcp_f32 v125,v125
v_rcp_f32 v126,v126
v_rcp_f32 v127,v127
v_mul_f32 v16,v16,v120
v_mul_f32 v17,v17,v121
v_mul_f32 v18,v18,v122
v_mul_f32 v19,v19,v123
v_mul_f32 v20,v20,v124
v_mul_f32 v21,v21,v125
v_mul_f32 v22,v22,v126
v_mul_f32 v23,v23,v127
s_cmp_ge_u32 s96,27
s_cbranch_scc1 LZ22_%=
CP v72,v8,v9
CP v73,v10,v11
CP v74,v12,v13
CP v75,v14,v15
s_lshl_b32 s100,s96,10
s_add_u32 s100,s100,s78
buffer_store_dwordx4 v[72:75],v231,s[48:51],s100 offen
CP v76,v16,v17
CP v77,v18,v19
CP v78,v20,v21
CP v79,v22,v23
s_lshl_b32 s100,s96,10
s_add_u32 s100,s100,s78
s_add_u32 s100,s100,27648
buffer_store_dwordx4 v[76:79],v231,s[48:51],s100 offen
s_branch LN2_%=
LZ22_%=:
CP v72,v8,v9
CP v73,v10,v11
CP v74,v12,v13
CP v75,v14,v15
s_nop 0
v_permlane32_swap_b32 v72,v74
v_permlane32_swap_b32 v73,v75
s_nop 0
v_permlane16_swap_b32 v72,v74
v_permlane16_swap_b32 v73,v75
s_sub_u32 s100,s96,27
s_lshl_b32 s100,s100,6
s_add_u32 s100,s100,s77
buffer_store_dwordx4 v[72:75],v131,s[36:39],s100 offen sc1
CP v76,v16,v17
CP v77,v18,v19
CP v78,v20,v21
CP v79,v22,v23
s_nop 0
v_permlane32_swap_b32 v76,v78
v_permlane32_swap_b32 v77,v79
s_nop 0
v_permlane16_swap_b32 v76,v78
v_permlane16_swap_b32 v77,v79
s_sub_u32 s100,s96,27
s_lshl_b32 s100,s100,6
s_add_u32 s100,s100,s77
s_add_u32 s100,s100,28672
buffer_store_dwordx4 v[76:79],v131,s[36:39],s100 offen sc1
LN2_%=:
s_lshl_b32 s96,s71,2
s_add_u32 s96,s96,3
s_cmp_ge_u32 s96,54
s_cbranch_scc1 LZEND_%=
AR v8,a152
AR v9,a153
AR v10,a154
AR v11,a155
AR v12,a156
AR v13,a157
AR v14,a158
AR v15,a159
v_mul_f32 v120,v8,v8
v_mul_f32 v121,v9,v9
v_mul_f32 v122,v10,v10
v_mul_f32 v123,v11,v11
v_mul_f32 v124,v12,v12
v_mul_f32 v125,v13,v13
v_mul_f32 v126,v14,v14
v_mul_f32 v127,v15,v15
v_fma_f32 v120,v120,v129,v128
v_fma_f32 v121,v121,v129,v128
v_fma_f32 v122,v122,v129,v128
v_fma_f32 v123,v123,v129,v128
v_fma_f32 v124,v124,v129,v128
v_fma_f32 v125,v125,v129,v128
v_fma_f32 v126,v126,v129,v128
v_fma_f32 v127,v127,v129,v128
v_mul_f32 v120,v120,v8
v_mul_f32 v121,v121,v9
v_mul_f32 v122,v122,v10
v_mul_f32 v123,v123,v11
v_mul_f32 v124,v124,v12
v_mul_f32 v125,v125,v13
v_mul_f32 v126,v126,v14
v_mul_f32 v127,v127,v15
v_exp_f32 v120,v120
v_exp_f32 v121,v121
v_exp_f32 v122,v122
v_exp_f32 v123,v123
v_exp_f32 v124,v124
v_exp_f32 v125,v125
v_exp_f32 v126,v126
v_exp_f32 v127,v127
v_fma_f32 v120,v120,v130,v130
v_fma_f32 v121,v121,v130,v130
v_fma_f32 v122,v122,v130,v130
v_fma_f32 v123,v123,v130,v130
v_fma_f32 v124,v124,v130,v130
v_fma_f32 v125,v125,v130,v130
v_fma_f32 v126,v126,v130,v130
v_fma_f32 v127,v127,v130,v130
v_rcp_f32 v120,v120
v_rcp_f32 v121,v121
v_rcp_f32 v122,v122
v_rcp_f32 v123,v123
v_rcp_f32 v124,v124
v_rcp_f32 v125,v125
v_rcp_f32 v126,v126
v_rcp_f32 v127,v127
v_mul_f32 v8,v8,v120
v_mul_f32 v9,v9,v121
v_mul_f32 v10,v10,v122
v_mul_f32 v11,v11,v123
v_mul_f32 v12,v12,v124
v_mul_f32 v13,v13,v125
v_mul_f32 v14,v14,v126
v_mul_f32 v15,v15,v127
AR v16,a184
AR v17,a185
AR v18,a186
AR v19,a187
AR v20,a188
AR v21,a189
AR v22,a190
AR v23,a191
v_mul_f32 v120,v16,v16
v_mul_f32 v121,v17,v17
v_mul_f32 v122,v18,v18
v_mul_f32 v123,v19,v19
v_mul_f32 v124,v20,v20
v_mul_f32 v125,v21,v21
v_mul_f32 v126,v22,v22
v_mul_f32 v127,v23,v23
v_fma_f32 v120,v120,v129,v128
v_fma_f32 v121,v121,v129,v128
v_fma_f32 v122,v122,v129,v128
v_fma_f32 v123,v123,v129,v128
v_fma_f32 v124,v124,v129,v128
v_fma_f32 v125,v125,v129,v128
v_fma_f32 v126,v126,v129,v128
v_fma_f32 v127,v127,v129,v128
v_mul_f32 v120,v120,v16
v_mul_f32 v121,v121,v17
v_mul_f32 v122,v122,v18
v_mul_f32 v123,v123,v19
v_mul_f32 v124,v124,v20
v_mul_f32 v125,v125,v21
v_mul_f32 v126,v126,v22
v_mul_f32 v127,v127,v23
v_exp_f32 v120,v120
v_exp_f32 v121,v121
v_exp_f32 v122,v122
v_exp_f32 v123,v123
v_exp_f32 v124,v124
v_exp_f32 v125,v125
v_exp_f32 v126,v126
v_exp_f32 v127,v127
v_fma_f32 v120,v120,v130,v130
v_fma_f32 v121,v121,v130,v130
v_fma_f32 v122,v122,v130,v130
v_fma_f32 v123,v123,v130,v130
v_fma_f32 v124,v124,v130,v130
v_fma_f32 v125,v125,v130,v130
v_fma_f32 v126,v126,v130,v130
v_fma_f32 v127,v127,v130,v130
v_rcp_f32 v120,v120
v_rcp_f32 v121,v121
v_rcp_f32 v122,v122
v_rcp_f32 v123,v123
v_rcp_f32 v124,v124
v_rcp_f32 v125,v125
v_rcp_f32 v126,v126
v_rcp_f32 v127,v127
v_mul_f32 v16,v16,v120
v_mul_f32 v17,v17,v121
v_mul_f32 v18,v18,v122
v_mul_f32 v19,v19,v123
v_mul_f32 v20,v20,v124
v_mul_f32 v21,v21,v125
v_mul_f32 v22,v22,v126
v_mul_f32 v23,v23,v127
s_cmp_ge_u32 s96,27
s_cbranch_scc1 LZ23_%=
CP v72,v8,v9
CP v73,v10,v11
CP v74,v12,v13
CP v75,v14,v15
s_lshl_b32 s100,s96,10
s_add_u32 s100,s100,s78
buffer_store_dwordx4 v[72:75],v231,s[48:51],s100 offen
CP v76,v16,v17
CP v77,v18,v19
CP v78,v20,v21
CP v79,v22,v23
s_lshl_b32 s100,s96,10
s_add_u32 s100,s100,s78
s_add_u32 s100,s100,27648
buffer_store_dwordx4 v[76:79],v231,s[48:51],s100 offen
s_branch LN3_%=
LZ23_%=:
CP v72,v8,v9
CP v73,v10,v11
CP v74,v12,v13
CP v75,v14,v15
s_nop 0
v_permlane32_swap_b32 v72,v74
v_permlane32_swap_b32 v73,v75
s_nop 0
v_permlane16_swap_b32 v72,v74
v_permlane16_swap_b32 v73,v75
s_sub_u32 s100,s96,27
s_lshl_b32 s100,s100,6
s_add_u32 s100,s100,s77
buffer_store_dwordx4 v[72:75],v131,s[36:39],s100 offen sc1
CP v76,v16,v17
CP v77,v18,v19
CP v78,v20,v21
CP v79,v22,v23
s_nop 0
v_permlane32_swap_b32 v76,v78
v_permlane32_swap_b32 v77,v79
s_nop 0
v_permlane16_swap_b32 v76,v78
v_permlane16_swap_b32 v77,v79
s_sub_u32 s100,s96,27
s_lshl_b32 s100,s100,6
s_add_u32 s100,s100,s77
s_add_u32 s100,s100,28672
buffer_store_dwordx4 v[76:79],v131,s[36:39],s100 offen sc1
LN3_%=:
LZEND_%=:
s_barrier
s_add_u32 m0,s74,32768
s_nop 0
buffer_load_dwordx4 v230,s[40:43],s72 offen lds
buffer_load_dwordx4 v230,s[40:43],s72 offen offset:1024 lds
buffer_load_dwordx4 v230,s[40:43],s72 offen offset:2048 lds
buffer_load_dwordx4 v230,s[40:43],s72 offen offset:3072 lds
s_add_u32 s72,s72,16384
s_add_u32 m0,s74,81920
s_nop 0
buffer_load_dwordx4 v230,s[44:47],s73 offen lds
buffer_load_dwordx4 v230,s[44:47],s73 offen offset:1024 lds
buffer_load_dwordx4 v230,s[44:47],s73 offen offset:2048 lds
buffer_load_dwordx4 v230,s[44:47],s73 offen offset:3072 lds
s_add_u32 s73,s73,16384
s_mov_b32 s48,s16
s_and_b32 s49,s17,0xffff
s_mov_b32 s50,20447232
s_mov_b32 s51,0x20000
ds_read_b128 a[192:195],v228
ds_read_b128 a[196:199],v228 offset:1024
ds_read_b128 a[200:203],v228 offset:2048
ds_read_b128 a[204:207],v228 offset:3072
ds_read_b128 a[208:211],v228 offset:4096
ds_read_b128 a[212:215],v228 offset:5120
ds_read_b128 a[216:219],v228 offset:6144
ds_read_b128 a[220:223],v228 offset:7168
ds_read_b128 a[224:227],v228 offset:8192
ds_read_b128 a[228:231],v228 offset:9216
ds_read_b128 a[232:235],v228 offset:10240
ds_read_b128 a[236:239],v228 offset:11264
ds_read_b128 a[240:243],v228 offset:12288
ds_read_b128 a[244:247],v228 offset:13312
ds_read_b128 a[248:251],v228 offset:14336
ds_read_b128 a[252:255],v228 offset:15360
s_cmp_eq_u32 s70,1
s_cbranch_scc1 LQSW_%=
AR v72,a0
AR v73,a1
AR v74,a2
AR v75,a3
AR v76,a4
AR v77,a5
AR v78,a6
AR v79,a7
CP v88,v72,v73
CP v89,v74,v75
CP v90,v76,v77
CP v91,v78,v79
v_accvgpr_write_b32 a128,v88
v_accvgpr_write_b32 a129,v89
v_accvgpr_write_b32 a130,v90
v_accvgpr_write_b32 a131,v91
AR v80,a8
AR v81,a9
AR v82,a10
AR v83,a11
AR v84,a12
AR v85,a13
AR v86,a14
AR v87,a15
CP v92,v80,v81
CP v93,v82,v83
CP v94,v84,v85
CP v95,v86,v87
v_accvgpr_write_b32 a132,v92
v_accvgpr_write_b32 a133,v93
v_accvgpr_write_b32 a134,v94
v_accvgpr_write_b32 a135,v95
AR v72,a16
AR v73,a17
AR v74,a18
AR v75,a19
AR v76,a20
AR v77,a21
AR v78,a22
AR v79,a23
CP v88,v72,v73
CP v89,v74,v75
CP v90,v76,v77
CP v91,v78,v79
v_accvgpr_write_b32 a136,v88
v_accvgpr_write_b32 a137,v89
v_accvgpr_write_b32 a138,v90
v_accvgpr_write_b32 a139,v91
AR v80,a24
AR v81,a25
AR v82,a26
AR v83,a27
AR v84,a28
AR v85,a29
AR v86,a30
AR v87,a31
CP v92,v80,v81
CP v93,v82,v83
CP v94,v84,v85
CP v95,v86,v87
v_accvgpr_write_b32 a140,v92
v_accvgpr_write_b32 a141,v93
v_accvgpr_write_b32 a142,v94
v_accvgpr_write_b32 a143,v95
s_waitcnt lgkmcnt(0)
MF v[8:11],a[192:195],a[128:131],0
AR v72,a32
AR v73,a33
AR v74,a34
MF v[8:11],a[196:199],a[132:135],v[8:11]
AR v75,a35
AR v76,a36
AR v77,a37
MF v[8:11],a[200:203],a[136:139],v[8:11]
AR v78,a38
AR v79,a39
CP v88,v72,v73
MF v[8:11],a[204:207],a[140:143],v[8:11]
CP v89,v74,v75
CP v90,v76,v77
CP v91,v78,v79
MF v[12:15],a[208:211],a[128:131],0
v_accvgpr_write_b32 a144,v88
v_accvgpr_write_b32 a145,v89
v_accvgpr_write_b32 a146,v90
MF v[12:15],a[212:215],a[132:135],v[12:15]
v_accvgpr_write_b32 a147,v91
AR v80,a40
AR v81,a41
MF v[12:15],a[216:219],a[136:139],v[12:15]
AR v82,a42
AR v83,a43
AR v84,a44
MF v[12:15],a[220:223],a[140:143],v[12:15]
AR v85,a45
AR v86,a46
AR v87,a47
MF v[40:43],a[224:227],a[128:131],0
CP v92,v80,v81
CP v93,v82,v83
CP v94,v84,v85
MF v[40:43],a[228:231],a[132:135],v[40:43]
CP v95,v86,v87
v_accvgpr_write_b32 a148,v92
v_accvgpr_write_b32 a149,v93
MF v[40:43],a[232:235],a[136:139],v[40:43]
v_accvgpr_write_b32 a150,v94
v_accvgpr_write_b32 a151,v95
AR v72,a48
MF v[40:43],a[236:239],a[140:143],v[40:43]
AR v73,a49
AR v74,a50
AR v75,a51
MF v[44:47],a[240:243],a[128:131],0
AR v76,a52
AR v77,a53
AR v78,a54
MF v[44:47],a[244:247],a[132:135],v[44:47]
AR v79,a55
CP v88,v72,v73
CP v89,v74,v75
MF v[44:47],a[248:251],a[136:139],v[44:47]
CP v90,v76,v77
CP v91,v78,v79
v_accvgpr_write_b32 a152,v88
MF v[44:47],a[252:255],a[140:143],v[44:47]
v_accvgpr_write_b32 a153,v89
v_accvgpr_write_b32 a154,v90
v_accvgpr_write_b32 a155,v91
AR v80,a56
AR v81,a57
AR v82,a58
AR v83,a59
AR v84,a60
AR v85,a61
AR v86,a62
AR v87,a63
CP v92,v80,v81
CP v93,v82,v83
CP v94,v84,v85
CP v95,v86,v87
v_accvgpr_write_b32 a156,v92
v_accvgpr_write_b32 a157,v93
v_accvgpr_write_b32 a158,v94
v_accvgpr_write_b32 a159,v95
MF v[16:19],a[192:195],a[144:147],0
AR v72,a64
AR v73,a65
AR v74,a66
MF v[16:19],a[196:199],a[148:151],v[16:19]
AR v75,a67
AR v76,a68
AR v77,a69
MF v[16:19],a[200:203],a[152:155],v[16:19]
AR v78,a70
AR v79,a71
CP v88,v72,v73
MF v[16:19],a[204:207],a[156:159],v[16:19]
CP v89,v74,v75
CP v90,v76,v77
CP v91,v78,v79
MF v[20:23],a[208:211],a[144:147],0
v_accvgpr_write_b32 a160,v88
v_accvgpr_write_b32 a161,v89
v_accvgpr_write_b32 a162,v90
MF v[20:23],a[212:215],a[148:151],v[20:23]
v_accvgpr_write_b32 a163,v91
AR v80,a72
AR v81,a73
MF v[20:23],a[216:219],a[152:155],v[20:23]
AR v82,a74
AR v83,a75
AR v84,a76
MF v[20:23],a[220:223],a[156:159],v[20:23]
AR v85,a77
AR v86,a78
AR v87,a79
MF v[48:51],a[224:227],a[144:147],0
CP v92,v80,v81
CP v93,v82,v83
CP v94,v84,v85
MF v[48:51],a[228:231],a[148:151],v[48:51]
CP v95,v86,v87
v_accvgpr_write_b32 a164,v92
v_accvgpr_write_b32 a165,v93
MF v[48:51],a[232:235],a[152:155],v[48:51]
v_accvgpr_write_b32 a166,v94
v_accvgpr_write_b32 a167,v95
AR v72,a80
MF v[48:51],a[236:239],a[156:159],v[48:51]
AR v73,a81
AR v74,a82
AR v75,a83
MF v[52:55],a[240:243],a[144:147],0
AR v76,a84
AR v77,a85
AR v78,a86
MF v[52:55],a[244:247],a[148:151],v[52:55]
AR v79,a87
CP v88,v72,v73
CP v89,v74,v75
MF v[52:55],a[248:251],a[152:155],v[52:55]
CP v90,v76,v77
CP v91,v78,v79
v_accvgpr_write_b32 a168,v88
MF v[52:55],a[252:255],a[156:159],v[52:55]
v_accvgpr_write_b32 a169,v89
v_accvgpr_write_b32 a170,v90
v_accvgpr_write_b32 a171,v91
AR v80,a88
AR v81,a89
AR v82,a90
AR v83,a91
AR v84,a92
AR v85,a93
AR v86,a94
AR v87,a95
CP v92,v80,v81
CP v93,v82,v83
CP v94,v84,v85
CP v95,v86,v87
v_accvgpr_write_b32 a172,v92
v_accvgpr_write_b32 a173,v93
v_accvgpr_write_b32 a174,v94
v_accvgpr_write_b32 a175,v95
MF v[24:27],a[192:195],a[160:163],0
AR v72,a96
AR v73,a97
AR v74,a98
MF v[24:27],a[196:199],a[164:167],v[24:27]
AR v75,a99
AR v76,a100
AR v77,a101
MF v[24:27],a[200:203],a[168:171],v[24:27]
AR v78,a102
AR v79,a103
CP v88,v72,v73
MF v[24:27],a[204:207],a[172:175],v[24:27]
CP v89,v74,v75
CP v90,v76,v77
CP v91,v78,v79
MF v[28:31],a[208:211],a[160:163],0
v_accvgpr_write_b32 a176,v88
v_accvgpr_write_b32 a177,v89
v_accvgpr_write_b32 a178,v90
MF v[28:31],a[212:215],a[164:167],v[28:31]
v_accvgpr_write_b32 a179,v91
AR v80,a104
AR v81,a105
MF v[28:31],a[216:219],a[168:171],v[28:31]
AR v82,a106
AR v83,a107
AR v84,a108
MF v[28:31],a[220:223],a[172:175],v[28:31]
AR v85,a109
AR v86,a110
AR v87,a111
MF v[56:59],a[224:227],a[160:163],0
CP v92,v80,v81
CP v93,v82,v83
CP v94,v84,v85
MF v[56:59],a[228:231],a[164:167],v[56:59]
CP v95,v86,v87
v_accvgpr_write_b32 a180,v92
v_accvgpr_write_b32 a181,v93
MF v[56:59],a[232:235],a[168:171],v[56:59]
v_accvgpr_write_b32 a182,v94
v_accvgpr_write_b32 a183,v95
AR v72,a112
MF v[56:59],a[236:239],a[172:175],v[56:59]
AR v73,a113
AR v74,a114
AR v75,a115
MF v[60:63],a[240:243],a[160:163],0
AR v76,a116
AR v77,a117
AR v78,a118
MF v[60:63],a[244:247],a[164:167],v[60:63]
AR v79,a119
CP v88,v72,v73
CP v89,v74,v75
MF v[60:63],a[248:251],a[168:171],v[60:63]
CP v90,v76,v77
CP v91,v78,v79
v_accvgpr_write_b32 a184,v88
MF v[60:63],a[252:255],a[172:175],v[60:63]
v_accvgpr_write_b32 a185,v89
v_accvgpr_write_b32 a186,v90
v_accvgpr_write_b32 a187,v91
AR v80,a120
AR v81,a121
AR v82,a122
AR v83,a123
AR v84,a124
AR v85,a125
AR v86,a126
AR v87,a127
CP v92,v80,v81
CP v93,v82,v83
CP v94,v84,v85
CP v95,v86,v87
v_accvgpr_write_b32 a188,v92
v_accvgpr_write_b32 a189,v93
v_accvgpr_write_b32 a190,v94
v_accvgpr_write_b32 a191,v95
MF v[32:35],a[192:195],a[176:179],0
MF v[32:35],a[196:199],a[180:183],v[32:35]
MF v[32:35],a[200:203],a[184:187],v[32:35]
MF v[32:35],a[204:207],a[188:191],v[32:35]
MF v[36:39],a[208:211],a[176:179],0
MF v[36:39],a[212:215],a[180:183],v[36:39]
MF v[36:39],a[216:219],a[184:187],v[36:39]
MF v[36:39],a[220:223],a[188:191],v[36:39]
MF v[64:67],a[224:227],a[176:179],0
MF v[64:67],a[228:231],a[180:183],v[64:67]
MF v[64:67],a[232:235],a[184:187],v[64:67]
MF v[64:67],a[236:239],a[188:191],v[64:67]
MF v[68:71],a[240:243],a[176:179],0
MF v[68:71],a[244:247],a[180:183],v[68:71]
MF v[68:71],a[248:251],a[184:187],v[68:71]
MF v[68:71],a[252:255],a[188:191],v[68:71]
s_branch LQE_%=
LQSW_%=:
AR v72,a64
AR v73,a65
AR v74,a66
AR v75,a67
AR v76,a68
AR v77,a69
AR v78,a70
AR v79,a71
CP v88,v72,v73
CP v89,v74,v75
CP v90,v76,v77
CP v91,v78,v79
v_accvgpr_write_b32 a128,v88
v_accvgpr_write_b32 a129,v89
v_accvgpr_write_b32 a130,v90
v_accvgpr_write_b32 a131,v91
AR v80,a72
AR v81,a73
AR v82,a74
AR v83,a75
AR v84,a76
AR v85,a77
AR v86,a78
AR v87,a79
CP v92,v80,v81
CP v93,v82,v83
CP v94,v84,v85
CP v95,v86,v87
v_accvgpr_write_b32 a132,v92
v_accvgpr_write_b32 a133,v93
v_accvgpr_write_b32 a134,v94
v_accvgpr_write_b32 a135,v95
AR v72,a80
AR v73,a81
AR v74,a82
AR v75,a83
AR v76,a84
AR v77,a85
AR v78,a86
AR v79,a87
CP v88,v72,v73
CP v89,v74,v75
CP v90,v76,v77
CP v91,v78,v79
v_accvgpr_write_b32 a136,v88
v_accvgpr_write_b32 a137,v89
v_accvgpr_write_b32 a138,v90
v_accvgpr_write_b32 a139,v91
AR v80,a88
AR v81,a89
AR v82,a90
AR v83,a91
AR v84,a92
AR v85,a93
AR v86,a94
AR v87,a95
CP v92,v80,v81
CP v93,v82,v83
CP v94,v84,v85
CP v95,v86,v87
v_accvgpr_write_b32 a140,v92
v_accvgpr_write_b32 a141,v93
v_accvgpr_write_b32 a142,v94
v_accvgpr_write_b32 a143,v95
s_waitcnt lgkmcnt(0)
MF v[8:11],a[192:195],a[128:131],0
AR v72,a96
AR v73,a97
AR v74,a98
MF v[8:11],a[196:199],a[132:135],v[8:11]
AR v75,a99
AR v76,a100
AR v77,a101
MF v[8:11],a[200:203],a[136:139],v[8:11]
AR v78,a102
AR v79,a103
CP v88,v72,v73
MF v[8:11],a[204:207],a[140:143],v[8:11]
CP v89,v74,v75
CP v90,v76,v77
CP v91,v78,v79
MF v[12:15],a[208:211],a[128:131],0
v_accvgpr_write_b32 a144,v88
v_accvgpr_write_b32 a145,v89
v_accvgpr_write_b32 a146,v90
MF v[12:15],a[212:215],a[132:135],v[12:15]
v_accvgpr_write_b32 a147,v91
AR v80,a104
AR v81,a105
MF v[12:15],a[216:219],a[136:139],v[12:15]
AR v82,a106
AR v83,a107
AR v84,a108
MF v[12:15],a[220:223],a[140:143],v[12:15]
AR v85,a109
AR v86,a110
AR v87,a111
MF v[40:43],a[224:227],a[128:131],0
CP v92,v80,v81
CP v93,v82,v83
CP v94,v84,v85
MF v[40:43],a[228:231],a[132:135],v[40:43]
CP v95,v86,v87
v_accvgpr_write_b32 a148,v92
v_accvgpr_write_b32 a149,v93
MF v[40:43],a[232:235],a[136:139],v[40:43]
v_accvgpr_write_b32 a150,v94
v_accvgpr_write_b32 a151,v95
AR v72,a112
MF v[40:43],a[236:239],a[140:143],v[40:43]
AR v73,a113
AR v74,a114
AR v75,a115
MF v[44:47],a[240:243],a[128:131],0
AR v76,a116
AR v77,a117
AR v78,a118
MF v[44:47],a[244:247],a[132:135],v[44:47]
AR v79,a119
CP v88,v72,v73
CP v89,v74,v75
MF v[44:47],a[248:251],a[136:139],v[44:47]
CP v90,v76,v77
CP v91,v78,v79
v_accvgpr_write_b32 a152,v88
MF v[44:47],a[252:255],a[140:143],v[44:47]
v_accvgpr_write_b32 a153,v89
v_accvgpr_write_b32 a154,v90
v_accvgpr_write_b32 a155,v91
AR v80,a120
AR v81,a121
AR v82,a122
AR v83,a123
AR v84,a124
AR v85,a125
AR v86,a126
AR v87,a127
CP v92,v80,v81
CP v93,v82,v83
CP v94,v84,v85
CP v95,v86,v87
v_accvgpr_write_b32 a156,v92
v_accvgpr_write_b32 a157,v93
v_accvgpr_write_b32 a158,v94
v_accvgpr_write_b32 a159,v95
MF v[16:19],a[192:195],a[144:147],0
AR v72,a0
AR v73,a1
AR v74,a2
MF v[16:19],a[196:199],a[148:151],v[16:19]
AR v75,a3
AR v76,a4
AR v77,a5
MF v[16:19],a[200:203],a[152:155],v[16:19]
AR v78,a6
AR v79,a7
CP v88,v72,v73
MF v[16:19],a[204:207],a[156:159],v[16:19]
CP v89,v74,v75
CP v90,v76,v77
CP v91,v78,v79
MF v[20:23],a[208:211],a[144:147],0
v_accvgpr_write_b32 a160,v88
v_accvgpr_write_b32 a161,v89
v_accvgpr_write_b32 a162,v90
MF v[20:23],a[212:215],a[148:151],v[20:23]
v_accvgpr_write_b32 a163,v91
AR v80,a8
AR v81,a9
MF v[20:23],a[216:219],a[152:155],v[20:23]
AR v82,a10
AR v83,a11
AR v84,a12
MF v[20:23],a[220:223],a[156:159],v[20:23]
AR v85,a13
AR v86,a14
AR v87,a15
MF v[48:51],a[224:227],a[144:147],0
CP v92,v80,v81
CP v93,v82,v83
CP v94,v84,v85
MF v[48:51],a[228:231],a[148:151],v[48:51]
CP v95,v86,v87
v_accvgpr_write_b32 a164,v92
v_accvgpr_write_b32 a165,v93
MF v[48:51],a[232:235],a[152:155],v[48:51]
v_accvgpr_write_b32 a166,v94
v_accvgpr_write_b32 a167,v95
AR v72,a16
MF v[48:51],a[236:239],a[156:159],v[48:51]
AR v73,a17
AR v74,a18
AR v75,a19
MF v[52:55],a[240:243],a[144:147],0
AR v76,a20
AR v77,a21
AR v78,a22
MF v[52:55],a[244:247],a[148:151],v[52:55]
AR v79,a23
CP v88,v72,v73
CP v89,v74,v75
MF v[52:55],a[248:251],a[152:155],v[52:55]
CP v90,v76,v77
CP v91,v78,v79
v_accvgpr_write_b32 a168,v88
MF v[52:55],a[252:255],a[156:159],v[52:55]
v_accvgpr_write_b32 a169,v89
v_accvgpr_write_b32 a170,v90
v_accvgpr_write_b32 a171,v91
AR v80,a24
AR v81,a25
AR v82,a26
AR v83,a27
AR v84,a28
AR v85,a29
AR v86,a30
AR v87,a31
CP v92,v80,v81
CP v93,v82,v83
CP v94,v84,v85
CP v95,v86,v87
v_accvgpr_write_b32 a172,v92
v_accvgpr_write_b32 a173,v93
v_accvgpr_write_b32 a174,v94
v_accvgpr_write_b32 a175,v95
MF v[24:27],a[192:195],a[160:163],0
AR v72,a32
AR v73,a33
AR v74,a34
MF v[24:27],a[196:199],a[164:167],v[24:27]
AR v75,a35
AR v76,a36
AR v77,a37
MF v[24:27],a[200:203],a[168:171],v[24:27]
AR v78,a38
AR v79,a39
CP v88,v72,v73
MF v[24:27],a[204:207],a[172:175],v[24:27]
CP v89,v74,v75
CP v90,v76,v77
CP v91,v78,v79
MF v[28:31],a[208:211],a[160:163],0
v_accvgpr_write_b32 a176,v88
v_accvgpr_write_b32 a177,v89
v_accvgpr_write_b32 a178,v90
MF v[28:31],a[212:215],a[164:167],v[28:31]
v_accvgpr_write_b32 a179,v91
AR v80,a40
AR v81,a41
MF v[28:31],a[216:219],a[168:171],v[28:31]
AR v82,a42
AR v83,a43
AR v84,a44
MF v[28:31],a[220:223],a[172:175],v[28:31]
AR v85,a45
AR v86,a46
AR v87,a47
MF v[56:59],a[224:227],a[160:163],0
CP v92,v80,v81
CP v93,v82,v83
CP v94,v84,v85
MF v[56:59],a[228:231],a[164:167],v[56:59]
CP v95,v86,v87
v_accvgpr_write_b32 a180,v92
v_accvgpr_write_b32 a181,v93
MF v[56:59],a[232:235],a[168:171],v[56:59]
v_accvgpr_write_b32 a182,v94
v_accvgpr_write_b32 a183,v95
AR v72,a48
MF v[56:59],a[236:239],a[172:175],v[56:59]
AR v73,a49
AR v74,a50
AR v75,a51
MF v[60:63],a[240:243],a[160:163],0
AR v76,a52
AR v77,a53
AR v78,a54
MF v[60:63],a[244:247],a[164:167],v[60:63]
AR v79,a55
CP v88,v72,v73
CP v89,v74,v75
MF v[60:63],a[248:251],a[168:171],v[60:63]
CP v90,v76,v77
CP v91,v78,v79
v_accvgpr_write_b32 a184,v88
MF v[60:63],a[252:255],a[172:175],v[60:63]
v_accvgpr_write_b32 a185,v89
v_accvgpr_write_b32 a186,v90
v_accvgpr_write_b32 a187,v91
AR v80,a56
AR v81,a57
AR v82,a58
AR v83,a59
AR v84,a60
AR v85,a61
AR v86,a62
AR v87,a63
CP v92,v80,v81
CP v93,v82,v83
CP v94,v84,v85
CP v95,v86,v87
v_accvgpr_write_b32 a188,v92
v_accvgpr_write_b32 a189,v93
v_accvgpr_write_b32 a190,v94
v_accvgpr_write_b32 a191,v95
MF v[32:35],a[192:195],a[176:179],0
MF v[32:35],a[196:199],a[180:183],v[32:35]
MF v[32:35],a[200:203],a[184:187],v[32:35]
MF v[32:35],a[204:207],a[188:191],v[32:35]
MF v[36:39],a[208:211],a[176:179],0
MF v[36:39],a[212:215],a[180:183],v[36:39]
MF v[36:39],a[216:219],a[184:187],v[36:39]
MF v[36:39],a[220:223],a[188:191],v[36:39]
MF v[64:67],a[224:227],a[176:179],0
MF v[64:67],a[228:231],a[180:183],v[64:67]
MF v[64:67],a[232:235],a[184:187],v[64:67]
MF v[64:67],a[236:239],a[188:191],v[64:67]
MF v[68:71],a[240:243],a[176:179],0
)A"
R"A(MF v[68:71],a[244:247],a[180:183],v[68:71]
MF v[68:71],a[248:251],a[184:187],v[68:71]
MF v[68:71],a[252:255],a[188:191],v[68:71]
LQE_%=:
s_nop 7
s_cmp_lg_u32 s58,0
s_cbranch_scc1 LNOM0_%=
s_lshl_b32 s77,s58,1
s_sub_u32 s77,s77,s75
s_lshl_b32 s77,s77,6
v_add_u32 v234,s77,v232
v_add_u32 v235,s77,v233
v_cmp_gt_i32_e64 s[82:83],0,v234
v_cmp_gt_i32_e64 s[84:85],1,v234
s_nop 0
v_cndmask_b32_e64 v8,v8,v236,s[82:83]
v_cmp_gt_i32_e64 s[82:83],2,v234
v_cndmask_b32_e64 v9,v9,v236,s[84:85]
v_cmp_gt_i32_e64 s[84:85],3,v234
v_cndmask_b32_e64 v10,v10,v236,s[82:83]
v_cmp_gt_i32_e64 s[82:83],16,v234
v_cndmask_b32_e64 v11,v11,v236,s[84:85]
v_cmp_gt_i32_e64 s[84:85],17,v234
v_cndmask_b32_e64 v12,v12,v236,s[82:83]
v_cmp_gt_i32_e64 s[82:83],18,v234
v_cndmask_b32_e64 v13,v13,v236,s[84:85]
v_cmp_gt_i32_e64 s[84:85],19,v234
v_cndmask_b32_e64 v14,v14,v236,s[82:83]
s_nop 0
v_cndmask_b32_e64 v15,v15,v236,s[84:85]
v_cmp_gt_i32_e64 s[82:83],32,v234
v_cmp_gt_i32_e64 s[84:85],33,v234
s_nop 0
v_cndmask_b32_e64 v40,v40,v236,s[82:83]
v_cmp_gt_i32_e64 s[82:83],34,v234
v_cndmask_b32_e64 v41,v41,v236,s[84:85]
v_cmp_gt_i32_e64 s[84:85],35,v234
v_cndmask_b32_e64 v42,v42,v236,s[82:83]
v_cmp_gt_i32_e64 s[82:83],48,v234
v_cndmask_b32_e64 v43,v43,v236,s[84:85]
v_cmp_gt_i32_e64 s[84:85],49,v234
v_cndmask_b32_e64 v44,v44,v236,s[82:83]
v_cmp_gt_i32_e64 s[82:83],50,v234
v_cndmask_b32_e64 v45,v45,v236,s[84:85]
v_cmp_gt_i32_e64 s[84:85],51,v234
v_cndmask_b32_e64 v46,v46,v236,s[82:83]
s_nop 0
v_cndmask_b32_e64 v47,v47,v236,s[84:85]
v_cmp_gt_i32_e64 s[82:83],0,v235
v_cmp_gt_i32_e64 s[84:85],1,v235
s_nop 0
v_cndmask_b32_e64 v16,v16,v236,s[82:83]
v_cmp_gt_i32_e64 s[82:83],2,v235
v_cndmask_b32_e64 v17,v17,v236,s[84:85]
v_cmp_gt_i32_e64 s[84:85],3,v235
v_cndmask_b32_e64 v18,v18,v236,s[82:83]
v_cmp_gt_i32_e64 s[82:83],16,v235
v_cndmask_b32_e64 v19,v19,v236,s[84:85]
v_cmp_gt_i32_e64 s[84:85],17,v235
v_cndmask_b32_e64 v20,v20,v236,s[82:83]
v_cmp_gt_i32_e64 s[82:83],18,v235
v_cndmask_b32_e64 v21,v21,v236,s[84:85]
v_cmp_gt_i32_e64 s[84:85],19,v235
v_cndmask_b32_e64 v22,v22,v236,s[82:83]
s_nop 0
v_cndmask_b32_e64 v23,v23,v236,s[84:85]
v_cmp_gt_i32_e64 s[82:83],32,v235
v_cmp_gt_i32_e64 s[84:85],33,v235
s_nop 0
v_cndmask_b32_e64 v48,v48,v236,s[82:83]
v_cmp_gt_i32_e64 s[82:83],34,v235
v_cndmask_b32_e64 v49,v49,v236,s[84:85]
v_cmp_gt_i32_e64 s[84:85],35,v235
v_cndmask_b32_e64 v50,v50,v236,s[82:83]
v_cmp_gt_i32_e64 s[82:83],48,v235
v_cndmask_b32_e64 v51,v51,v236,s[84:85]
v_cmp_gt_i32_e64 s[84:85],49,v235
v_cndmask_b32_e64 v52,v52,v236,s[82:83]
v_cmp_gt_i32_e64 s[82:83],50,v235
v_cndmask_b32_e64 v53,v53,v236,s[84:85]
v_cmp_gt_i32_e64 s[84:85],51,v235
v_cndmask_b32_e64 v54,v54,v236,s[82:83]
s_nop 0
v_cndmask_b32_e64 v55,v55,v236,s[84:85]
LNOM0_%=:
v_max3_f32 v216,v8,v9,v10
v_max3_f32 v216,v216,v11,v12
v_max3_f32 v216,v216,v13,v14
v_max3_f32 v216,v216,v15,v40
v_max3_f32 v216,v216,v41,v42
v_max3_f32 v216,v216,v43,v44
v_max3_f32 v216,v216,v45,v46
v_max_f32 v216,v216,v47
v_mov_b32 v217,v216
s_nop 1
v_permlane32_swap_b32 v216,v217
v_max_f32 v216,v216,v217
v_mov_b32 v217,v216
s_nop 1
v_permlane16_swap_b32 v216,v217
v_max_f32 v216,v216,v217
v_max3_f32 v218,v16,v17,v18
v_max3_f32 v218,v218,v19,v20
v_max3_f32 v218,v218,v21,v22
v_max3_f32 v218,v218,v23,v48
v_max3_f32 v218,v218,v49,v50
v_max3_f32 v218,v218,v51,v52
v_max3_f32 v218,v218,v53,v54
v_max_f32 v218,v218,v55
v_mov_b32 v219,v218
s_nop 1
v_permlane32_swap_b32 v218,v219
v_max_f32 v218,v218,v219
v_mov_b32 v219,v218
s_nop 1
v_permlane16_swap_b32 v218,v219
v_max_f32 v218,v218,v219
v_max3_f32 v220,v24,v25,v26
v_max3_f32 v220,v220,v27,v28
v_max3_f32 v220,v220,v29,v30
v_max3_f32 v220,v220,v31,v56
v_max3_f32 v220,v220,v57,v58
v_max3_f32 v220,v220,v59,v60
v_max3_f32 v220,v220,v61,v62
v_max_f32 v220,v220,v63
v_mov_b32 v221,v220
s_nop 1
v_permlane32_swap_b32 v220,v221
v_max_f32 v220,v220,v221
v_mov_b32 v221,v220
s_nop 1
v_permlane16_swap_b32 v220,v221
v_max_f32 v220,v220,v221
v_max3_f32 v222,v32,v33,v34
v_max3_f32 v222,v222,v35,v36
v_max3_f32 v222,v222,v37,v38
v_max3_f32 v222,v222,v39,v64
v_max3_f32 v222,v222,v65,v66
v_max3_f32 v222,v222,v67,v68
v_max3_f32 v222,v222,v69,v70
v_max_f32 v222,v222,v71
v_mov_b32 v223,v222
s_nop 1
v_permlane32_swap_b32 v222,v223
v_max_f32 v222,v222,v223
v_mov_b32 v223,v222
s_nop 1
v_permlane16_swap_b32 v222,v223
v_max_f32 v222,v222,v223
v_sub_f32 v200,0,v216
v_sub_f32 v201,0,v216
v_sub_f32 v202,0,v216
v_sub_f32 v203,0,v216
v_sub_f32 v204,0,v218
v_sub_f32 v205,0,v218
v_sub_f32 v206,0,v218
v_sub_f32 v207,0,v218
v_sub_f32 v208,0,v220
v_sub_f32 v209,0,v220
v_sub_f32 v210,0,v220
v_sub_f32 v211,0,v220
v_sub_f32 v212,0,v222
v_sub_f32 v213,0,v222
v_sub_f32 v214,0,v222
v_sub_f32 v215,0,v222
v_add_f32 v8,v8,v200
v_add_f32 v9,v9,v200
v_add_f32 v10,v10,v200
v_add_f32 v11,v11,v200
v_add_f32 v12,v12,v200
v_add_f32 v13,v13,v200
v_add_f32 v14,v14,v200
v_add_f32 v15,v15,v200
v_add_f32 v40,v40,v200
v_add_f32 v41,v41,v200
v_add_f32 v42,v42,v200
v_add_f32 v43,v43,v200
v_add_f32 v44,v44,v200
v_add_f32 v45,v45,v200
v_add_f32 v46,v46,v200
v_add_f32 v47,v47,v200
v_add_f32 v16,v16,v204
v_add_f32 v17,v17,v204
v_add_f32 v18,v18,v204
v_add_f32 v19,v19,v204
v_add_f32 v20,v20,v204
v_add_f32 v21,v21,v204
v_add_f32 v22,v22,v204
v_add_f32 v23,v23,v204
v_add_f32 v48,v48,v204
v_add_f32 v49,v49,v204
v_add_f32 v50,v50,v204
v_add_f32 v51,v51,v204
v_add_f32 v52,v52,v204
v_add_f32 v53,v53,v204
v_add_f32 v54,v54,v204
v_add_f32 v55,v55,v204
v_add_f32 v24,v24,v208
v_add_f32 v25,v25,v208
v_add_f32 v26,v26,v208
v_add_f32 v27,v27,v208
v_add_f32 v28,v28,v208
v_add_f32 v29,v29,v208
v_add_f32 v30,v30,v208
v_add_f32 v31,v31,v208
v_add_f32 v56,v56,v208
v_add_f32 v57,v57,v208
v_add_f32 v58,v58,v208
v_add_f32 v59,v59,v208
v_add_f32 v60,v60,v208
v_add_f32 v61,v61,v208
v_add_f32 v62,v62,v208
v_add_f32 v63,v63,v208
v_add_f32 v32,v32,v212
v_add_f32 v33,v33,v212
v_add_f32 v34,v34,v212
v_add_f32 v35,v35,v212
v_add_f32 v36,v36,v212
v_add_f32 v37,v37,v212
v_add_f32 v38,v38,v212
v_add_f32 v39,v39,v212
v_add_f32 v64,v64,v212
v_add_f32 v65,v65,v212
v_add_f32 v66,v66,v212
v_add_f32 v67,v67,v212
v_add_f32 v68,v68,v212
v_add_f32 v69,v69,v212
v_add_f32 v70,v70,v212
v_add_f32 v71,v71,v212
v_add_u32 v228,s59,v231
s_add_u32 m0,s74,s61
s_nop 0
buffer_load_dwordx4 v230,s[40:43],s72 offen lds
buffer_load_dwordx4 v230,s[40:43],s72 offen offset:1024 lds
buffer_load_dwordx4 v230,s[40:43],s72 offen offset:2048 lds
buffer_load_dwordx4 v230,s[40:43],s72 offen offset:3072 lds
s_add_u32 s72,s72,16384
s_add_u32 m0,s74,s62
s_nop 0
buffer_load_dwordx4 v230,s[44:47],s73 offen lds
buffer_load_dwordx4 v230,s[44:47],s73 offen offset:1024 lds
buffer_load_dwordx4 v230,s[44:47],s73 offen offset:2048 lds
buffer_load_dwordx4 v230,s[44:47],s73 offen offset:3072 lds
s_add_u32 s73,s73,16384
ds_read_b128 a[192:195],v228
ds_read_b128 a[196:199],v228 offset:1024
ds_read_b128 a[200:203],v228 offset:2048
ds_read_b128 a[204:207],v228 offset:3072
ds_read_b128 a[208:211],v228 offset:4096
ds_read_b128 a[212:215],v228 offset:5120
ds_read_b128 a[216:219],v228 offset:6144
ds_read_b128 a[220:223],v228 offset:7168
ds_read_b128 a[224:227],v228 offset:8192
ds_read_b128 a[228:231],v228 offset:9216
ds_read_b128 a[232:235],v228 offset:10240
ds_read_b128 a[236:239],v228 offset:11264
ds_read_b128 a[240:243],v228 offset:12288
ds_read_b128 a[244:247],v228 offset:13312
ds_read_b128 a[248:251],v228 offset:14336
ds_read_b128 a[252:255],v228 offset:15360
v_exp_f32 v8,v8
v_exp_f32 v9,v9
v_exp_f32 v10,v10
v_exp_f32 v11,v11
v_exp_f32 v12,v12
v_exp_f32 v13,v13
v_exp_f32 v14,v14
v_exp_f32 v15,v15
v_exp_f32 v16,v16
v_add_f32 v216,v8,v10
v_add_f32 v217,v9,v11
v_exp_f32 v17,v17
CP v8,v8,v9
v_exp_f32 v18,v18
v_add_f32 v216,v12,v216
v_exp_f32 v19,v19
CP v9,v10,v11
v_exp_f32 v20,v20
v_add_f32 v217,v13,v217
v_add_f32 v216,v14,v216
v_exp_f32 v21,v21
CP v10,v12,v13
v_exp_f32 v22,v22
v_add_f32 v217,v15,v217
v_exp_f32 v23,v23
CP v11,v14,v15
v_exp_f32 v24,v24
v_add_f32 v218,v16,v18
v_add_f32 v219,v17,v19
v_exp_f32 v25,v25
CP v16,v16,v17
v_exp_f32 v26,v26
v_add_f32 v218,v20,v218
v_exp_f32 v27,v27
CP v17,v18,v19
v_exp_f32 v28,v28
v_add_f32 v219,v21,v219
v_add_f32 v218,v22,v218
v_exp_f32 v29,v29
CP v18,v20,v21
v_exp_f32 v30,v30
v_add_f32 v219,v23,v219
v_exp_f32 v31,v31
CP v19,v22,v23
v_exp_f32 v32,v32
v_add_f32 v220,v24,v26
v_add_f32 v221,v25,v27
v_exp_f32 v33,v33
CP v24,v24,v25
v_exp_f32 v34,v34
v_add_f32 v220,v28,v220
v_exp_f32 v35,v35
CP v25,v26,v27
v_exp_f32 v36,v36
v_add_f32 v221,v29,v221
v_add_f32 v220,v30,v220
v_exp_f32 v37,v37
CP v26,v28,v29
v_exp_f32 v38,v38
v_add_f32 v221,v31,v221
v_exp_f32 v39,v39
CP v27,v30,v31
v_add_f32 v222,v32,v34
v_add_f32 v223,v33,v35
CP v32,v32,v33
v_add_f32 v222,v36,v222
CP v33,v34,v35
v_add_f32 v223,v37,v223
v_add_f32 v222,v38,v222
CP v34,v36,v37
v_add_f32 v223,v39,v223
CP v35,v38,v39
s_add_u32 s75,s75,1
s_add_u32 s59,s59,16384
s_and_b32 s59,s59,0xffff
s_add_u32 s61,s61,16384
s_and_b32 s61,s61,0xffff
s_add_u32 s60,s60,16384
s_and_b32 s60,s60,0xffff
s_or_b32 s60,s60,65536
s_add_u32 s62,s62,16384
s_and_b32 s62,s62,0xffff
s_or_b32 s62,s62,65536
v_add_u32 v229,s60,v231
s_waitcnt lgkmcnt(0)
MF v[72:75],a[192:195],a[128:131],v[200:203]
ds_read_b128 v[136:139],v229
v_exp_f32 v40,v40
MF v[80:83],a[192:195],a[144:147],v[204:207]
v_exp_f32 v41,v41
MF v[88:91],a[192:195],a[160:163],v[208:211]
v_exp_f32 v42,v42
MF v[96:99],a[192:195],a[176:179],v[212:215]
ds_read_b128 v[144:147],v229 offset:1024
v_exp_f32 v43,v43
MF v[72:75],a[196:199],a[132:135],v[72:75]
v_exp_f32 v44,v44
MF v[80:83],a[196:199],a[148:151],v[80:83]
v_exp_f32 v45,v45
MF v[88:91],a[196:199],a[164:167],v[88:91]
ds_read_b128 v[152:155],v229 offset:2048
v_exp_f32 v46,v46
MF v[96:99],a[196:199],a[180:183],v[96:99]
v_exp_f32 v47,v47
MF v[72:75],a[200:203],a[136:139],v[72:75]
v_exp_f32 v48,v48
v_add_f32 v216,v40,v216
MF v[80:83],a[200:203],a[152:155],v[80:83]
ds_read_b128 v[160:163],v229 offset:3072
v_add_f32 v217,v41,v217
MF v[88:91],a[200:203],a[168:171],v[88:91]
v_exp_f32 v49,v49
CP v40,v40,v41
MF v[96:99],a[200:203],a[184:187],v[96:99]
v_exp_f32 v50,v50
v_add_f32 v216,v42,v216
MF v[72:75],a[204:207],a[140:143],v[72:75]
ds_read_b128 v[168:171],v229 offset:4096
v_add_f32 v217,v43,v217
MF v[80:83],a[204:207],a[156:159],v[80:83]
v_exp_f32 v51,v51
CP v41,v42,v43
MF v[88:91],a[204:207],a[172:175],v[88:91]
v_exp_f32 v52,v52
MF v[96:99],a[204:207],a[188:191],v[96:99]
ds_read_b128 v[176:179],v229 offset:5120
v_add_f32 v216,v44,v216
MF v[76:79],a[208:211],a[128:131],v[200:203]
v_add_f32 v217,v45,v217
MF v[84:87],a[208:211],a[144:147],v[204:207]
v_exp_f32 v53,v53
MF v[92:95],a[208:211],a[160:163],v[208:211]
ds_read_b128 v[184:187],v229 offset:6144
CP v42,v44,v45
MF v[100:103],a[208:211],a[176:179],v[212:215]
v_exp_f32 v54,v54
MF v[76:79],a[212:215],a[132:135],v[76:79]
v_add_f32 v216,v46,v216
v_add_f32 v217,v47,v217
MF v[84:87],a[212:215],a[148:151],v[84:87]
ds_read_b128 v[192:195],v229 offset:7168
v_exp_f32 v55,v55
MF v[92:95],a[212:215],a[164:167],v[92:95]
CP v43,v46,v47
MF v[100:103],a[212:215],a[180:183],v[100:103]
v_exp_f32 v56,v56
MF v[76:79],a[216:219],a[136:139],v[76:79]
ds_read_b128 v[140:143],v229 offset:8192
v_add_f32 v218,v48,v218
MF v[84:87],a[216:219],a[152:155],v[84:87]
v_add_f32 v219,v49,v219
MF v[92:95],a[216:219],a[168:171],v[92:95]
v_exp_f32 v57,v57
MF v[100:103],a[216:219],a[184:187],v[100:103]
ds_read_b128 v[148:151],v229 offset:9216
CP v48,v48,v49
MF v[76:79],a[220:223],a[140:143],v[76:79]
v_exp_f32 v58,v58
MF v[84:87],a[220:223],a[156:159],v[84:87]
v_add_f32 v218,v50,v218
v_add_f32 v219,v51,v219
MF v[92:95],a[220:223],a[172:175],v[92:95]
ds_read_b128 v[156:159],v229 offset:10240
v_exp_f32 v59,v59
MF v[100:103],a[220:223],a[188:191],v[100:103]
CP v49,v50,v51
MF v[104:107],a[224:227],a[128:131],v[200:203]
v_exp_f32 v60,v60
MF v[112:115],a[224:227],a[144:147],v[204:207]
ds_read_b128 v[164:167],v229 offset:11264
v_add_f32 v218,v52,v218
MF v[120:123],a[224:227],a[160:163],v[208:211]
v_add_f32 v219,v53,v219
MF v[128:131],a[224:227],a[176:179],v[212:215]
v_exp_f32 v61,v61
MF v[104:107],a[228:231],a[132:135],v[104:107]
ds_read_b128 v[172:175],v229 offset:12288
CP v50,v52,v53
MF v[112:115],a[228:231],a[148:151],v[112:115]
v_exp_f32 v62,v62
MF v[120:123],a[228:231],a[164:167],v[120:123]
v_add_f32 v218,v54,v218
v_add_f32 v219,v55,v219
MF v[128:131],a[228:231],a[180:183],v[128:131]
ds_read_b128 v[180:183],v229 offset:13312
v_exp_f32 v63,v63
MF v[104:107],a[232:235],a[136:139],v[104:107]
CP v51,v54,v55
MF v[112:115],a[232:235],a[152:155],v[112:115]
v_exp_f32 v64,v64
MF v[120:123],a[232:235],a[168:171],v[120:123]
ds_read_b128 v[188:191],v229 offset:14336
v_add_f32 v220,v56,v220
MF v[128:131],a[232:235],a[184:187],v[128:131]
v_add_f32 v221,v57,v221
MF v[104:107],a[236:239],a[140:143],v[104:107]
v_exp_f32 v65,v65
MF v[112:115],a[236:239],a[156:159],v[112:115]
ds_read_b128 v[196:199],v229 offset:15360
CP v56,v56,v57
MF v[120:123],a[236:239],a[172:175],v[120:123]
v_exp_f32 v66,v66
MF v[128:131],a[236:239],a[188:191],v[128:131]
v_add_f32 v220,v58,v220
v_add_f32 v221,v59,v221
MF v[108:111],a[240:243],a[128:131],v[200:203]
v_exp_f32 v67,v67
CP v57,v58,v59
MF v[116:119],a[240:243],a[144:147],v[204:207]
v_exp_f32 v68,v68
MF v[124:127],a[240:243],a[160:163],v[208:211]
v_add_f32 v220,v60,v220
v_add_f32 v221,v61,v221
MF v[132:135],a[240:243],a[176:179],v[212:215]
v_exp_f32 v69,v69
CP v58,v60,v61
MF v[108:111],a[244:247],a[132:135],v[108:111]
v_exp_f32 v70,v70
MF v[116:119],a[244:247],a[148:151],v[116:119]
v_add_f32 v220,v62,v220
v_add_f32 v221,v63,v221
MF v[124:127],a[244:247],a[164:167],v[124:127]
v_exp_f32 v71,v71
CP v59,v62,v63
MF v[132:135],a[244:247],a[180:183],v[132:135]
v_add_f32 v222,v64,v222
v_add_f32 v223,v65,v223
MF v[108:111],a[248:251],a[136:139],v[108:111]
v_add_f32 v222,v66,v222
v_add_f32 v223,v67,v223
MF v[116:119],a[248:251],a[152:155],v[116:119]
v_add_f32 v222,v68,v222
v_add_f32 v223,v69,v223
MF v[124:127],a[248:251],a[168:171],v[124:127]
v_add_f32 v222,v70,v222
v_add_f32 v223,v71,v223
MF v[132:135],a[248:251],a[184:187],v[132:135]
v_max3_f32 v2,v216,v217,v218
v_max3_f32 v2,v2,v219,v220
MF v[108:111],a[252:255],a[140:143],v[108:111]
v_max3_f32 v2,v2,v221,v222
v_max_f32 v2,v2,v223
MF v[116:119],a[252:255],a[156:159],v[116:119]
v_cmp_nge_f32 vcc,0x5d800000,v2
CP v64,v64,v65
MF v[124:127],a[252:255],a[172:175],v[124:127]
CP v65,v66,v67
CP v66,v68,v69
MF v[132:135],a[252:255],a[188:191],v[132:135]
CP v67,v70,v71
v_add_u32 v228,s59,v231
s_cmp_eq_u64 vcc,0
s_cbranch_scc0 LSTUB1_%=
LSK1_%=:
s_waitcnt vmcnt(8) lgkmcnt(0)
s_barrier
s_cmp_lg_u32 s56,0
s_cbranch_scc1 LNOSIG_%=
s_cmp_ge_u32 s57,28
s_cbranch_scc1 LNOSIG_%=
s_cmp_lt_u32 s71,6
s_cbranch_scc1 LNOSIG_%=
s_lshr_b32 s100,s88,3
s_lshl_b32 s100,s100,6
v_mov_b32 v3,s100
v_mov_b32 v4,1
s_mov_b64 exec,1
global_atomic_add v3,v4,s[26:27]
s_mov_b64 exec,-1
LNOSIG_%=:
v_min_u32 v252,65535,v251
v_lshlrev_b32 v252,7,v252
s_lshl_b32 s100,s56,8
MF a[0:3],v[136:139],v[8:11],0
ds_read_b128 a[192:195],v228
v_add_f32 v216,v216,v217
v_add_f32 v218,v218,v219
MF a[32:35],v[136:139],v[16:19],0
v_add_f32 v220,v220,v221
v_add_f32 v222,v222,v223
s_add_u32 m0,s74,s61
MF a[64:67],v[136:139],v[24:27],0
buffer_load_dwordx4 v230,s[40:43],s72 offen lds
v_add_f32 v224,v224,v216
MF a[96:99],v[136:139],v[32:35],0
v_add_f32 v225,v225,v218
v_add_f32 v226,v226,v220
v_add_f32 v227,v227,v222
MF a[4:7],v[144:147],v[8:11],0
ds_read_b128 a[196:199],v228 offset:1024
s_lshl_b32 s77,s58,1
s_sub_u32 s77,s77,s75
MF a[36:39],v[144:147],v[16:19],0
s_lshl_b32 s77,s77,6
v_add_u32 v234,s77,v232
v_add_u32 v235,s77,v233
MF a[68:71],v[144:147],v[24:27],0
buffer_load_dwordx4 v230,s[40:43],s72 offen offset:1024 lds
v_cmp_gt_i32_e64 s[82:83],0,v234
MF a[100:103],v[144:147],v[32:35],0
v_cmp_gt_i32_e64 s[84:85],1,v234
v_cndmask_b32_e64 v72,v72,v236,s[82:83]
v_cmp_gt_i32_e64 s[82:83],2,v234
MF a[8:11],v[152:155],v[8:11],0
ds_read_b128 a[200:203],v228 offset:2048
v_cndmask_b32_e64 v73,v73,v236,s[84:85]
v_cmp_gt_i32_e64 s[84:85],3,v234
MF a[40:43],v[152:155],v[16:19],0
v_cndmask_b32_e64 v74,v74,v236,s[82:83]
v_cmp_gt_i32_e64 s[82:83],16,v234
v_cndmask_b32_e64 v75,v75,v236,s[84:85]
MF a[72:75],v[152:155],v[24:27],0
buffer_load_dwordx4 v230,s[40:43],s72 offen offset:2048 lds
v_cmp_gt_i32_e64 s[84:85],17,v234
MF a[104:107],v[152:155],v[32:35],0
v_cndmask_b32_e64 v76,v76,v236,s[82:83]
v_cmp_gt_i32_e64 s[82:83],18,v234
v_cndmask_b32_e64 v77,v77,v236,s[84:85]
MF a[12:15],v[160:163],v[8:11],0
ds_read_b128 a[204:207],v228 offset:3072
v_cmp_gt_i32_e64 s[84:85],19,v234
v_cndmask_b32_e64 v78,v78,v236,s[82:83]
MF a[44:47],v[160:163],v[16:19],0
v_cndmask_b32_e64 v79,v79,v236,s[84:85]
v_exp_f32 v72,v72
MF a[76:79],v[160:163],v[24:27],0
ds_read_b128 a[208:211],v228 offset:4096
v_exp_f32 v73,v73
MF a[108:111],v[160:163],v[32:35],0
v_exp_f32 v74,v74
MF a[16:19],v[168:171],v[8:11],0
buffer_load_dwordx4 v230,s[40:43],s72 offen offset:3072 lds
MF a[48:51],v[168:171],v[16:19],0
v_exp_f32 v75,v75
MF a[80:83],v[168:171],v[24:27],0
ds_read_b128 a[212:215],v228 offset:5120
v_exp_f32 v76,v76
MF a[112:115],v[168:171],v[32:35],0
v_exp_f32 v77,v77
MF a[20:23],v[176:179],v[8:11],0
v_exp_f32 v78,v78
s_add_u32 s72,s72,16384
MF a[52:55],v[176:179],v[16:19],0
v_exp_f32 v79,v79
v_cmp_gt_i32_e64 s[82:83],0,v235
MF a[84:87],v[176:179],v[24:27],0
ds_read_b128 a[216:219],v228 offset:6144
v_cmp_gt_i32_e64 s[84:85],1,v235
v_cndmask_b32_e64 v80,v80,v236,s[82:83]
MF a[116:119],v[176:179],v[32:35],0
v_cmp_gt_i32_e64 s[82:83],2,v235
v_cndmask_b32_e64 v81,v81,v236,s[84:85]
v_cmp_gt_i32_e64 s[84:85],3,v235
MF a[24:27],v[184:187],v[8:11],0
ds_read_b128 a[220:223],v228 offset:7168
v_cndmask_b32_e64 v82,v82,v236,s[82:83]
v_cmp_gt_i32_e64 s[82:83],16,v235
MF a[56:59],v[184:187],v[16:19],0
v_cndmask_b32_e64 v83,v83,v236,s[84:85]
v_cmp_gt_i32_e64 s[84:85],17,v235
s_add_u32 m0,s74,s62
MF a[88:91],v[184:187],v[24:27],0
buffer_load_dwordx4 v230,s[44:47],s73 offen lds
v_cndmask_b32_e64 v84,v84,v236,s[82:83]
MF a[120:123],v[184:187],v[32:35],0
v_cmp_gt_i32_e64 s[82:83],18,v235
v_cndmask_b32_e64 v85,v85,v236,s[84:85]
v_cmp_gt_i32_e64 s[84:85],19,v235
MF a[28:31],v[192:195],v[8:11],0
v_cndmask_b32_e64 v86,v86,v236,s[82:83]
v_cndmask_b32_e64 v87,v87,v236,s[84:85]
MF a[60:63],v[192:195],v[16:19],0
ds_read_b128 a[224:227],v228 offset:8192
v_exp_f32 v80,v80
MF a[92:95],v[192:195],v[24:27],0
v_add_f32 v216,v72,v74
v_add_f32 v217,v73,v75
MF a[124:127],v[192:195],v[32:35],0
buffer_load_dwordx4 v230,s[44:47],s73 offen offset:1024 lds
v_exp_f32 v81,v81
CP v72,v72,v73
v_exp_f32 v82,v82
v_add_f32 v216,v76,v216
v_exp_f32 v83,v83
MF a[0:3],v[140:143],v[40:43],a[0:3]
CP v73,v74,v75
v_exp_f32 v84,v84
MF a[32:35],v[140:143],v[48:51],a[32:35]
ds_read_b128 a[228:231],v228 offset:9216
v_add_f32 v217,v77,v217
v_add_f32 v216,v78,v216
MF a[64:67],v[140:143],v[56:59],a[64:67]
v_exp_f32 v85,v85
CP v74,v76,v77
MF a[96:99],v[140:143],v[64:67],a[96:99]
ds_read_b128 a[232:235],v228 offset:10240
v_exp_f32 v86,v86
MF a[4:7],v[148:151],v[40:43],a[4:7]
v_add_f32 v217,v79,v217
v_exp_f32 v87,v87
MF a[36:39],v[148:151],v[48:51],a[36:39]
buffer_load_dwordx4 v230,s[44:47],s73 offen offset:2048 lds
MF a[68:71],v[148:151],v[56:59],a[68:71]
CP v75,v78,v79
v_exp_f32 v88,v88
MF a[100:103],v[148:151],v[64:67],a[100:103]
ds_read_b128 a[236:239],v228 offset:11264
v_add_f32 v218,v80,v82
MF a[8:11],v[156:159],v[40:43],a[8:11]
v_add_f32 v219,v81,v83
v_exp_f32 v89,v89
MF a[40:43],v[156:159],v[48:51],a[40:43]
buffer_load_dwordx4 v230,s[44:47],s73 offen offset:3072 lds
CP v80,v80,v81
MF a[72:75],v[156:159],v[56:59],a[72:75]
v_exp_f32 v90,v90
v_add_f32 v218,v84,v218
MF a[104:107],v[156:159],v[64:67],a[104:107]
ds_read_b128 a[240:243],v228 offset:12288
v_exp_f32 v91,v91
MF a[12:15],v[164:167],v[40:43],a[12:15]
CP v81,v82,v83
v_exp_f32 v92,v92
MF a[44:47],v[164:167],v[48:51],a[44:47]
ds_read_b128 a[244:247],v228 offset:13312
v_add_f32 v219,v85,v219
v_add_f32 v218,v86,v218
MF a[76:79],v[164:167],v[56:59],a[76:79]
v_exp_f32 v93,v93
CP v82,v84,v85
MF a[108:111],v[164:167],v[64:67],a[108:111]
v_exp_f32 v94,v94
s_add_u32 s73,s73,16384
MF a[16:19],v[172:175],v[40:43],a[16:19]
v_add_f32 v219,v87,v219
v_exp_f32 v95,v95
MF a[48:51],v[172:175],v[48:51],a[48:51]
ds_read_b128 a[248:251],v228 offset:14336
CP v83,v86,v87
MF a[80:83],v[172:175],v[56:59],a[80:83]
v_exp_f32 v96,v96
s_add_u32 m0,s100,147456
MF a[112:115],v[172:175],v[64:67],a[112:115]
global_load_lds_dword v252,s[8:9]
v_add_f32 v220,v88,v90
MF a[20:23],v[180:183],v[40:43],a[20:23]
v_add_f32 v221,v89,v91
v_exp_f32 v97,v97
MF a[52:55],v[180:183],v[48:51],a[52:55]
ds_read_b128 a[252:255],v228 offset:15360
CP v88,v88,v89
MF a[84:87],v[180:183],v[56:59],a[84:87]
v_exp_f32 v98,v98
v_add_f32 v220,v92,v220
MF a[116:119],v[180:183],v[64:67],a[116:119]
v_exp_f32 v99,v99
CP v89,v90,v91
MF a[24:27],v[188:191],v[40:43],a[24:27]
s_add_u32 s75,s75,1
s_add_u32 s59,s59,16384
MF a[56:59],v[188:191],v[48:51],a[56:59]
v_exp_f32 v100,v100
s_and_b32 s59,s59,0xffff
MF a[88:91],v[188:191],v[56:59],a[88:91]
v_add_f32 v221,v93,v221
s_add_u32 s61,s61,16384
s_and_b32 s61,s61,0xffff
MF a[120:123],v[188:191],v[64:67],a[120:123]
v_add_f32 v220,v94,v220
s_add_u32 s60,s60,16384
MF a[28:31],v[196:199],v[40:43],a[28:31]
s_and_b32 s60,s60,0xffff
s_or_b32 s60,s60,65536
MF a[60:63],v[196:199],v[48:51],a[60:63]
v_exp_f32 v101,v101
s_add_u32 s62,s62,16384
MF a[92:95],v[196:199],v[56:59],a[92:95]
CP v90,v92,v93
s_and_b32 s62,s62,0xffff
s_or_b32 s62,s62,65536
MF a[124:127],v[196:199],v[64:67],a[124:127]
v_exp_f32 v102,v102
v_add_u32 v229,s60,v231
v_add_f32 v221,v95,v221
v_exp_f32 v103,v103
CP v91,v94,v95
v_add_f32 v222,v96,v98
v_add_f32 v223,v97,v99
CP v96,v96,v97
v_add_f32 v222,v100,v222
CP v97,v98,v99
v_add_f32 v223,v101,v223
v_add_f32 v222,v102,v222
CP v98,v100,v101
v_add_f32 v223,v103,v223
CP v99,v102,v103
s_waitcnt lgkmcnt(0)
s_cmp_eq_u32 s58,0
s_cbranch_scc1 LTRANS_%=
s_sub_u32 s76,s58,1
s_cmp_eq_u32 s76,0
s_cbranch_scc1 LDMASK_%=
LDLOOP_%=:
MF v[8:11],a[192:195],a[128:131],v[200:203]
ds_read_b128 v[136:139],v229
v_exp_f32 v104,v104
MF v[16:19],a[192:195],a[144:147],v[204:207]
v_exp_f32 v105,v105
MF v[24:27],a[192:195],a[160:163],v[208:211]
v_exp_f32 v106,v106
MF v[32:35],a[192:195],a[176:179],v[212:215]
ds_read_b128 v[144:147],v229 offset:1024
v_exp_f32 v107,v107
MF v[8:11],a[196:199],a[132:135],v[8:11]
v_exp_f32 v108,v108
MF v[16:19],a[196:199],a[148:151],v[16:19]
v_exp_f32 v109,v109
MF v[24:27],a[196:199],a[164:167],v[24:27]
ds_read_b128 v[152:155],v229 offset:2048
v_exp_f32 v110,v110
MF v[32:35],a[196:199],a[180:183],v[32:35]
v_exp_f32 v111,v111
MF v[8:11],a[200:203],a[136:139],v[8:11]
v_exp_f32 v112,v112
v_add_f32 v216,v104,v216
MF v[16:19],a[200:203],a[152:155],v[16:19]
ds_read_b128 v[160:163],v229 offset:3072
v_add_f32 v217,v105,v217
MF v[24:27],a[200:203],a[168:171],v[24:27]
v_exp_f32 v113,v113
CP v104,v104,v105
MF v[32:35],a[200:203],a[184:187],v[32:35]
v_exp_f32 v114,v114
v_add_f32 v216,v106,v216
MF v[8:11],a[204:207],a[140:143],v[8:11]
ds_read_b128 v[168:171],v229 offset:4096
v_add_f32 v217,v107,v217
MF v[16:19],a[204:207],a[156:159],v[16:19]
v_exp_f32 v115,v115
CP v105,v106,v107
MF v[24:27],a[204:207],a[172:175],v[24:27]
v_exp_f32 v116,v116
MF v[32:35],a[204:207],a[188:191],v[32:35]
ds_read_b128 v[176:179],v229 offset:5120
v_add_f32 v216,v108,v216
MF v[12:15],a[208:211],a[128:131],v[200:203]
v_add_f32 v217,v109,v217
MF v[20:23],a[208:211],a[144:147],v[204:207]
v_exp_f32 v117,v117
MF v[28:31],a[208:211],a[160:163],v[208:211]
ds_read_b128 v[184:187],v229 offset:6144
CP v106,v108,v109
MF v[36:39],a[208:211],a[176:179],v[212:215]
v_exp_f32 v118,v118
MF v[12:15],a[212:215],a[132:135],v[12:15]
v_add_f32 v216,v110,v216
v_add_f32 v217,v111,v217
MF v[20:23],a[212:215],a[148:151],v[20:23]
ds_read_b128 v[192:195],v229 offset:7168
v_exp_f32 v119,v119
MF v[28:31],a[212:215],a[164:167],v[28:31]
CP v107,v110,v111
MF v[36:39],a[212:215],a[180:183],v[36:39]
v_exp_f32 v120,v120
MF v[12:15],a[216:219],a[136:139],v[12:15]
ds_read_b128 v[140:143],v229 offset:8192
v_add_f32 v218,v112,v218
MF v[20:23],a[216:219],a[152:155],v[20:23]
v_add_f32 v219,v113,v219
MF v[28:31],a[216:219],a[168:171],v[28:31]
v_exp_f32 v121,v121
MF v[36:39],a[216:219],a[184:187],v[36:39]
ds_read_b128 v[148:151],v229 offset:9216
CP v112,v112,v113
MF v[12:15],a[220:223],a[140:143],v[12:15]
v_exp_f32 v122,v122
MF v[20:23],a[220:223],a[156:159],v[20:23]
v_add_f32 v218,v114,v218
v_add_f32 v219,v115,v219
MF v[28:31],a[220:223],a[172:175],v[28:31]
ds_read_b128 v[156:159],v229 offset:10240
v_exp_f32 v123,v123
MF v[36:39],a[220:223],a[188:191],v[36:39]
CP v113,v114,v115
MF v[40:43],a[224:227],a[128:131],v[200:203]
v_exp_f32 v124,v124
MF v[48:51],a[224:227],a[144:147],v[204:207]
ds_read_b128 v[164:167],v229 offset:11264
v_add_f32 v218,v116,v218
MF v[56:59],a[224:227],a[160:163],v[208:211]
v_add_f32 v219,v117,v219
MF v[64:67],a[224:227],a[176:179],v[212:215]
v_exp_f32 v125,v125
MF v[40:43],a[228:231],a[132:135],v[40:43]
ds_read_b128 v[172:175],v229 offset:12288
CP v114,v116,v117
MF v[48:51],a[228:231],a[148:151],v[48:51]
v_exp_f32 v126,v126
MF v[56:59],a[228:231],a[164:167],v[56:59]
v_add_f32 v218,v118,v218
v_add_f32 v219,v119,v219
MF v[64:67],a[228:231],a[180:183],v[64:67]
ds_read_b128 v[180:183],v229 offset:13312
v_exp_f32 v127,v127
MF v[40:43],a[232:235],a[136:139],v[40:43]
CP v115,v118,v119
MF v[48:51],a[232:235],a[152:155],v[48:51]
v_exp_f32 v128,v128
MF v[56:59],a[232:235],a[168:171],v[56:59]
ds_read_b128 v[188:191],v229 offset:14336
v_add_f32 v220,v120,v220
MF v[64:67],a[232:235],a[184:187],v[64:67]
v_add_f32 v221,v121,v221
MF v[40:43],a[236:239],a[140:143],v[40:43]
v_exp_f32 v129,v129
MF v[48:51],a[236:239],a[156:159],v[48:51]
ds_read_b128 v[196:199],v229 offset:15360
CP v120,v120,v121
MF v[56:59],a[236:239],a[172:175],v[56:59]
v_exp_f32 v130,v130
MF v[64:67],a[236:239],a[188:191],v[64:67]
v_add_f32 v220,v122,v220
v_add_f32 v221,v123,v221
MF v[44:47],a[240:243],a[128:131],v[200:203]
v_exp_f32 v131,v131
CP v121,v122,v123
MF v[52:55],a[240:243],a[144:147],v[204:207]
v_exp_f32 v132,v132
MF v[60:63],a[240:243],a[160:163],v[208:211]
v_add_f32 v220,v124,v220
v_add_f32 v221,v125,v221
MF v[68:71],a[240:243],a[176:179],v[212:215]
v_exp_f32 v133,v133
CP v122,v124,v125
MF v[44:47],a[244:247],a[132:135],v[44:47]
v_exp_f32 v134,v134
MF v[52:55],a[244:247],a[148:151],v[52:55]
v_add_f32 v220,v126,v220
v_add_f32 v221,v127,v221
MF v[60:63],a[244:247],a[164:167],v[60:63]
v_exp_f32 v135,v135
CP v123,v126,v127
MF v[68:71],a[244:247],a[180:183],v[68:71]
v_add_f32 v222,v128,v222
v_add_f32 v223,v129,v223
MF v[44:47],a[248:251],a[136:139],v[44:47]
v_add_f32 v222,v130,v222
v_add_f32 v223,v131,v223
MF v[52:55],a[248:251],a[152:155],v[52:55]
v_add_f32 v222,v132,v222
v_add_f32 v223,v133,v223
MF v[60:63],a[248:251],a[168:171],v[60:63]
v_add_f32 v222,v134,v222
v_add_f32 v223,v135,v223
MF v[68:71],a[248:251],a[184:187],v[68:71]
v_max3_f32 v2,v216,v217,v218
v_max3_f32 v2,v2,v219,v220
MF v[44:47],a[252:255],a[140:143],v[44:47]
v_max3_f32 v2,v2,v221,v222
v_max_f32 v2,v2,v223
MF v[52:55],a[252:255],a[156:159],v[52:55]
v_cmp_nge_f32 vcc,0x5d800000,v2
CP v128,v128,v129
MF v[60:63],a[252:255],a[172:175],v[60:63]
CP v129,v130,v131
CP v130,v132,v133
MF v[68:71],a[252:255],a[188:191],v[68:71]
CP v131,v134,v135
v_add_u32 v228,s59,v231
s_cmp_eq_u64 vcc,0
s_cbranch_scc0 LSTUB2_%=
LSK2_%=:
s_waitcnt vmcnt(8) lgkmcnt(0)
s_barrier
MF a[0:3],v[136:139],v[72:75],a[0:3]
ds_read_b128 a[192:195],v228
v_add_f32 v216,v216,v217
v_add_f32 v218,v218,v219
MF a[32:35],v[136:139],v[80:83],a[32:35]
v_add_f32 v220,v220,v221
v_add_f32 v222,v222,v223
s_add_u32 m0,s74,s61
MF a[64:67],v[136:139],v[88:91],a[64:67]
buffer_load_dwordx4 v230,s[40:43],s72 offen lds
v_add_f32 v224,v224,v216
MF a[96:99],v[136:139],v[96:99],a[96:99]
v_add_f32 v225,v225,v218
v_add_f32 v226,v226,v220
v_add_f32 v227,v227,v222
MF a[4:7],v[144:147],v[72:75],a[4:7]
ds_read_b128 a[196:199],v228 offset:1024
v_exp_f32 v8,v8
MF a[36:39],v[144:147],v[80:83],a[36:39]
v_exp_f32 v9,v9
MF a[68:71],v[144:147],v[88:91],a[68:71]
buffer_load_dwordx4 v230,s[40:43],s72 offen offset:1024 lds
MF a[100:103],v[144:147],v[96:99],a[100:103]
v_exp_f32 v10,v10
MF a[8:11],v[152:155],v[72:75],a[8:11]
ds_read_b128 a[200:203],v228 offset:2048
v_exp_f32 v11,v11
MF a[40:43],v[152:155],v[80:83],a[40:43]
v_exp_f32 v12,v12
MF a[72:75],v[152:155],v[88:91],a[72:75]
ds_read_b128 a[204:207],v228 offset:3072
v_exp_f32 v13,v13
MF a[104:107],v[152:155],v[96:99],a[104:107]
v_exp_f32 v14,v14
MF a[12:15],v[160:163],v[72:75],a[12:15]
buffer_load_dwordx4 v230,s[40:43],s72 offen offset:2048 lds
MF a[44:47],v[160:163],v[80:83],a[44:47]
v_exp_f32 v15,v15
MF a[76:79],v[160:163],v[88:91],a[76:79]
v_exp_f32 v16,v16
v_add_f32 v216,v8,v10
MF a[108:111],v[160:163],v[96:99],a[108:111]
ds_read_b128 a[208:211],v228 offset:4096
v_add_f32 v217,v9,v11
MF a[16:19],v[168:171],v[72:75],a[16:19]
v_exp_f32 v17,v17
CP v8,v8,v9
MF a[48:51],v[168:171],v[80:83],a[48:51]
buffer_load_dwordx4 v230,s[40:43],s72 offen offset:3072 lds
MF a[80:83],v[168:171],v[88:91],a[80:83]
v_exp_f32 v18,v18
v_add_f32 v216,v12,v216
MF a[112:115],v[168:171],v[96:99],a[112:115]
ds_read_b128 a[212:215],v228 offset:5120
v_exp_f32 v19,v19
MF a[20:23],v[176:179],v[72:75],a[20:23]
CP v9,v10,v11
v_exp_f32 v20,v20
MF a[52:55],v[176:179],v[80:83],a[52:55]
ds_read_b128 a[216:219],v228 offset:6144
v_add_f32 v217,v13,v217
MF a[84:87],v[176:179],v[88:91],a[84:87]
v_add_f32 v216,v14,v216
v_exp_f32 v21,v21
MF a[116:119],v[176:179],v[96:99],a[116:119]
CP v10,v12,v13
s_add_u32 s72,s72,16384
MF a[24:27],v[184:187],v[72:75],a[24:27]
v_exp_f32 v22,v22
MF a[56:59],v[184:187],v[80:83],a[56:59]
ds_read_b128 a[220:223],v228 offset:7168
v_add_f32 v217,v15,v217
MF a[88:91],v[184:187],v[88:91],a[88:91]
v_exp_f32 v23,v23
CP v11,v14,v15
MF a[120:123],v[184:187],v[96:99],a[120:123]
v_exp_f32 v24,v24
MF a[28:31],v[192:195],v[72:75],a[28:31]
ds_read_b128 a[224:227],v228 offset:8192
v_add_f32 v218,v16,v18
v_add_f32 v219,v17,v19
MF a[60:63],v[192:195],v[80:83],a[60:63]
v_exp_f32 v25,v25
s_add_u32 m0,s74,s62
MF a[92:95],v[192:195],v[88:91],a[92:95]
buffer_load_dwordx4 v230,s[44:47],s73 offen lds
CP v16,v16,v17
MF a[124:127],v[192:195],v[96:99],a[124:127]
)A"
R"A(v_exp_f32 v26,v26
MF a[0:3],v[140:143],v[104:107],a[0:3]
ds_read_b128 a[228:231],v228 offset:9216
v_add_f32 v218,v20,v218
MF a[32:35],v[140:143],v[112:115],a[32:35]
v_exp_f32 v27,v27
MF a[64:67],v[140:143],v[120:123],a[64:67]
buffer_load_dwordx4 v230,s[44:47],s73 offen offset:1024 lds
CP v17,v18,v19
MF a[96:99],v[140:143],v[128:131],a[96:99]
v_exp_f32 v28,v28
v_add_f32 v219,v21,v219
MF a[4:7],v[148:151],v[104:107],a[4:7]
ds_read_b128 a[232:235],v228 offset:10240
v_add_f32 v218,v22,v218
MF a[36:39],v[148:151],v[112:115],a[36:39]
v_exp_f32 v29,v29
MF a[68:71],v[148:151],v[120:123],a[68:71]
ds_read_b128 a[236:239],v228 offset:11264
CP v18,v20,v21
MF a[100:103],v[148:151],v[128:131],a[100:103]
v_exp_f32 v30,v30
v_add_f32 v219,v23,v219
MF a[8:11],v[156:159],v[104:107],a[8:11]
buffer_load_dwordx4 v230,s[44:47],s73 offen offset:2048 lds
MF a[40:43],v[156:159],v[112:115],a[40:43]
v_exp_f32 v31,v31
CP v19,v22,v23
MF a[72:75],v[156:159],v[120:123],a[72:75]
v_exp_f32 v32,v32
v_add_f32 v220,v24,v26
MF a[104:107],v[156:159],v[128:131],a[104:107]
ds_read_b128 a[240:243],v228 offset:12288
v_add_f32 v221,v25,v27
MF a[12:15],v[164:167],v[104:107],a[12:15]
v_exp_f32 v33,v33
MF a[44:47],v[164:167],v[112:115],a[44:47]
buffer_load_dwordx4 v230,s[44:47],s73 offen offset:3072 lds
CP v24,v24,v25
MF a[76:79],v[164:167],v[120:123],a[76:79]
v_exp_f32 v34,v34
MF a[108:111],v[164:167],v[128:131],a[108:111]
ds_read_b128 a[244:247],v228 offset:13312
v_add_f32 v220,v28,v220
MF a[16:19],v[172:175],v[104:107],a[16:19]
v_exp_f32 v35,v35
CP v25,v26,v27
MF a[48:51],v[172:175],v[112:115],a[48:51]
ds_read_b128 a[248:251],v228 offset:14336
v_exp_f32 v36,v36
MF a[80:83],v[172:175],v[120:123],a[80:83]
v_add_f32 v221,v29,v221
v_add_f32 v220,v30,v220
MF a[112:115],v[172:175],v[128:131],a[112:115]
v_exp_f32 v37,v37
s_add_u32 s73,s73,16384
MF a[20:23],v[180:183],v[104:107],a[20:23]
CP v26,v28,v29
v_exp_f32 v38,v38
MF a[52:55],v[180:183],v[112:115],a[52:55]
ds_read_b128 a[252:255],v228 offset:15360
v_add_f32 v221,v31,v221
MF a[84:87],v[180:183],v[120:123],a[84:87]
v_exp_f32 v39,v39
MF a[116:119],v[180:183],v[128:131],a[116:119]
CP v27,v30,v31
v_add_f32 v222,v32,v34
MF a[24:27],v[188:191],v[104:107],a[24:27]
s_add_u32 s75,s75,1
s_add_u32 s59,s59,16384
MF a[56:59],v[188:191],v[112:115],a[56:59]
v_add_f32 v223,v33,v35
CP v32,v32,v33
s_and_b32 s59,s59,0xffff
MF a[88:91],v[188:191],v[120:123],a[88:91]
v_add_f32 v222,v36,v222
s_add_u32 s61,s61,16384
s_and_b32 s61,s61,0xffff
MF a[120:123],v[188:191],v[128:131],a[120:123]
CP v33,v34,v35
s_add_u32 s60,s60,16384
MF a[28:31],v[196:199],v[104:107],a[28:31]
v_add_f32 v223,v37,v223
s_and_b32 s60,s60,0xffff
s_or_b32 s60,s60,65536
MF a[60:63],v[196:199],v[112:115],a[60:63]
v_add_f32 v222,v38,v222
CP v34,v36,v37
s_add_u32 s62,s62,16384
MF a[92:95],v[196:199],v[120:123],a[92:95]
v_add_f32 v223,v39,v223
s_and_b32 s62,s62,0xffff
s_or_b32 s62,s62,65536
MF a[124:127],v[196:199],v[128:131],a[124:127]
CP v35,v38,v39
v_add_u32 v229,s60,v231
s_waitcnt lgkmcnt(0)
MF v[72:75],a[192:195],a[128:131],v[200:203]
ds_read_b128 v[136:139],v229
v_exp_f32 v40,v40
MF v[80:83],a[192:195],a[144:147],v[204:207]
v_exp_f32 v41,v41
MF v[88:91],a[192:195],a[160:163],v[208:211]
v_exp_f32 v42,v42
MF v[96:99],a[192:195],a[176:179],v[212:215]
ds_read_b128 v[144:147],v229 offset:1024
v_exp_f32 v43,v43
MF v[72:75],a[196:199],a[132:135],v[72:75]
v_exp_f32 v44,v44
MF v[80:83],a[196:199],a[148:151],v[80:83]
v_exp_f32 v45,v45
MF v[88:91],a[196:199],a[164:167],v[88:91]
ds_read_b128 v[152:155],v229 offset:2048
v_exp_f32 v46,v46
MF v[96:99],a[196:199],a[180:183],v[96:99]
v_exp_f32 v47,v47
MF v[72:75],a[200:203],a[136:139],v[72:75]
v_exp_f32 v48,v48
v_add_f32 v216,v40,v216
MF v[80:83],a[200:203],a[152:155],v[80:83]
ds_read_b128 v[160:163],v229 offset:3072
v_add_f32 v217,v41,v217
MF v[88:91],a[200:203],a[168:171],v[88:91]
v_exp_f32 v49,v49
CP v40,v40,v41
MF v[96:99],a[200:203],a[184:187],v[96:99]
v_exp_f32 v50,v50
v_add_f32 v216,v42,v216
MF v[72:75],a[204:207],a[140:143],v[72:75]
ds_read_b128 v[168:171],v229 offset:4096
v_add_f32 v217,v43,v217
MF v[80:83],a[204:207],a[156:159],v[80:83]
v_exp_f32 v51,v51
CP v41,v42,v43
MF v[88:91],a[204:207],a[172:175],v[88:91]
v_exp_f32 v52,v52
MF v[96:99],a[204:207],a[188:191],v[96:99]
ds_read_b128 v[176:179],v229 offset:5120
v_add_f32 v216,v44,v216
MF v[76:79],a[208:211],a[128:131],v[200:203]
v_add_f32 v217,v45,v217
MF v[84:87],a[208:211],a[144:147],v[204:207]
v_exp_f32 v53,v53
MF v[92:95],a[208:211],a[160:163],v[208:211]
ds_read_b128 v[184:187],v229 offset:6144
CP v42,v44,v45
MF v[100:103],a[208:211],a[176:179],v[212:215]
v_exp_f32 v54,v54
MF v[76:79],a[212:215],a[132:135],v[76:79]
v_add_f32 v216,v46,v216
v_add_f32 v217,v47,v217
MF v[84:87],a[212:215],a[148:151],v[84:87]
ds_read_b128 v[192:195],v229 offset:7168
v_exp_f32 v55,v55
MF v[92:95],a[212:215],a[164:167],v[92:95]
CP v43,v46,v47
MF v[100:103],a[212:215],a[180:183],v[100:103]
v_exp_f32 v56,v56
MF v[76:79],a[216:219],a[136:139],v[76:79]
ds_read_b128 v[140:143],v229 offset:8192
v_add_f32 v218,v48,v218
MF v[84:87],a[216:219],a[152:155],v[84:87]
v_add_f32 v219,v49,v219
MF v[92:95],a[216:219],a[168:171],v[92:95]
v_exp_f32 v57,v57
MF v[100:103],a[216:219],a[184:187],v[100:103]
ds_read_b128 v[148:151],v229 offset:9216
CP v48,v48,v49
MF v[76:79],a[220:223],a[140:143],v[76:79]
v_exp_f32 v58,v58
MF v[84:87],a[220:223],a[156:159],v[84:87]
v_add_f32 v218,v50,v218
v_add_f32 v219,v51,v219
MF v[92:95],a[220:223],a[172:175],v[92:95]
ds_read_b128 v[156:159],v229 offset:10240
v_exp_f32 v59,v59
MF v[100:103],a[220:223],a[188:191],v[100:103]
CP v49,v50,v51
MF v[104:107],a[224:227],a[128:131],v[200:203]
v_exp_f32 v60,v60
MF v[112:115],a[224:227],a[144:147],v[204:207]
ds_read_b128 v[164:167],v229 offset:11264
v_add_f32 v218,v52,v218
MF v[120:123],a[224:227],a[160:163],v[208:211]
v_add_f32 v219,v53,v219
MF v[128:131],a[224:227],a[176:179],v[212:215]
v_exp_f32 v61,v61
MF v[104:107],a[228:231],a[132:135],v[104:107]
ds_read_b128 v[172:175],v229 offset:12288
CP v50,v52,v53
MF v[112:115],a[228:231],a[148:151],v[112:115]
v_exp_f32 v62,v62
MF v[120:123],a[228:231],a[164:167],v[120:123]
v_add_f32 v218,v54,v218
v_add_f32 v219,v55,v219
MF v[128:131],a[228:231],a[180:183],v[128:131]
ds_read_b128 v[180:183],v229 offset:13312
v_exp_f32 v63,v63
MF v[104:107],a[232:235],a[136:139],v[104:107]
CP v51,v54,v55
MF v[112:115],a[232:235],a[152:155],v[112:115]
v_exp_f32 v64,v64
MF v[120:123],a[232:235],a[168:171],v[120:123]
ds_read_b128 v[188:191],v229 offset:14336
v_add_f32 v220,v56,v220
MF v[128:131],a[232:235],a[184:187],v[128:131]
v_add_f32 v221,v57,v221
MF v[104:107],a[236:239],a[140:143],v[104:107]
v_exp_f32 v65,v65
MF v[112:115],a[236:239],a[156:159],v[112:115]
ds_read_b128 v[196:199],v229 offset:15360
CP v56,v56,v57
MF v[120:123],a[236:239],a[172:175],v[120:123]
v_exp_f32 v66,v66
MF v[128:131],a[236:239],a[188:191],v[128:131]
v_add_f32 v220,v58,v220
v_add_f32 v221,v59,v221
MF v[108:111],a[240:243],a[128:131],v[200:203]
v_exp_f32 v67,v67
CP v57,v58,v59
MF v[116:119],a[240:243],a[144:147],v[204:207]
v_exp_f32 v68,v68
MF v[124:127],a[240:243],a[160:163],v[208:211]
v_add_f32 v220,v60,v220
v_add_f32 v221,v61,v221
MF v[132:135],a[240:243],a[176:179],v[212:215]
v_exp_f32 v69,v69
CP v58,v60,v61
MF v[108:111],a[244:247],a[132:135],v[108:111]
v_exp_f32 v70,v70
MF v[116:119],a[244:247],a[148:151],v[116:119]
v_add_f32 v220,v62,v220
v_add_f32 v221,v63,v221
MF v[124:127],a[244:247],a[164:167],v[124:127]
v_exp_f32 v71,v71
CP v59,v62,v63
MF v[132:135],a[244:247],a[180:183],v[132:135]
v_add_f32 v222,v64,v222
v_add_f32 v223,v65,v223
MF v[108:111],a[248:251],a[136:139],v[108:111]
v_add_f32 v222,v66,v222
v_add_f32 v223,v67,v223
MF v[116:119],a[248:251],a[152:155],v[116:119]
v_add_f32 v222,v68,v222
v_add_f32 v223,v69,v223
MF v[124:127],a[248:251],a[168:171],v[124:127]
v_add_f32 v222,v70,v222
v_add_f32 v223,v71,v223
MF v[132:135],a[248:251],a[184:187],v[132:135]
v_max3_f32 v2,v216,v217,v218
v_max3_f32 v2,v2,v219,v220
MF v[108:111],a[252:255],a[140:143],v[108:111]
v_max3_f32 v2,v2,v221,v222
v_max_f32 v2,v2,v223
MF v[116:119],a[252:255],a[156:159],v[116:119]
v_cmp_nge_f32 vcc,0x5d800000,v2
CP v64,v64,v65
MF v[124:127],a[252:255],a[172:175],v[124:127]
CP v65,v66,v67
CP v66,v68,v69
MF v[132:135],a[252:255],a[188:191],v[132:135]
CP v67,v70,v71
v_add_u32 v228,s59,v231
s_cmp_eq_u64 vcc,0
s_cbranch_scc0 LSTUB3_%=
LSK3_%=:
s_waitcnt vmcnt(8) lgkmcnt(0)
s_barrier
MF a[0:3],v[136:139],v[8:11],a[0:3]
ds_read_b128 a[192:195],v228
v_add_f32 v216,v216,v217
v_add_f32 v218,v218,v219
MF a[32:35],v[136:139],v[16:19],a[32:35]
v_add_f32 v220,v220,v221
v_add_f32 v222,v222,v223
s_add_u32 m0,s74,s61
MF a[64:67],v[136:139],v[24:27],a[64:67]
buffer_load_dwordx4 v230,s[40:43],s72 offen lds
v_add_f32 v224,v224,v216
MF a[96:99],v[136:139],v[32:35],a[96:99]
v_add_f32 v225,v225,v218
v_add_f32 v226,v226,v220
v_add_f32 v227,v227,v222
MF a[4:7],v[144:147],v[8:11],a[4:7]
ds_read_b128 a[196:199],v228 offset:1024
v_exp_f32 v72,v72
MF a[36:39],v[144:147],v[16:19],a[36:39]
v_exp_f32 v73,v73
MF a[68:71],v[144:147],v[24:27],a[68:71]
buffer_load_dwordx4 v230,s[40:43],s72 offen offset:1024 lds
MF a[100:103],v[144:147],v[32:35],a[100:103]
v_exp_f32 v74,v74
MF a[8:11],v[152:155],v[8:11],a[8:11]
ds_read_b128 a[200:203],v228 offset:2048
v_exp_f32 v75,v75
MF a[40:43],v[152:155],v[16:19],a[40:43]
v_exp_f32 v76,v76
MF a[72:75],v[152:155],v[24:27],a[72:75]
ds_read_b128 a[204:207],v228 offset:3072
v_exp_f32 v77,v77
MF a[104:107],v[152:155],v[32:35],a[104:107]
v_exp_f32 v78,v78
MF a[12:15],v[160:163],v[8:11],a[12:15]
buffer_load_dwordx4 v230,s[40:43],s72 offen offset:2048 lds
MF a[44:47],v[160:163],v[16:19],a[44:47]
v_exp_f32 v79,v79
MF a[76:79],v[160:163],v[24:27],a[76:79]
v_exp_f32 v80,v80
v_add_f32 v216,v72,v74
MF a[108:111],v[160:163],v[32:35],a[108:111]
ds_read_b128 a[208:211],v228 offset:4096
v_add_f32 v217,v73,v75
MF a[16:19],v[168:171],v[8:11],a[16:19]
v_exp_f32 v81,v81
CP v72,v72,v73
MF a[48:51],v[168:171],v[16:19],a[48:51]
buffer_load_dwordx4 v230,s[40:43],s72 offen offset:3072 lds
MF a[80:83],v[168:171],v[24:27],a[80:83]
v_exp_f32 v82,v82
v_add_f32 v216,v76,v216
MF a[112:115],v[168:171],v[32:35],a[112:115]
ds_read_b128 a[212:215],v228 offset:5120
v_exp_f32 v83,v83
MF a[20:23],v[176:179],v[8:11],a[20:23]
CP v73,v74,v75
v_exp_f32 v84,v84
MF a[52:55],v[176:179],v[16:19],a[52:55]
ds_read_b128 a[216:219],v228 offset:6144
v_add_f32 v217,v77,v217
MF a[84:87],v[176:179],v[24:27],a[84:87]
v_add_f32 v216,v78,v216
v_exp_f32 v85,v85
MF a[116:119],v[176:179],v[32:35],a[116:119]
CP v74,v76,v77
s_add_u32 s72,s72,16384
MF a[24:27],v[184:187],v[8:11],a[24:27]
v_exp_f32 v86,v86
MF a[56:59],v[184:187],v[16:19],a[56:59]
ds_read_b128 a[220:223],v228 offset:7168
v_add_f32 v217,v79,v217
MF a[88:91],v[184:187],v[24:27],a[88:91]
v_exp_f32 v87,v87
CP v75,v78,v79
MF a[120:123],v[184:187],v[32:35],a[120:123]
v_exp_f32 v88,v88
MF a[28:31],v[192:195],v[8:11],a[28:31]
ds_read_b128 a[224:227],v228 offset:8192
v_add_f32 v218,v80,v82
v_add_f32 v219,v81,v83
MF a[60:63],v[192:195],v[16:19],a[60:63]
v_exp_f32 v89,v89
s_add_u32 m0,s74,s62
MF a[92:95],v[192:195],v[24:27],a[92:95]
buffer_load_dwordx4 v230,s[44:47],s73 offen lds
CP v80,v80,v81
MF a[124:127],v[192:195],v[32:35],a[124:127]
v_exp_f32 v90,v90
MF a[0:3],v[140:143],v[40:43],a[0:3]
ds_read_b128 a[228:231],v228 offset:9216
v_add_f32 v218,v84,v218
MF a[32:35],v[140:143],v[48:51],a[32:35]
v_exp_f32 v91,v91
MF a[64:67],v[140:143],v[56:59],a[64:67]
buffer_load_dwordx4 v230,s[44:47],s73 offen offset:1024 lds
CP v81,v82,v83
MF a[96:99],v[140:143],v[64:67],a[96:99]
v_exp_f32 v92,v92
v_add_f32 v219,v85,v219
MF a[4:7],v[148:151],v[40:43],a[4:7]
ds_read_b128 a[232:235],v228 offset:10240
v_add_f32 v218,v86,v218
MF a[36:39],v[148:151],v[48:51],a[36:39]
v_exp_f32 v93,v93
MF a[68:71],v[148:151],v[56:59],a[68:71]
ds_read_b128 a[236:239],v228 offset:11264
CP v82,v84,v85
MF a[100:103],v[148:151],v[64:67],a[100:103]
v_exp_f32 v94,v94
v_add_f32 v219,v87,v219
MF a[8:11],v[156:159],v[40:43],a[8:11]
buffer_load_dwordx4 v230,s[44:47],s73 offen offset:2048 lds
MF a[40:43],v[156:159],v[48:51],a[40:43]
v_exp_f32 v95,v95
CP v83,v86,v87
MF a[72:75],v[156:159],v[56:59],a[72:75]
v_exp_f32 v96,v96
v_add_f32 v220,v88,v90
MF a[104:107],v[156:159],v[64:67],a[104:107]
ds_read_b128 a[240:243],v228 offset:12288
v_add_f32 v221,v89,v91
MF a[12:15],v[164:167],v[40:43],a[12:15]
v_exp_f32 v97,v97
MF a[44:47],v[164:167],v[48:51],a[44:47]
buffer_load_dwordx4 v230,s[44:47],s73 offen offset:3072 lds
CP v88,v88,v89
MF a[76:79],v[164:167],v[56:59],a[76:79]
v_exp_f32 v98,v98
MF a[108:111],v[164:167],v[64:67],a[108:111]
ds_read_b128 a[244:247],v228 offset:13312
v_add_f32 v220,v92,v220
MF a[16:19],v[172:175],v[40:43],a[16:19]
v_exp_f32 v99,v99
CP v89,v90,v91
MF a[48:51],v[172:175],v[48:51],a[48:51]
ds_read_b128 a[248:251],v228 offset:14336
v_exp_f32 v100,v100
MF a[80:83],v[172:175],v[56:59],a[80:83]
v_add_f32 v221,v93,v221
v_add_f32 v220,v94,v220
MF a[112:115],v[172:175],v[64:67],a[112:115]
v_exp_f32 v101,v101
s_add_u32 s73,s73,16384
MF a[20:23],v[180:183],v[40:43],a[20:23]
CP v90,v92,v93
v_exp_f32 v102,v102
MF a[52:55],v[180:183],v[48:51],a[52:55]
ds_read_b128 a[252:255],v228 offset:15360
v_add_f32 v221,v95,v221
MF a[84:87],v[180:183],v[56:59],a[84:87]
v_exp_f32 v103,v103
MF a[116:119],v[180:183],v[64:67],a[116:119]
CP v91,v94,v95
v_add_f32 v222,v96,v98
MF a[24:27],v[188:191],v[40:43],a[24:27]
s_add_u32 s75,s75,1
s_add_u32 s59,s59,16384
MF a[56:59],v[188:191],v[48:51],a[56:59]
v_add_f32 v223,v97,v99
CP v96,v96,v97
s_and_b32 s59,s59,0xffff
MF a[88:91],v[188:191],v[56:59],a[88:91]
v_add_f32 v222,v100,v222
s_add_u32 s61,s61,16384
s_and_b32 s61,s61,0xffff
MF a[120:123],v[188:191],v[64:67],a[120:123]
CP v97,v98,v99
s_add_u32 s60,s60,16384
MF a[28:31],v[196:199],v[40:43],a[28:31]
v_add_f32 v223,v101,v223
s_and_b32 s60,s60,0xffff
s_or_b32 s60,s60,65536
MF a[60:63],v[196:199],v[48:51],a[60:63]
v_add_f32 v222,v102,v222
CP v98,v100,v101
s_add_u32 s62,s62,16384
MF a[92:95],v[196:199],v[56:59],a[92:95]
v_add_f32 v223,v103,v223
s_and_b32 s62,s62,0xffff
s_or_b32 s62,s62,65536
MF a[124:127],v[196:199],v[64:67],a[124:127]
CP v99,v102,v103
v_add_u32 v229,s60,v231
s_waitcnt lgkmcnt(0)
s_sub_u32 s76,s76,1
s_cmp_lg_u32 s76,0
s_cbranch_scc1 LDLOOP_%=
LDMASK_%=:
MF v[8:11],a[192:195],a[128:131],v[200:203]
ds_read_b128 v[136:139],v229
v_exp_f32 v104,v104
MF v[16:19],a[192:195],a[144:147],v[204:207]
v_exp_f32 v105,v105
MF v[24:27],a[192:195],a[160:163],v[208:211]
v_exp_f32 v106,v106
MF v[32:35],a[192:195],a[176:179],v[212:215]
ds_read_b128 v[144:147],v229 offset:1024
v_exp_f32 v107,v107
MF v[8:11],a[196:199],a[132:135],v[8:11]
v_exp_f32 v108,v108
MF v[16:19],a[196:199],a[148:151],v[16:19]
v_exp_f32 v109,v109
MF v[24:27],a[196:199],a[164:167],v[24:27]
ds_read_b128 v[152:155],v229 offset:2048
v_exp_f32 v110,v110
MF v[32:35],a[196:199],a[180:183],v[32:35]
v_exp_f32 v111,v111
MF v[8:11],a[200:203],a[136:139],v[8:11]
v_exp_f32 v112,v112
v_add_f32 v216,v104,v216
MF v[16:19],a[200:203],a[152:155],v[16:19]
ds_read_b128 v[160:163],v229 offset:3072
v_add_f32 v217,v105,v217
MF v[24:27],a[200:203],a[168:171],v[24:27]
v_exp_f32 v113,v113
CP v104,v104,v105
MF v[32:35],a[200:203],a[184:187],v[32:35]
v_exp_f32 v114,v114
v_add_f32 v216,v106,v216
MF v[8:11],a[204:207],a[140:143],v[8:11]
ds_read_b128 v[168:171],v229 offset:4096
v_add_f32 v217,v107,v217
MF v[16:19],a[204:207],a[156:159],v[16:19]
v_exp_f32 v115,v115
CP v105,v106,v107
MF v[24:27],a[204:207],a[172:175],v[24:27]
v_exp_f32 v116,v116
MF v[32:35],a[204:207],a[188:191],v[32:35]
ds_read_b128 v[176:179],v229 offset:5120
v_add_f32 v216,v108,v216
MF v[12:15],a[208:211],a[128:131],v[200:203]
v_add_f32 v217,v109,v217
MF v[20:23],a[208:211],a[144:147],v[204:207]
v_exp_f32 v117,v117
MF v[28:31],a[208:211],a[160:163],v[208:211]
ds_read_b128 v[184:187],v229 offset:6144
CP v106,v108,v109
MF v[36:39],a[208:211],a[176:179],v[212:215]
v_exp_f32 v118,v118
MF v[12:15],a[212:215],a[132:135],v[12:15]
v_add_f32 v216,v110,v216
v_add_f32 v217,v111,v217
MF v[20:23],a[212:215],a[148:151],v[20:23]
ds_read_b128 v[192:195],v229 offset:7168
v_exp_f32 v119,v119
MF v[28:31],a[212:215],a[164:167],v[28:31]
CP v107,v110,v111
MF v[36:39],a[212:215],a[180:183],v[36:39]
v_exp_f32 v120,v120
MF v[12:15],a[216:219],a[136:139],v[12:15]
ds_read_b128 v[140:143],v229 offset:8192
v_add_f32 v218,v112,v218
MF v[20:23],a[216:219],a[152:155],v[20:23]
v_add_f32 v219,v113,v219
MF v[28:31],a[216:219],a[168:171],v[28:31]
v_exp_f32 v121,v121
MF v[36:39],a[216:219],a[184:187],v[36:39]
ds_read_b128 v[148:151],v229 offset:9216
CP v112,v112,v113
MF v[12:15],a[220:223],a[140:143],v[12:15]
v_exp_f32 v122,v122
MF v[20:23],a[220:223],a[156:159],v[20:23]
v_add_f32 v218,v114,v218
v_add_f32 v219,v115,v219
MF v[28:31],a[220:223],a[172:175],v[28:31]
ds_read_b128 v[156:159],v229 offset:10240
v_exp_f32 v123,v123
MF v[36:39],a[220:223],a[188:191],v[36:39]
CP v113,v114,v115
MF v[40:43],a[224:227],a[128:131],v[200:203]
v_exp_f32 v124,v124
MF v[48:51],a[224:227],a[144:147],v[204:207]
ds_read_b128 v[164:167],v229 offset:11264
v_add_f32 v218,v116,v218
MF v[56:59],a[224:227],a[160:163],v[208:211]
v_add_f32 v219,v117,v219
MF v[64:67],a[224:227],a[176:179],v[212:215]
v_exp_f32 v125,v125
MF v[40:43],a[228:231],a[132:135],v[40:43]
ds_read_b128 v[172:175],v229 offset:12288
CP v114,v116,v117
MF v[48:51],a[228:231],a[148:151],v[48:51]
v_exp_f32 v126,v126
MF v[56:59],a[228:231],a[164:167],v[56:59]
v_add_f32 v218,v118,v218
v_add_f32 v219,v119,v219
MF v[64:67],a[228:231],a[180:183],v[64:67]
ds_read_b128 v[180:183],v229 offset:13312
v_exp_f32 v127,v127
MF v[40:43],a[232:235],a[136:139],v[40:43]
CP v115,v118,v119
MF v[48:51],a[232:235],a[152:155],v[48:51]
v_exp_f32 v128,v128
MF v[56:59],a[232:235],a[168:171],v[56:59]
ds_read_b128 v[188:191],v229 offset:14336
v_add_f32 v220,v120,v220
MF v[64:67],a[232:235],a[184:187],v[64:67]
v_add_f32 v221,v121,v221
MF v[40:43],a[236:239],a[140:143],v[40:43]
v_exp_f32 v129,v129
MF v[48:51],a[236:239],a[156:159],v[48:51]
ds_read_b128 v[196:199],v229 offset:15360
CP v120,v120,v121
MF v[56:59],a[236:239],a[172:175],v[56:59]
v_exp_f32 v130,v130
MF v[64:67],a[236:239],a[188:191],v[64:67]
v_add_f32 v220,v122,v220
v_add_f32 v221,v123,v221
MF v[44:47],a[240:243],a[128:131],v[200:203]
v_exp_f32 v131,v131
CP v121,v122,v123
MF v[52:55],a[240:243],a[144:147],v[204:207]
v_exp_f32 v132,v132
MF v[60:63],a[240:243],a[160:163],v[208:211]
v_add_f32 v220,v124,v220
v_add_f32 v221,v125,v221
MF v[68:71],a[240:243],a[176:179],v[212:215]
v_exp_f32 v133,v133
CP v122,v124,v125
MF v[44:47],a[244:247],a[132:135],v[44:47]
v_exp_f32 v134,v134
MF v[52:55],a[244:247],a[148:151],v[52:55]
v_add_f32 v220,v126,v220
v_add_f32 v221,v127,v221
MF v[60:63],a[244:247],a[164:167],v[60:63]
v_exp_f32 v135,v135
CP v123,v126,v127
MF v[68:71],a[244:247],a[180:183],v[68:71]
v_add_f32 v222,v128,v222
v_add_f32 v223,v129,v223
MF v[44:47],a[248:251],a[136:139],v[44:47]
v_add_f32 v222,v130,v222
v_add_f32 v223,v131,v223
MF v[52:55],a[248:251],a[152:155],v[52:55]
v_add_f32 v222,v132,v222
v_add_f32 v223,v133,v223
MF v[60:63],a[248:251],a[168:171],v[60:63]
v_add_f32 v222,v134,v222
v_add_f32 v223,v135,v223
MF v[68:71],a[248:251],a[184:187],v[68:71]
v_max3_f32 v2,v216,v217,v218
v_max3_f32 v2,v2,v219,v220
MF v[44:47],a[252:255],a[140:143],v[44:47]
v_max3_f32 v2,v2,v221,v222
v_max_f32 v2,v2,v223
MF v[52:55],a[252:255],a[156:159],v[52:55]
v_cmp_nge_f32 vcc,0x5d800000,v2
CP v128,v128,v129
MF v[60:63],a[252:255],a[172:175],v[60:63]
CP v129,v130,v131
CP v130,v132,v133
MF v[68:71],a[252:255],a[188:191],v[68:71]
CP v131,v134,v135
v_add_u32 v228,s59,v231
s_cmp_eq_u64 vcc,0
s_cbranch_scc0 LSTUB4_%=
LSK4_%=:
s_waitcnt vmcnt(8) lgkmcnt(0)
s_barrier
MF a[0:3],v[136:139],v[72:75],a[0:3]
ds_read_b128 a[192:195],v228
v_add_f32 v216,v216,v217
v_add_f32 v218,v218,v219
MF a[32:35],v[136:139],v[80:83],a[32:35]
v_add_f32 v220,v220,v221
v_add_f32 v222,v222,v223
s_add_u32 m0,s74,s61
MF a[64:67],v[136:139],v[88:91],a[64:67]
buffer_load_dwordx4 v230,s[40:43],s72 offen lds
v_add_f32 v224,v224,v216
MF a[96:99],v[136:139],v[96:99],a[96:99]
v_add_f32 v225,v225,v218
v_add_f32 v226,v226,v220
v_add_f32 v227,v227,v222
MF a[4:7],v[144:147],v[72:75],a[4:7]
ds_read_b128 a[196:199],v228 offset:1024
s_lshl_b32 s77,s58,1
s_sub_u32 s77,s77,s75
MF a[36:39],v[144:147],v[80:83],a[36:39]
s_lshl_b32 s77,s77,6
v_add_u32 v234,s77,v232
v_add_u32 v235,s77,v233
MF a[68:71],v[144:147],v[88:91],a[68:71]
buffer_load_dwordx4 v230,s[40:43],s72 offen offset:1024 lds
v_cmp_gt_i32_e64 s[82:83],0,v234
MF a[100:103],v[144:147],v[96:99],a[100:103]
v_cmp_gt_i32_e64 s[84:85],1,v234
v_cndmask_b32_e64 v8,v8,v236,s[82:83]
v_cmp_gt_i32_e64 s[82:83],2,v234
MF a[8:11],v[152:155],v[72:75],a[8:11]
ds_read_b128 a[200:203],v228 offset:2048
v_cndmask_b32_e64 v9,v9,v236,s[84:85]
v_cmp_gt_i32_e64 s[84:85],3,v234
MF a[40:43],v[152:155],v[80:83],a[40:43]
v_cndmask_b32_e64 v10,v10,v236,s[82:83]
v_cmp_gt_i32_e64 s[82:83],16,v234
v_cndmask_b32_e64 v11,v11,v236,s[84:85]
MF a[72:75],v[152:155],v[88:91],a[72:75]
ds_read_b128 a[204:207],v228 offset:3072
v_cmp_gt_i32_e64 s[84:85],17,v234
v_cndmask_b32_e64 v12,v12,v236,s[82:83]
MF a[104:107],v[152:155],v[96:99],a[104:107]
v_cmp_gt_i32_e64 s[82:83],18,v234
v_cndmask_b32_e64 v13,v13,v236,s[84:85]
v_cmp_gt_i32_e64 s[84:85],19,v234
MF a[12:15],v[160:163],v[72:75],a[12:15]
buffer_load_dwordx4 v230,s[40:43],s72 offen offset:2048 lds
v_cndmask_b32_e64 v14,v14,v236,s[82:83]
MF a[44:47],v[160:163],v[80:83],a[44:47]
v_cndmask_b32_e64 v15,v15,v236,s[84:85]
v_exp_f32 v8,v8
MF a[76:79],v[160:163],v[88:91],a[76:79]
v_exp_f32 v9,v9
MF a[108:111],v[160:163],v[96:99],a[108:111]
ds_read_b128 a[208:211],v228 offset:4096
v_exp_f32 v10,v10
MF a[16:19],v[168:171],v[72:75],a[16:19]
v_exp_f32 v11,v11
MF a[48:51],v[168:171],v[80:83],a[48:51]
buffer_load_dwordx4 v230,s[40:43],s72 offen offset:3072 lds
MF a[80:83],v[168:171],v[88:91],a[80:83]
v_exp_f32 v12,v12
MF a[112:115],v[168:171],v[96:99],a[112:115]
ds_read_b128 a[212:215],v228 offset:5120
v_exp_f32 v13,v13
MF a[20:23],v[176:179],v[72:75],a[20:23]
v_exp_f32 v14,v14
MF a[52:55],v[176:179],v[80:83],a[52:55]
ds_read_b128 a[216:219],v228 offset:6144
v_exp_f32 v15,v15
MF a[84:87],v[176:179],v[88:91],a[84:87]
v_cmp_gt_i32_e64 s[82:83],0,v235
v_cmp_gt_i32_e64 s[84:85],1,v235
s_nop 0
v_cndmask_b32_e64 v16,v16,v236,s[82:83]
MF a[116:119],v[176:179],v[96:99],a[116:119]
v_cmp_gt_i32_e64 s[82:83],2,v235
v_cndmask_b32_e64 v17,v17,v236,s[84:85]
s_add_u32 s72,s72,16384
MF a[24:27],v[184:187],v[72:75],a[24:27]
v_cmp_gt_i32_e64 s[84:85],3,v235
v_cndmask_b32_e64 v18,v18,v236,s[82:83]
v_cmp_gt_i32_e64 s[82:83],16,v235
MF a[56:59],v[184:187],v[80:83],a[56:59]
ds_read_b128 a[220:223],v228 offset:7168
v_cndmask_b32_e64 v19,v19,v236,s[84:85]
v_cmp_gt_i32_e64 s[84:85],17,v235
MF a[88:91],v[184:187],v[88:91],a[88:91]
v_cndmask_b32_e64 v20,v20,v236,s[82:83]
v_cmp_gt_i32_e64 s[82:83],18,v235
v_cndmask_b32_e64 v21,v21,v236,s[84:85]
MF a[120:123],v[184:187],v[96:99],a[120:123]
v_cmp_gt_i32_e64 s[84:85],19,v235
v_cndmask_b32_e64 v22,v22,v236,s[82:83]
s_nop 0
v_cndmask_b32_e64 v23,v23,v236,s[84:85]
MF a[28:31],v[192:195],v[72:75],a[28:31]
ds_read_b128 a[224:227],v228 offset:8192
v_exp_f32 v16,v16
MF a[60:63],v[192:195],v[80:83],a[60:63]
v_add_f32 v216,v8,v10
s_add_u32 m0,s74,s62
MF a[92:95],v[192:195],v[88:91],a[92:95]
buffer_load_dwordx4 v230,s[44:47],s73 offen lds
v_add_f32 v217,v9,v11
MF a[124:127],v[192:195],v[96:99],a[124:127]
v_exp_f32 v17,v17
CP v8,v8,v9
MF a[0:3],v[140:143],v[104:107],a[0:3]
ds_read_b128 a[228:231],v228 offset:9216
v_exp_f32 v18,v18
MF a[32:35],v[140:143],v[112:115],a[32:35]
v_add_f32 v216,v12,v216
v_exp_f32 v19,v19
MF a[64:67],v[140:143],v[120:123],a[64:67]
buffer_load_dwordx4 v230,s[44:47],s73 offen offset:1024 lds
CP v9,v10,v11
MF a[96:99],v[140:143],v[128:131],a[96:99]
v_exp_f32 v20,v20
v_add_f32 v217,v13,v217
MF a[4:7],v[148:151],v[104:107],a[4:7]
ds_read_b128 a[232:235],v228 offset:10240
v_add_f32 v216,v14,v216
MF a[36:39],v[148:151],v[112:115],a[36:39]
v_exp_f32 v21,v21
CP v10,v12,v13
MF a[68:71],v[148:151],v[120:123],a[68:71]
ds_read_b128 a[236:239],v228 offset:11264
v_exp_f32 v22,v22
MF a[100:103],v[148:151],v[128:131],a[100:103]
v_add_f32 v217,v15,v217
v_exp_f32 v23,v23
MF a[8:11],v[156:159],v[104:107],a[8:11]
buffer_load_dwordx4 v230,s[44:47],s73 offen offset:2048 lds
CP v11,v14,v15
MF a[40:43],v[156:159],v[112:115],a[40:43]
v_exp_f32 v24,v24
v_add_f32 v218,v16,v18
MF a[72:75],v[156:159],v[120:123],a[72:75]
v_add_f32 v219,v17,v19
v_exp_f32 v25,v25
MF a[104:107],v[156:159],v[128:131],a[104:107]
ds_read_b128 a[240:243],v228 offset:12288
CP v16,v16,v17
MF a[12:15],v[164:167],v[104:107],a[12:15]
v_exp_f32 v26,v26
v_add_f32 v218,v20,v218
MF a[44:47],v[164:167],v[112:115],a[44:47]
buffer_load_dwordx4 v230,s[44:47],s73 offen offset:3072 lds
MF a[76:79],v[164:167],v[120:123],a[76:79]
v_exp_f32 v27,v27
CP v17,v18,v19
MF a[108:111],v[164:167],v[128:131],a[108:111]
ds_read_b128 a[244:247],v228 offset:13312
v_exp_f32 v28,v28
MF a[16:19],v[172:175],v[104:107],a[16:19]
v_add_f32 v219,v21,v219
v_add_f32 v218,v22,v218
MF a[48:51],v[172:175],v[112:115],a[48:51]
ds_read_b128 a[248:251],v228 offset:14336
v_exp_f32 v29,v29
MF a[80:83],v[172:175],v[120:123],a[80:83]
CP v18,v20,v21
v_exp_f32 v30,v30
MF a[112:115],v[172:175],v[128:131],a[112:115]
v_add_f32 v219,v23,v219
s_add_u32 s73,s73,16384
MF a[20:23],v[180:183],v[104:107],a[20:23]
v_exp_f32 v31,v31
CP v19,v22,v23
MF a[52:55],v[180:183],v[112:115],a[52:55]
ds_read_b128 a[252:255],v228 offset:15360
v_exp_f32 v32,v32
MF a[84:87],v[180:183],v[120:123],a[84:87]
v_add_f32 v220,v24,v26
v_add_f32 v221,v25,v27
MF a[116:119],v[180:183],v[128:131],a[116:119]
v_exp_f32 v33,v33
CP v24,v24,v25
MF a[24:27],v[188:191],v[104:107],a[24:27]
s_add_u32 s75,s75,1
s_add_u32 s59,s59,16384
MF a[56:59],v[188:191],v[112:115],a[56:59]
v_exp_f32 v34,v34
s_and_b32 s59,s59,0xffff
MF a[88:91],v[188:191],v[120:123],a[88:91]
v_add_f32 v220,v28,v220
s_add_u32 s61,s61,16384
s_and_b32 s61,s61,0xffff
MF a[120:123],v[188:191],v[128:131],a[120:123]
v_exp_f32 v35,v35
s_add_u32 s60,s60,16384
MF a[28:31],v[196:199],v[104:107],a[28:31]
CP v25,v26,v27
s_and_b32 s60,s60,0xffff
s_or_b32 s60,s60,65536
MF a[60:63],v[196:199],v[112:115],a[60:63]
v_exp_f32 v36,v36
s_add_u32 s62,s62,16384
MF a[92:95],v[196:199],v[120:123],a[92:95]
v_add_f32 v221,v29,v221
s_and_b32 s62,s62,0xffff
s_or_b32 s62,s62,65536
MF a[124:127],v[196:199],v[128:131],a[124:127]
v_add_f32 v220,v30,v220
v_add_u32 v229,s60,v231
v_exp_f32 v37,v37
CP v26,v28,v29
v_exp_f32 v38,v38
v_add_f32 v221,v31,v221
v_exp_f32 v39,v39
CP v27,v30,v31
v_add_f32 v222,v32,v34
v_add_f32 v223,v33,v35
CP v32,v32,v33
v_add_f32 v222,v36,v222
CP v33,v34,v35
v_add_f32 v223,v37,v223
v_add_f32 v222,v38,v222
CP v34,v36,v37
v_add_f32 v223,v39,v223
CP v35,v38,v39
s_waitcnt lgkmcnt(0)
MF v[72:75],a[192:195],a[128:131],v[200:203]
ds_read_b128 v[136:139],v229
s_lshl_b32 s77,s58,1
s_sub_u32 s77,s77,s75
MF v[80:83],a[192:195],a[144:147],v[204:207]
s_add_u32 s77,s77,1
s_lshl_b32 s77,s77,6
v_add_u32 v234,s77,v232
MF v[88:91],a[192:195],a[160:163],v[208:211]
v_add_u32 v235,s77,v233
v_cmp_gt_i32_e64 s[82:83],32,v234
v_cmp_gt_i32_e64 s[84:85],33,v234
MF v[96:99],a[192:195],a[176:179],v[212:215]
ds_read_b128 v[144:147],v229 offset:1024
v_cndmask_b32_e64 v40,v40,v236,s[82:83]
v_cmp_gt_i32_e64 s[82:83],34,v234
MF v[72:75],a[196:199],a[132:135],v[72:75]
v_cndmask_b32_e64 v41,v41,v236,s[84:85]
v_cmp_gt_i32_e64 s[84:85],35,v234
v_cndmask_b32_e64 v42,v42,v236,s[82:83]
MF v[80:83],a[196:199],a[148:151],v[80:83]
v_cmp_gt_i32_e64 s[82:83],48,v234
v_cndmask_b32_e64 v43,v43,v236,s[84:85]
v_cmp_gt_i32_e64 s[84:85],49,v234
MF v[88:91],a[196:199],a[164:167],v[88:91]
ds_read_b128 v[152:155],v229 offset:2048
v_cndmask_b32_e64 v44,v44,v236,s[82:83]
v_cmp_gt_i32_e64 s[82:83],50,v234
MF v[96:99],a[196:199],a[180:183],v[96:99]
v_cndmask_b32_e64 v45,v45,v236,s[84:85]
v_cmp_gt_i32_e64 s[84:85],51,v234
v_cndmask_b32_e64 v46,v46,v236,s[82:83]
MF v[72:75],a[200:203],a[136:139],v[72:75]
v_cndmask_b32_e64 v47,v47,v236,s[84:85]
v_exp_f32 v40,v40
MF v[80:83],a[200:203],a[152:155],v[80:83]
ds_read_b128 v[160:163],v229 offset:3072
v_exp_f32 v41,v41
MF v[88:91],a[200:203],a[168:171],v[88:91]
v_exp_f32 v42,v42
MF v[96:99],a[200:203],a[184:187],v[96:99]
v_exp_f32 v43,v43
MF v[72:75],a[204:207],a[140:143],v[72:75]
ds_read_b128 v[168:171],v229 offset:4096
v_exp_f32 v44,v44
MF v[80:83],a[204:207],a[156:159],v[80:83]
v_exp_f32 v45,v45
MF v[88:91],a[204:207],a[172:175],v[88:91]
v_exp_f32 v46,v46
MF v[96:99],a[204:207],a[188:191],v[96:99]
ds_read_b128 v[176:179],v229 offset:5120
v_exp_f32 v47,v47
MF v[76:79],a[208:211],a[128:131],v[200:203]
v_cmp_gt_i32_e64 s[82:83],32,v235
v_cmp_gt_i32_e64 s[84:85],33,v235
s_nop 0
v_cndmask_b32_e64 v48,v48,v236,s[82:83]
MF v[84:87],a[208:211],a[144:147],v[204:207]
v_cmp_gt_i32_e64 s[82:83],34,v235
v_cndmask_b32_e64 v49,v49,v236,s[84:85]
v_cmp_gt_i32_e64 s[84:85],35,v235
MF v[92:95],a[208:211],a[160:163],v[208:211]
ds_read_b128 v[184:187],v229 offset:6144
v_cndmask_b32_e64 v50,v50,v236,s[82:83]
v_cmp_gt_i32_e64 s[82:83],48,v235
MF v[100:103],a[208:211],a[176:179],v[212:215]
v_cndmask_b32_e64 v51,v51,v236,s[84:85]
v_cmp_gt_i32_e64 s[84:85],49,v235
v_cndmask_b32_e64 v52,v52,v236,s[82:83]
MF v[76:79],a[212:215],a[132:135],v[76:79]
v_cmp_gt_i32_e64 s[82:83],50,v235
v_cndmask_b32_e64 v53,v53,v236,s[84:85]
v_cmp_gt_i32_e64 s[84:85],51,v235
MF v[84:87],a[212:215],a[148:151],v[84:87]
ds_read_b128 v[192:195],v229 offset:7168
v_cndmask_b32_e64 v54,v54,v236,s[82:83]
v_cndmask_b32_e64 v55,v55,v236,s[84:85]
MF v[92:95],a[212:215],a[164:167],v[92:95]
v_exp_f32 v48,v48
v_add_f32 v216,v40,v216
MF v[100:103],a[212:215],a[180:183],v[100:103]
v_add_f32 v217,v41,v217
v_exp_f32 v49,v49
MF v[76:79],a[216:219],a[136:139],v[76:79]
ds_read_b128 v[140:143],v229 offset:8192
CP v40,v40,v41
MF v[84:87],a[216:219],a[152:155],v[84:87]
v_exp_f32 v50,v50
v_add_f32 v216,v42,v216
MF v[92:95],a[216:219],a[168:171],v[92:95]
v_add_f32 v217,v43,v217
v_exp_f32 v51,v51
MF v[100:103],a[216:219],a[184:187],v[100:103]
ds_read_b128 v[148:151],v229 offset:9216
)A"
R"A(CP v41,v42,v43
MF v[76:79],a[220:223],a[140:143],v[76:79]
v_exp_f32 v52,v52
v_add_f32 v216,v44,v216
MF v[84:87],a[220:223],a[156:159],v[84:87]
v_add_f32 v217,v45,v217
v_exp_f32 v53,v53
MF v[92:95],a[220:223],a[172:175],v[92:95]
ds_read_b128 v[156:159],v229 offset:10240
CP v42,v44,v45
MF v[100:103],a[220:223],a[188:191],v[100:103]
v_exp_f32 v54,v54
v_add_f32 v216,v46,v216
MF v[104:107],a[224:227],a[128:131],v[200:203]
v_add_f32 v217,v47,v217
v_exp_f32 v55,v55
MF v[112:115],a[224:227],a[144:147],v[204:207]
ds_read_b128 v[164:167],v229 offset:11264
CP v43,v46,v47
MF v[120:123],a[224:227],a[160:163],v[208:211]
v_exp_f32 v56,v56
v_add_f32 v218,v48,v218
MF v[128:131],a[224:227],a[176:179],v[212:215]
v_add_f32 v219,v49,v219
v_exp_f32 v57,v57
MF v[104:107],a[228:231],a[132:135],v[104:107]
ds_read_b128 v[172:175],v229 offset:12288
CP v48,v48,v49
MF v[112:115],a[228:231],a[148:151],v[112:115]
v_exp_f32 v58,v58
v_add_f32 v218,v50,v218
MF v[120:123],a[228:231],a[164:167],v[120:123]
v_add_f32 v219,v51,v219
v_exp_f32 v59,v59
MF v[128:131],a[228:231],a[180:183],v[128:131]
ds_read_b128 v[180:183],v229 offset:13312
CP v49,v50,v51
MF v[104:107],a[232:235],a[136:139],v[104:107]
v_exp_f32 v60,v60
v_add_f32 v218,v52,v218
MF v[112:115],a[232:235],a[152:155],v[112:115]
v_add_f32 v219,v53,v219
v_exp_f32 v61,v61
MF v[120:123],a[232:235],a[168:171],v[120:123]
ds_read_b128 v[188:191],v229 offset:14336
CP v50,v52,v53
MF v[128:131],a[232:235],a[184:187],v[128:131]
v_exp_f32 v62,v62
v_add_f32 v218,v54,v218
MF v[104:107],a[236:239],a[140:143],v[104:107]
v_add_f32 v219,v55,v219
v_exp_f32 v63,v63
MF v[112:115],a[236:239],a[156:159],v[112:115]
ds_read_b128 v[196:199],v229 offset:15360
CP v51,v54,v55
MF v[120:123],a[236:239],a[172:175],v[120:123]
v_exp_f32 v64,v64
v_add_f32 v220,v56,v220
MF v[128:131],a[236:239],a[188:191],v[128:131]
v_add_f32 v221,v57,v221
v_exp_f32 v65,v65
MF v[108:111],a[240:243],a[128:131],v[200:203]
CP v56,v56,v57
v_exp_f32 v66,v66
MF v[116:119],a[240:243],a[144:147],v[204:207]
v_add_f32 v220,v58,v220
v_add_f32 v221,v59,v221
MF v[124:127],a[240:243],a[160:163],v[208:211]
v_exp_f32 v67,v67
CP v57,v58,v59
MF v[132:135],a[240:243],a[176:179],v[212:215]
v_exp_f32 v68,v68
v_add_f32 v220,v60,v220
MF v[108:111],a[244:247],a[132:135],v[108:111]
v_add_f32 v221,v61,v221
v_exp_f32 v69,v69
MF v[116:119],a[244:247],a[148:151],v[116:119]
CP v58,v60,v61
v_exp_f32 v70,v70
MF v[124:127],a[244:247],a[164:167],v[124:127]
v_add_f32 v220,v62,v220
v_add_f32 v221,v63,v221
MF v[132:135],a[244:247],a[180:183],v[132:135]
v_exp_f32 v71,v71
CP v59,v62,v63
MF v[108:111],a[248:251],a[136:139],v[108:111]
v_add_f32 v222,v64,v222
v_add_f32 v223,v65,v223
v_add_f32 v222,v66,v222
MF v[116:119],a[248:251],a[152:155],v[116:119]
v_add_f32 v223,v67,v223
v_add_f32 v222,v68,v222
v_add_f32 v223,v69,v223
MF v[124:127],a[248:251],a[168:171],v[124:127]
v_add_f32 v222,v70,v222
v_add_f32 v223,v71,v223
MF v[132:135],a[248:251],a[184:187],v[132:135]
v_max3_f32 v2,v216,v217,v218
v_max3_f32 v2,v2,v219,v220
MF v[108:111],a[252:255],a[140:143],v[108:111]
v_max3_f32 v2,v2,v221,v222
v_max_f32 v2,v2,v223
MF v[116:119],a[252:255],a[156:159],v[116:119]
v_cmp_nge_f32 vcc,0x5d800000,v2
CP v64,v64,v65
MF v[124:127],a[252:255],a[172:175],v[124:127]
CP v65,v66,v67
CP v66,v68,v69
MF v[132:135],a[252:255],a[188:191],v[132:135]
CP v67,v70,v71
v_add_u32 v228,s59,v231
s_cmp_eq_u64 vcc,0
s_cbranch_scc0 LSTUB5_%=
LSK5_%=:
s_waitcnt vmcnt(8) lgkmcnt(0)
s_barrier
MF a[0:3],v[136:139],v[8:11],a[0:3]
ds_read_b128 a[192:195],v228
v_add_f32 v216,v216,v217
v_add_f32 v218,v218,v219
MF a[32:35],v[136:139],v[16:19],a[32:35]
v_add_f32 v220,v220,v221
v_add_f32 v222,v222,v223
s_add_u32 m0,s74,s61
MF a[64:67],v[136:139],v[24:27],a[64:67]
buffer_load_dwordx4 v230,s[40:43],s72 offen lds
v_add_f32 v224,v224,v216
MF a[96:99],v[136:139],v[32:35],a[96:99]
v_add_f32 v225,v225,v218
v_add_f32 v226,v226,v220
v_add_f32 v227,v227,v222
MF a[4:7],v[144:147],v[8:11],a[4:7]
ds_read_b128 a[196:199],v228 offset:1024
s_lshl_b32 s77,s58,1
s_sub_u32 s77,s77,s75
MF a[36:39],v[144:147],v[16:19],a[36:39]
s_lshl_b32 s77,s77,6
v_add_u32 v234,s77,v232
v_add_u32 v235,s77,v233
MF a[68:71],v[144:147],v[24:27],a[68:71]
buffer_load_dwordx4 v230,s[40:43],s72 offen offset:1024 lds
v_cmp_gt_i32_e64 s[82:83],0,v234
MF a[100:103],v[144:147],v[32:35],a[100:103]
v_cmp_gt_i32_e64 s[84:85],1,v234
v_cndmask_b32_e64 v72,v72,v236,s[82:83]
v_cmp_gt_i32_e64 s[82:83],2,v234
MF a[8:11],v[152:155],v[8:11],a[8:11]
ds_read_b128 a[200:203],v228 offset:2048
v_cndmask_b32_e64 v73,v73,v236,s[84:85]
v_cmp_gt_i32_e64 s[84:85],3,v234
MF a[40:43],v[152:155],v[16:19],a[40:43]
v_cndmask_b32_e64 v74,v74,v236,s[82:83]
v_cmp_gt_i32_e64 s[82:83],16,v234
v_cndmask_b32_e64 v75,v75,v236,s[84:85]
MF a[72:75],v[152:155],v[24:27],a[72:75]
ds_read_b128 a[204:207],v228 offset:3072
v_cmp_gt_i32_e64 s[84:85],17,v234
v_cndmask_b32_e64 v76,v76,v236,s[82:83]
MF a[104:107],v[152:155],v[32:35],a[104:107]
v_cmp_gt_i32_e64 s[82:83],18,v234
v_cndmask_b32_e64 v77,v77,v236,s[84:85]
v_cmp_gt_i32_e64 s[84:85],19,v234
MF a[12:15],v[160:163],v[8:11],a[12:15]
buffer_load_dwordx4 v230,s[40:43],s72 offen offset:2048 lds
v_cndmask_b32_e64 v78,v78,v236,s[82:83]
MF a[44:47],v[160:163],v[16:19],a[44:47]
v_cndmask_b32_e64 v79,v79,v236,s[84:85]
v_exp_f32 v72,v72
MF a[76:79],v[160:163],v[24:27],a[76:79]
v_exp_f32 v73,v73
MF a[108:111],v[160:163],v[32:35],a[108:111]
ds_read_b128 a[208:211],v228 offset:4096
v_exp_f32 v74,v74
MF a[16:19],v[168:171],v[8:11],a[16:19]
v_exp_f32 v75,v75
MF a[48:51],v[168:171],v[16:19],a[48:51]
buffer_load_dwordx4 v230,s[40:43],s72 offen offset:3072 lds
MF a[80:83],v[168:171],v[24:27],a[80:83]
v_exp_f32 v76,v76
MF a[112:115],v[168:171],v[32:35],a[112:115]
ds_read_b128 a[212:215],v228 offset:5120
v_exp_f32 v77,v77
MF a[20:23],v[176:179],v[8:11],a[20:23]
v_exp_f32 v78,v78
MF a[52:55],v[176:179],v[16:19],a[52:55]
ds_read_b128 a[216:219],v228 offset:6144
v_exp_f32 v79,v79
MF a[84:87],v[176:179],v[24:27],a[84:87]
v_cmp_gt_i32_e64 s[82:83],0,v235
v_cmp_gt_i32_e64 s[84:85],1,v235
s_nop 0
v_cndmask_b32_e64 v80,v80,v236,s[82:83]
MF a[116:119],v[176:179],v[32:35],a[116:119]
v_cmp_gt_i32_e64 s[82:83],2,v235
v_cndmask_b32_e64 v81,v81,v236,s[84:85]
s_add_u32 s72,s72,16384
MF a[24:27],v[184:187],v[8:11],a[24:27]
v_cmp_gt_i32_e64 s[84:85],3,v235
v_cndmask_b32_e64 v82,v82,v236,s[82:83]
v_cmp_gt_i32_e64 s[82:83],16,v235
MF a[56:59],v[184:187],v[16:19],a[56:59]
ds_read_b128 a[220:223],v228 offset:7168
v_cndmask_b32_e64 v83,v83,v236,s[84:85]
v_cmp_gt_i32_e64 s[84:85],17,v235
MF a[88:91],v[184:187],v[24:27],a[88:91]
v_cndmask_b32_e64 v84,v84,v236,s[82:83]
v_cmp_gt_i32_e64 s[82:83],18,v235
v_cndmask_b32_e64 v85,v85,v236,s[84:85]
MF a[120:123],v[184:187],v[32:35],a[120:123]
v_cmp_gt_i32_e64 s[84:85],19,v235
v_cndmask_b32_e64 v86,v86,v236,s[82:83]
s_nop 0
v_cndmask_b32_e64 v87,v87,v236,s[84:85]
MF a[28:31],v[192:195],v[8:11],a[28:31]
ds_read_b128 a[224:227],v228 offset:8192
v_exp_f32 v80,v80
MF a[60:63],v[192:195],v[16:19],a[60:63]
v_add_f32 v216,v72,v74
s_add_u32 m0,s74,s62
MF a[92:95],v[192:195],v[24:27],a[92:95]
buffer_load_dwordx4 v230,s[44:47],s73 offen lds
v_add_f32 v217,v73,v75
MF a[124:127],v[192:195],v[32:35],a[124:127]
v_exp_f32 v81,v81
CP v72,v72,v73
MF a[0:3],v[140:143],v[40:43],a[0:3]
ds_read_b128 a[228:231],v228 offset:9216
v_exp_f32 v82,v82
MF a[32:35],v[140:143],v[48:51],a[32:35]
v_add_f32 v216,v76,v216
v_exp_f32 v83,v83
MF a[64:67],v[140:143],v[56:59],a[64:67]
buffer_load_dwordx4 v230,s[44:47],s73 offen offset:1024 lds
CP v73,v74,v75
MF a[96:99],v[140:143],v[64:67],a[96:99]
v_exp_f32 v84,v84
v_add_f32 v217,v77,v217
MF a[4:7],v[148:151],v[40:43],a[4:7]
ds_read_b128 a[232:235],v228 offset:10240
v_add_f32 v216,v78,v216
MF a[36:39],v[148:151],v[48:51],a[36:39]
v_exp_f32 v85,v85
CP v74,v76,v77
MF a[68:71],v[148:151],v[56:59],a[68:71]
ds_read_b128 a[236:239],v228 offset:11264
v_exp_f32 v86,v86
MF a[100:103],v[148:151],v[64:67],a[100:103]
v_add_f32 v217,v79,v217
v_exp_f32 v87,v87
MF a[8:11],v[156:159],v[40:43],a[8:11]
buffer_load_dwordx4 v230,s[44:47],s73 offen offset:2048 lds
CP v75,v78,v79
MF a[40:43],v[156:159],v[48:51],a[40:43]
v_exp_f32 v88,v88
v_add_f32 v218,v80,v82
MF a[72:75],v[156:159],v[56:59],a[72:75]
v_add_f32 v219,v81,v83
v_exp_f32 v89,v89
MF a[104:107],v[156:159],v[64:67],a[104:107]
ds_read_b128 a[240:243],v228 offset:12288
CP v80,v80,v81
MF a[12:15],v[164:167],v[40:43],a[12:15]
v_exp_f32 v90,v90
v_add_f32 v218,v84,v218
MF a[44:47],v[164:167],v[48:51],a[44:47]
buffer_load_dwordx4 v230,s[44:47],s73 offen offset:3072 lds
MF a[76:79],v[164:167],v[56:59],a[76:79]
v_exp_f32 v91,v91
CP v81,v82,v83
MF a[108:111],v[164:167],v[64:67],a[108:111]
ds_read_b128 a[244:247],v228 offset:13312
v_exp_f32 v92,v92
MF a[16:19],v[172:175],v[40:43],a[16:19]
v_add_f32 v219,v85,v219
v_add_f32 v218,v86,v218
MF a[48:51],v[172:175],v[48:51],a[48:51]
ds_read_b128 a[248:251],v228 offset:14336
v_exp_f32 v93,v93
MF a[80:83],v[172:175],v[56:59],a[80:83]
CP v82,v84,v85
v_exp_f32 v94,v94
MF a[112:115],v[172:175],v[64:67],a[112:115]
v_add_f32 v219,v87,v219
s_add_u32 s73,s73,16384
MF a[20:23],v[180:183],v[40:43],a[20:23]
v_exp_f32 v95,v95
CP v83,v86,v87
MF a[52:55],v[180:183],v[48:51],a[52:55]
ds_read_b128 a[252:255],v228 offset:15360
v_exp_f32 v96,v96
MF a[84:87],v[180:183],v[56:59],a[84:87]
v_add_f32 v220,v88,v90
v_add_f32 v221,v89,v91
MF a[116:119],v[180:183],v[64:67],a[116:119]
v_exp_f32 v97,v97
CP v88,v88,v89
MF a[24:27],v[188:191],v[40:43],a[24:27]
s_add_u32 s75,s75,1
s_add_u32 s59,s59,16384
MF a[56:59],v[188:191],v[48:51],a[56:59]
v_exp_f32 v98,v98
s_and_b32 s59,s59,0xffff
MF a[88:91],v[188:191],v[56:59],a[88:91]
v_add_f32 v220,v92,v220
s_add_u32 s61,s61,16384
s_and_b32 s61,s61,0xffff
MF a[120:123],v[188:191],v[64:67],a[120:123]
v_exp_f32 v99,v99
s_add_u32 s60,s60,16384
MF a[28:31],v[196:199],v[40:43],a[28:31]
CP v89,v90,v91
s_and_b32 s60,s60,0xffff
s_or_b32 s60,s60,65536
MF a[60:63],v[196:199],v[48:51],a[60:63]
v_exp_f32 v100,v100
s_add_u32 s62,s62,16384
MF a[92:95],v[196:199],v[56:59],a[92:95]
v_add_f32 v221,v93,v221
s_and_b32 s62,s62,0xffff
s_or_b32 s62,s62,65536
MF a[124:127],v[196:199],v[64:67],a[124:127]
v_add_f32 v220,v94,v220
v_add_u32 v229,s60,v231
v_exp_f32 v101,v101
CP v90,v92,v93
v_exp_f32 v102,v102
v_add_f32 v221,v95,v221
v_exp_f32 v103,v103
CP v91,v94,v95
v_add_f32 v222,v96,v98
v_add_f32 v223,v97,v99
CP v96,v96,v97
v_add_f32 v222,v100,v222
CP v97,v98,v99
v_add_f32 v223,v101,v223
v_add_f32 v222,v102,v222
CP v98,v100,v101
v_add_f32 v223,v103,v223
CP v99,v102,v103
s_waitcnt lgkmcnt(0)
LTRANS_%=:
MF v[24:27],a[192:195],a[160:163],v[208:211]
ds_read_b128 v[136:139],v229
s_lshl_b32 s77,s58,1
s_sub_u32 s77,s77,s75
MF v[32:35],a[192:195],a[176:179],v[212:215]
ds_read_b128 v[144:147],v229 offset:1024
s_add_u32 s77,s77,1
s_lshl_b32 s77,s77,6
MF v[24:27],a[196:199],a[164:167],v[24:27]
v_add_u32 v234,s77,v232
v_add_u32 v235,s77,v233
v_cmp_gt_i32_e64 s[82:83],32,v234
MF v[32:35],a[196:199],a[180:183],v[32:35]
ds_read_b128 v[152:155],v229 offset:2048
v_cmp_gt_i32_e64 s[84:85],33,v234
v_cndmask_b32_e64 v104,v104,v236,s[82:83]
MF v[24:27],a[200:203],a[168:171],v[24:27]
ds_read_b128 v[160:163],v229 offset:3072
v_cmp_gt_i32_e64 s[82:83],34,v234
v_cndmask_b32_e64 v105,v105,v236,s[84:85]
MF v[32:35],a[200:203],a[184:187],v[32:35]
v_cmp_gt_i32_e64 s[84:85],35,v234
v_cndmask_b32_e64 v106,v106,v236,s[82:83]
v_cmp_gt_i32_e64 s[82:83],48,v234
MF v[24:27],a[204:207],a[172:175],v[24:27]
ds_read_b128 v[168:171],v229 offset:4096
v_cndmask_b32_e64 v107,v107,v236,s[84:85]
v_cmp_gt_i32_e64 s[84:85],49,v234
MF v[32:35],a[204:207],a[188:191],v[32:35]
ds_read_b128 v[176:179],v229 offset:5120
v_cndmask_b32_e64 v108,v108,v236,s[82:83]
v_cmp_gt_i32_e64 s[82:83],50,v234
MF v[28:31],a[208:211],a[160:163],v[208:211]
v_cndmask_b32_e64 v109,v109,v236,s[84:85]
v_cmp_gt_i32_e64 s[84:85],51,v234
v_cndmask_b32_e64 v110,v110,v236,s[82:83]
MF v[36:39],a[208:211],a[176:179],v[212:215]
ds_read_b128 v[184:187],v229 offset:6144
v_cndmask_b32_e64 v111,v111,v236,s[84:85]
v_exp_f32 v104,v104
v_exp_f32 v105,v105
v_exp_f32 v106,v106
v_exp_f32 v107,v107
v_exp_f32 v108,v108
MF v[28:31],a[212:215],a[164:167],v[28:31]
ds_read_b128 v[192:195],v229 offset:7168
v_exp_f32 v109,v109
MF v[36:39],a[212:215],a[180:183],v[36:39]
v_exp_f32 v110,v110
MF v[28:31],a[216:219],a[168:171],v[28:31]
ds_read_b128 v[140:143],v229 offset:8192
v_exp_f32 v111,v111
MF v[36:39],a[216:219],a[184:187],v[36:39]
ds_read_b128 v[148:151],v229 offset:9216
v_cmp_gt_i32_e64 s[82:83],32,v235
v_cmp_gt_i32_e64 s[84:85],33,v235
MF v[28:31],a[220:223],a[172:175],v[28:31]
v_cndmask_b32_e64 v112,v112,v236,s[82:83]
v_cmp_gt_i32_e64 s[82:83],34,v235
v_cndmask_b32_e64 v113,v113,v236,s[84:85]
MF v[36:39],a[220:223],a[188:191],v[36:39]
ds_read_b128 v[156:159],v229 offset:10240
v_cmp_gt_i32_e64 s[84:85],35,v235
v_cndmask_b32_e64 v114,v114,v236,s[82:83]
MF v[56:59],a[224:227],a[160:163],v[208:211]
ds_read_b128 v[164:167],v229 offset:11264
v_cmp_gt_i32_e64 s[82:83],48,v235
v_cndmask_b32_e64 v115,v115,v236,s[84:85]
MF v[64:67],a[224:227],a[176:179],v[212:215]
v_cmp_gt_i32_e64 s[84:85],49,v235
v_cndmask_b32_e64 v116,v116,v236,s[82:83]
v_cmp_gt_i32_e64 s[82:83],50,v235
MF v[56:59],a[228:231],a[164:167],v[56:59]
ds_read_b128 v[172:175],v229 offset:12288
v_cndmask_b32_e64 v117,v117,v236,s[84:85]
v_cmp_gt_i32_e64 s[84:85],51,v235
MF v[64:67],a[228:231],a[180:183],v[64:67]
ds_read_b128 v[180:183],v229 offset:13312
v_cndmask_b32_e64 v118,v118,v236,s[82:83]
v_cndmask_b32_e64 v119,v119,v236,s[84:85]
MF v[56:59],a[232:235],a[168:171],v[56:59]
v_exp_f32 v112,v112
v_add_f32 v216,v104,v216
MF v[64:67],a[232:235],a[184:187],v[64:67]
ds_read_b128 v[188:191],v229 offset:14336
v_add_f32 v217,v105,v217
MF v[56:59],a[236:239],a[172:175],v[56:59]
ds_read_b128 v[196:199],v229 offset:15360
v_exp_f32 v113,v113
MF v[64:67],a[236:239],a[188:191],v[64:67]
CP v104,v104,v105
v_exp_f32 v114,v114
MF v[60:63],a[240:243],a[160:163],v[208:211]
v_add_f32 v216,v106,v216
v_add_f32 v217,v107,v217
MF v[68:71],a[240:243],a[176:179],v[212:215]
v_exp_f32 v115,v115
CP v105,v106,v107
MF v[60:63],a[244:247],a[164:167],v[60:63]
v_exp_f32 v116,v116
v_add_f32 v216,v108,v216
MF v[68:71],a[244:247],a[180:183],v[68:71]
v_add_f32 v217,v109,v217
v_exp_f32 v117,v117
MF v[60:63],a[248:251],a[168:171],v[60:63]
CP v106,v108,v109
v_exp_f32 v118,v118
MF v[68:71],a[248:251],a[184:187],v[68:71]
v_add_f32 v216,v110,v216
v_add_f32 v217,v111,v217
MF v[60:63],a[252:255],a[172:175],v[60:63]
v_exp_f32 v119,v119
CP v107,v110,v111
MF v[68:71],a[252:255],a[188:191],v[68:71]
v_exp_f32 v120,v120
v_add_f32 v218,v112,v218
v_add_f32 v219,v113,v219
v_exp_f32 v121,v121
CP v112,v112,v113
v_exp_f32 v122,v122
v_add_f32 v218,v114,v218
v_add_f32 v219,v115,v219
v_exp_f32 v123,v123
CP v113,v114,v115
v_exp_f32 v124,v124
v_add_f32 v218,v116,v218
v_add_f32 v219,v117,v219
v_exp_f32 v125,v125
CP v114,v116,v117
v_exp_f32 v126,v126
v_add_f32 v218,v118,v218
v_add_f32 v219,v119,v219
v_exp_f32 v127,v127
CP v115,v118,v119
v_exp_f32 v128,v128
v_add_f32 v220,v120,v220
v_add_f32 v221,v121,v221
v_exp_f32 v129,v129
CP v120,v120,v121
v_exp_f32 v130,v130
v_add_f32 v220,v122,v220
v_add_f32 v221,v123,v221
v_exp_f32 v131,v131
CP v121,v122,v123
v_exp_f32 v132,v132
v_add_f32 v220,v124,v220
v_add_f32 v221,v125,v221
v_exp_f32 v133,v133
CP v122,v124,v125
v_exp_f32 v134,v134
v_add_f32 v220,v126,v220
v_add_f32 v221,v127,v221
v_exp_f32 v135,v135
CP v123,v126,v127
v_add_f32 v222,v128,v222
v_add_f32 v223,v129,v223
v_add_f32 v222,v130,v222
v_add_f32 v223,v131,v223
v_add_f32 v222,v132,v222
v_add_f32 v223,v133,v223
v_add_f32 v222,v134,v222
v_add_f32 v223,v135,v223
v_max3_f32 v2,v216,v217,v218
v_max3_f32 v2,v2,v219,v220
v_max3_f32 v2,v2,v221,v222
v_max_f32 v2,v2,v223
v_cmp_nge_f32 vcc,0x5d800000,v2
CP v128,v128,v129
CP v129,v130,v131
CP v130,v132,v133
CP v131,v134,v135
v_add_u32 v228,s59,v231
s_cmp_eq_u64 vcc,0
s_cbranch_scc0 LSTUB6_%=
LSK6_%=:
s_waitcnt vmcnt(8) lgkmcnt(0)
s_barrier
v_min_u32 v252,40959,v251
v_lshlrev_b32 v252,7,v252
s_lshl_b32 s100,s56,8
MF a[0:3],v[136:139],v[72:75],a[0:3]
ds_read_b128 a[192:195],v228
v_add_f32 v216,v216,v217
MF a[32:35],v[136:139],v[80:83],a[32:35]
v_add_f32 v218,v218,v219
v_add_f32 v220,v220,v221
s_add_u32 m0,s74,s61
MF a[64:67],v[136:139],v[88:91],a[64:67]
buffer_load_dwordx4 v230,s[40:43],s72 offen lds
MF a[96:99],v[136:139],v[96:99],a[96:99]
v_add_f32 v222,v222,v223
v_add_f32 v224,v224,v216
MF a[4:7],v[144:147],v[72:75],a[4:7]
ds_read_b128 a[196:199],v228 offset:1024
v_add_f32 v225,v225,v218
MF a[36:39],v[144:147],v[80:83],a[36:39]
v_add_f32 v226,v226,v220
v_add_f32 v227,v227,v222
MF a[68:71],v[144:147],v[88:91],a[68:71]
buffer_load_dwordx4 v230,s[40:43],s72 offen offset:1024 lds
MF a[100:103],v[144:147],v[96:99],a[100:103]
s_sub_u32 s77,30,s58
s_sub_u32 s77,s77,s58
MF a[8:11],v[152:155],v[72:75],a[8:11]
ds_read_b128 a[200:203],v228 offset:2048
s_sub_u32 s77,s77,s75
MF a[40:43],v[152:155],v[80:83],a[40:43]
s_lshl_b32 s77,s77,6
v_add_u32 v234,s77,v232
MF a[72:75],v[152:155],v[88:91],a[72:75]
buffer_load_dwordx4 v230,s[40:43],s72 offen offset:2048 lds
MF a[104:107],v[152:155],v[96:99],a[104:107]
v_add_u32 v235,s77,v233
v_cmp_gt_i32_e64 s[82:83],0,v234
MF a[12:15],v[160:163],v[72:75],a[12:15]
ds_read_b128 a[204:207],v228 offset:3072
v_cmp_gt_i32_e64 s[84:85],1,v234
MF a[44:47],v[160:163],v[80:83],a[44:47]
v_cndmask_b32_e64 v24,v24,v236,s[82:83]
v_cmp_gt_i32_e64 s[82:83],2,v234
MF a[76:79],v[160:163],v[88:91],a[76:79]
ds_read_b128 a[208:211],v228 offset:4096
v_cndmask_b32_e64 v25,v25,v236,s[84:85]
MF a[108:111],v[160:163],v[96:99],a[108:111]
v_cmp_gt_i32_e64 s[84:85],3,v234
v_cndmask_b32_e64 v26,v26,v236,s[82:83]
MF a[16:19],v[168:171],v[72:75],a[16:19]
buffer_load_dwordx4 v230,s[40:43],s72 offen offset:3072 lds
MF a[48:51],v[168:171],v[80:83],a[48:51]
v_cmp_gt_i32_e64 s[82:83],16,v234
v_cndmask_b32_e64 v27,v27,v236,s[84:85]
MF a[80:83],v[168:171],v[88:91],a[80:83]
ds_read_b128 a[212:215],v228 offset:5120
v_cmp_gt_i32_e64 s[84:85],17,v234
MF a[112:115],v[168:171],v[96:99],a[112:115]
v_cndmask_b32_e64 v28,v28,v236,s[82:83]
v_cmp_gt_i32_e64 s[82:83],18,v234
MF a[20:23],v[176:179],v[72:75],a[20:23]
v_cndmask_b32_e64 v29,v29,v236,s[84:85]
s_add_u32 s72,s72,16384
MF a[52:55],v[176:179],v[80:83],a[52:55]
v_cmp_gt_i32_e64 s[84:85],19,v234
v_cndmask_b32_e64 v30,v30,v236,s[82:83]
s_nop 0
v_cndmask_b32_e64 v31,v31,v236,s[84:85]
MF a[84:87],v[176:179],v[88:91],a[84:87]
ds_read_b128 a[216:219],v228 offset:6144
v_exp_f32 v24,v24
MF a[116:119],v[176:179],v[96:99],a[116:119]
v_exp_f32 v25,v25
MF a[24:27],v[184:187],v[72:75],a[24:27]
ds_read_b128 a[220:223],v228 offset:7168
v_exp_f32 v26,v26
MF a[56:59],v[184:187],v[80:83],a[56:59]
v_exp_f32 v27,v27
s_add_u32 m0,s74,s62
MF a[88:91],v[184:187],v[88:91],a[88:91]
buffer_load_dwordx4 v230,s[44:47],s73 offen lds
MF a[120:123],v[184:187],v[96:99],a[120:123]
v_exp_f32 v28,v28
MF a[28:31],v[192:195],v[72:75],a[28:31]
v_exp_f32 v29,v29
MF a[60:63],v[192:195],v[80:83],a[60:63]
ds_read_b128 a[224:227],v228 offset:8192
v_exp_f32 v30,v30
MF a[92:95],v[192:195],v[88:91],a[92:95]
v_exp_f32 v31,v31
v_cmp_gt_i32_e64 s[82:83],0,v235
MF a[124:127],v[192:195],v[96:99],a[124:127]
buffer_load_dwordx4 v230,s[44:47],s73 offen offset:1024 lds
v_cmp_gt_i32_e64 s[84:85],1,v235
MF a[0:3],v[140:143],v[104:107],a[0:3]
v_cndmask_b32_e64 v32,v32,v236,s[82:83]
v_cmp_gt_i32_e64 s[82:83],2,v235
MF a[32:35],v[140:143],v[112:115],a[32:35]
ds_read_b128 a[228:231],v228 offset:9216
v_cndmask_b32_e64 v33,v33,v236,s[84:85]
MF a[64:67],v[140:143],v[120:123],a[64:67]
v_cmp_gt_i32_e64 s[84:85],3,v235
v_cndmask_b32_e64 v34,v34,v236,s[82:83]
MF a[96:99],v[140:143],v[128:131],a[96:99]
ds_read_b128 a[232:235],v228 offset:10240
v_cmp_gt_i32_e64 s[82:83],16,v235
MF a[4:7],v[148:151],v[104:107],a[4:7]
v_cndmask_b32_e64 v35,v35,v236,s[84:85]
v_cmp_gt_i32_e64 s[84:85],17,v235
MF a[36:39],v[148:151],v[112:115],a[36:39]
buffer_load_dwordx4 v230,s[44:47],s73 offen offset:2048 lds
MF a[68:71],v[148:151],v[120:123],a[68:71]
v_cndmask_b32_e64 v36,v36,v236,s[82:83]
v_cmp_gt_i32_e64 s[82:83],18,v235
MF a[100:103],v[148:151],v[128:131],a[100:103]
ds_read_b128 a[236:239],v228 offset:11264
v_cndmask_b32_e64 v37,v37,v236,s[84:85]
MF a[8:11],v[156:159],v[104:107],a[8:11]
v_cmp_gt_i32_e64 s[84:85],19,v235
v_cndmask_b32_e64 v38,v38,v236,s[82:83]
MF a[40:43],v[156:159],v[112:115],a[40:43]
buffer_load_dwordx4 v230,s[44:47],s73 offen offset:3072 lds
v_cndmask_b32_e64 v39,v39,v236,s[84:85]
MF a[72:75],v[156:159],v[120:123],a[72:75]
v_exp_f32 v32,v32
MF a[104:107],v[156:159],v[128:131],a[104:107]
ds_read_b128 a[240:243],v228 offset:12288
v_add_f32 v220,v24,v26
MF a[12:15],v[164:167],v[104:107],a[12:15]
v_add_f32 v221,v25,v27
v_exp_f32 v33,v33
MF a[44:47],v[164:167],v[112:115],a[44:47]
ds_read_b128 a[244:247],v228 offset:13312
CP v24,v24,v25
MF a[76:79],v[164:167],v[120:123],a[76:79]
v_exp_f32 v34,v34
MF a[108:111],v[164:167],v[128:131],a[108:111]
v_add_f32 v220,v28,v220
s_add_u32 s73,s73,16384
MF a[16:19],v[172:175],v[104:107],a[16:19]
v_exp_f32 v35,v35
CP v25,v26,v27
MF a[48:51],v[172:175],v[112:115],a[48:51]
ds_read_b128 a[248:251],v228 offset:14336
v_exp_f32 v36,v36
MF a[80:83],v[172:175],v[120:123],a[80:83]
v_add_f32 v221,v29,v221
s_add_u32 m0,s100,147456
MF a[112:115],v[172:175],v[128:131],a[112:115]
global_load_lds_dword v252,s[10:11]
v_add_f32 v220,v30,v220
MF a[20:23],v[180:183],v[104:107],a[20:23]
v_exp_f32 v37,v37
CP v26,v28,v29
MF a[52:55],v[180:183],v[112:115],a[52:55]
ds_read_b128 a[252:255],v228 offset:15360
v_exp_f32 v38,v38
MF a[84:87],v[180:183],v[120:123],a[84:87]
v_add_f32 v221,v31,v221
v_exp_f32 v39,v39
MF a[116:119],v[180:183],v[128:131],a[116:119]
CP v27,v30,v31
v_add_f32 v222,v32,v34
MF a[24:27],v[188:191],v[104:107],a[24:27]
s_add_u32 s75,s75,1
s_add_u32 s59,s59,16384
MF a[56:59],v[188:191],v[112:115],a[56:59]
v_add_f32 v223,v33,v35
s_and_b32 s59,s59,0xffff
MF a[88:91],v[188:191],v[120:123],a[88:91]
CP v32,v32,v33
s_add_u32 s61,s61,16384
s_and_b32 s61,s61,0xffff
MF a[120:123],v[188:191],v[128:131],a[120:123]
v_add_f32 v222,v36,v222
CP v33,v34,v35
s_add_u32 s60,s60,16384
MF a[28:31],v[196:199],v[104:107],a[28:31]
v_add_f32 v223,v37,v223
s_and_b32 s60,s60,0xffff
s_or_b32 s60,s60,65536
MF a[60:63],v[196:199],v[112:115],a[60:63]
v_add_f32 v222,v38,v222
CP v34,v36,v37
s_add_u32 s62,s62,16384
MF a[92:95],v[196:199],v[120:123],a[92:95]
v_add_f32 v223,v39,v223
s_and_b32 s62,s62,0xffff
s_or_b32 s62,s62,65536
MF a[124:127],v[196:199],v[128:131],a[124:127]
CP v35,v38,v39
v_add_u32 v229,s60,v231
s_waitcnt lgkmcnt(0)
s_nop 7
s_lshl_b32 s100,s58,3
s_lshl_b32 s101,s56,1
s_add_u32 s100,s100,s101
s_mul_i32 s100,s100,159744
s_lshl_b32 s101,s57,12
s_add_u32 s78,s100,s101
v_mov_b32 v76,v224
v_mov_b32 v78,v224
s_nop 1
v_permlane32_swap_b32 v76,v78
v_add_f32 v76,v76,v78
v_mov_b32 v78,v76
s_nop 1
v_permlane16_swap_b32 v76,v78
v_add_f32 v76,v76,v78
v_rcp_f32 v76,v76
v_mov_b32 v77,v225
v_mov_b32 v79,v225
s_nop 1
v_permlane32_swap_b32 v77,v79
v_add_f32 v77,v77,v79
v_mov_b32 v79,v77
s_nop 1
v_permlane16_swap_b32 v77,v79
v_add_f32 v77,v77,v79
v_rcp_f32 v77,v77
AR v8,a0
AR v9,a1
AR v10,a2
AR v11,a3
AR v12,a4
AR v13,a5
AR v14,a6
AR v15,a7
v_mul_f32 v8,v8,v76
v_mul_f32 v9,v9,v76
v_mul_f32 v10,v10,v76
v_mul_f32 v11,v11,v76
v_mul_f32 v12,v12,v76
v_mul_f32 v13,v13,v76
v_mul_f32 v14,v14,v76
v_mul_f32 v15,v15,v76
CP v72,v8,v9
CP v73,v10,v11
CP v74,v12,v13
CP v75,v14,v15
buffer_store_dwordx4 v[72:75],v231,s[48:51],s78 offen
AR v16,a8
AR v17,a9
AR v18,a10
AR v19,a11
AR v20,a12
AR v21,a13
AR v22,a14
AR v23,a15
v_mul_f32 v16,v16,v76
v_mul_f32 v17,v17,v76
v_mul_f32 v18,v18,v76
v_mul_f32 v19,v19,v76
v_mul_f32 v20,v20,v76
v_mul_f32 v21,v21,v76
v_mul_f32 v22,v22,v76
v_mul_f32 v23,v23,v76
CP v80,v16,v17
CP v81,v18,v19
CP v82,v20,v21
CP v83,v22,v23
buffer_store_dwordx4 v[80:83],v231,s[48:51],s78 offen offset:1024
AR v40,a16
AR v41,a17
AR v42,a18
AR v43,a19
AR v44,a20
AR v45,a21
AR v46,a22
AR v47,a23
v_mul_f32 v40,v40,v76
v_mul_f32 v41,v41,v76
v_mul_f32 v42,v42,v76
v_mul_f32 v43,v43,v76
v_mul_f32 v44,v44,v76
v_mul_f32 v45,v45,v76
v_mul_f32 v46,v46,v76
v_mul_f32 v47,v47,v76
CP v104,v40,v41
CP v105,v42,v43
CP v106,v44,v45
CP v107,v46,v47
buffer_store_dwordx4 v[104:107],v231,s[48:51],s78 offen offset:2048
AR v48,a24
AR v49,a25
AR v50,a26
AR v51,a27
AR v52,a28
AR v53,a29
AR v54,a30
AR v55,a31
v_mul_f32 v48,v48,v76
v_mul_f32 v49,v49,v76
v_mul_f32 v50,v50,v76
v_mul_f32 v51,v51,v76
v_mul_f32 v52,v52,v76
v_mul_f32 v53,v53,v76
v_mul_f32 v54,v54,v76
v_mul_f32 v55,v55,v76
CP v112,v48,v49
CP v113,v50,v51
CP v114,v52,v53
CP v115,v54,v55
buffer_store_dwordx4 v[112:115],v231,s[48:51],s78 offen offset:3072
s_add_u32 s78,s78,159744
AR v8,a32
AR v9,a33
AR v10,a34
AR v11,a35
AR v12,a36
AR v13,a37
AR v14,a38
AR v15,a39
v_mul_f32 v8,v8,v77
v_mul_f32 v9,v9,v77
v_mul_f32 v10,v10,v77
v_mul_f32 v11,v11,v77
v_mul_f32 v12,v12,v77
v_mul_f32 v13,v13,v77
v_mul_f32 v14,v14,v77
v_mul_f32 v15,v15,v77
CP v72,v8,v9
CP v73,v10,v11
CP v74,v12,v13
CP v75,v14,v15
buffer_store_dwordx4 v[72:75],v231,s[48:51],s78 offen
AR v16,a40
AR v17,a41
AR v18,a42
AR v19,a43
AR v20,a44
AR v21,a45
AR v22,a46
AR v23,a47
v_mul_f32 v16,v16,v77
v_mul_f32 v17,v17,v77
v_mul_f32 v18,v18,v77
v_mul_f32 v19,v19,v77
v_mul_f32 v20,v20,v77
v_mul_f32 v21,v21,v77
v_mul_f32 v22,v22,v77
v_mul_f32 v23,v23,v77
CP v80,v16,v17
CP v81,v18,v19
CP v82,v20,v21
CP v83,v22,v23
buffer_store_dwordx4 v[80:83],v231,s[48:51],s78 offen offset:1024
AR v40,a48
AR v41,a49
AR v42,a50
AR v43,a51
AR v44,a52
AR v45,a53
AR v46,a54
AR v47,a55
v_mul_f32 v40,v40,v77
v_mul_f32 v41,v41,v77
v_mul_f32 v42,v42,v77
v_mul_f32 v43,v43,v77
v_mul_f32 v44,v44,v77
v_mul_f32 v45,v45,v77
v_mul_f32 v46,v46,v77
v_mul_f32 v47,v47,v77
CP v104,v40,v41
CP v105,v42,v43
CP v106,v44,v45
CP v107,v46,v47
buffer_store_dwordx4 v[104:107],v231,s[48:51],s78 offen offset:2048
AR v48,a56
AR v49,a57
AR v50,a58
AR v51,a59
AR v52,a60
AR v53,a61
AR v54,a62
AR v55,a63
v_mul_f32 v48,v48,v77
v_mul_f32 v49,v49,v77
v_mul_f32 v50,v50,v77
v_mul_f32 v51,v51,v77
v_mul_f32 v52,v52,v77
v_mul_f32 v53,v53,v77
v_mul_f32 v54,v54,v77
v_mul_f32 v55,v55,v77
CP v112,v48,v49
CP v113,v50,v51
CP v114,v52,v53
CP v115,v54,v55
buffer_store_dwordx4 v[112:115],v231,s[48:51],s78 offen offset:3072
MF v[88:91],a[192:195],a[160:163],v[208:211]
ds_read_b128 v[136:139],v229
s_sub_u32 s77,30,s58
s_sub_u32 s77,s77,s58
MF v[96:99],a[192:195],a[176:179],v[212:215]
ds_read_b128 v[144:147],v229 offset:1024
s_sub_u32 s77,s77,s75
s_add_u32 s77,s77,1
MF v[88:91],a[196:199],a[164:167],v[88:91]
s_lshl_b32 s77,s77,6
v_add_u32 v234,s77,v232
v_add_u32 v235,s77,v233
MF v[96:99],a[196:199],a[180:183],v[96:99]
ds_read_b128 v[152:155],v229 offset:2048
v_cmp_gt_i32_e64 s[82:83],32,v234
v_cmp_gt_i32_e64 s[84:85],33,v234
MF v[88:91],a[200:203],a[168:171],v[88:91]
ds_read_b128 v[160:163],v229 offset:3072
v_cndmask_b32_e64 v56,v56,v236,s[82:83]
v_cmp_gt_i32_e64 s[82:83],34,v234
MF v[96:99],a[200:203],a[184:187],v[96:99]
v_cndmask_b32_e64 v57,v57,v236,s[84:85]
v_cmp_gt_i32_e64 s[84:85],35,v234
v_cndmask_b32_e64 v58,v58,v236,s[82:83]
MF v[88:91],a[204:207],a[172:175],v[88:91]
ds_read_b128 v[168:171],v229 offset:4096
v_cmp_gt_i32_e64 s[82:83],48,v234
v_cndmask_b32_e64 v59,v59,v236,s[84:85]
MF v[96:99],a[204:207],a[188:191],v[96:99]
ds_read_b128 v[176:179],v229 offset:5120
v_cmp_gt_i32_e64 s[84:85],49,v234
v_cndmask_b32_e64 v60,v60,v236,s[82:83]
MF v[92:95],a[208:211],a[160:163],v[208:211]
v_cmp_gt_i32_e64 s[82:83],50,v234
v_cndmask_b32_e64 v61,v61,v236,s[84:85]
v_cmp_gt_i32_e64 s[84:85],51,v234
MF v[100:103],a[208:211],a[176:179],v[212:215]
ds_read_b128 v[184:187],v229 offset:6144
v_cndmask_b32_e64 v62,v62,v236,s[82:83]
v_cndmask_b32_e64 v63,v63,v236,s[84:85]
MF v[92:95],a[212:215],a[164:167],v[92:95]
ds_read_b128 v[192:195],v229 offset:7168
v_exp_f32 v56,v56
v_exp_f32 v57,v57
v_exp_f32 v58,v58
v_exp_f32 v59,v59
v_exp_f32 v60,v60
v_exp_f32 v61,v61
MF v[100:103],a[212:215],a[180:183],v[100:103]
v_exp_f32 v62,v62
MF v[92:95],a[216:219],a[168:171],v[92:95]
ds_read_b128 v[140:143],v229 offset:8192
v_exp_f32 v63,v63
MF v[100:103],a[216:219],a[184:187],v[100:103]
ds_read_b128 v[148:151],v229 offset:9216
v_cmp_gt_i32_e64 s[82:83],32,v235
v_cmp_gt_i32_e64 s[84:85],33,v235
MF v[92:95],a[220:223],a[172:175],v[92:95]
v_cndmask_b32_e64 v64,v64,v236,s[82:83]
v_cmp_gt_i32_e64 s[82:83],34,v235
v_cndmask_b32_e64 v65,v65,v236,s[84:85]
MF v[100:103],a[220:223],a[188:191],v[100:103]
ds_read_b128 v[156:159],v229 offset:10240
v_cmp_gt_i32_e64 s[84:85],35,v235
v_cndmask_b32_e64 v66,v66,v236,s[82:83]
MF v[120:123],a[224:227],a[160:163],v[208:211]
ds_read_b128 v[164:167],v229 offset:11264
v_cmp_gt_i32_e64 s[82:83],48,v235
v_cndmask_b32_e64 v67,v67,v236,s[84:85]
MF v[128:131],a[224:227],a[176:179],v[212:215]
v_cmp_gt_i32_e64 s[84:85],49,v235
v_cndmask_b32_e64 v68,v68,v236,s[82:83]
v_cmp_gt_i32_e64 s[82:83],50,v235
MF v[120:123],a[228:231],a[164:167],v[120:123]
ds_read_b128 v[172:175],v229 offset:12288
v_cndmask_b32_e64 v69,v69,v236,s[84:85]
v_cmp_gt_i32_e64 s[84:85],51,v235
MF v[128:131],a[228:231],a[180:183],v[128:131]
ds_read_b128 v[180:183],v229 offset:13312
v_cndmask_b32_e64 v70,v70,v236,s[82:83]
v_cndmask_b32_e64 v71,v71,v236,s[84:85]
MF v[120:123],a[232:235],a[168:171],v[120:123]
v_exp_f32 v64,v64
v_add_f32 v220,v56,v220
MF v[128:131],a[232:235],a[184:187],v[128:131]
ds_read_b128 v[188:191],v229 offset:14336
v_add_f32 v221,v57,v221
MF v[120:123],a[236:239],a[172:175],v[120:123]
ds_read_b128 v[196:199],v229 offset:15360
v_exp_f32 v65,v65
MF v[128:131],a[236:239],a[188:191],v[128:131]
CP v56,v56,v57
v_exp_f32 v66,v66
MF v[124:127],a[240:243],a[160:163],v[208:211]
v_add_f32 v220,v58,v220
v_add_f32 v221,v59,v221
MF v[132:135],a[240:243],a[176:179],v[212:215]
v_exp_f32 v67,v67
CP v57,v58,v59
MF v[124:127],a[244:247],a[164:167],v[124:127]
v_exp_f32 v68,v68
v_add_f32 v220,v60,v220
MF v[132:135],a[244:247],a[180:183],v[132:135]
v_add_f32 v221,v61,v221
v_exp_f32 v69,v69
MF v[124:127],a[248:251],a[168:171],v[124:127]
CP v58,v60,v61
v_exp_f32 v70,v70
MF v[132:135],a[248:251],a[184:187],v[132:135]
v_add_f32 v220,v62,v220
v_add_f32 v221,v63,v221
MF v[124:127],a[252:255],a[172:175],v[124:127]
)A"
R"A(v_exp_f32 v71,v71
CP v59,v62,v63
MF v[132:135],a[252:255],a[188:191],v[132:135]
v_add_f32 v222,v64,v222
v_add_f32 v223,v65,v223
v_add_f32 v222,v66,v222
v_add_f32 v223,v67,v223
v_add_f32 v222,v68,v222
v_add_f32 v223,v69,v223
v_add_f32 v222,v70,v222
v_add_f32 v223,v71,v223
v_max3_f32 v2,v220,v221,v222
v_max_f32 v2,v2,v223
v_cmp_nge_f32 vcc,0x5d800000,v2
CP v64,v64,v65
CP v65,v66,v67
CP v66,v68,v69
CP v67,v70,v71
v_add_u32 v228,s59,v231
s_cmp_eq_u64 vcc,0
s_cbranch_scc0 LSTUB7_%=
LSK7_%=:
s_waitcnt vmcnt(9) lgkmcnt(0)
s_barrier
v_min_u32 v252,8639,v251
v_lshlrev_b32 v252,7,v252
s_lshl_b32 s100,s56,8
MF a[64:67],v[136:139],v[24:27],a[64:67]
ds_read_b128 a[192:195],v228
v_add_f32 v220,v220,v221
s_add_u32 m0,s74,s61
MF a[96:99],v[136:139],v[32:35],a[96:99]
buffer_load_dwordx4 v230,s[40:43],s72 offen lds
v_add_f32 v222,v222,v223
v_add_f32 v226,v226,v220
v_add_f32 v227,v227,v222
s_sub_u32 s77,30,s58
s_sub_u32 s77,s77,s58
s_sub_u32 s77,s77,s75
MF a[68:71],v[144:147],v[24:27],a[68:71]
ds_read_b128 a[196:199],v228 offset:1024
s_lshl_b32 s77,s77,6
v_add_u32 v234,s77,v232
MF a[100:103],v[144:147],v[32:35],a[100:103]
buffer_load_dwordx4 v230,s[40:43],s72 offen offset:1024 lds
v_add_u32 v235,s77,v233
MF a[72:75],v[152:155],v[24:27],a[72:75]
ds_read_b128 a[200:203],v228 offset:2048
v_cmp_gt_i32_e64 s[82:83],0,v234
v_cmp_gt_i32_e64 s[84:85],1,v234
MF a[104:107],v[152:155],v[32:35],a[104:107]
buffer_load_dwordx4 v230,s[40:43],s72 offen offset:2048 lds
v_cndmask_b32_e64 v88,v88,v236,s[82:83]
MF a[76:79],v[160:163],v[24:27],a[76:79]
ds_read_b128 a[204:207],v228 offset:3072
v_cmp_gt_i32_e64 s[82:83],2,v234
v_cndmask_b32_e64 v89,v89,v236,s[84:85]
MF a[108:111],v[160:163],v[32:35],a[108:111]
ds_read_b128 a[208:211],v228 offset:4096
v_cmp_gt_i32_e64 s[84:85],3,v234
v_cndmask_b32_e64 v90,v90,v236,s[82:83]
MF a[80:83],v[168:171],v[24:27],a[80:83]
buffer_load_dwordx4 v230,s[40:43],s72 offen offset:3072 lds
v_cmp_gt_i32_e64 s[82:83],16,v234
MF a[112:115],v[168:171],v[32:35],a[112:115]
ds_read_b128 a[212:215],v228 offset:5120
v_cndmask_b32_e64 v91,v91,v236,s[84:85]
v_cmp_gt_i32_e64 s[84:85],17,v234
MF a[84:87],v[176:179],v[24:27],a[84:87]
v_cndmask_b32_e64 v92,v92,v236,s[82:83]
v_cmp_gt_i32_e64 s[82:83],18,v234
s_add_u32 s72,s72,16384
MF a[116:119],v[176:179],v[32:35],a[116:119]
ds_read_b128 a[216:219],v228 offset:6144
v_cndmask_b32_e64 v93,v93,v236,s[84:85]
v_cmp_gt_i32_e64 s[84:85],19,v234
MF a[88:91],v[184:187],v[24:27],a[88:91]
ds_read_b128 a[220:223],v228 offset:7168
v_cndmask_b32_e64 v94,v94,v236,s[82:83]
s_add_u32 m0,s74,s62
MF a[120:123],v[184:187],v[32:35],a[120:123]
buffer_load_dwordx4 v230,s[44:47],s73 offen lds
v_cndmask_b32_e64 v95,v95,v236,s[84:85]
MF a[92:95],v[192:195],v[24:27],a[92:95]
ds_read_b128 a[224:227],v228 offset:8192
v_exp_f32 v88,v88
MF a[124:127],v[192:195],v[32:35],a[124:127]
buffer_load_dwordx4 v230,s[44:47],s73 offen offset:1024 lds
v_exp_f32 v89,v89
v_exp_f32 v90,v90
v_exp_f32 v91,v91
v_exp_f32 v92,v92
v_exp_f32 v93,v93
v_exp_f32 v94,v94
MF a[64:67],v[140:143],v[56:59],a[64:67]
ds_read_b128 a[228:231],v228 offset:9216
v_exp_f32 v95,v95
MF a[96:99],v[140:143],v[64:67],a[96:99]
ds_read_b128 a[232:235],v228 offset:10240
v_cmp_gt_i32_e64 s[82:83],0,v235
v_cmp_gt_i32_e64 s[84:85],1,v235
MF a[68:71],v[148:151],v[56:59],a[68:71]
buffer_load_dwordx4 v230,s[44:47],s73 offen offset:2048 lds
v_cndmask_b32_e64 v96,v96,v236,s[82:83]
MF a[100:103],v[148:151],v[64:67],a[100:103]
ds_read_b128 a[236:239],v228 offset:11264
v_cmp_gt_i32_e64 s[82:83],2,v235
v_cndmask_b32_e64 v97,v97,v236,s[84:85]
MF a[72:75],v[156:159],v[56:59],a[72:75]
buffer_load_dwordx4 v230,s[44:47],s73 offen offset:3072 lds
v_cmp_gt_i32_e64 s[84:85],3,v235
MF a[104:107],v[156:159],v[64:67],a[104:107]
ds_read_b128 a[240:243],v228 offset:12288
v_cndmask_b32_e64 v98,v98,v236,s[82:83]
v_cmp_gt_i32_e64 s[82:83],16,v235
MF a[76:79],v[164:167],v[56:59],a[76:79]
ds_read_b128 a[244:247],v228 offset:13312
v_cndmask_b32_e64 v99,v99,v236,s[84:85]
v_cmp_gt_i32_e64 s[84:85],17,v235
MF a[108:111],v[164:167],v[64:67],a[108:111]
v_cndmask_b32_e64 v100,v100,v236,s[82:83]
v_cmp_gt_i32_e64 s[82:83],18,v235
s_add_u32 s73,s73,16384
MF a[80:83],v[172:175],v[56:59],a[80:83]
ds_read_b128 a[248:251],v228 offset:14336
v_cndmask_b32_e64 v101,v101,v236,s[84:85]
s_add_u32 m0,s100,147456
MF a[112:115],v[172:175],v[64:67],a[112:115]
global_load_lds_dword v252,s[18:19]
v_cmp_gt_i32_e64 s[84:85],19,v235
MF a[84:87],v[180:183],v[56:59],a[84:87]
ds_read_b128 a[252:255],v228 offset:15360
v_cndmask_b32_e64 v102,v102,v236,s[82:83]
v_cndmask_b32_e64 v103,v103,v236,s[84:85]
MF a[116:119],v[180:183],v[64:67],a[116:119]
v_exp_f32 v96,v96
v_add_f32 v220,v88,v90
MF a[88:91],v[188:191],v[56:59],a[88:91]
s_add_u32 s75,s75,1
s_add_u32 s59,s59,16384
s_and_b32 s59,s59,0xffff
MF a[120:123],v[188:191],v[64:67],a[120:123]
s_add_u32 s61,s61,16384
s_and_b32 s61,s61,0xffff
s_add_u32 s60,s60,16384
MF a[92:95],v[196:199],v[56:59],a[92:95]
s_and_b32 s60,s60,0xffff
s_or_b32 s60,s60,65536
s_add_u32 s62,s62,16384
MF a[124:127],v[196:199],v[64:67],a[124:127]
s_and_b32 s62,s62,0xffff
s_or_b32 s62,s62,65536
v_add_u32 v229,s60,v231
v_add_f32 v221,v89,v91
v_exp_f32 v97,v97
CP v88,v88,v89
v_exp_f32 v98,v98
v_add_f32 v220,v92,v220
v_exp_f32 v99,v99
CP v89,v90,v91
v_exp_f32 v100,v100
v_add_f32 v221,v93,v221
v_add_f32 v220,v94,v220
v_exp_f32 v101,v101
CP v90,v92,v93
v_exp_f32 v102,v102
v_add_f32 v221,v95,v221
v_exp_f32 v103,v103
CP v91,v94,v95
v_add_f32 v222,v96,v98
v_add_f32 v223,v97,v99
CP v96,v96,v97
v_add_f32 v222,v100,v222
CP v97,v98,v99
v_add_f32 v223,v101,v223
v_add_f32 v222,v102,v222
CP v98,v100,v101
v_add_f32 v223,v103,v223
CP v99,v102,v103
s_waitcnt lgkmcnt(0)
s_cmp_eq_u32 s58,7
s_cbranch_scc1 LDRAIN_%=
s_lshl_b32 s76,s58,1
s_sub_u32 s76,13,s76
s_cmp_eq_u32 s76,0
s_cbranch_scc1 LSMASK_%=
LSLOOP_%=:
MF v[24:27],a[192:195],a[160:163],v[208:211]
ds_read_b128 v[136:139],v229
v_exp_f32 v120,v120
MF v[32:35],a[192:195],a[176:179],v[212:215]
ds_read_b128 v[144:147],v229 offset:1024
v_exp_f32 v121,v121
MF v[24:27],a[196:199],a[164:167],v[24:27]
v_exp_f32 v122,v122
MF v[32:35],a[196:199],a[180:183],v[32:35]
ds_read_b128 v[152:155],v229 offset:2048
v_exp_f32 v123,v123
MF v[24:27],a[200:203],a[168:171],v[24:27]
ds_read_b128 v[160:163],v229 offset:3072
v_exp_f32 v124,v124
MF v[32:35],a[200:203],a[184:187],v[32:35]
v_exp_f32 v125,v125
MF v[24:27],a[204:207],a[172:175],v[24:27]
ds_read_b128 v[168:171],v229 offset:4096
v_exp_f32 v126,v126
MF v[32:35],a[204:207],a[188:191],v[32:35]
ds_read_b128 v[176:179],v229 offset:5120
v_exp_f32 v127,v127
MF v[28:31],a[208:211],a[160:163],v[208:211]
v_exp_f32 v128,v128
v_add_f32 v220,v120,v220
MF v[36:39],a[208:211],a[176:179],v[212:215]
ds_read_b128 v[184:187],v229 offset:6144
v_add_f32 v221,v121,v221
MF v[28:31],a[212:215],a[164:167],v[28:31]
ds_read_b128 v[192:195],v229 offset:7168
v_exp_f32 v129,v129
MF v[36:39],a[212:215],a[180:183],v[36:39]
CP v120,v120,v121
v_exp_f32 v130,v130
MF v[28:31],a[216:219],a[168:171],v[28:31]
ds_read_b128 v[140:143],v229 offset:8192
v_add_f32 v220,v122,v220
MF v[36:39],a[216:219],a[184:187],v[36:39]
ds_read_b128 v[148:151],v229 offset:9216
v_add_f32 v221,v123,v221
MF v[28:31],a[220:223],a[172:175],v[28:31]
v_exp_f32 v131,v131
MF v[36:39],a[220:223],a[188:191],v[36:39]
ds_read_b128 v[156:159],v229 offset:10240
CP v121,v122,v123
MF v[56:59],a[224:227],a[160:163],v[208:211]
ds_read_b128 v[164:167],v229 offset:11264
v_exp_f32 v132,v132
MF v[64:67],a[224:227],a[176:179],v[212:215]
v_add_f32 v220,v124,v220
v_add_f32 v221,v125,v221
MF v[56:59],a[228:231],a[164:167],v[56:59]
ds_read_b128 v[172:175],v229 offset:12288
v_exp_f32 v133,v133
MF v[64:67],a[228:231],a[180:183],v[64:67]
ds_read_b128 v[180:183],v229 offset:13312
CP v122,v124,v125
MF v[56:59],a[232:235],a[168:171],v[56:59]
v_exp_f32 v134,v134
MF v[64:67],a[232:235],a[184:187],v[64:67]
ds_read_b128 v[188:191],v229 offset:14336
v_add_f32 v220,v126,v220
MF v[56:59],a[236:239],a[172:175],v[56:59]
ds_read_b128 v[196:199],v229 offset:15360
v_add_f32 v221,v127,v221
MF v[64:67],a[236:239],a[188:191],v[64:67]
v_exp_f32 v135,v135
CP v123,v126,v127
MF v[60:63],a[240:243],a[160:163],v[208:211]
v_add_f32 v222,v128,v222
v_add_f32 v223,v129,v223
MF v[68:71],a[240:243],a[176:179],v[212:215]
v_add_f32 v222,v130,v222
v_add_f32 v223,v131,v223
MF v[60:63],a[244:247],a[164:167],v[60:63]
v_add_f32 v222,v132,v222
v_add_f32 v223,v133,v223
MF v[68:71],a[244:247],a[180:183],v[68:71]
v_add_f32 v222,v134,v222
v_add_f32 v223,v135,v223
MF v[60:63],a[248:251],a[168:171],v[60:63]
v_max3_f32 v2,v220,v221,v222
v_max_f32 v2,v2,v223
MF v[68:71],a[248:251],a[184:187],v[68:71]
v_cmp_nge_f32 vcc,0x5d800000,v2
CP v128,v128,v129
MF v[60:63],a[252:255],a[172:175],v[60:63]
CP v129,v130,v131
CP v130,v132,v133
MF v[68:71],a[252:255],a[188:191],v[68:71]
CP v131,v134,v135
v_add_u32 v228,s59,v231
s_cmp_eq_u64 vcc,0
s_cbranch_scc0 LSTUB8_%=
LSK8_%=:
s_waitcnt vmcnt(8) lgkmcnt(0)
s_barrier
MF a[64:67],v[136:139],v[88:91],a[64:67]
ds_read_b128 a[192:195],v228
v_add_f32 v220,v220,v221
s_add_u32 m0,s74,s61
MF a[96:99],v[136:139],v[96:99],a[96:99]
buffer_load_dwordx4 v230,s[40:43],s72 offen lds
v_add_f32 v222,v222,v223
MF a[68:71],v[144:147],v[88:91],a[68:71]
ds_read_b128 a[196:199],v228 offset:1024
v_add_f32 v226,v226,v220
v_add_f32 v227,v227,v222
MF a[100:103],v[144:147],v[96:99],a[100:103]
buffer_load_dwordx4 v230,s[40:43],s72 offen offset:1024 lds
MF a[72:75],v[152:155],v[88:91],a[72:75]
ds_read_b128 a[200:203],v228 offset:2048
v_exp_f32 v24,v24
MF a[104:107],v[152:155],v[96:99],a[104:107]
ds_read_b128 a[204:207],v228 offset:3072
v_exp_f32 v25,v25
MF a[76:79],v[160:163],v[88:91],a[76:79]
buffer_load_dwordx4 v230,s[40:43],s72 offen offset:2048 lds
MF a[108:111],v[160:163],v[96:99],a[108:111]
ds_read_b128 a[208:211],v228 offset:4096
v_exp_f32 v26,v26
MF a[80:83],v[168:171],v[88:91],a[80:83]
buffer_load_dwordx4 v230,s[40:43],s72 offen offset:3072 lds
v_exp_f32 v27,v27
v_exp_f32 v28,v28
v_exp_f32 v29,v29
v_exp_f32 v30,v30
v_exp_f32 v31,v31
v_exp_f32 v32,v32
MF a[112:115],v[168:171],v[96:99],a[112:115]
ds_read_b128 a[212:215],v228 offset:5120
v_add_f32 v220,v24,v26
v_add_f32 v221,v25,v27
MF a[84:87],v[176:179],v[88:91],a[84:87]
ds_read_b128 a[216:219],v228 offset:6144
v_exp_f32 v33,v33
MF a[116:119],v[176:179],v[96:99],a[116:119]
CP v24,v24,v25
s_add_u32 s72,s72,16384
MF a[88:91],v[184:187],v[88:91],a[88:91]
ds_read_b128 a[220:223],v228 offset:7168
v_exp_f32 v34,v34
MF a[120:123],v[184:187],v[96:99],a[120:123]
v_add_f32 v220,v28,v220
v_exp_f32 v35,v35
MF a[92:95],v[192:195],v[88:91],a[92:95]
ds_read_b128 a[224:227],v228 offset:8192
s_add_u32 m0,s74,s62
MF a[124:127],v[192:195],v[96:99],a[124:127]
buffer_load_dwordx4 v230,s[44:47],s73 offen lds
CP v25,v26,v27
MF a[64:67],v[140:143],v[120:123],a[64:67]
ds_read_b128 a[228:231],v228 offset:9216
v_exp_f32 v36,v36
MF a[96:99],v[140:143],v[128:131],a[96:99]
buffer_load_dwordx4 v230,s[44:47],s73 offen offset:1024 lds
MF a[68:71],v[148:151],v[120:123],a[68:71]
ds_read_b128 a[232:235],v228 offset:10240
v_add_f32 v221,v29,v221
v_add_f32 v220,v30,v220
MF a[100:103],v[148:151],v[128:131],a[100:103]
ds_read_b128 a[236:239],v228 offset:11264
v_exp_f32 v37,v37
MF a[72:75],v[156:159],v[120:123],a[72:75]
buffer_load_dwordx4 v230,s[44:47],s73 offen offset:2048 lds
CP v26,v28,v29
MF a[104:107],v[156:159],v[128:131],a[104:107]
ds_read_b128 a[240:243],v228 offset:12288
v_exp_f32 v38,v38
MF a[76:79],v[164:167],v[120:123],a[76:79]
buffer_load_dwordx4 v230,s[44:47],s73 offen offset:3072 lds
v_add_f32 v221,v31,v221
MF a[108:111],v[164:167],v[128:131],a[108:111]
ds_read_b128 a[244:247],v228 offset:13312
v_exp_f32 v39,v39
MF a[80:83],v[172:175],v[120:123],a[80:83]
ds_read_b128 a[248:251],v228 offset:14336
CP v27,v30,v31
v_add_f32 v222,v32,v34
MF a[112:115],v[172:175],v[128:131],a[112:115]
v_add_f32 v223,v33,v35
s_add_u32 s73,s73,16384
MF a[84:87],v[180:183],v[120:123],a[84:87]
ds_read_b128 a[252:255],v228 offset:15360
CP v32,v32,v33
v_add_f32 v222,v36,v222
MF a[116:119],v[180:183],v[128:131],a[116:119]
CP v33,v34,v35
v_add_f32 v223,v37,v223
v_add_f32 v222,v38,v222
MF a[88:91],v[188:191],v[120:123],a[88:91]
s_add_u32 s75,s75,1
s_add_u32 s59,s59,16384
s_and_b32 s59,s59,0xffff
MF a[120:123],v[188:191],v[128:131],a[120:123]
s_add_u32 s61,s61,16384
s_and_b32 s61,s61,0xffff
s_add_u32 s60,s60,16384
MF a[92:95],v[196:199],v[120:123],a[92:95]
s_and_b32 s60,s60,0xffff
s_or_b32 s60,s60,65536
s_add_u32 s62,s62,16384
MF a[124:127],v[196:199],v[128:131],a[124:127]
s_and_b32 s62,s62,0xffff
s_or_b32 s62,s62,65536
v_add_u32 v229,s60,v231
CP v34,v36,v37
v_add_f32 v223,v39,v223
CP v35,v38,v39
s_waitcnt lgkmcnt(0)
MF v[88:91],a[192:195],a[160:163],v[208:211]
ds_read_b128 v[136:139],v229
v_exp_f32 v56,v56
MF v[96:99],a[192:195],a[176:179],v[212:215]
ds_read_b128 v[144:147],v229 offset:1024
v_exp_f32 v57,v57
MF v[88:91],a[196:199],a[164:167],v[88:91]
v_exp_f32 v58,v58
MF v[96:99],a[196:199],a[180:183],v[96:99]
ds_read_b128 v[152:155],v229 offset:2048
v_exp_f32 v59,v59
MF v[88:91],a[200:203],a[168:171],v[88:91]
ds_read_b128 v[160:163],v229 offset:3072
v_exp_f32 v60,v60
MF v[96:99],a[200:203],a[184:187],v[96:99]
v_exp_f32 v61,v61
MF v[88:91],a[204:207],a[172:175],v[88:91]
ds_read_b128 v[168:171],v229 offset:4096
v_exp_f32 v62,v62
MF v[96:99],a[204:207],a[188:191],v[96:99]
ds_read_b128 v[176:179],v229 offset:5120
v_exp_f32 v63,v63
MF v[92:95],a[208:211],a[160:163],v[208:211]
v_exp_f32 v64,v64
v_add_f32 v220,v56,v220
MF v[100:103],a[208:211],a[176:179],v[212:215]
ds_read_b128 v[184:187],v229 offset:6144
v_add_f32 v221,v57,v221
MF v[92:95],a[212:215],a[164:167],v[92:95]
ds_read_b128 v[192:195],v229 offset:7168
v_exp_f32 v65,v65
MF v[100:103],a[212:215],a[180:183],v[100:103]
CP v56,v56,v57
v_exp_f32 v66,v66
MF v[92:95],a[216:219],a[168:171],v[92:95]
ds_read_b128 v[140:143],v229 offset:8192
v_add_f32 v220,v58,v220
MF v[100:103],a[216:219],a[184:187],v[100:103]
ds_read_b128 v[148:151],v229 offset:9216
v_add_f32 v221,v59,v221
MF v[92:95],a[220:223],a[172:175],v[92:95]
v_exp_f32 v67,v67
MF v[100:103],a[220:223],a[188:191],v[100:103]
ds_read_b128 v[156:159],v229 offset:10240
CP v57,v58,v59
MF v[120:123],a[224:227],a[160:163],v[208:211]
ds_read_b128 v[164:167],v229 offset:11264
v_exp_f32 v68,v68
MF v[128:131],a[224:227],a[176:179],v[212:215]
v_add_f32 v220,v60,v220
v_add_f32 v221,v61,v221
MF v[120:123],a[228:231],a[164:167],v[120:123]
ds_read_b128 v[172:175],v229 offset:12288
v_exp_f32 v69,v69
MF v[128:131],a[228:231],a[180:183],v[128:131]
ds_read_b128 v[180:183],v229 offset:13312
CP v58,v60,v61
MF v[120:123],a[232:235],a[168:171],v[120:123]
v_exp_f32 v70,v70
MF v[128:131],a[232:235],a[184:187],v[128:131]
ds_read_b128 v[188:191],v229 offset:14336
v_add_f32 v220,v62,v220
MF v[120:123],a[236:239],a[172:175],v[120:123]
ds_read_b128 v[196:199],v229 offset:15360
v_add_f32 v221,v63,v221
MF v[128:131],a[236:239],a[188:191],v[128:131]
v_exp_f32 v71,v71
CP v59,v62,v63
MF v[124:127],a[240:243],a[160:163],v[208:211]
v_add_f32 v222,v64,v222
v_add_f32 v223,v65,v223
MF v[132:135],a[240:243],a[176:179],v[212:215]
v_add_f32 v222,v66,v222
v_add_f32 v223,v67,v223
MF v[124:127],a[244:247],a[164:167],v[124:127]
v_add_f32 v222,v68,v222
v_add_f32 v223,v69,v223
MF v[132:135],a[244:247],a[180:183],v[132:135]
v_add_f32 v222,v70,v222
v_add_f32 v223,v71,v223
MF v[124:127],a[248:251],a[168:171],v[124:127]
v_max3_f32 v2,v220,v221,v222
v_max_f32 v2,v2,v223
MF v[132:135],a[248:251],a[184:187],v[132:135]
v_cmp_nge_f32 vcc,0x5d800000,v2
CP v64,v64,v65
MF v[124:127],a[252:255],a[172:175],v[124:127]
CP v65,v66,v67
CP v66,v68,v69
MF v[132:135],a[252:255],a[188:191],v[132:135]
CP v67,v70,v71
v_add_u32 v228,s59,v231
s_cmp_eq_u64 vcc,0
s_cbranch_scc0 LSTUB9_%=
LSK9_%=:
s_waitcnt vmcnt(8) lgkmcnt(0)
s_barrier
MF a[64:67],v[136:139],v[24:27],a[64:67]
ds_read_b128 a[192:195],v228
v_add_f32 v220,v220,v221
s_add_u32 m0,s74,s61
MF a[96:99],v[136:139],v[32:35],a[96:99]
buffer_load_dwordx4 v230,s[40:43],s72 offen lds
v_add_f32 v222,v222,v223
MF a[68:71],v[144:147],v[24:27],a[68:71]
ds_read_b128 a[196:199],v228 offset:1024
v_add_f32 v226,v226,v220
v_add_f32 v227,v227,v222
MF a[100:103],v[144:147],v[32:35],a[100:103]
buffer_load_dwordx4 v230,s[40:43],s72 offen offset:1024 lds
MF a[72:75],v[152:155],v[24:27],a[72:75]
ds_read_b128 a[200:203],v228 offset:2048
v_exp_f32 v88,v88
MF a[104:107],v[152:155],v[32:35],a[104:107]
ds_read_b128 a[204:207],v228 offset:3072
v_exp_f32 v89,v89
MF a[76:79],v[160:163],v[24:27],a[76:79]
buffer_load_dwordx4 v230,s[40:43],s72 offen offset:2048 lds
MF a[108:111],v[160:163],v[32:35],a[108:111]
ds_read_b128 a[208:211],v228 offset:4096
v_exp_f32 v90,v90
MF a[80:83],v[168:171],v[24:27],a[80:83]
buffer_load_dwordx4 v230,s[40:43],s72 offen offset:3072 lds
v_exp_f32 v91,v91
v_exp_f32 v92,v92
v_exp_f32 v93,v93
v_exp_f32 v94,v94
v_exp_f32 v95,v95
v_exp_f32 v96,v96
MF a[112:115],v[168:171],v[32:35],a[112:115]
ds_read_b128 a[212:215],v228 offset:5120
v_add_f32 v220,v88,v90
v_add_f32 v221,v89,v91
MF a[84:87],v[176:179],v[24:27],a[84:87]
ds_read_b128 a[216:219],v228 offset:6144
v_exp_f32 v97,v97
MF a[116:119],v[176:179],v[32:35],a[116:119]
CP v88,v88,v89
s_add_u32 s72,s72,16384
MF a[88:91],v[184:187],v[24:27],a[88:91]
ds_read_b128 a[220:223],v228 offset:7168
v_exp_f32 v98,v98
MF a[120:123],v[184:187],v[32:35],a[120:123]
v_add_f32 v220,v92,v220
v_exp_f32 v99,v99
MF a[92:95],v[192:195],v[24:27],a[92:95]
ds_read_b128 a[224:227],v228 offset:8192
s_add_u32 m0,s74,s62
MF a[124:127],v[192:195],v[32:35],a[124:127]
buffer_load_dwordx4 v230,s[44:47],s73 offen lds
CP v89,v90,v91
MF a[64:67],v[140:143],v[56:59],a[64:67]
ds_read_b128 a[228:231],v228 offset:9216
v_exp_f32 v100,v100
MF a[96:99],v[140:143],v[64:67],a[96:99]
buffer_load_dwordx4 v230,s[44:47],s73 offen offset:1024 lds
MF a[68:71],v[148:151],v[56:59],a[68:71]
ds_read_b128 a[232:235],v228 offset:10240
v_add_f32 v221,v93,v221
v_add_f32 v220,v94,v220
MF a[100:103],v[148:151],v[64:67],a[100:103]
ds_read_b128 a[236:239],v228 offset:11264
v_exp_f32 v101,v101
MF a[72:75],v[156:159],v[56:59],a[72:75]
buffer_load_dwordx4 v230,s[44:47],s73 offen offset:2048 lds
CP v90,v92,v93
MF a[104:107],v[156:159],v[64:67],a[104:107]
ds_read_b128 a[240:243],v228 offset:12288
v_exp_f32 v102,v102
MF a[76:79],v[164:167],v[56:59],a[76:79]
buffer_load_dwordx4 v230,s[44:47],s73 offen offset:3072 lds
v_add_f32 v221,v95,v221
MF a[108:111],v[164:167],v[64:67],a[108:111]
ds_read_b128 a[244:247],v228 offset:13312
v_exp_f32 v103,v103
MF a[80:83],v[172:175],v[56:59],a[80:83]
ds_read_b128 a[248:251],v228 offset:14336
CP v91,v94,v95
v_add_f32 v222,v96,v98
MF a[112:115],v[172:175],v[64:67],a[112:115]
v_add_f32 v223,v97,v99
s_add_u32 s73,s73,16384
MF a[84:87],v[180:183],v[56:59],a[84:87]
ds_read_b128 a[252:255],v228 offset:15360
CP v96,v96,v97
v_add_f32 v222,v100,v222
MF a[116:119],v[180:183],v[64:67],a[116:119]
CP v97,v98,v99
v_add_f32 v223,v101,v223
v_add_f32 v222,v102,v222
MF a[88:91],v[188:191],v[56:59],a[88:91]
s_add_u32 s75,s75,1
s_add_u32 s59,s59,16384
s_and_b32 s59,s59,0xffff
MF a[120:123],v[188:191],v[64:67],a[120:123]
s_add_u32 s61,s61,16384
s_and_b32 s61,s61,0xffff
s_add_u32 s60,s60,16384
MF a[92:95],v[196:199],v[56:59],a[92:95]
s_and_b32 s60,s60,0xffff
s_or_b32 s60,s60,65536
s_add_u32 s62,s62,16384
MF a[124:127],v[196:199],v[64:67],a[124:127]
s_and_b32 s62,s62,0xffff
s_or_b32 s62,s62,65536
v_add_u32 v229,s60,v231
CP v98,v100,v101
v_add_f32 v223,v103,v223
CP v99,v102,v103
s_waitcnt lgkmcnt(0)
s_sub_u32 s76,s76,1
s_cmp_lg_u32 s76,0
s_cbranch_scc1 LSLOOP_%=
LSMASK_%=:
MF v[24:27],a[192:195],a[160:163],v[208:211]
ds_read_b128 v[136:139],v229
v_exp_f32 v120,v120
MF v[32:35],a[192:195],a[176:179],v[212:215]
ds_read_b128 v[144:147],v229 offset:1024
v_exp_f32 v121,v121
MF v[24:27],a[196:199],a[164:167],v[24:27]
v_exp_f32 v122,v122
MF v[32:35],a[196:199],a[180:183],v[32:35]
ds_read_b128 v[152:155],v229 offset:2048
v_exp_f32 v123,v123
MF v[24:27],a[200:203],a[168:171],v[24:27]
ds_read_b128 v[160:163],v229 offset:3072
v_exp_f32 v124,v124
MF v[32:35],a[200:203],a[184:187],v[32:35]
v_exp_f32 v125,v125
MF v[24:27],a[204:207],a[172:175],v[24:27]
ds_read_b128 v[168:171],v229 offset:4096
v_exp_f32 v126,v126
MF v[32:35],a[204:207],a[188:191],v[32:35]
ds_read_b128 v[176:179],v229 offset:5120
v_exp_f32 v127,v127
MF v[28:31],a[208:211],a[160:163],v[208:211]
v_exp_f32 v128,v128
v_add_f32 v220,v120,v220
MF v[36:39],a[208:211],a[176:179],v[212:215]
ds_read_b128 v[184:187],v229 offset:6144
v_add_f32 v221,v121,v221
MF v[28:31],a[212:215],a[164:167],v[28:31]
ds_read_b128 v[192:195],v229 offset:7168
v_exp_f32 v129,v129
MF v[36:39],a[212:215],a[180:183],v[36:39]
CP v120,v120,v121
v_exp_f32 v130,v130
MF v[28:31],a[216:219],a[168:171],v[28:31]
ds_read_b128 v[140:143],v229 offset:8192
v_add_f32 v220,v122,v220
MF v[36:39],a[216:219],a[184:187],v[36:39]
ds_read_b128 v[148:151],v229 offset:9216
v_add_f32 v221,v123,v221
MF v[28:31],a[220:223],a[172:175],v[28:31]
v_exp_f32 v131,v131
MF v[36:39],a[220:223],a[188:191],v[36:39]
ds_read_b128 v[156:159],v229 offset:10240
CP v121,v122,v123
MF v[56:59],a[224:227],a[160:163],v[208:211]
ds_read_b128 v[164:167],v229 offset:11264
v_exp_f32 v132,v132
MF v[64:67],a[224:227],a[176:179],v[212:215]
v_add_f32 v220,v124,v220
v_add_f32 v221,v125,v221
MF v[56:59],a[228:231],a[164:167],v[56:59]
ds_read_b128 v[172:175],v229 offset:12288
v_exp_f32 v133,v133
MF v[64:67],a[228:231],a[180:183],v[64:67]
ds_read_b128 v[180:183],v229 offset:13312
CP v122,v124,v125
MF v[56:59],a[232:235],a[168:171],v[56:59]
v_exp_f32 v134,v134
MF v[64:67],a[232:235],a[184:187],v[64:67]
ds_read_b128 v[188:191],v229 offset:14336
v_add_f32 v220,v126,v220
MF v[56:59],a[236:239],a[172:175],v[56:59]
ds_read_b128 v[196:199],v229 offset:15360
v_add_f32 v221,v127,v221
MF v[64:67],a[236:239],a[188:191],v[64:67]
v_exp_f32 v135,v135
CP v123,v126,v127
MF v[60:63],a[240:243],a[160:163],v[208:211]
v_add_f32 v222,v128,v222
v_add_f32 v223,v129,v223
MF v[68:71],a[240:243],a[176:179],v[212:215]
v_add_f32 v222,v130,v222
v_add_f32 v223,v131,v223
MF v[60:63],a[244:247],a[164:167],v[60:63]
v_add_f32 v222,v132,v222
v_add_f32 v223,v133,v223
MF v[68:71],a[244:247],a[180:183],v[68:71]
v_add_f32 v222,v134,v222
v_add_f32 v223,v135,v223
MF v[60:63],a[248:251],a[168:171],v[60:63]
v_max3_f32 v2,v220,v221,v222
v_max_f32 v2,v2,v223
MF v[68:71],a[248:251],a[184:187],v[68:71]
v_cmp_nge_f32 vcc,0x5d800000,v2
CP v128,v128,v129
MF v[60:63],a[252:255],a[172:175],v[60:63]
CP v129,v130,v131
CP v130,v132,v133
MF v[68:71],a[252:255],a[188:191],v[68:71]
CP v131,v134,v135
v_add_u32 v228,s59,v231
s_cmp_eq_u64 vcc,0
s_cbranch_scc0 LSTUB10_%=
LSK10_%=:
s_waitcnt vmcnt(8) lgkmcnt(0)
s_barrier
MF a[64:67],v[136:139],v[88:91],a[64:67]
ds_read_b128 a[192:195],v228
v_add_f32 v220,v220,v221
v_add_f32 v222,v222,v223
MF a[96:99],v[136:139],v[96:99],a[96:99]
ds_read_b128 a[196:199],v228 offset:1024
v_add_f32 v226,v226,v220
v_add_f32 v227,v227,v222
MF a[68:71],v[144:147],v[88:91],a[68:71]
s_sub_u32 s77,30,s58
s_sub_u32 s77,s77,s58
s_sub_u32 s77,s77,s75
MF a[100:103],v[144:147],v[96:99],a[100:103]
ds_read_b128 a[200:203],v228 offset:2048
s_lshl_b32 s77,s77,6
v_add_u32 v234,s77,v232
MF a[72:75],v[152:155],v[88:91],a[72:75]
v_add_u32 v235,s77,v233
v_cmp_gt_i32_e64 s[82:83],0,v234
v_cmp_gt_i32_e64 s[84:85],1,v234
MF a[104:107],v[152:155],v[96:99],a[104:107]
ds_read_b128 a[204:207],v228 offset:3072
v_cndmask_b32_e64 v24,v24,v236,s[82:83]
v_cmp_gt_i32_e64 s[82:83],2,v234
MF a[76:79],v[160:163],v[88:91],a[76:79]
v_cndmask_b32_e64 v25,v25,v236,s[84:85]
v_cmp_gt_i32_e64 s[84:85],3,v234
v_cndmask_b32_e64 v26,v26,v236,s[82:83]
MF a[108:111],v[160:163],v[96:99],a[108:111]
ds_read_b128 a[208:211],v228 offset:4096
v_cmp_gt_i32_e64 s[82:83],16,v234
v_cndmask_b32_e64 v27,v27,v236,s[84:85]
MF a[80:83],v[168:171],v[88:91],a[80:83]
ds_read_b128 a[212:215],v228 offset:5120
v_cmp_gt_i32_e64 s[84:85],17,v234
v_cndmask_b32_e64 v28,v28,v236,s[82:83]
MF a[112:115],v[168:171],v[96:99],a[112:115]
v_cmp_gt_i32_e64 s[82:83],18,v234
v_cndmask_b32_e64 v29,v29,v236,s[84:85]
v_cmp_gt_i32_e64 s[84:85],19,v234
MF a[84:87],v[176:179],v[88:91],a[84:87]
ds_read_b128 a[216:219],v228 offset:6144
v_cndmask_b32_e64 v30,v30,v236,s[82:83]
v_cndmask_b32_e64 v31,v31,v236,s[84:85]
MF a[116:119],v[176:179],v[96:99],a[116:119]
v_exp_f32 v24,v24
MF a[88:91],v[184:187],v[88:91],a[88:91]
ds_read_b128 a[220:223],v228 offset:7168
v_exp_f32 v25,v25
v_exp_f32 v26,v26
v_exp_f32 v27,v27
v_exp_f32 v28,v28
v_exp_f32 v29,v29
v_exp_f32 v30,v30
MF a[120:123],v[184:187],v[96:99],a[120:123]
v_exp_f32 v31,v31
v_cmp_gt_i32_e64 s[82:83],0,v235
MF a[92:95],v[192:195],v[88:91],a[92:95]
ds_read_b128 a[224:227],v228 offset:8192
v_cmp_gt_i32_e64 s[84:85],1,v235
v_cndmask_b32_e64 v32,v32,v236,s[82:83]
MF a[124:127],v[192:195],v[96:99],a[124:127]
ds_read_b128 a[228:231],v228 offset:9216
v_cmp_gt_i32_e64 s[82:83],2,v235
v_cndmask_b32_e64 v33,v33,v236,s[84:85]
MF a[64:67],v[140:143],v[120:123],a[64:67]
v_cmp_gt_i32_e64 s[84:85],3,v235
v_cndmask_b32_e64 v34,v34,v236,s[82:83]
v_cmp_gt_i32_e64 s[82:83],16,v235
MF a[96:99],v[140:143],v[128:131],a[96:99]
ds_read_b128 a[232:235],v228 offset:10240
v_cndmask_b32_e64 v35,v35,v236,s[84:85]
v_cmp_gt_i32_e64 s[84:85],17,v235
MF a[68:71],v[148:151],v[120:123],a[68:71]
v_cndmask_b32_e64 v36,v36,v236,s[82:83]
v_cmp_gt_i32_e64 s[82:83],18,v235
v_cndmask_b32_e64 v37,v37,v236,s[84:85]
MF a[100:103],v[148:151],v[128:131],a[100:103]
ds_read_b128 a[236:239],v228 offset:11264
v_cmp_gt_i32_e64 s[84:85],19,v235
v_cndmask_b32_e64 v38,v38,v236,s[82:83]
MF a[72:75],v[156:159],v[120:123],a[72:75]
v_cndmask_b32_e64 v39,v39,v236,s[84:85]
v_exp_f32 v32,v32
MF a[104:107],v[156:159],v[128:131],a[104:107]
ds_read_b128 a[240:243],v228 offset:12288
v_add_f32 v220,v24,v26
v_add_f32 v221,v25,v27
MF a[76:79],v[164:167],v[120:123],a[76:79]
ds_read_b128 a[244:247],v228 offset:13312
v_exp_f32 v33,v33
MF a[108:111],v[164:167],v[128:131],a[108:111]
CP v24,v24,v25
v_exp_f32 v34,v34
MF a[80:83],v[172:175],v[120:123],a[80:83]
ds_read_b128 a[248:251],v228 offset:14336
v_add_f32 v220,v28,v220
MF a[112:115],v[172:175],v[128:131],a[112:115]
v_exp_f32 v35,v35
CP v25,v26,v27
MF a[84:87],v[180:183],v[120:123],a[84:87]
ds_read_b128 a[252:255],v228 offset:15360
v_exp_f32 v36,v36
MF a[116:119],v[180:183],v[128:131],a[116:119]
v_add_f32 v221,v29,v221
v_add_f32 v220,v30,v220
MF a[88:91],v[188:191],v[120:123],a[88:91]
s_add_u32 s75,s75,1
s_add_u32 s59,s59,16384
s_and_b32 s59,s59,0xffff
MF a[120:123],v[188:191],v[128:131],a[120:123]
s_add_u32 s61,s61,16384
s_and_b32 s61,s61,0xffff
s_add_u32 s60,s60,16384
MF a[92:95],v[196:199],v[120:123],a[92:95]
s_and_b32 s60,s60,0xffff
s_or_b32 s60,s60,65536
s_add_u32 s62,s62,16384
MF a[124:127],v[196:199],v[128:131],a[124:127]
s_and_b32 s62,s62,0xffff
s_or_b32 s62,s62,65536
v_add_u32 v229,s60,v231
v_exp_f32 v37,v37
CP v26,v28,v29
v_exp_f32 v38,v38
v_add_f32 v221,v31,v221
v_exp_f32 v39,v39
CP v27,v30,v31
v_add_f32 v222,v32,v34
v_add_f32 v223,v33,v35
CP v32,v32,v33
v_add_f32 v222,v36,v222
CP v33,v34,v35
v_add_f32 v223,v37,v223
v_add_f32 v222,v38,v222
CP v34,v36,v37
v_add_f32 v223,v39,v223
CP v35,v38,v39
s_waitcnt lgkmcnt(0)
MF v[88:91],a[192:195],a[160:163],v[208:211]
ds_read_b128 v[136:139],v229
s_sub_u32 s77,30,s58
s_sub_u32 s77,s77,s58
MF v[96:99],a[192:195],a[176:179],v[212:215]
ds_read_b128 v[144:147],v229 offset:1024
s_sub_u32 s77,s77,s75
s_add_u32 s77,s77,1
MF v[88:91],a[196:199],a[164:167],v[88:91]
s_lshl_b32 s77,s77,6
v_add_u32 v234,s77,v232
v_add_u32 v235,s77,v233
MF v[96:99],a[196:199],a[180:183],v[96:99]
ds_read_b128 v[152:155],v229 offset:2048
v_cmp_gt_i32_e64 s[82:83],32,v234
v_cmp_gt_i32_e64 s[84:85],33,v234
MF v[88:91],a[200:203],a[168:171],v[88:91]
ds_read_b128 v[160:163],v229 offset:3072
v_cndmask_b32_e64 v56,v56,v236,s[82:83]
v_cmp_gt_i32_e64 s[82:83],34,v234
MF v[96:99],a[200:203],a[184:187],v[96:99]
v_cndmask_b32_e64 v57,v57,v236,s[84:85]
v_cmp_gt_i32_e64 s[84:85],35,v234
v_cndmask_b32_e64 v58,v58,v236,s[82:83]
MF v[88:91],a[204:207],a[172:175],v[88:91]
ds_read_b128 v[168:171],v229 offset:4096
v_cmp_gt_i32_e64 s[82:83],48,v234
v_cndmask_b32_e64 v59,v59,v236,s[84:85]
MF v[96:99],a[204:207],a[188:191],v[96:99]
ds_read_b128 v[176:179],v229 offset:5120
v_cmp_gt_i32_e64 s[84:85],49,v234
v_cndmask_b32_e64 v60,v60,v236,s[82:83]
MF v[92:95],a[208:211],a[160:163],v[208:211]
v_cmp_gt_i32_e64 s[82:83],50,v234
v_cndmask_b32_e64 v61,v61,v236,s[84:85]
v_cmp_gt_i32_e64 s[84:85],51,v234
MF v[100:103],a[208:211],a[176:179],v[212:215]
ds_read_b128 v[184:187],v229 offset:6144
v_cndmask_b32_e64 v62,v62,v236,s[82:83]
v_cndmask_b32_e64 v63,v63,v236,s[84:85]
MF v[92:95],a[212:215],a[164:167],v[92:95]
ds_read_b128 v[192:195],v229 offset:7168
v_exp_f32 v56,v56
v_exp_f32 v57,v57
v_exp_f32 v58,v58
v_exp_f32 v59,v59
v_exp_f32 v60,v60
v_exp_f32 v61,v61
MF v[100:103],a[212:215],a[180:183],v[100:103]
v_exp_f32 v62,v62
MF v[92:95],a[216:219],a[168:171],v[92:95]
ds_read_b128 v[140:143],v229 offset:8192
v_exp_f32 v63,v63
MF v[100:103],a[216:219],a[184:187],v[100:103]
ds_read_b128 v[148:151],v229 offset:9216
v_cmp_gt_i32_e64 s[82:83],32,v235
v_cmp_gt_i32_e64 s[84:85],33,v235
MF v[92:95],a[220:223],a[172:175],v[92:95]
v_cndmask_b32_e64 v64,v64,v236,s[82:83]
v_cmp_gt_i32_e64 s[82:83],34,v235
v_cndmask_b32_e64 v65,v65,v236,s[84:85]
MF v[100:103],a[220:223],a[188:191],v[100:103]
ds_read_b128 v[156:159],v229 offset:10240
v_cmp_gt_i32_e64 s[84:85],35,v235
v_cndmask_b32_e64 v66,v66,v236,s[82:83]
MF v[120:123],a[224:227],a[160:163],v[208:211]
ds_read_b128 v[164:167],v229 offset:11264
v_cmp_gt_i32_e64 s[82:83],48,v235
v_cndmask_b32_e64 v67,v67,v236,s[84:85]
MF v[128:131],a[224:227],a[176:179],v[212:215]
v_cmp_gt_i32_e64 s[84:85],49,v235
v_cndmask_b32_e64 v68,v68,v236,s[82:83]
v_cmp_gt_i32_e64 s[82:83],50,v235
MF v[120:123],a[228:231],a[164:167],v[120:123]
ds_read_b128 v[172:175],v229 offset:12288
v_cndmask_b32_e64 v69,v69,v236,s[84:85]
v_cmp_gt_i32_e64 s[84:85],51,v235
MF v[128:131],a[228:231],a[180:183],v[128:131]
ds_read_b128 v[180:183],v229 offset:13312
v_cndmask_b32_e64 v70,v70,v236,s[82:83]
v_cndmask_b32_e64 v71,v71,v236,s[84:85]
MF v[120:123],a[232:235],a[168:171],v[120:123]
)A"
R"A(v_exp_f32 v64,v64
v_add_f32 v220,v56,v220
MF v[128:131],a[232:235],a[184:187],v[128:131]
ds_read_b128 v[188:191],v229 offset:14336
v_add_f32 v221,v57,v221
MF v[120:123],a[236:239],a[172:175],v[120:123]
ds_read_b128 v[196:199],v229 offset:15360
v_exp_f32 v65,v65
MF v[128:131],a[236:239],a[188:191],v[128:131]
CP v56,v56,v57
v_exp_f32 v66,v66
MF v[124:127],a[240:243],a[160:163],v[208:211]
v_add_f32 v220,v58,v220
v_add_f32 v221,v59,v221
MF v[132:135],a[240:243],a[176:179],v[212:215]
v_exp_f32 v67,v67
CP v57,v58,v59
MF v[124:127],a[244:247],a[164:167],v[124:127]
v_exp_f32 v68,v68
v_add_f32 v220,v60,v220
MF v[132:135],a[244:247],a[180:183],v[132:135]
v_add_f32 v221,v61,v221
v_exp_f32 v69,v69
MF v[124:127],a[248:251],a[168:171],v[124:127]
CP v58,v60,v61
v_exp_f32 v70,v70
MF v[132:135],a[248:251],a[184:187],v[132:135]
v_add_f32 v220,v62,v220
v_add_f32 v221,v63,v221
MF v[124:127],a[252:255],a[172:175],v[124:127]
v_exp_f32 v71,v71
CP v59,v62,v63
MF v[132:135],a[252:255],a[188:191],v[132:135]
v_add_f32 v222,v64,v222
v_add_f32 v223,v65,v223
v_add_f32 v222,v66,v222
v_add_f32 v223,v67,v223
v_add_f32 v222,v68,v222
v_add_f32 v223,v69,v223
v_add_f32 v222,v70,v222
v_add_f32 v223,v71,v223
v_max3_f32 v2,v220,v221,v222
v_max_f32 v2,v2,v223
v_cmp_nge_f32 vcc,0x5d800000,v2
CP v64,v64,v65
CP v65,v66,v67
CP v66,v68,v69
CP v67,v70,v71
v_add_u32 v228,s59,v231
s_cmp_eq_u64 vcc,0
s_cbranch_scc0 LSTUB11_%=
LSK11_%=:
s_waitcnt vmcnt(0) lgkmcnt(0)
s_barrier
MF a[64:67],v[136:139],v[24:27],a[64:67]
v_add_f32 v220,v220,v221
v_add_f32 v222,v222,v223
MF a[96:99],v[136:139],v[32:35],a[96:99]
v_add_f32 v226,v226,v220
v_add_f32 v227,v227,v222
s_sub_u32 s77,30,s58
MF a[68:71],v[144:147],v[24:27],a[68:71]
s_sub_u32 s77,s77,s58
s_sub_u32 s77,s77,s75
MF a[100:103],v[144:147],v[32:35],a[100:103]
s_lshl_b32 s77,s77,6
v_add_u32 v234,s77,v232
v_add_u32 v235,s77,v233
MF a[72:75],v[152:155],v[24:27],a[72:75]
v_cmp_gt_i32_e64 s[82:83],0,v234
v_cmp_gt_i32_e64 s[84:85],1,v234
s_nop 0
v_cndmask_b32_e64 v88,v88,v236,s[82:83]
MF a[104:107],v[152:155],v[32:35],a[104:107]
v_cmp_gt_i32_e64 s[82:83],2,v234
v_cndmask_b32_e64 v89,v89,v236,s[84:85]
v_cmp_gt_i32_e64 s[84:85],3,v234
MF a[76:79],v[160:163],v[24:27],a[76:79]
v_cndmask_b32_e64 v90,v90,v236,s[82:83]
v_cmp_gt_i32_e64 s[82:83],16,v234
v_cndmask_b32_e64 v91,v91,v236,s[84:85]
MF a[108:111],v[160:163],v[32:35],a[108:111]
v_cmp_gt_i32_e64 s[84:85],17,v234
v_cndmask_b32_e64 v92,v92,v236,s[82:83]
v_cmp_gt_i32_e64 s[82:83],18,v234
MF a[80:83],v[168:171],v[24:27],a[80:83]
v_cndmask_b32_e64 v93,v93,v236,s[84:85]
v_cmp_gt_i32_e64 s[84:85],19,v234
v_cndmask_b32_e64 v94,v94,v236,s[82:83]
MF a[112:115],v[168:171],v[32:35],a[112:115]
v_cndmask_b32_e64 v95,v95,v236,s[84:85]
v_exp_f32 v88,v88
MF a[84:87],v[176:179],v[24:27],a[84:87]
v_exp_f32 v89,v89
MF a[116:119],v[176:179],v[32:35],a[116:119]
v_exp_f32 v90,v90
MF a[88:91],v[184:187],v[24:27],a[88:91]
v_exp_f32 v91,v91
MF a[120:123],v[184:187],v[32:35],a[120:123]
v_exp_f32 v92,v92
MF a[92:95],v[192:195],v[24:27],a[92:95]
v_exp_f32 v93,v93
MF a[124:127],v[192:195],v[32:35],a[124:127]
v_exp_f32 v94,v94
MF a[64:67],v[140:143],v[56:59],a[64:67]
v_exp_f32 v95,v95
v_cmp_gt_i32_e64 s[82:83],0,v235
MF a[96:99],v[140:143],v[64:67],a[96:99]
v_cmp_gt_i32_e64 s[84:85],1,v235
v_cndmask_b32_e64 v96,v96,v236,s[82:83]
v_cmp_gt_i32_e64 s[82:83],2,v235
MF a[68:71],v[148:151],v[56:59],a[68:71]
v_cndmask_b32_e64 v97,v97,v236,s[84:85]
v_cmp_gt_i32_e64 s[84:85],3,v235
v_cndmask_b32_e64 v98,v98,v236,s[82:83]
MF a[100:103],v[148:151],v[64:67],a[100:103]
v_cmp_gt_i32_e64 s[82:83],16,v235
v_cndmask_b32_e64 v99,v99,v236,s[84:85]
v_cmp_gt_i32_e64 s[84:85],17,v235
MF a[72:75],v[156:159],v[56:59],a[72:75]
v_cndmask_b32_e64 v100,v100,v236,s[82:83]
v_cmp_gt_i32_e64 s[82:83],18,v235
v_cndmask_b32_e64 v101,v101,v236,s[84:85]
MF a[104:107],v[156:159],v[64:67],a[104:107]
v_cmp_gt_i32_e64 s[84:85],19,v235
v_cndmask_b32_e64 v102,v102,v236,s[82:83]
s_nop 0
v_cndmask_b32_e64 v103,v103,v236,s[84:85]
MF a[76:79],v[164:167],v[56:59],a[76:79]
v_exp_f32 v96,v96
v_add_f32 v220,v88,v90
MF a[108:111],v[164:167],v[64:67],a[108:111]
v_add_f32 v221,v89,v91
v_exp_f32 v97,v97
MF a[80:83],v[172:175],v[56:59],a[80:83]
CP v88,v88,v89
v_exp_f32 v98,v98
MF a[112:115],v[172:175],v[64:67],a[112:115]
v_add_f32 v220,v92,v220
v_exp_f32 v99,v99
MF a[84:87],v[180:183],v[56:59],a[84:87]
CP v89,v90,v91
v_exp_f32 v100,v100
MF a[116:119],v[180:183],v[64:67],a[116:119]
v_add_f32 v221,v93,v221
v_add_f32 v220,v94,v220
MF a[88:91],v[188:191],v[56:59],a[88:91]
s_add_u32 s75,s75,1
s_add_u32 s59,s59,16384
s_and_b32 s59,s59,0xffff
MF a[120:123],v[188:191],v[64:67],a[120:123]
s_add_u32 s61,s61,16384
s_and_b32 s61,s61,0xffff
s_add_u32 s60,s60,16384
MF a[92:95],v[196:199],v[56:59],a[92:95]
s_and_b32 s60,s60,0xffff
s_or_b32 s60,s60,65536
s_add_u32 s62,s62,16384
MF a[124:127],v[196:199],v[64:67],a[124:127]
s_and_b32 s62,s62,0xffff
s_or_b32 s62,s62,65536
v_add_u32 v229,s60,v231
v_exp_f32 v101,v101
CP v90,v92,v93
v_exp_f32 v102,v102
v_add_f32 v221,v95,v221
v_exp_f32 v103,v103
CP v91,v94,v95
v_add_f32 v222,v96,v98
v_add_f32 v223,v97,v99
CP v96,v96,v97
v_add_f32 v222,v100,v222
CP v97,v98,v99
v_add_f32 v223,v101,v223
v_add_f32 v222,v102,v222
CP v98,v100,v101
v_add_f32 v223,v103,v223
CP v99,v102,v103
LDRAIN_%=:
ds_read_b128 v[136:139],v229
ds_read_b128 v[144:147],v229 offset:1024
ds_read_b128 v[152:155],v229 offset:2048
ds_read_b128 v[160:163],v229 offset:3072
ds_read_b128 v[168:171],v229 offset:4096
ds_read_b128 v[176:179],v229 offset:5120
ds_read_b128 v[184:187],v229 offset:6144
ds_read_b128 v[192:195],v229 offset:7168
ds_read_b128 v[140:143],v229 offset:8192
ds_read_b128 v[148:151],v229 offset:9216
ds_read_b128 v[156:159],v229 offset:10240
ds_read_b128 v[164:167],v229 offset:11264
ds_read_b128 v[172:175],v229 offset:12288
ds_read_b128 v[180:183],v229 offset:13312
ds_read_b128 v[188:191],v229 offset:14336
ds_read_b128 v[196:199],v229 offset:15360
s_sub_u32 s77,30,s58
s_sub_u32 s77,s77,s58
s_sub_u32 s77,s77,s75
s_add_u32 s77,s77,1
s_lshl_b32 s77,s77,6
v_add_u32 v234,s77,v232
v_add_u32 v235,s77,v233
v_cmp_gt_i32_e64 s[82:83],32,v234
v_cmp_gt_i32_e64 s[84:85],33,v234
s_nop 0
v_cndmask_b32_e64 v120,v120,v236,s[82:83]
v_cmp_gt_i32_e64 s[82:83],34,v234
v_cndmask_b32_e64 v121,v121,v236,s[84:85]
v_cmp_gt_i32_e64 s[84:85],35,v234
v_cndmask_b32_e64 v122,v122,v236,s[82:83]
v_cmp_gt_i32_e64 s[82:83],48,v234
v_cndmask_b32_e64 v123,v123,v236,s[84:85]
v_cmp_gt_i32_e64 s[84:85],49,v234
v_cndmask_b32_e64 v124,v124,v236,s[82:83]
v_cmp_gt_i32_e64 s[82:83],50,v234
v_cndmask_b32_e64 v125,v125,v236,s[84:85]
v_cmp_gt_i32_e64 s[84:85],51,v234
v_cndmask_b32_e64 v126,v126,v236,s[82:83]
s_nop 0
v_cndmask_b32_e64 v127,v127,v236,s[84:85]
v_exp_f32 v120,v120
v_exp_f32 v121,v121
v_exp_f32 v122,v122
v_exp_f32 v123,v123
v_exp_f32 v124,v124
v_exp_f32 v125,v125
v_exp_f32 v126,v126
v_exp_f32 v127,v127
v_cmp_gt_i32_e64 s[82:83],32,v235
v_cmp_gt_i32_e64 s[84:85],33,v235
s_nop 0
v_cndmask_b32_e64 v128,v128,v236,s[82:83]
v_cmp_gt_i32_e64 s[82:83],34,v235
v_cndmask_b32_e64 v129,v129,v236,s[84:85]
v_cmp_gt_i32_e64 s[84:85],35,v235
v_cndmask_b32_e64 v130,v130,v236,s[82:83]
v_cmp_gt_i32_e64 s[82:83],48,v235
v_cndmask_b32_e64 v131,v131,v236,s[84:85]
v_cmp_gt_i32_e64 s[84:85],49,v235
v_cndmask_b32_e64 v132,v132,v236,s[82:83]
v_cmp_gt_i32_e64 s[82:83],50,v235
v_cndmask_b32_e64 v133,v133,v236,s[84:85]
v_cmp_gt_i32_e64 s[84:85],51,v235
v_cndmask_b32_e64 v134,v134,v236,s[82:83]
s_nop 0
v_cndmask_b32_e64 v135,v135,v236,s[84:85]
v_exp_f32 v128,v128
v_add_f32 v220,v120,v220
v_add_f32 v221,v121,v221
v_exp_f32 v129,v129
CP v120,v120,v121
v_exp_f32 v130,v130
v_add_f32 v220,v122,v220
v_add_f32 v221,v123,v221
v_exp_f32 v131,v131
CP v121,v122,v123
v_exp_f32 v132,v132
v_add_f32 v220,v124,v220
v_add_f32 v221,v125,v221
v_exp_f32 v133,v133
CP v122,v124,v125
v_exp_f32 v134,v134
v_add_f32 v220,v126,v220
v_add_f32 v221,v127,v221
v_exp_f32 v135,v135
CP v123,v126,v127
v_add_f32 v222,v128,v222
v_add_f32 v223,v129,v223
v_add_f32 v222,v130,v222
v_add_f32 v223,v131,v223
v_add_f32 v222,v132,v222
v_add_f32 v223,v133,v223
v_add_f32 v222,v134,v222
v_add_f32 v223,v135,v223
v_max3_f32 v2,v220,v221,v222
v_max_f32 v2,v2,v223
v_cmp_nge_f32 vcc,0x5d800000,v2
CP v128,v128,v129
CP v129,v130,v131
CP v130,v132,v133
CP v131,v134,v135
v_add_u32 v228,s59,v231
s_cmp_eq_u64 vcc,0
s_cbranch_scc0 LSTUB12_%=
LSK12_%=:
s_waitcnt vmcnt(10) lgkmcnt(0)
s_barrier
MF a[64:67],v[136:139],v[88:91],a[64:67]
MF a[96:99],v[136:139],v[96:99],a[96:99]
MF a[68:71],v[144:147],v[88:91],a[68:71]
MF a[100:103],v[144:147],v[96:99],a[100:103]
MF a[72:75],v[152:155],v[88:91],a[72:75]
v_add_f32 v220,v220,v221
MF a[104:107],v[152:155],v[96:99],a[104:107]
MF a[76:79],v[160:163],v[88:91],a[76:79]
MF a[108:111],v[160:163],v[96:99],a[108:111]
MF a[80:83],v[168:171],v[88:91],a[80:83]
MF a[112:115],v[168:171],v[96:99],a[112:115]
MF a[84:87],v[176:179],v[88:91],a[84:87]
MF a[116:119],v[176:179],v[96:99],a[116:119]
MF a[88:91],v[184:187],v[88:91],a[88:91]
v_add_f32 v222,v222,v223
MF a[120:123],v[184:187],v[96:99],a[120:123]
MF a[92:95],v[192:195],v[88:91],a[92:95]
MF a[124:127],v[192:195],v[96:99],a[124:127]
MF a[64:67],v[140:143],v[120:123],a[64:67]
MF a[96:99],v[140:143],v[128:131],a[96:99]
MF a[68:71],v[148:151],v[120:123],a[68:71]
MF a[100:103],v[148:151],v[128:131],a[100:103]
MF a[72:75],v[156:159],v[120:123],a[72:75]
v_add_f32 v226,v226,v220
MF a[104:107],v[156:159],v[128:131],a[104:107]
MF a[76:79],v[164:167],v[120:123],a[76:79]
MF a[108:111],v[164:167],v[128:131],a[108:111]
MF a[80:83],v[172:175],v[120:123],a[80:83]
MF a[112:115],v[172:175],v[128:131],a[112:115]
MF a[84:87],v[180:183],v[120:123],a[84:87]
MF a[116:119],v[180:183],v[128:131],a[116:119]
v_add_f32 v227,v227,v222
MF a[88:91],v[188:191],v[120:123],a[88:91]
s_add_u32 s75,s75,1
s_add_u32 s59,s59,16384
s_and_b32 s59,s59,0xffff
MF a[120:123],v[188:191],v[128:131],a[120:123]
s_add_u32 s61,s61,16384
s_and_b32 s61,s61,0xffff
s_add_u32 s60,s60,16384
MF a[92:95],v[196:199],v[120:123],a[92:95]
s_and_b32 s60,s60,0xffff
s_or_b32 s60,s60,65536
s_add_u32 s62,s62,16384
MF a[124:127],v[196:199],v[128:131],a[124:127]
s_and_b32 s62,s62,0xffff
s_or_b32 s62,s62,65536
v_add_u32 v229,s60,v231
s_nop 7
s_sub_u32 s100,15,s58
s_lshl_b32 s100,s100,3
s_lshl_b32 s101,s56,1
s_add_u32 s100,s100,s101
s_mul_i32 s100,s100,159744
s_lshl_b32 s101,s57,12
s_add_u32 s78,s100,s101
v_mov_b32 v76,v226
v_mov_b32 v78,v226
s_nop 1
v_permlane32_swap_b32 v76,v78
v_add_f32 v76,v76,v78
v_mov_b32 v78,v76
s_nop 1
v_permlane16_swap_b32 v76,v78
v_add_f32 v76,v76,v78
v_rcp_f32 v76,v76
v_mov_b32 v77,v227
v_mov_b32 v79,v227
s_nop 1
v_permlane32_swap_b32 v77,v79
v_add_f32 v77,v77,v79
v_mov_b32 v79,v77
s_nop 1
v_permlane16_swap_b32 v77,v79
v_add_f32 v77,v77,v79
v_rcp_f32 v77,v77
AR v8,a64
AR v9,a65
AR v10,a66
AR v11,a67
AR v12,a68
AR v13,a69
AR v14,a70
AR v15,a71
v_mul_f32 v8,v8,v76
v_mul_f32 v9,v9,v76
v_mul_f32 v10,v10,v76
v_mul_f32 v11,v11,v76
v_mul_f32 v12,v12,v76
v_mul_f32 v13,v13,v76
v_mul_f32 v14,v14,v76
v_mul_f32 v15,v15,v76
CP v72,v8,v9
CP v73,v10,v11
CP v74,v12,v13
CP v75,v14,v15
buffer_store_dwordx4 v[72:75],v231,s[48:51],s78 offen
AR v16,a72
AR v17,a73
AR v18,a74
AR v19,a75
AR v20,a76
AR v21,a77
AR v22,a78
AR v23,a79
v_mul_f32 v16,v16,v76
v_mul_f32 v17,v17,v76
v_mul_f32 v18,v18,v76
v_mul_f32 v19,v19,v76
v_mul_f32 v20,v20,v76
v_mul_f32 v21,v21,v76
v_mul_f32 v22,v22,v76
v_mul_f32 v23,v23,v76
CP v80,v16,v17
CP v81,v18,v19
CP v82,v20,v21
CP v83,v22,v23
buffer_store_dwordx4 v[80:83],v231,s[48:51],s78 offen offset:1024
AR v40,a80
AR v41,a81
AR v42,a82
AR v43,a83
AR v44,a84
AR v45,a85
AR v46,a86
AR v47,a87
v_mul_f32 v40,v40,v76
v_mul_f32 v41,v41,v76
v_mul_f32 v42,v42,v76
v_mul_f32 v43,v43,v76
v_mul_f32 v44,v44,v76
v_mul_f32 v45,v45,v76
v_mul_f32 v46,v46,v76
v_mul_f32 v47,v47,v76
CP v104,v40,v41
CP v105,v42,v43
CP v106,v44,v45
CP v107,v46,v47
buffer_store_dwordx4 v[104:107],v231,s[48:51],s78 offen offset:2048
AR v48,a88
AR v49,a89
AR v50,a90
AR v51,a91
AR v52,a92
AR v53,a93
AR v54,a94
AR v55,a95
v_mul_f32 v48,v48,v76
v_mul_f32 v49,v49,v76
v_mul_f32 v50,v50,v76
v_mul_f32 v51,v51,v76
v_mul_f32 v52,v52,v76
v_mul_f32 v53,v53,v76
v_mul_f32 v54,v54,v76
v_mul_f32 v55,v55,v76
CP v112,v48,v49
CP v113,v50,v51
CP v114,v52,v53
CP v115,v54,v55
buffer_store_dwordx4 v[112:115],v231,s[48:51],s78 offen offset:3072
s_add_u32 s78,s78,159744
AR v8,a96
AR v9,a97
AR v10,a98
AR v11,a99
AR v12,a100
AR v13,a101
AR v14,a102
AR v15,a103
v_mul_f32 v8,v8,v77
v_mul_f32 v9,v9,v77
v_mul_f32 v10,v10,v77
v_mul_f32 v11,v11,v77
v_mul_f32 v12,v12,v77
v_mul_f32 v13,v13,v77
v_mul_f32 v14,v14,v77
v_mul_f32 v15,v15,v77
CP v72,v8,v9
CP v73,v10,v11
CP v74,v12,v13
CP v75,v14,v15
buffer_store_dwordx4 v[72:75],v231,s[48:51],s78 offen
AR v16,a104
AR v17,a105
AR v18,a106
AR v19,a107
AR v20,a108
AR v21,a109
AR v22,a110
AR v23,a111
v_mul_f32 v16,v16,v77
v_mul_f32 v17,v17,v77
v_mul_f32 v18,v18,v77
v_mul_f32 v19,v19,v77
v_mul_f32 v20,v20,v77
v_mul_f32 v21,v21,v77
v_mul_f32 v22,v22,v77
v_mul_f32 v23,v23,v77
CP v80,v16,v17
CP v81,v18,v19
CP v82,v20,v21
CP v83,v22,v23
buffer_store_dwordx4 v[80:83],v231,s[48:51],s78 offen offset:1024
AR v40,a112
AR v41,a113
AR v42,a114
AR v43,a115
AR v44,a116
AR v45,a117
AR v46,a118
AR v47,a119
v_mul_f32 v40,v40,v77
v_mul_f32 v41,v41,v77
v_mul_f32 v42,v42,v77
v_mul_f32 v43,v43,v77
v_mul_f32 v44,v44,v77
v_mul_f32 v45,v45,v77
v_mul_f32 v46,v46,v77
v_mul_f32 v47,v47,v77
CP v104,v40,v41
CP v105,v42,v43
CP v106,v44,v45
CP v107,v46,v47
buffer_store_dwordx4 v[104:107],v231,s[48:51],s78 offen offset:2048
AR v48,a120
AR v49,a121
AR v50,a122
AR v51,a123
AR v52,a124
AR v53,a125
AR v54,a126
AR v55,a127
v_mul_f32 v48,v48,v77
v_mul_f32 v49,v49,v77
v_mul_f32 v50,v50,v77
v_mul_f32 v51,v51,v77
v_mul_f32 v52,v52,v77
v_mul_f32 v53,v53,v77
v_mul_f32 v54,v54,v77
v_mul_f32 v55,v55,v77
CP v112,v48,v49
CP v113,v50,v51
CP v114,v52,v53
CP v115,v54,v55
buffer_store_dwordx4 v[112:115],v231,s[48:51],s78 offen offset:3072
s_cmp_lt_u32 s58,4
s_cbranch_scc1 LFIN_%=
s_sub_u32 s96,s58,4
s_lshl_b32 s96,s96,5
s_add_u32 s96,s96,s57
s_cmp_lg_u32 s56,0
s_cbranch_scc1 LPOLLED_%=
s_lshr_b32 s100,s96,3
s_lshl_b32 s100,s100,6
v_mov_b32 v3,s100
s_mov_b32 s76,0
LPOLL_%=:
global_load_dword v4,v3,s[26:27] sc1
s_waitcnt vmcnt(0)
v_readfirstlane_b32 s100,v4
s_add_u32 s76,s76,1
s_cmp_ge_u32 s100,8
s_cbranch_scc1 LGOT_%=
s_sleep 8
s_cmp_lt_u32 s76,0x10000
s_cbranch_scc1 LPOLL_%=
LGOT_%=:
LPOLLED_%=:
s_barrier
s_mov_b32 s36,s22
s_and_b32 s37,s23,0xffff
s_mov_b32 s38,3670016
s_mov_b32 s39,0x20000
s_lshl_b32 s100,s96,4
s_lshl_b32 s101,s56,2
s_add_u32 s100,s100,s101
v_lshrrev_b32 v2,4,v1
v_add_u32 v2,s100,v2
v_mul_u32_u24 v104,1792,v2
v_and_b32 v2,15,v1
v_lshl_add_u32 v104,v2,4,v104
v_cmp_gt_u32 vcc,12,v2
buffer_load_dwordx4 v[8:11],v104,s[36:39],0 offen sc1
buffer_load_dwordx4 v[12:15],v104,s[36:39],0 offen offset:256 sc1
buffer_load_dwordx4 v[16:19],v104,s[36:39],0 offen offset:512 sc1
buffer_load_dwordx4 v[20:23],v104,s[36:39],0 offen offset:768 sc1
buffer_load_dwordx4 v[24:27],v104,s[36:39],0 offen offset:1024 sc1
buffer_load_dwordx4 v[28:31],v104,s[36:39],0 offen offset:1280 sc1
buffer_load_dwordx4 v[32:35],v104,s[36:39],0 offen offset:1536 sc1
s_waitcnt vmcnt(6)
v_lshlrev_b32 v40,16,v8
v_and_b32 v41,0xffff0000,v8
v_lshlrev_b32 v42,16,v9
v_and_b32 v43,0xffff0000,v9
v_lshlrev_b32 v44,16,v10
v_and_b32 v45,0xffff0000,v10
v_lshlrev_b32 v46,16,v11
v_and_b32 v47,0xffff0000,v11
s_waitcnt vmcnt(5)
v_lshlrev_b32 v48,16,v12
v_and_b32 v49,0xffff0000,v12
v_lshlrev_b32 v50,16,v13
v_and_b32 v51,0xffff0000,v13
v_lshlrev_b32 v52,16,v14
v_and_b32 v53,0xffff0000,v14
v_lshlrev_b32 v54,16,v15
v_and_b32 v55,0xffff0000,v15
s_waitcnt vmcnt(4)
v_lshlrev_b32 v56,16,v16
v_and_b32 v57,0xffff0000,v16
v_lshlrev_b32 v58,16,v17
v_and_b32 v59,0xffff0000,v17
v_lshlrev_b32 v60,16,v18
v_and_b32 v61,0xffff0000,v18
v_lshlrev_b32 v62,16,v19
v_and_b32 v63,0xffff0000,v19
s_waitcnt vmcnt(3)
v_lshlrev_b32 v64,16,v20
v_and_b32 v65,0xffff0000,v20
v_lshlrev_b32 v66,16,v21
v_and_b32 v67,0xffff0000,v21
v_lshlrev_b32 v68,16,v22
v_and_b32 v69,0xffff0000,v22
v_lshlrev_b32 v70,16,v23
v_and_b32 v71,0xffff0000,v23
s_waitcnt vmcnt(2)
v_lshlrev_b32 v72,16,v24
v_and_b32 v73,0xffff0000,v24
v_lshlrev_b32 v74,16,v25
v_and_b32 v75,0xffff0000,v25
v_lshlrev_b32 v76,16,v26
v_and_b32 v77,0xffff0000,v26
v_lshlrev_b32 v78,16,v27
v_and_b32 v79,0xffff0000,v27
s_waitcnt vmcnt(1)
v_lshlrev_b32 v80,16,v28
v_and_b32 v81,0xffff0000,v28
v_lshlrev_b32 v82,16,v29
v_and_b32 v83,0xffff0000,v29
v_lshlrev_b32 v84,16,v30
v_and_b32 v85,0xffff0000,v30
v_lshlrev_b32 v86,16,v31
v_and_b32 v87,0xffff0000,v31
s_waitcnt vmcnt(0)
v_lshlrev_b32 v88,16,v32
v_and_b32 v89,0xffff0000,v32
v_lshlrev_b32 v90,16,v33
v_and_b32 v91,0xffff0000,v33
v_lshlrev_b32 v92,16,v34
v_and_b32 v93,0xffff0000,v34
v_lshlrev_b32 v94,16,v35
v_and_b32 v95,0xffff0000,v35
v_cndmask_b32 v88,0,v88,vcc
v_cndmask_b32 v89,0,v89,vcc
v_cndmask_b32 v90,0,v90,vcc
v_cndmask_b32 v91,0,v91,vcc
v_cndmask_b32 v92,0,v92,vcc
v_cndmask_b32 v93,0,v93,vcc
v_cndmask_b32 v94,0,v94,vcc
v_cndmask_b32 v95,0,v95,vcc
v_add_f32 v105,v40,v41
v_add_f32 v106,v42,v43
v_add_f32 v105,v44,v105
v_add_f32 v106,v45,v106
v_add_f32 v105,v46,v105
v_add_f32 v106,v47,v106
v_add_f32 v105,v48,v105
v_add_f32 v106,v49,v106
v_add_f32 v105,v50,v105
v_add_f32 v106,v51,v106
v_add_f32 v105,v52,v105
v_add_f32 v106,v53,v106
v_add_f32 v105,v54,v105
v_add_f32 v106,v55,v106
v_add_f32 v105,v56,v105
v_add_f32 v106,v57,v106
v_add_f32 v105,v58,v105
v_add_f32 v106,v59,v106
v_add_f32 v105,v60,v105
v_add_f32 v106,v61,v106
v_add_f32 v105,v62,v105
v_add_f32 v106,v63,v106
v_add_f32 v105,v64,v105
v_add_f32 v106,v65,v106
v_add_f32 v105,v66,v105
v_add_f32 v106,v67,v106
v_add_f32 v105,v68,v105
v_add_f32 v106,v69,v106
v_add_f32 v105,v70,v105
v_add_f32 v106,v71,v106
v_add_f32 v105,v72,v105
v_add_f32 v106,v73,v106
v_add_f32 v105,v74,v105
v_add_f32 v106,v75,v106
v_add_f32 v105,v76,v105
v_add_f32 v106,v77,v106
v_add_f32 v105,v78,v105
v_add_f32 v106,v79,v106
v_add_f32 v105,v80,v105
v_add_f32 v106,v81,v106
v_add_f32 v105,v82,v105
v_add_f32 v106,v83,v106
v_add_f32 v105,v84,v105
v_add_f32 v106,v85,v106
v_add_f32 v105,v86,v105
v_add_f32 v106,v87,v106
v_add_f32 v105,v88,v105
v_add_f32 v106,v89,v106
v_add_f32 v105,v90,v105
v_add_f32 v106,v91,v106
v_add_f32 v105,v92,v105
v_add_f32 v106,v93,v106
v_add_f32 v105,v94,v105
v_add_f32 v106,v95,v106
v_add_f32 v105,v105,v106
s_nop 1
v_add_f32_dpp v105,v105,v105 row_ror:8 row_mask:0xf bank_mask:0xf
s_nop 1
v_add_f32_dpp v105,v105,v105 row_ror:4 row_mask:0xf bank_mask:0xf
s_nop 1
v_add_f32_dpp v105,v105,v105 row_ror:2 row_mask:0xf bank_mask:0xf
s_nop 1
v_add_f32_dpp v105,v105,v105 row_ror:1 row_mask:0xf bank_mask:0xf
v_mul_f32 v105,0x3a97b426,v105
v_sub_f32 v40,v40,v105
v_sub_f32 v41,v41,v105
v_sub_f32 v42,v42,v105
v_sub_f32 v43,v43,v105
v_sub_f32 v44,v44,v105
v_sub_f32 v45,v45,v105
v_sub_f32 v46,v46,v105
v_sub_f32 v47,v47,v105
v_sub_f32 v48,v48,v105
v_sub_f32 v49,v49,v105
v_sub_f32 v50,v50,v105
v_sub_f32 v51,v51,v105
v_sub_f32 v52,v52,v105
v_sub_f32 v53,v53,v105
v_sub_f32 v54,v54,v105
v_sub_f32 v55,v55,v105
v_sub_f32 v56,v56,v105
v_sub_f32 v57,v57,v105
v_sub_f32 v58,v58,v105
v_sub_f32 v59,v59,v105
v_sub_f32 v60,v60,v105
v_sub_f32 v61,v61,v105
v_sub_f32 v62,v62,v105
v_sub_f32 v63,v63,v105
v_sub_f32 v64,v64,v105
v_sub_f32 v65,v65,v105
v_sub_f32 v66,v66,v105
v_sub_f32 v67,v67,v105
v_sub_f32 v68,v68,v105
v_sub_f32 v69,v69,v105
v_sub_f32 v70,v70,v105
v_sub_f32 v71,v71,v105
v_sub_f32 v72,v72,v105
v_sub_f32 v73,v73,v105
v_sub_f32 v74,v74,v105
v_sub_f32 v75,v75,v105
v_sub_f32 v76,v76,v105
v_sub_f32 v77,v77,v105
v_sub_f32 v78,v78,v105
v_sub_f32 v79,v79,v105
v_sub_f32 v80,v80,v105
v_sub_f32 v81,v81,v105
v_sub_f32 v82,v82,v105
v_sub_f32 v83,v83,v105
v_sub_f32 v84,v84,v105
v_sub_f32 v85,v85,v105
v_sub_f32 v86,v86,v105
v_sub_f32 v87,v87,v105
v_sub_f32 v88,v88,v105
v_sub_f32 v89,v89,v105
v_sub_f32 v90,v90,v105
v_sub_f32 v91,v91,v105
v_sub_f32 v92,v92,v105
v_sub_f32 v93,v93,v105
v_sub_f32 v94,v94,v105
v_sub_f32 v95,v95,v105
v_cndmask_b32 v88,0,v88,vcc
v_cndmask_b32 v89,0,v89,vcc
v_cndmask_b32 v90,0,v90,vcc
v_cndmask_b32 v91,0,v91,vcc
v_cndmask_b32 v92,0,v92,vcc
v_cndmask_b32 v93,0,v93,vcc
v_cndmask_b32 v94,0,v94,vcc
v_cndmask_b32 v95,0,v95,vcc
v_mul_f32 v105,v40,v40
v_mul_f32 v106,v41,v41
v_fmac_f32 v105,v42,v42
v_fmac_f32 v106,v43,v43
v_fmac_f32 v105,v44,v44
v_fmac_f32 v106,v45,v45
v_fmac_f32 v105,v46,v46
v_fmac_f32 v106,v47,v47
v_fmac_f32 v105,v48,v48
v_fmac_f32 v106,v49,v49
v_fmac_f32 v105,v50,v50
v_fmac_f32 v106,v51,v51
v_fmac_f32 v105,v52,v52
v_fmac_f32 v106,v53,v53
v_fmac_f32 v105,v54,v54
v_fmac_f32 v106,v55,v55
v_fmac_f32 v105,v56,v56
v_fmac_f32 v106,v57,v57
v_fmac_f32 v105,v58,v58
v_fmac_f32 v106,v59,v59
v_fmac_f32 v105,v60,v60
v_fmac_f32 v106,v61,v61
v_fmac_f32 v105,v62,v62
v_fmac_f32 v106,v63,v63
v_fmac_f32 v105,v64,v64
v_fmac_f32 v106,v65,v65
v_fmac_f32 v105,v66,v66
v_fmac_f32 v106,v67,v67
v_fmac_f32 v105,v68,v68
v_fmac_f32 v106,v69,v69
v_fmac_f32 v105,v70,v70
v_fmac_f32 v106,v71,v71
v_fmac_f32 v105,v72,v72
v_fmac_f32 v106,v73,v73
v_fmac_f32 v105,v74,v74
v_fmac_f32 v106,v75,v75
v_fmac_f32 v105,v76,v76
v_fmac_f32 v106,v77,v77
v_fmac_f32 v105,v78,v78
v_fmac_f32 v106,v79,v79
v_fmac_f32 v105,v80,v80
v_fmac_f32 v106,v81,v81
v_fmac_f32 v105,v82,v82
v_fmac_f32 v106,v83,v83
v_fmac_f32 v105,v84,v84
v_fmac_f32 v106,v85,v85
v_fmac_f32 v105,v86,v86
v_fmac_f32 v106,v87,v87
v_fmac_f32 v105,v88,v88
v_fmac_f32 v106,v89,v89
v_fmac_f32 v105,v90,v90
v_fmac_f32 v106,v91,v91
v_fmac_f32 v105,v92,v92
v_fmac_f32 v106,v93,v93
v_fmac_f32 v105,v94,v94
v_fmac_f32 v106,v95,v95
v_add_f32 v105,v105,v106
s_nop 1
v_add_f32_dpp v105,v105,v105 row_ror:8 row_mask:0xf bank_mask:0xf
s_nop 1
v_add_f32_dpp v105,v105,v105 row_ror:4 row_mask:0xf bank_mask:0xf
s_nop 1
v_add_f32_dpp v105,v105,v105 row_ror:2 row_mask:0xf bank_mask:0xf
s_nop 1
v_add_f32_dpp v105,v105,v105 row_ror:1 row_mask:0xf bank_mask:0xf
v_mov_b32 v106,0x3727c5ac
v_fmac_f32 v106,0x3a97b426,v105
v_rsq_f32 v106,v106
s_nop 0
v_mul_f32 v40,v40,v106
v_mul_f32 v41,v41,v106
v_mul_f32 v42,v42,v106
v_mul_f32 v43,v43,v106
v_mul_f32 v44,v44,v106
v_mul_f32 v45,v45,v106
v_mul_f32 v46,v46,v106
v_mul_f32 v47,v47,v106
v_mul_f32 v48,v48,v106
v_mul_f32 v49,v49,v106
v_mul_f32 v50,v50,v106
v_mul_f32 v51,v51,v106
v_mul_f32 v52,v52,v106
v_mul_f32 v53,v53,v106
v_mul_f32 v54,v54,v106
v_mul_f32 v55,v55,v106
v_mul_f32 v56,v56,v106
v_mul_f32 v57,v57,v106
v_mul_f32 v58,v58,v106
v_mul_f32 v59,v59,v106
v_mul_f32 v60,v60,v106
v_mul_f32 v61,v61,v106
v_mul_f32 v62,v62,v106
v_mul_f32 v63,v63,v106
v_mul_f32 v64,v64,v106
v_mul_f32 v65,v65,v106
v_mul_f32 v66,v66,v106
v_mul_f32 v67,v67,v106
v_mul_f32 v68,v68,v106
v_mul_f32 v69,v69,v106
v_mul_f32 v70,v70,v106
v_mul_f32 v71,v71,v106
v_mul_f32 v72,v72,v106
v_mul_f32 v73,v73,v106
v_mul_f32 v74,v74,v106
v_mul_f32 v75,v75,v106
v_mul_f32 v76,v76,v106
v_mul_f32 v77,v77,v106
v_mul_f32 v78,v78,v106
v_mul_f32 v79,v79,v106
v_mul_f32 v80,v80,v106
v_mul_f32 v81,v81,v106
v_mul_f32 v82,v82,v106
v_mul_f32 v83,v83,v106
v_mul_f32 v84,v84,v106
v_mul_f32 v85,v85,v106
v_mul_f32 v86,v86,v106
v_mul_f32 v87,v87,v106
v_mul_f32 v88,v88,v106
v_mul_f32 v89,v89,v106
v_mul_f32 v90,v90,v106
v_mul_f32 v91,v91,v106
v_mul_f32 v92,v92,v106
v_mul_f32 v93,v93,v106
v_mul_f32 v94,v94,v106
v_mul_f32 v95,v95,v106
CP v8,v40,v41
CP v9,v42,v43
CP v10,v44,v45
CP v11,v46,v47
CP v12,v48,v49
CP v13,v50,v51
CP v14,v52,v53
CP v15,v54,v55
CP v16,v56,v57
CP v17,v58,v59
CP v18,v60,v61
CP v19,v62,v63
CP v20,v64,v65
CP v21,v66,v67
CP v22,v68,v69
CP v23,v70,v71
CP v24,v72,v73
CP v25,v74,v75
CP v26,v76,v77
CP v27,v78,v79
CP v28,v80,v81
CP v29,v82,v83
CP v30,v84,v85
CP v31,v86,v87
CP v32,v88,v89
CP v33,v90,v91
CP v34,v92,v93
CP v35,v94,v95
s_mov_b32 s36,s24
s_and_b32 s37,s25,0xffff
s_mov_b32 s38,3670016
s_mov_b32 s39,0x20000
buffer_store_dwordx4 v[8:11],v104,s[36:39],0 offen
buffer_store_dwordx4 v[12:15],v104,s[36:39],0 offen offset:256
buffer_store_dwordx4 v[16:19],v104,s[36:39],0 offen offset:512
buffer_store_dwordx4 v[20:23],v104,s[36:39],0 offen offset:768
buffer_store_dwordx4 v[24:27],v104,s[36:39],0 offen offset:1024
buffer_store_dwordx4 v[28:31],v104,s[36:39],0 offen offset:1280
buffer_store_dwordx4 v[32:35],v104,s[36:39],0 offen offset:1536
LFIN_%=:
s_endpgm
LSTUB1_%=:
s_getpc_b64 s[80:81]
s_branch LRARE0D_%=
s_branch LSK1_%=
LSTUB2_%=:
s_getpc_b64 s[80:81]
s_branch LRARE1D_%=
s_branch LSK2_%=
LSTUB3_%=:
s_getpc_b64 s[80:81]
s_branch LRARE0D_%=
s_branch LSK3_%=
LSTUB4_%=:
s_getpc_b64 s[80:81]
s_branch LRARE1D_%=
s_branch LSK4_%=
LSTUB5_%=:
s_getpc_b64 s[80:81]
s_branch LRARE0D_%=
s_branch LSK5_%=
LSTUB6_%=:
s_getpc_b64 s[80:81]
s_branch LRARE1D_%=
s_branch LSK6_%=
LSTUB7_%=:
s_getpc_b64 s[80:81]
s_branch LRARE0S_%=
s_branch LSK7_%=
LSTUB8_%=:
s_getpc_b64 s[80:81]
s_branch LRARE1S_%=
s_branch LSK8_%=
LSTUB9_%=:
s_getpc_b64 s[80:81]
s_branch LRARE0S_%=
s_branch LSK9_%=
LSTUB10_%=:
s_getpc_b64 s[80:81]
s_branch LRARE1S_%=
s_branch LSK10_%=
LSTUB11_%=:
s_getpc_b64 s[80:81]
s_branch LRARE0S_%=
s_branch LSK11_%=
LSTUB12_%=:
s_getpc_b64 s[80:81]
s_branch LRARE1S_%=
s_branch LSK12_%=
LRARE0D_%=:
s_waitcnt vmcnt(0) lgkmcnt(0)
s_nop 15
s_sub_u32 s100,s75,1
s_lshl_b32 s100,s100,14
s_lshl_b32 s101,s57,19
s_add_u32 s100,s100,s101
buffer_load_dwordx4 a[192:195],v231,s[40:43],s100 offen
buffer_load_dwordx4 a[196:199],v231,s[40:43],s100 offen offset:1024
buffer_load_dwordx4 a[200:203],v231,s[40:43],s100 offen offset:2048
buffer_load_dwordx4 a[204:207],v231,s[40:43],s100 offen offset:3072
s_add_u32 s100,s100,4096
buffer_load_dwordx4 a[208:211],v231,s[40:43],s100 offen
buffer_load_dwordx4 a[212:215],v231,s[40:43],s100 offen offset:1024
buffer_load_dwordx4 a[216:219],v231,s[40:43],s100 offen offset:2048
buffer_load_dwordx4 a[220:223],v231,s[40:43],s100 offen offset:3072
s_add_u32 s100,s100,4096
buffer_load_dwordx4 a[224:227],v231,s[40:43],s100 offen
buffer_load_dwordx4 a[228:231],v231,s[40:43],s100 offen offset:1024
buffer_load_dwordx4 a[232:235],v231,s[40:43],s100 offen offset:2048
buffer_load_dwordx4 a[236:239],v231,s[40:43],s100 offen offset:3072
s_add_u32 s100,s100,4096
buffer_load_dwordx4 a[240:243],v231,s[40:43],s100 offen
buffer_load_dwordx4 a[244:247],v231,s[40:43],s100 offen offset:1024
buffer_load_dwordx4 a[248:251],v231,s[40:43],s100 offen offset:2048
buffer_load_dwordx4 a[252:255],v231,s[40:43],s100 offen offset:3072
s_add_u32 s100,s100,4096
s_waitcnt vmcnt(0)
MF v[8:11],a[192:195],a[128:131],0
MF v[16:19],a[192:195],a[144:147],0
MF v[24:27],a[192:195],a[160:163],0
MF v[32:35],a[192:195],a[176:179],0
MF v[8:11],a[196:199],a[132:135],v[8:11]
MF v[16:19],a[196:199],a[148:151],v[16:19]
MF v[24:27],a[196:199],a[164:167],v[24:27]
MF v[32:35],a[196:199],a[180:183],v[32:35]
MF v[8:11],a[200:203],a[136:139],v[8:11]
MF v[16:19],a[200:203],a[152:155],v[16:19]
MF v[24:27],a[200:203],a[168:171],v[24:27]
MF v[32:35],a[200:203],a[184:187],v[32:35]
MF v[8:11],a[204:207],a[140:143],v[8:11]
MF v[16:19],a[204:207],a[156:159],v[16:19]
MF v[24:27],a[204:207],a[172:175],v[24:27]
MF v[32:35],a[204:207],a[188:191],v[32:35]
MF v[12:15],a[208:211],a[128:131],0
MF v[20:23],a[208:211],a[144:147],0
MF v[28:31],a[208:211],a[160:163],0
MF v[36:39],a[208:211],a[176:179],0
MF v[12:15],a[212:215],a[132:135],v[12:15]
MF v[20:23],a[212:215],a[148:151],v[20:23]
MF v[28:31],a[212:215],a[164:167],v[28:31]
MF v[36:39],a[212:215],a[180:183],v[36:39]
MF v[12:15],a[216:219],a[136:139],v[12:15]
MF v[20:23],a[216:219],a[152:155],v[20:23]
MF v[28:31],a[216:219],a[168:171],v[28:31]
MF v[36:39],a[216:219],a[184:187],v[36:39]
MF v[12:15],a[220:223],a[140:143],v[12:15]
MF v[20:23],a[220:223],a[156:159],v[20:23]
MF v[28:31],a[220:223],a[172:175],v[28:31]
MF v[36:39],a[220:223],a[188:191],v[36:39]
MF v[40:43],a[224:227],a[128:131],0
MF v[48:51],a[224:227],a[144:147],0
MF v[56:59],a[224:227],a[160:163],0
MF v[64:67],a[224:227],a[176:179],0
MF v[40:43],a[228:231],a[132:135],v[40:43]
MF v[48:51],a[228:231],a[148:151],v[48:51]
MF v[56:59],a[228:231],a[164:167],v[56:59]
MF v[64:67],a[228:231],a[180:183],v[64:67]
MF v[40:43],a[232:235],a[136:139],v[40:43]
MF v[48:51],a[232:235],a[152:155],v[48:51]
MF v[56:59],a[232:235],a[168:171],v[56:59]
MF v[64:67],a[232:235],a[184:187],v[64:67]
MF v[40:43],a[236:239],a[140:143],v[40:43]
MF v[48:51],a[236:239],a[156:159],v[48:51]
MF v[56:59],a[236:239],a[172:175],v[56:59]
MF v[64:67],a[236:239],a[188:191],v[64:67]
MF v[44:47],a[240:243],a[128:131],0
MF v[52:55],a[240:243],a[144:147],0
MF v[60:63],a[240:243],a[160:163],0
MF v[68:71],a[240:243],a[176:179],0
MF v[44:47],a[244:247],a[132:135],v[44:47]
MF v[52:55],a[244:247],a[148:151],v[52:55]
MF v[60:63],a[244:247],a[164:167],v[60:63]
MF v[68:71],a[244:247],a[180:183],v[68:71]
MF v[44:47],a[248:251],a[136:139],v[44:47]
MF v[52:55],a[248:251],a[152:155],v[52:55]
MF v[60:63],a[248:251],a[168:171],v[60:63]
MF v[68:71],a[248:251],a[184:187],v[68:71]
MF v[44:47],a[252:255],a[140:143],v[44:47]
MF v[52:55],a[252:255],a[156:159],v[52:55]
MF v[60:63],a[252:255],a[172:175],v[60:63]
MF v[68:71],a[252:255],a[188:191],v[68:71]
s_nop 15
s_lshl_b32 s77,s58,1
s_sub_u32 s77,s77,s75
s_add_u32 s77,s77,1
s_lshl_b32 s77,s77,6
v_add_u32 v234,s77,v232
v_add_u32 v235,s77,v233
v_cmp_gt_i32_e64 s[82:83],0,v234
v_cmp_gt_i32_e64 s[84:85],1,v234
s_nop 0
v_cndmask_b32_e64 v8,v8,v236,s[82:83]
v_cmp_gt_i32_e64 s[82:83],2,v234
v_cndmask_b32_e64 v9,v9,v236,s[84:85]
v_cmp_gt_i32_e64 s[84:85],3,v234
v_cndmask_b32_e64 v10,v10,v236,s[82:83]
v_cmp_gt_i32_e64 s[82:83],16,v234
v_cndmask_b32_e64 v11,v11,v236,s[84:85]
v_cmp_gt_i32_e64 s[84:85],17,v234
v_cndmask_b32_e64 v12,v12,v236,s[82:83]
v_cmp_gt_i32_e64 s[82:83],18,v234
v_cndmask_b32_e64 v13,v13,v236,s[84:85]
v_cmp_gt_i32_e64 s[84:85],19,v234
v_cndmask_b32_e64 v14,v14,v236,s[82:83]
s_nop 0
v_cndmask_b32_e64 v15,v15,v236,s[84:85]
v_cmp_gt_i32_e64 s[82:83],32,v234
v_cmp_gt_i32_e64 s[84:85],33,v234
s_nop 0
v_cndmask_b32_e64 v40,v40,v236,s[82:83]
v_cmp_gt_i32_e64 s[82:83],34,v234
v_cndmask_b32_e64 v41,v41,v236,s[84:85]
v_cmp_gt_i32_e64 s[84:85],35,v234
v_cndmask_b32_e64 v42,v42,v236,s[82:83]
v_cmp_gt_i32_e64 s[82:83],48,v234
v_cndmask_b32_e64 v43,v43,v236,s[84:85]
v_cmp_gt_i32_e64 s[84:85],49,v234
)A"
R"A(v_cndmask_b32_e64 v44,v44,v236,s[82:83]
v_cmp_gt_i32_e64 s[82:83],50,v234
v_cndmask_b32_e64 v45,v45,v236,s[84:85]
v_cmp_gt_i32_e64 s[84:85],51,v234
v_cndmask_b32_e64 v46,v46,v236,s[82:83]
s_nop 0
v_cndmask_b32_e64 v47,v47,v236,s[84:85]
v_cmp_gt_i32_e64 s[82:83],0,v235
v_cmp_gt_i32_e64 s[84:85],1,v235
s_nop 0
v_cndmask_b32_e64 v16,v16,v236,s[82:83]
v_cmp_gt_i32_e64 s[82:83],2,v235
v_cndmask_b32_e64 v17,v17,v236,s[84:85]
v_cmp_gt_i32_e64 s[84:85],3,v235
v_cndmask_b32_e64 v18,v18,v236,s[82:83]
v_cmp_gt_i32_e64 s[82:83],16,v235
v_cndmask_b32_e64 v19,v19,v236,s[84:85]
v_cmp_gt_i32_e64 s[84:85],17,v235
v_cndmask_b32_e64 v20,v20,v236,s[82:83]
v_cmp_gt_i32_e64 s[82:83],18,v235
v_cndmask_b32_e64 v21,v21,v236,s[84:85]
v_cmp_gt_i32_e64 s[84:85],19,v235
v_cndmask_b32_e64 v22,v22,v236,s[82:83]
s_nop 0
v_cndmask_b32_e64 v23,v23,v236,s[84:85]
v_cmp_gt_i32_e64 s[82:83],32,v235
v_cmp_gt_i32_e64 s[84:85],33,v235
s_nop 0
v_cndmask_b32_e64 v48,v48,v236,s[82:83]
v_cmp_gt_i32_e64 s[82:83],34,v235
v_cndmask_b32_e64 v49,v49,v236,s[84:85]
v_cmp_gt_i32_e64 s[84:85],35,v235
v_cndmask_b32_e64 v50,v50,v236,s[82:83]
v_cmp_gt_i32_e64 s[82:83],48,v235
v_cndmask_b32_e64 v51,v51,v236,s[84:85]
v_cmp_gt_i32_e64 s[84:85],49,v235
v_cndmask_b32_e64 v52,v52,v236,s[82:83]
v_cmp_gt_i32_e64 s[82:83],50,v235
v_cndmask_b32_e64 v53,v53,v236,s[84:85]
v_cmp_gt_i32_e64 s[84:85],51,v235
v_cndmask_b32_e64 v54,v54,v236,s[82:83]
s_nop 0
v_cndmask_b32_e64 v55,v55,v236,s[84:85]
s_sub_u32 s77,30,s58
s_sub_u32 s77,s77,s58
s_sub_u32 s77,s77,s75
s_add_u32 s77,s77,1
s_lshl_b32 s77,s77,6
v_add_u32 v234,s77,v232
v_add_u32 v235,s77,v233
v_cmp_gt_i32_e64 s[82:83],0,v234
v_cmp_gt_i32_e64 s[84:85],1,v234
s_nop 0
v_cndmask_b32_e64 v24,v24,v236,s[82:83]
v_cmp_gt_i32_e64 s[82:83],2,v234
v_cndmask_b32_e64 v25,v25,v236,s[84:85]
v_cmp_gt_i32_e64 s[84:85],3,v234
v_cndmask_b32_e64 v26,v26,v236,s[82:83]
v_cmp_gt_i32_e64 s[82:83],16,v234
v_cndmask_b32_e64 v27,v27,v236,s[84:85]
v_cmp_gt_i32_e64 s[84:85],17,v234
v_cndmask_b32_e64 v28,v28,v236,s[82:83]
v_cmp_gt_i32_e64 s[82:83],18,v234
v_cndmask_b32_e64 v29,v29,v236,s[84:85]
v_cmp_gt_i32_e64 s[84:85],19,v234
v_cndmask_b32_e64 v30,v30,v236,s[82:83]
s_nop 0
v_cndmask_b32_e64 v31,v31,v236,s[84:85]
v_cmp_gt_i32_e64 s[82:83],32,v234
v_cmp_gt_i32_e64 s[84:85],33,v234
s_nop 0
v_cndmask_b32_e64 v56,v56,v236,s[82:83]
v_cmp_gt_i32_e64 s[82:83],34,v234
v_cndmask_b32_e64 v57,v57,v236,s[84:85]
v_cmp_gt_i32_e64 s[84:85],35,v234
v_cndmask_b32_e64 v58,v58,v236,s[82:83]
v_cmp_gt_i32_e64 s[82:83],48,v234
v_cndmask_b32_e64 v59,v59,v236,s[84:85]
v_cmp_gt_i32_e64 s[84:85],49,v234
v_cndmask_b32_e64 v60,v60,v236,s[82:83]
v_cmp_gt_i32_e64 s[82:83],50,v234
v_cndmask_b32_e64 v61,v61,v236,s[84:85]
v_cmp_gt_i32_e64 s[84:85],51,v234
v_cndmask_b32_e64 v62,v62,v236,s[82:83]
s_nop 0
v_cndmask_b32_e64 v63,v63,v236,s[84:85]
v_cmp_gt_i32_e64 s[82:83],0,v235
v_cmp_gt_i32_e64 s[84:85],1,v235
s_nop 0
v_cndmask_b32_e64 v32,v32,v236,s[82:83]
v_cmp_gt_i32_e64 s[82:83],2,v235
v_cndmask_b32_e64 v33,v33,v236,s[84:85]
v_cmp_gt_i32_e64 s[84:85],3,v235
v_cndmask_b32_e64 v34,v34,v236,s[82:83]
v_cmp_gt_i32_e64 s[82:83],16,v235
v_cndmask_b32_e64 v35,v35,v236,s[84:85]
v_cmp_gt_i32_e64 s[84:85],17,v235
v_cndmask_b32_e64 v36,v36,v236,s[82:83]
v_cmp_gt_i32_e64 s[82:83],18,v235
v_cndmask_b32_e64 v37,v37,v236,s[84:85]
v_cmp_gt_i32_e64 s[84:85],19,v235
v_cndmask_b32_e64 v38,v38,v236,s[82:83]
s_nop 0
v_cndmask_b32_e64 v39,v39,v236,s[84:85]
v_cmp_gt_i32_e64 s[82:83],32,v235
v_cmp_gt_i32_e64 s[84:85],33,v235
s_nop 0
v_cndmask_b32_e64 v64,v64,v236,s[82:83]
v_cmp_gt_i32_e64 s[82:83],34,v235
v_cndmask_b32_e64 v65,v65,v236,s[84:85]
v_cmp_gt_i32_e64 s[84:85],35,v235
v_cndmask_b32_e64 v66,v66,v236,s[82:83]
v_cmp_gt_i32_e64 s[82:83],48,v235
v_cndmask_b32_e64 v67,v67,v236,s[84:85]
v_cmp_gt_i32_e64 s[84:85],49,v235
v_cndmask_b32_e64 v68,v68,v236,s[82:83]
v_cmp_gt_i32_e64 s[82:83],50,v235
v_cndmask_b32_e64 v69,v69,v236,s[84:85]
v_cmp_gt_i32_e64 s[84:85],51,v235
v_cndmask_b32_e64 v70,v70,v236,s[82:83]
s_nop 0
v_cndmask_b32_e64 v71,v71,v236,s[84:85]
v_max3_f32 v216,v8,v9,v10
v_max3_f32 v216,v216,v11,v12
v_max3_f32 v216,v216,v13,v14
v_max3_f32 v216,v216,v15,v40
v_max3_f32 v216,v216,v41,v42
v_max3_f32 v216,v216,v43,v44
v_max3_f32 v216,v216,v45,v46
v_max_f32 v216,v216,v47
v_mov_b32 v217,v216
s_nop 1
v_permlane32_swap_b32 v216,v217
v_max_f32 v216,v216,v217
v_mov_b32 v217,v216
s_nop 1
v_permlane16_swap_b32 v216,v217
v_max_f32 v216,v216,v217
v_max3_f32 v218,v16,v17,v18
v_max3_f32 v218,v218,v19,v20
v_max3_f32 v218,v218,v21,v22
v_max3_f32 v218,v218,v23,v48
v_max3_f32 v218,v218,v49,v50
v_max3_f32 v218,v218,v51,v52
v_max3_f32 v218,v218,v53,v54
v_max_f32 v218,v218,v55
v_mov_b32 v219,v218
s_nop 1
v_permlane32_swap_b32 v218,v219
v_max_f32 v218,v218,v219
v_mov_b32 v219,v218
s_nop 1
v_permlane16_swap_b32 v218,v219
v_max_f32 v218,v218,v219
v_max3_f32 v220,v24,v25,v26
v_max3_f32 v220,v220,v27,v28
v_max3_f32 v220,v220,v29,v30
v_max3_f32 v220,v220,v31,v56
v_max3_f32 v220,v220,v57,v58
v_max3_f32 v220,v220,v59,v60
v_max3_f32 v220,v220,v61,v62
v_max_f32 v220,v220,v63
v_mov_b32 v221,v220
s_nop 1
v_permlane32_swap_b32 v220,v221
v_max_f32 v220,v220,v221
v_mov_b32 v221,v220
s_nop 1
v_permlane16_swap_b32 v220,v221
v_max_f32 v220,v220,v221
v_max3_f32 v222,v32,v33,v34
v_max3_f32 v222,v222,v35,v36
v_max3_f32 v222,v222,v37,v38
v_max3_f32 v222,v222,v39,v64
v_max3_f32 v222,v222,v65,v66
v_max3_f32 v222,v222,v67,v68
v_max3_f32 v222,v222,v69,v70
v_max_f32 v222,v222,v71
v_mov_b32 v223,v222
s_nop 1
v_permlane32_swap_b32 v222,v223
v_max_f32 v222,v222,v223
v_mov_b32 v223,v222
s_nop 1
v_permlane16_swap_b32 v222,v223
v_max_f32 v222,v222,v223
v_add_f32 v216,v216,v200
v_max_f32 v216,0,v216
v_sub_f32 v200,v200,v216
v_sub_f32 v201,v201,v216
v_sub_f32 v202,v202,v216
v_sub_f32 v203,v203,v216
v_sub_f32 v5,0,v216
v_exp_f32 v5,v5
s_nop 0
v_mul_f32 v224,v224,v5
AR v6,a0
v_mul_f32 v6,v6,v5
v_accvgpr_write_b32 a0,v6
AR v6,a1
v_mul_f32 v6,v6,v5
v_accvgpr_write_b32 a1,v6
AR v6,a2
v_mul_f32 v6,v6,v5
v_accvgpr_write_b32 a2,v6
AR v6,a3
v_mul_f32 v6,v6,v5
v_accvgpr_write_b32 a3,v6
AR v6,a4
v_mul_f32 v6,v6,v5
v_accvgpr_write_b32 a4,v6
AR v6,a5
v_mul_f32 v6,v6,v5
v_accvgpr_write_b32 a5,v6
AR v6,a6
v_mul_f32 v6,v6,v5
v_accvgpr_write_b32 a6,v6
AR v6,a7
v_mul_f32 v6,v6,v5
v_accvgpr_write_b32 a7,v6
AR v6,a8
v_mul_f32 v6,v6,v5
v_accvgpr_write_b32 a8,v6
AR v6,a9
v_mul_f32 v6,v6,v5
v_accvgpr_write_b32 a9,v6
AR v6,a10
v_mul_f32 v6,v6,v5
v_accvgpr_write_b32 a10,v6
AR v6,a11
v_mul_f32 v6,v6,v5
v_accvgpr_write_b32 a11,v6
AR v6,a12
v_mul_f32 v6,v6,v5
v_accvgpr_write_b32 a12,v6
AR v6,a13
v_mul_f32 v6,v6,v5
v_accvgpr_write_b32 a13,v6
AR v6,a14
v_mul_f32 v6,v6,v5
v_accvgpr_write_b32 a14,v6
AR v6,a15
v_mul_f32 v6,v6,v5
v_accvgpr_write_b32 a15,v6
AR v6,a16
v_mul_f32 v6,v6,v5
v_accvgpr_write_b32 a16,v6
AR v6,a17
v_mul_f32 v6,v6,v5
v_accvgpr_write_b32 a17,v6
AR v6,a18
v_mul_f32 v6,v6,v5
v_accvgpr_write_b32 a18,v6
AR v6,a19
v_mul_f32 v6,v6,v5
v_accvgpr_write_b32 a19,v6
AR v6,a20
v_mul_f32 v6,v6,v5
v_accvgpr_write_b32 a20,v6
AR v6,a21
v_mul_f32 v6,v6,v5
v_accvgpr_write_b32 a21,v6
AR v6,a22
v_mul_f32 v6,v6,v5
v_accvgpr_write_b32 a22,v6
AR v6,a23
v_mul_f32 v6,v6,v5
v_accvgpr_write_b32 a23,v6
AR v6,a24
v_mul_f32 v6,v6,v5
v_accvgpr_write_b32 a24,v6
AR v6,a25
v_mul_f32 v6,v6,v5
v_accvgpr_write_b32 a25,v6
AR v6,a26
v_mul_f32 v6,v6,v5
v_accvgpr_write_b32 a26,v6
AR v6,a27
v_mul_f32 v6,v6,v5
v_accvgpr_write_b32 a27,v6
AR v6,a28
v_mul_f32 v6,v6,v5
v_accvgpr_write_b32 a28,v6
AR v6,a29
v_mul_f32 v6,v6,v5
v_accvgpr_write_b32 a29,v6
AR v6,a30
v_mul_f32 v6,v6,v5
v_accvgpr_write_b32 a30,v6
AR v6,a31
v_mul_f32 v6,v6,v5
v_accvgpr_write_b32 a31,v6
v_sub_f32 v72,v72,v216
v_add_f32 v8,v8,v200
v_sub_f32 v73,v73,v216
v_add_f32 v9,v9,v200
v_sub_f32 v74,v74,v216
v_add_f32 v10,v10,v200
v_sub_f32 v75,v75,v216
v_add_f32 v11,v11,v200
v_sub_f32 v76,v76,v216
v_add_f32 v12,v12,v200
v_sub_f32 v77,v77,v216
v_add_f32 v13,v13,v200
v_sub_f32 v78,v78,v216
v_add_f32 v14,v14,v200
v_sub_f32 v79,v79,v216
v_add_f32 v15,v15,v200
v_sub_f32 v104,v104,v216
v_add_f32 v40,v40,v200
v_sub_f32 v105,v105,v216
v_add_f32 v41,v41,v200
v_sub_f32 v106,v106,v216
v_add_f32 v42,v42,v200
v_sub_f32 v107,v107,v216
v_add_f32 v43,v43,v200
v_sub_f32 v108,v108,v216
v_add_f32 v44,v44,v200
v_sub_f32 v109,v109,v216
v_add_f32 v45,v45,v200
v_sub_f32 v110,v110,v216
v_add_f32 v46,v46,v200
v_sub_f32 v111,v111,v216
v_add_f32 v47,v47,v200
v_add_f32 v218,v218,v204
v_max_f32 v218,0,v218
v_sub_f32 v204,v204,v218
v_sub_f32 v205,v205,v218
v_sub_f32 v206,v206,v218
v_sub_f32 v207,v207,v218
v_sub_f32 v5,0,v218
v_exp_f32 v5,v5
s_nop 0
v_mul_f32 v225,v225,v5
AR v6,a32
v_mul_f32 v6,v6,v5
v_accvgpr_write_b32 a32,v6
AR v6,a33
v_mul_f32 v6,v6,v5
v_accvgpr_write_b32 a33,v6
AR v6,a34
v_mul_f32 v6,v6,v5
v_accvgpr_write_b32 a34,v6
AR v6,a35
v_mul_f32 v6,v6,v5
v_accvgpr_write_b32 a35,v6
AR v6,a36
v_mul_f32 v6,v6,v5
v_accvgpr_write_b32 a36,v6
AR v6,a37
v_mul_f32 v6,v6,v5
v_accvgpr_write_b32 a37,v6
AR v6,a38
v_mul_f32 v6,v6,v5
v_accvgpr_write_b32 a38,v6
AR v6,a39
v_mul_f32 v6,v6,v5
v_accvgpr_write_b32 a39,v6
AR v6,a40
v_mul_f32 v6,v6,v5
v_accvgpr_write_b32 a40,v6
AR v6,a41
v_mul_f32 v6,v6,v5
v_accvgpr_write_b32 a41,v6
AR v6,a42
v_mul_f32 v6,v6,v5
v_accvgpr_write_b32 a42,v6
AR v6,a43
v_mul_f32 v6,v6,v5
v_accvgpr_write_b32 a43,v6
AR v6,a44
v_mul_f32 v6,v6,v5
v_accvgpr_write_b32 a44,v6
AR v6,a45
v_mul_f32 v6,v6,v5
v_accvgpr_write_b32 a45,v6
AR v6,a46
v_mul_f32 v6,v6,v5
v_accvgpr_write_b32 a46,v6
AR v6,a47
v_mul_f32 v6,v6,v5
v_accvgpr_write_b32 a47,v6
AR v6,a48
v_mul_f32 v6,v6,v5
v_accvgpr_write_b32 a48,v6
AR v6,a49
v_mul_f32 v6,v6,v5
v_accvgpr_write_b32 a49,v6
AR v6,a50
v_mul_f32 v6,v6,v5
v_accvgpr_write_b32 a50,v6
AR v6,a51
v_mul_f32 v6,v6,v5
v_accvgpr_write_b32 a51,v6
AR v6,a52
v_mul_f32 v6,v6,v5
v_accvgpr_write_b32 a52,v6
AR v6,a53
v_mul_f32 v6,v6,v5
v_accvgpr_write_b32 a53,v6
AR v6,a54
v_mul_f32 v6,v6,v5
v_accvgpr_write_b32 a54,v6
AR v6,a55
v_mul_f32 v6,v6,v5
v_accvgpr_write_b32 a55,v6
AR v6,a56
v_mul_f32 v6,v6,v5
v_accvgpr_write_b32 a56,v6
AR v6,a57
v_mul_f32 v6,v6,v5
v_accvgpr_write_b32 a57,v6
AR v6,a58
v_mul_f32 v6,v6,v5
v_accvgpr_write_b32 a58,v6
AR v6,a59
v_mul_f32 v6,v6,v5
v_accvgpr_write_b32 a59,v6
AR v6,a60
v_mul_f32 v6,v6,v5
v_accvgpr_write_b32 a60,v6
AR v6,a61
v_mul_f32 v6,v6,v5
v_accvgpr_write_b32 a61,v6
AR v6,a62
v_mul_f32 v6,v6,v5
v_accvgpr_write_b32 a62,v6
AR v6,a63
v_mul_f32 v6,v6,v5
v_accvgpr_write_b32 a63,v6
v_sub_f32 v80,v80,v218
v_add_f32 v16,v16,v204
v_sub_f32 v81,v81,v218
v_add_f32 v17,v17,v204
v_sub_f32 v82,v82,v218
v_add_f32 v18,v18,v204
v_sub_f32 v83,v83,v218
v_add_f32 v19,v19,v204
v_sub_f32 v84,v84,v218
v_add_f32 v20,v20,v204
v_sub_f32 v85,v85,v218
v_add_f32 v21,v21,v204
v_sub_f32 v86,v86,v218
v_add_f32 v22,v22,v204
v_sub_f32 v87,v87,v218
v_add_f32 v23,v23,v204
v_sub_f32 v112,v112,v218
v_add_f32 v48,v48,v204
v_sub_f32 v113,v113,v218
v_add_f32 v49,v49,v204
v_sub_f32 v114,v114,v218
v_add_f32 v50,v50,v204
v_sub_f32 v115,v115,v218
v_add_f32 v51,v51,v204
v_sub_f32 v116,v116,v218
v_add_f32 v52,v52,v204
v_sub_f32 v117,v117,v218
v_add_f32 v53,v53,v204
v_sub_f32 v118,v118,v218
v_add_f32 v54,v54,v204
v_sub_f32 v119,v119,v218
v_add_f32 v55,v55,v204
v_add_f32 v220,v220,v208
v_max_f32 v220,0,v220
v_sub_f32 v208,v208,v220
v_sub_f32 v209,v209,v220
v_sub_f32 v210,v210,v220
v_sub_f32 v211,v211,v220
v_sub_f32 v5,0,v220
v_exp_f32 v5,v5
s_nop 0
v_mul_f32 v226,v226,v5
AR v6,a64
v_mul_f32 v6,v6,v5
v_accvgpr_write_b32 a64,v6
AR v6,a65
v_mul_f32 v6,v6,v5
v_accvgpr_write_b32 a65,v6
AR v6,a66
v_mul_f32 v6,v6,v5
v_accvgpr_write_b32 a66,v6
AR v6,a67
v_mul_f32 v6,v6,v5
v_accvgpr_write_b32 a67,v6
AR v6,a68
v_mul_f32 v6,v6,v5
v_accvgpr_write_b32 a68,v6
AR v6,a69
v_mul_f32 v6,v6,v5
v_accvgpr_write_b32 a69,v6
AR v6,a70
v_mul_f32 v6,v6,v5
v_accvgpr_write_b32 a70,v6
AR v6,a71
v_mul_f32 v6,v6,v5
v_accvgpr_write_b32 a71,v6
AR v6,a72
v_mul_f32 v6,v6,v5
v_accvgpr_write_b32 a72,v6
AR v6,a73
v_mul_f32 v6,v6,v5
v_accvgpr_write_b32 a73,v6
AR v6,a74
v_mul_f32 v6,v6,v5
v_accvgpr_write_b32 a74,v6
AR v6,a75
v_mul_f32 v6,v6,v5
v_accvgpr_write_b32 a75,v6
AR v6,a76
v_mul_f32 v6,v6,v5
v_accvgpr_write_b32 a76,v6
AR v6,a77
v_mul_f32 v6,v6,v5
v_accvgpr_write_b32 a77,v6
AR v6,a78
v_mul_f32 v6,v6,v5
v_accvgpr_write_b32 a78,v6
AR v6,a79
v_mul_f32 v6,v6,v5
v_accvgpr_write_b32 a79,v6
AR v6,a80
v_mul_f32 v6,v6,v5
v_accvgpr_write_b32 a80,v6
AR v6,a81
v_mul_f32 v6,v6,v5
v_accvgpr_write_b32 a81,v6
AR v6,a82
v_mul_f32 v6,v6,v5
v_accvgpr_write_b32 a82,v6
AR v6,a83
v_mul_f32 v6,v6,v5
v_accvgpr_write_b32 a83,v6
AR v6,a84
v_mul_f32 v6,v6,v5
v_accvgpr_write_b32 a84,v6
AR v6,a85
v_mul_f32 v6,v6,v5
v_accvgpr_write_b32 a85,v6
AR v6,a86
v_mul_f32 v6,v6,v5
v_accvgpr_write_b32 a86,v6
AR v6,a87
v_mul_f32 v6,v6,v5
v_accvgpr_write_b32 a87,v6
AR v6,a88
v_mul_f32 v6,v6,v5
v_accvgpr_write_b32 a88,v6
AR v6,a89
v_mul_f32 v6,v6,v5
v_accvgpr_write_b32 a89,v6
AR v6,a90
v_mul_f32 v6,v6,v5
v_accvgpr_write_b32 a90,v6
AR v6,a91
v_mul_f32 v6,v6,v5
v_accvgpr_write_b32 a91,v6
AR v6,a92
v_mul_f32 v6,v6,v5
v_accvgpr_write_b32 a92,v6
AR v6,a93
v_mul_f32 v6,v6,v5
v_accvgpr_write_b32 a93,v6
AR v6,a94
v_mul_f32 v6,v6,v5
v_accvgpr_write_b32 a94,v6
AR v6,a95
v_mul_f32 v6,v6,v5
v_accvgpr_write_b32 a95,v6
v_sub_f32 v88,v88,v220
v_add_f32 v24,v24,v208
v_sub_f32 v89,v89,v220
v_add_f32 v25,v25,v208
v_sub_f32 v90,v90,v220
v_add_f32 v26,v26,v208
v_sub_f32 v91,v91,v220
v_add_f32 v27,v27,v208
v_sub_f32 v92,v92,v220
v_add_f32 v28,v28,v208
v_sub_f32 v93,v93,v220
v_add_f32 v29,v29,v208
v_sub_f32 v94,v94,v220
v_add_f32 v30,v30,v208
v_sub_f32 v95,v95,v220
v_add_f32 v31,v31,v208
v_sub_f32 v120,v120,v220
v_add_f32 v56,v56,v208
v_sub_f32 v121,v121,v220
v_add_f32 v57,v57,v208
v_sub_f32 v122,v122,v220
v_add_f32 v58,v58,v208
v_sub_f32 v123,v123,v220
v_add_f32 v59,v59,v208
v_sub_f32 v124,v124,v220
v_add_f32 v60,v60,v208
v_sub_f32 v125,v125,v220
v_add_f32 v61,v61,v208
v_sub_f32 v126,v126,v220
v_add_f32 v62,v62,v208
v_sub_f32 v127,v127,v220
v_add_f32 v63,v63,v208
v_add_f32 v222,v222,v212
v_max_f32 v222,0,v222
v_sub_f32 v212,v212,v222
v_sub_f32 v213,v213,v222
v_sub_f32 v214,v214,v222
v_sub_f32 v215,v215,v222
v_sub_f32 v5,0,v222
v_exp_f32 v5,v5
s_nop 0
v_mul_f32 v227,v227,v5
AR v6,a96
v_mul_f32 v6,v6,v5
v_accvgpr_write_b32 a96,v6
AR v6,a97
v_mul_f32 v6,v6,v5
v_accvgpr_write_b32 a97,v6
AR v6,a98
v_mul_f32 v6,v6,v5
v_accvgpr_write_b32 a98,v6
AR v6,a99
v_mul_f32 v6,v6,v5
v_accvgpr_write_b32 a99,v6
AR v6,a100
v_mul_f32 v6,v6,v5
v_accvgpr_write_b32 a100,v6
AR v6,a101
v_mul_f32 v6,v6,v5
v_accvgpr_write_b32 a101,v6
AR v6,a102
v_mul_f32 v6,v6,v5
v_accvgpr_write_b32 a102,v6
AR v6,a103
v_mul_f32 v6,v6,v5
v_accvgpr_write_b32 a103,v6
AR v6,a104
v_mul_f32 v6,v6,v5
v_accvgpr_write_b32 a104,v6
AR v6,a105
v_mul_f32 v6,v6,v5
v_accvgpr_write_b32 a105,v6
AR v6,a106
v_mul_f32 v6,v6,v5
v_accvgpr_write_b32 a106,v6
AR v6,a107
v_mul_f32 v6,v6,v5
v_accvgpr_write_b32 a107,v6
AR v6,a108
v_mul_f32 v6,v6,v5
v_accvgpr_write_b32 a108,v6
AR v6,a109
v_mul_f32 v6,v6,v5
v_accvgpr_write_b32 a109,v6
AR v6,a110
v_mul_f32 v6,v6,v5
v_accvgpr_write_b32 a110,v6
AR v6,a111
v_mul_f32 v6,v6,v5
v_accvgpr_write_b32 a111,v6
AR v6,a112
v_mul_f32 v6,v6,v5
v_accvgpr_write_b32 a112,v6
AR v6,a113
v_mul_f32 v6,v6,v5
v_accvgpr_write_b32 a113,v6
AR v6,a114
v_mul_f32 v6,v6,v5
v_accvgpr_write_b32 a114,v6
AR v6,a115
v_mul_f32 v6,v6,v5
v_accvgpr_write_b32 a115,v6
AR v6,a116
v_mul_f32 v6,v6,v5
v_accvgpr_write_b32 a116,v6
AR v6,a117
v_mul_f32 v6,v6,v5
v_accvgpr_write_b32 a117,v6
AR v6,a118
v_mul_f32 v6,v6,v5
v_accvgpr_write_b32 a118,v6
AR v6,a119
v_mul_f32 v6,v6,v5
v_accvgpr_write_b32 a119,v6
AR v6,a120
v_mul_f32 v6,v6,v5
v_accvgpr_write_b32 a120,v6
AR v6,a121
v_mul_f32 v6,v6,v5
v_accvgpr_write_b32 a121,v6
AR v6,a122
v_mul_f32 v6,v6,v5
v_accvgpr_write_b32 a122,v6
AR v6,a123
v_mul_f32 v6,v6,v5
v_accvgpr_write_b32 a123,v6
AR v6,a124
v_mul_f32 v6,v6,v5
v_accvgpr_write_b32 a124,v6
AR v6,a125
v_mul_f32 v6,v6,v5
v_accvgpr_write_b32 a125,v6
AR v6,a126
v_mul_f32 v6,v6,v5
v_accvgpr_write_b32 a126,v6
AR v6,a127
v_mul_f32 v6,v6,v5
v_accvgpr_write_b32 a127,v6
v_sub_f32 v96,v96,v222
v_add_f32 v32,v32,v212
v_sub_f32 v97,v97,v222
v_add_f32 v33,v33,v212
v_sub_f32 v98,v98,v222
v_add_f32 v34,v34,v212
v_sub_f32 v99,v99,v222
v_add_f32 v35,v35,v212
v_sub_f32 v100,v100,v222
v_add_f32 v36,v36,v212
v_sub_f32 v101,v101,v222
v_add_f32 v37,v37,v212
v_sub_f32 v102,v102,v222
v_add_f32 v38,v38,v212
v_sub_f32 v103,v103,v222
v_add_f32 v39,v39,v212
v_sub_f32 v128,v128,v222
v_add_f32 v64,v64,v212
v_sub_f32 v129,v129,v222
v_add_f32 v65,v65,v212
v_sub_f32 v130,v130,v222
v_add_f32 v66,v66,v212
v_sub_f32 v131,v131,v222
v_add_f32 v67,v67,v212
v_sub_f32 v132,v132,v222
v_add_f32 v68,v68,v212
v_sub_f32 v133,v133,v222
v_add_f32 v69,v69,v212
v_sub_f32 v134,v134,v222
v_add_f32 v70,v70,v212
v_sub_f32 v135,v135,v222
v_add_f32 v71,v71,v212
v_exp_f32 v8,v8
v_exp_f32 v9,v9
v_exp_f32 v10,v10
v_exp_f32 v11,v11
v_exp_f32 v12,v12
v_exp_f32 v13,v13
v_exp_f32 v14,v14
v_exp_f32 v15,v15
v_exp_f32 v16,v16
v_add_f32 v216,v8,v10
v_add_f32 v217,v9,v11
v_exp_f32 v17,v17
CP v8,v8,v9
v_exp_f32 v18,v18
v_add_f32 v216,v12,v216
v_exp_f32 v19,v19
CP v9,v10,v11
v_exp_f32 v20,v20
v_add_f32 v217,v13,v217
v_add_f32 v216,v14,v216
v_exp_f32 v21,v21
CP v10,v12,v13
v_exp_f32 v22,v22
v_add_f32 v217,v15,v217
v_exp_f32 v23,v23
CP v11,v14,v15
v_exp_f32 v24,v24
v_add_f32 v218,v16,v18
v_add_f32 v219,v17,v19
v_exp_f32 v25,v25
CP v16,v16,v17
v_exp_f32 v26,v26
v_add_f32 v218,v20,v218
v_exp_f32 v27,v27
CP v17,v18,v19
v_exp_f32 v28,v28
v_add_f32 v219,v21,v219
v_add_f32 v218,v22,v218
v_exp_f32 v29,v29
CP v18,v20,v21
v_exp_f32 v30,v30
v_add_f32 v219,v23,v219
v_exp_f32 v31,v31
CP v19,v22,v23
v_exp_f32 v32,v32
v_add_f32 v220,v24,v26
v_add_f32 v221,v25,v27
v_exp_f32 v33,v33
CP v24,v24,v25
v_exp_f32 v34,v34
v_add_f32 v220,v28,v220
v_exp_f32 v35,v35
CP v25,v26,v27
v_exp_f32 v36,v36
v_add_f32 v221,v29,v221
v_add_f32 v220,v30,v220
v_exp_f32 v37,v37
CP v26,v28,v29
v_exp_f32 v38,v38
v_add_f32 v221,v31,v221
v_exp_f32 v39,v39
CP v27,v30,v31
v_exp_f32 v40,v40
v_add_f32 v222,v32,v34
v_add_f32 v223,v33,v35
v_exp_f32 v41,v41
CP v32,v32,v33
v_exp_f32 v42,v42
v_add_f32 v222,v36,v222
v_exp_f32 v43,v43
CP v33,v34,v35
v_exp_f32 v44,v44
v_add_f32 v223,v37,v223
v_add_f32 v222,v38,v222
v_exp_f32 v45,v45
CP v34,v36,v37
v_exp_f32 v46,v46
v_add_f32 v223,v39,v223
v_exp_f32 v47,v47
CP v35,v38,v39
v_exp_f32 v48,v48
v_add_f32 v216,v40,v216
v_add_f32 v217,v41,v217
v_exp_f32 v49,v49
CP v40,v40,v41
v_exp_f32 v50,v50
v_add_f32 v216,v42,v216
v_add_f32 v217,v43,v217
v_exp_f32 v51,v51
CP v41,v42,v43
v_exp_f32 v52,v52
v_add_f32 v216,v44,v216
v_add_f32 v217,v45,v217
v_exp_f32 v53,v53
CP v42,v44,v45
v_exp_f32 v54,v54
v_add_f32 v216,v46,v216
v_add_f32 v217,v47,v217
v_exp_f32 v55,v55
CP v43,v46,v47
v_exp_f32 v56,v56
v_add_f32 v218,v48,v218
v_add_f32 v219,v49,v219
v_exp_f32 v57,v57
CP v48,v48,v49
v_exp_f32 v58,v58
v_add_f32 v218,v50,v218
v_add_f32 v219,v51,v219
v_exp_f32 v59,v59
CP v49,v50,v51
v_exp_f32 v60,v60
v_add_f32 v218,v52,v218
v_add_f32 v219,v53,v219
v_exp_f32 v61,v61
CP v50,v52,v53
v_exp_f32 v62,v62
v_add_f32 v218,v54,v218
v_add_f32 v219,v55,v219
v_exp_f32 v63,v63
CP v51,v54,v55
v_exp_f32 v64,v64
v_add_f32 v220,v56,v220
v_add_f32 v221,v57,v221
v_exp_f32 v65,v65
CP v56,v56,v57
v_exp_f32 v66,v66
v_add_f32 v220,v58,v220
v_add_f32 v221,v59,v221
v_exp_f32 v67,v67
CP v57,v58,v59
v_exp_f32 v68,v68
v_add_f32 v220,v60,v220
v_add_f32 v221,v61,v221
v_exp_f32 v69,v69
CP v58,v60,v61
v_exp_f32 v70,v70
v_add_f32 v220,v62,v220
v_add_f32 v221,v63,v221
v_exp_f32 v71,v71
CP v59,v62,v63
v_add_f32 v222,v64,v222
v_add_f32 v223,v65,v223
CP v64,v64,v65
v_add_f32 v222,v66,v222
v_add_f32 v223,v67,v223
CP v65,v66,v67
v_add_f32 v222,v68,v222
v_add_f32 v223,v69,v223
CP v66,v68,v69
v_add_f32 v222,v70,v222
v_add_f32 v223,v71,v223
CP v67,v70,v71
s_nop 3
s_add_u32 s80,s80,4
s_addc_u32 s81,s81,0
s_setpc_b64 s[80:81]
LRARE0S_%=:
s_waitcnt vmcnt(0) lgkmcnt(0)
s_nop 15
s_sub_u32 s100,s75,1
s_lshl_b32 s100,s100,14
s_lshl_b32 s101,s57,19
s_add_u32 s100,s100,s101
buffer_load_dwordx4 a[192:195],v231,s[40:43],s100 offen
buffer_load_dwordx4 a[196:199],v231,s[40:43],s100 offen offset:1024
buffer_load_dwordx4 a[200:203],v231,s[40:43],s100 offen offset:2048
buffer_load_dwordx4 a[204:207],v231,s[40:43],s100 offen offset:3072
s_add_u32 s100,s100,4096
buffer_load_dwordx4 a[208:211],v231,s[40:43],s100 offen
buffer_load_dwordx4 a[212:215],v231,s[40:43],s100 offen offset:1024
buffer_load_dwordx4 a[216:219],v231,s[40:43],s100 offen offset:2048
buffer_load_dwordx4 a[220:223],v231,s[40:43],s100 offen offset:3072
s_add_u32 s100,s100,4096
buffer_load_dwordx4 a[224:227],v231,s[40:43],s100 offen
buffer_load_dwordx4 a[228:231],v231,s[40:43],s100 offen offset:1024
buffer_load_dwordx4 a[232:235],v231,s[40:43],s100 offen offset:2048
buffer_load_dwordx4 a[236:239],v231,s[40:43],s100 offen offset:3072
s_add_u32 s100,s100,4096
buffer_load_dwordx4 a[240:243],v231,s[40:43],s100 offen
buffer_load_dwordx4 a[244:247],v231,s[40:43],s100 offen offset:1024
buffer_load_dwordx4 a[248:251],v231,s[40:43],s100 offen offset:2048
buffer_load_dwordx4 a[252:255],v231,s[40:43],s100 offen offset:3072
s_add_u32 s100,s100,4096
s_waitcnt vmcnt(0)
MF v[24:27],a[192:195],a[160:163],0
MF v[32:35],a[192:195],a[176:179],0
MF v[24:27],a[196:199],a[164:167],v[24:27]
MF v[32:35],a[196:199],a[180:183],v[32:35]
MF v[24:27],a[200:203],a[168:171],v[24:27]
MF v[32:35],a[200:203],a[184:187],v[32:35]
MF v[24:27],a[204:207],a[172:175],v[24:27]
MF v[32:35],a[204:207],a[188:191],v[32:35]
MF v[28:31],a[208:211],a[160:163],0
MF v[36:39],a[208:211],a[176:179],0
MF v[28:31],a[212:215],a[164:167],v[28:31]
MF v[36:39],a[212:215],a[180:183],v[36:39]
MF v[28:31],a[216:219],a[168:171],v[28:31]
MF v[36:39],a[216:219],a[184:187],v[36:39]
MF v[28:31],a[220:223],a[172:175],v[28:31]
MF v[36:39],a[220:223],a[188:191],v[36:39]
MF v[56:59],a[224:227],a[160:163],0
MF v[64:67],a[224:227],a[176:179],0
MF v[56:59],a[228:231],a[164:167],v[56:59]
MF v[64:67],a[228:231],a[180:183],v[64:67]
MF v[56:59],a[232:235],a[168:171],v[56:59]
MF v[64:67],a[232:235],a[184:187],v[64:67]
MF v[56:59],a[236:239],a[172:175],v[56:59]
MF v[64:67],a[236:239],a[188:191],v[64:67]
MF v[60:63],a[240:243],a[160:163],0
MF v[68:71],a[240:243],a[176:179],0
MF v[60:63],a[244:247],a[164:167],v[60:63]
MF v[68:71],a[244:247],a[180:183],v[68:71]
MF v[60:63],a[248:251],a[168:171],v[60:63]
MF v[68:71],a[248:251],a[184:187],v[68:71]
MF v[60:63],a[252:255],a[172:175],v[60:63]
MF v[68:71],a[252:255],a[188:191],v[68:71]
s_nop 15
s_sub_u32 s77,30,s58
s_sub_u32 s77,s77,s58
s_sub_u32 s77,s77,s75
s_add_u32 s77,s77,1
s_lshl_b32 s77,s77,6
v_add_u32 v234,s77,v232
v_add_u32 v235,s77,v233
v_cmp_gt_i32_e64 s[82:83],0,v234
v_cmp_gt_i32_e64 s[84:85],1,v234
s_nop 0
v_cndmask_b32_e64 v24,v24,v236,s[82:83]
v_cmp_gt_i32_e64 s[82:83],2,v234
v_cndmask_b32_e64 v25,v25,v236,s[84:85]
v_cmp_gt_i32_e64 s[84:85],3,v234
v_cndmask_b32_e64 v26,v26,v236,s[82:83]
v_cmp_gt_i32_e64 s[82:83],16,v234
v_cndmask_b32_e64 v27,v27,v236,s[84:85]
v_cmp_gt_i32_e64 s[84:85],17,v234
v_cndmask_b32_e64 v28,v28,v236,s[82:83]
v_cmp_gt_i32_e64 s[82:83],18,v234
v_cndmask_b32_e64 v29,v29,v236,s[84:85]
v_cmp_gt_i32_e64 s[84:85],19,v234
v_cndmask_b32_e64 v30,v30,v236,s[82:83]
s_nop 0
v_cndmask_b32_e64 v31,v31,v236,s[84:85]
v_cmp_gt_i32_e64 s[82:83],32,v234
v_cmp_gt_i32_e64 s[84:85],33,v234
s_nop 0
v_cndmask_b32_e64 v56,v56,v236,s[82:83]
v_cmp_gt_i32_e64 s[82:83],34,v234
v_cndmask_b32_e64 v57,v57,v236,s[84:85]
v_cmp_gt_i32_e64 s[84:85],35,v234
v_cndmask_b32_e64 v58,v58,v236,s[82:83]
v_cmp_gt_i32_e64 s[82:83],48,v234
v_cndmask_b32_e64 v59,v59,v236,s[84:85]
v_cmp_gt_i32_e64 s[84:85],49,v234
v_cndmask_b32_e64 v60,v60,v236,s[82:83]
v_cmp_gt_i32_e64 s[82:83],50,v234
v_cndmask_b32_e64 v61,v61,v236,s[84:85]
v_cmp_gt_i32_e64 s[84:85],51,v234
v_cndmask_b32_e64 v62,v62,v236,s[82:83]
s_nop 0
v_cndmask_b32_e64 v63,v63,v236,s[84:85]
v_cmp_gt_i32_e64 s[82:83],0,v235
v_cmp_gt_i32_e64 s[84:85],1,v235
s_nop 0
v_cndmask_b32_e64 v32,v32,v236,s[82:83]
v_cmp_gt_i32_e64 s[82:83],2,v235
v_cndmask_b32_e64 v33,v33,v236,s[84:85]
v_cmp_gt_i32_e64 s[84:85],3,v235
v_cndmask_b32_e64 v34,v34,v236,s[82:83]
v_cmp_gt_i32_e64 s[82:83],16,v235
v_cndmask_b32_e64 v35,v35,v236,s[84:85]
v_cmp_gt_i32_e64 s[84:85],17,v235
v_cndmask_b32_e64 v36,v36,v236,s[82:83]
v_cmp_gt_i32_e64 s[82:83],18,v235
v_cndmask_b32_e64 v37,v37,v236,s[84:85]
v_cmp_gt_i32_e64 s[84:85],19,v235
v_cndmask_b32_e64 v38,v38,v236,s[82:83]
s_nop 0
v_cndmask_b32_e64 v39,v39,v236,s[84:85]
v_cmp_gt_i32_e64 s[82:83],32,v235
v_cmp_gt_i32_e64 s[84:85],33,v235
s_nop 0
v_cndmask_b32_e64 v64,v64,v236,s[82:83]
v_cmp_gt_i32_e64 s[82:83],34,v235
v_cndmask_b32_e64 v65,v65,v236,s[84:85]
v_cmp_gt_i32_e64 s[84:85],35,v235
v_cndmask_b32_e64 v66,v66,v236,s[82:83]
v_cmp_gt_i32_e64 s[82:83],48,v235
v_cndmask_b32_e64 v67,v67,v236,s[84:85]
v_cmp_gt_i32_e64 s[84:85],49,v235
v_cndmask_b32_e64 v68,v68,v236,s[82:83]
v_cmp_gt_i32_e64 s[82:83],50,v235
v_cndmask_b32_e64 v69,v69,v236,s[84:85]
v_cmp_gt_i32_e64 s[84:85],51,v235
v_cndmask_b32_e64 v70,v70,v236,s[82:83]
s_nop 0
v_cndmask_b32_e64 v71,v71,v236,s[84:85]
v_max3_f32 v220,v24,v25,v26
v_max3_f32 v220,v220,v27,v28
v_max3_f32 v220,v220,v29,v30
v_max3_f32 v220,v220,v31,v56
v_max3_f32 v220,v220,v57,v58
v_max3_f32 v220,v220,v59,v60
v_max3_f32 v220,v220,v61,v62
v_max_f32 v220,v220,v63
v_mov_b32 v221,v220
s_nop 1
v_permlane32_swap_b32 v220,v221
v_max_f32 v220,v220,v221
v_mov_b32 v221,v220
s_nop 1
v_permlane16_swap_b32 v220,v221
v_max_f32 v220,v220,v221
v_max3_f32 v222,v32,v33,v34
v_max3_f32 v222,v222,v35,v36
v_max3_f32 v222,v222,v37,v38
v_max3_f32 v222,v222,v39,v64
v_max3_f32 v222,v222,v65,v66
v_max3_f32 v222,v222,v67,v68
v_max3_f32 v222,v222,v69,v70
v_max_f32 v222,v222,v71
v_mov_b32 v223,v222
s_nop 1
v_permlane32_swap_b32 v222,v223
v_max_f32 v222,v222,v223
v_mov_b32 v223,v222
s_nop 1
v_permlane16_swap_b32 v222,v223
v_max_f32 v222,v222,v223
v_add_f32 v220,v220,v208
v_max_f32 v220,0,v220
v_sub_f32 v208,v208,v220
v_sub_f32 v209,v209,v220
v_sub_f32 v210,v210,v220
v_sub_f32 v211,v211,v220
v_sub_f32 v5,0,v220
v_exp_f32 v5,v5
s_nop 0
v_mul_f32 v226,v226,v5
AR v6,a64
v_mul_f32 v6,v6,v5
v_accvgpr_write_b32 a64,v6
AR v6,a65
v_mul_f32 v6,v6,v5
v_accvgpr_write_b32 a65,v6
AR v6,a66
v_mul_f32 v6,v6,v5
v_accvgpr_write_b32 a66,v6
AR v6,a67
v_mul_f32 v6,v6,v5
v_accvgpr_write_b32 a67,v6
AR v6,a68
v_mul_f32 v6,v6,v5
v_accvgpr_write_b32 a68,v6
AR v6,a69
v_mul_f32 v6,v6,v5
v_accvgpr_write_b32 a69,v6
AR v6,a70
v_mul_f32 v6,v6,v5
v_accvgpr_write_b32 a70,v6
AR v6,a71
v_mul_f32 v6,v6,v5
v_accvgpr_write_b32 a71,v6
AR v6,a72
v_mul_f32 v6,v6,v5
v_accvgpr_write_b32 a72,v6
AR v6,a73
v_mul_f32 v6,v6,v5
v_accvgpr_write_b32 a73,v6
AR v6,a74
v_mul_f32 v6,v6,v5
v_accvgpr_write_b32 a74,v6
AR v6,a75
v_mul_f32 v6,v6,v5
v_accvgpr_write_b32 a75,v6
AR v6,a76
v_mul_f32 v6,v6,v5
v_accvgpr_write_b32 a76,v6
AR v6,a77
v_mul_f32 v6,v6,v5
v_accvgpr_write_b32 a77,v6
AR v6,a78
v_mul_f32 v6,v6,v5
v_accvgpr_write_b32 a78,v6
AR v6,a79
v_mul_f32 v6,v6,v5
v_accvgpr_write_b32 a79,v6
AR v6,a80
v_mul_f32 v6,v6,v5
v_accvgpr_write_b32 a80,v6
AR v6,a81
v_mul_f32 v6,v6,v5
v_accvgpr_write_b32 a81,v6
AR v6,a82
v_mul_f32 v6,v6,v5
v_accvgpr_write_b32 a82,v6
AR v6,a83
v_mul_f32 v6,v6,v5
v_accvgpr_write_b32 a83,v6
AR v6,a84
v_mul_f32 v6,v6,v5
v_accvgpr_write_b32 a84,v6
AR v6,a85
v_mul_f32 v6,v6,v5
v_accvgpr_write_b32 a85,v6
AR v6,a86
v_mul_f32 v6,v6,v5
v_accvgpr_write_b32 a86,v6
AR v6,a87
v_mul_f32 v6,v6,v5
v_accvgpr_write_b32 a87,v6
AR v6,a88
v_mul_f32 v6,v6,v5
v_accvgpr_write_b32 a88,v6
AR v6,a89
v_mul_f32 v6,v6,v5
v_accvgpr_write_b32 a89,v6
AR v6,a90
v_mul_f32 v6,v6,v5
v_accvgpr_write_b32 a90,v6
AR v6,a91
v_mul_f32 v6,v6,v5
v_accvgpr_write_b32 a91,v6
AR v6,a92
v_mul_f32 v6,v6,v5
v_accvgpr_write_b32 a92,v6
AR v6,a93
v_mul_f32 v6,v6,v5
v_accvgpr_write_b32 a93,v6
AR v6,a94
v_mul_f32 v6,v6,v5
v_accvgpr_write_b32 a94,v6
AR v6,a95
v_mul_f32 v6,v6,v5
v_accvgpr_write_b32 a95,v6
v_sub_f32 v88,v88,v220
v_add_f32 v24,v24,v208
v_sub_f32 v89,v89,v220
v_add_f32 v25,v25,v208
v_sub_f32 v90,v90,v220
v_add_f32 v26,v26,v208
v_sub_f32 v91,v91,v220
v_add_f32 v27,v27,v208
v_sub_f32 v92,v92,v220
v_add_f32 v28,v28,v208
v_sub_f32 v93,v93,v220
v_add_f32 v29,v29,v208
v_sub_f32 v94,v94,v220
v_add_f32 v30,v30,v208
v_sub_f32 v95,v95,v220
v_add_f32 v31,v31,v208
v_sub_f32 v120,v120,v220
v_add_f32 v56,v56,v208
v_sub_f32 v121,v121,v220
v_add_f32 v57,v57,v208
v_sub_f32 v122,v122,v220
v_add_f32 v58,v58,v208
v_sub_f32 v123,v123,v220
v_add_f32 v59,v59,v208
v_sub_f32 v124,v124,v220
v_add_f32 v60,v60,v208
v_sub_f32 v125,v125,v220
v_add_f32 v61,v61,v208
v_sub_f32 v126,v126,v220
v_add_f32 v62,v62,v208
v_sub_f32 v127,v127,v220
v_add_f32 v63,v63,v208
v_add_f32 v222,v222,v212
v_max_f32 v222,0,v222
v_sub_f32 v212,v212,v222
v_sub_f32 v213,v213,v222
v_sub_f32 v214,v214,v222
v_sub_f32 v215,v215,v222
v_sub_f32 v5,0,v222
v_exp_f32 v5,v5
s_nop 0
v_mul_f32 v227,v227,v5
AR v6,a96
v_mul_f32 v6,v6,v5
v_accvgpr_write_b32 a96,v6
AR v6,a97
v_mul_f32 v6,v6,v5
v_accvgpr_write_b32 a97,v6
AR v6,a98
v_mul_f32 v6,v6,v5
v_accvgpr_write_b32 a98,v6
AR v6,a99
v_mul_f32 v6,v6,v5
v_accvgpr_write_b32 a99,v6
AR v6,a100
v_mul_f32 v6,v6,v5
v_accvgpr_write_b32 a100,v6
AR v6,a101
v_mul_f32 v6,v6,v5
v_accvgpr_write_b32 a101,v6
AR v6,a102
v_mul_f32 v6,v6,v5
v_accvgpr_write_b32 a102,v6
AR v6,a103
v_mul_f32 v6,v6,v5
v_accvgpr_write_b32 a103,v6
AR v6,a104
v_mul_f32 v6,v6,v5
v_accvgpr_write_b32 a104,v6
AR v6,a105
v_mul_f32 v6,v6,v5
v_accvgpr_write_b32 a105,v6
AR v6,a106
v_mul_f32 v6,v6,v5
v_accvgpr_write_b32 a106,v6
AR v6,a107
v_mul_f32 v6,v6,v5
v_accvgpr_write_b32 a107,v6
AR v6,a108
v_mul_f32 v6,v6,v5
v_accvgpr_write_b32 a108,v6
AR v6,a109
v_mul_f32 v6,v6,v5
v_accvgpr_write_b32 a109,v6
AR v6,a110
v_mul_f32 v6,v6,v5
v_accvgpr_write_b32 a110,v6
AR v6,a111
v_mul_f32 v6,v6,v5
v_accvgpr_write_b32 a111,v6
AR v6,a112
v_mul_f32 v6,v6,v5
v_accvgpr_write_b32 a112,v6
AR v6,a113
v_mul_f32 v6,v6,v5
v_accvgpr_write_b32 a113,v6
AR v6,a114
v_mul_f32 v6,v6,v5
v_accvgpr_write_b32 a114,v6
AR v6,a115
v_mul_f32 v6,v6,v5
v_accvgpr_write_b32 a115,v6
AR v6,a116
v_mul_f32 v6,v6,v5
v_accvgpr_write_b32 a116,v6
AR v6,a117
v_mul_f32 v6,v6,v5
v_accvgpr_write_b32 a117,v6
AR v6,a118
v_mul_f32 v6,v6,v5
v_accvgpr_write_b32 a118,v6
)A"
R"A(AR v6,a119
v_mul_f32 v6,v6,v5
v_accvgpr_write_b32 a119,v6
AR v6,a120
v_mul_f32 v6,v6,v5
v_accvgpr_write_b32 a120,v6
AR v6,a121
v_mul_f32 v6,v6,v5
v_accvgpr_write_b32 a121,v6
AR v6,a122
v_mul_f32 v6,v6,v5
v_accvgpr_write_b32 a122,v6
AR v6,a123
v_mul_f32 v6,v6,v5
v_accvgpr_write_b32 a123,v6
AR v6,a124
v_mul_f32 v6,v6,v5
v_accvgpr_write_b32 a124,v6
AR v6,a125
v_mul_f32 v6,v6,v5
v_accvgpr_write_b32 a125,v6
AR v6,a126
v_mul_f32 v6,v6,v5
v_accvgpr_write_b32 a126,v6
AR v6,a127
v_mul_f32 v6,v6,v5
v_accvgpr_write_b32 a127,v6
v_sub_f32 v96,v96,v222
v_add_f32 v32,v32,v212
v_sub_f32 v97,v97,v222
v_add_f32 v33,v33,v212
v_sub_f32 v98,v98,v222
v_add_f32 v34,v34,v212
v_sub_f32 v99,v99,v222
v_add_f32 v35,v35,v212
v_sub_f32 v100,v100,v222
v_add_f32 v36,v36,v212
v_sub_f32 v101,v101,v222
v_add_f32 v37,v37,v212
v_sub_f32 v102,v102,v222
v_add_f32 v38,v38,v212
v_sub_f32 v103,v103,v222
v_add_f32 v39,v39,v212
v_sub_f32 v128,v128,v222
v_add_f32 v64,v64,v212
v_sub_f32 v129,v129,v222
v_add_f32 v65,v65,v212
v_sub_f32 v130,v130,v222
v_add_f32 v66,v66,v212
v_sub_f32 v131,v131,v222
v_add_f32 v67,v67,v212
v_sub_f32 v132,v132,v222
v_add_f32 v68,v68,v212
v_sub_f32 v133,v133,v222
v_add_f32 v69,v69,v212
v_sub_f32 v134,v134,v222
v_add_f32 v70,v70,v212
v_sub_f32 v135,v135,v222
v_add_f32 v71,v71,v212
v_exp_f32 v24,v24
v_exp_f32 v25,v25
v_exp_f32 v26,v26
v_exp_f32 v27,v27
v_exp_f32 v28,v28
v_exp_f32 v29,v29
v_exp_f32 v30,v30
v_exp_f32 v31,v31
v_exp_f32 v32,v32
v_add_f32 v220,v24,v26
v_add_f32 v221,v25,v27
v_exp_f32 v33,v33
CP v24,v24,v25
v_exp_f32 v34,v34
v_add_f32 v220,v28,v220
v_exp_f32 v35,v35
CP v25,v26,v27
v_exp_f32 v36,v36
v_add_f32 v221,v29,v221
v_add_f32 v220,v30,v220
v_exp_f32 v37,v37
CP v26,v28,v29
v_exp_f32 v38,v38
v_add_f32 v221,v31,v221
v_exp_f32 v39,v39
CP v27,v30,v31
v_exp_f32 v56,v56
v_add_f32 v222,v32,v34
v_add_f32 v223,v33,v35
v_exp_f32 v57,v57
CP v32,v32,v33
v_exp_f32 v58,v58
v_add_f32 v222,v36,v222
v_exp_f32 v59,v59
CP v33,v34,v35
v_exp_f32 v60,v60
v_add_f32 v223,v37,v223
v_add_f32 v222,v38,v222
v_exp_f32 v61,v61
CP v34,v36,v37
v_exp_f32 v62,v62
v_add_f32 v223,v39,v223
v_exp_f32 v63,v63
CP v35,v38,v39
v_exp_f32 v64,v64
v_add_f32 v220,v56,v220
v_add_f32 v221,v57,v221
v_exp_f32 v65,v65
CP v56,v56,v57
v_exp_f32 v66,v66
v_add_f32 v220,v58,v220
v_add_f32 v221,v59,v221
v_exp_f32 v67,v67
CP v57,v58,v59
v_exp_f32 v68,v68
v_add_f32 v220,v60,v220
v_add_f32 v221,v61,v221
v_exp_f32 v69,v69
CP v58,v60,v61
v_exp_f32 v70,v70
v_add_f32 v220,v62,v220
v_add_f32 v221,v63,v221
v_exp_f32 v71,v71
CP v59,v62,v63
v_add_f32 v222,v64,v222
v_add_f32 v223,v65,v223
CP v64,v64,v65
v_add_f32 v222,v66,v222
v_add_f32 v223,v67,v223
CP v65,v66,v67
v_add_f32 v222,v68,v222
v_add_f32 v223,v69,v223
CP v66,v68,v69
v_add_f32 v222,v70,v222
v_add_f32 v223,v71,v223
CP v67,v70,v71
s_nop 3
s_add_u32 s80,s80,4
s_addc_u32 s81,s81,0
s_setpc_b64 s[80:81]
LRARE1D_%=:
s_waitcnt vmcnt(0) lgkmcnt(0)
s_nop 15
s_sub_u32 s100,s75,1
s_lshl_b32 s100,s100,14
s_lshl_b32 s101,s57,19
s_add_u32 s100,s100,s101
buffer_load_dwordx4 a[192:195],v231,s[40:43],s100 offen
buffer_load_dwordx4 a[196:199],v231,s[40:43],s100 offen offset:1024
buffer_load_dwordx4 a[200:203],v231,s[40:43],s100 offen offset:2048
buffer_load_dwordx4 a[204:207],v231,s[40:43],s100 offen offset:3072
s_add_u32 s100,s100,4096
buffer_load_dwordx4 a[208:211],v231,s[40:43],s100 offen
buffer_load_dwordx4 a[212:215],v231,s[40:43],s100 offen offset:1024
buffer_load_dwordx4 a[216:219],v231,s[40:43],s100 offen offset:2048
buffer_load_dwordx4 a[220:223],v231,s[40:43],s100 offen offset:3072
s_add_u32 s100,s100,4096
buffer_load_dwordx4 a[224:227],v231,s[40:43],s100 offen
buffer_load_dwordx4 a[228:231],v231,s[40:43],s100 offen offset:1024
buffer_load_dwordx4 a[232:235],v231,s[40:43],s100 offen offset:2048
buffer_load_dwordx4 a[236:239],v231,s[40:43],s100 offen offset:3072
s_add_u32 s100,s100,4096
buffer_load_dwordx4 a[240:243],v231,s[40:43],s100 offen
buffer_load_dwordx4 a[244:247],v231,s[40:43],s100 offen offset:1024
buffer_load_dwordx4 a[248:251],v231,s[40:43],s100 offen offset:2048
buffer_load_dwordx4 a[252:255],v231,s[40:43],s100 offen offset:3072
s_add_u32 s100,s100,4096
s_waitcnt vmcnt(0)
MF v[72:75],a[192:195],a[128:131],0
MF v[80:83],a[192:195],a[144:147],0
MF v[88:91],a[192:195],a[160:163],0
MF v[96:99],a[192:195],a[176:179],0
MF v[72:75],a[196:199],a[132:135],v[72:75]
MF v[80:83],a[196:199],a[148:151],v[80:83]
MF v[88:91],a[196:199],a[164:167],v[88:91]
MF v[96:99],a[196:199],a[180:183],v[96:99]
MF v[72:75],a[200:203],a[136:139],v[72:75]
MF v[80:83],a[200:203],a[152:155],v[80:83]
MF v[88:91],a[200:203],a[168:171],v[88:91]
MF v[96:99],a[200:203],a[184:187],v[96:99]
MF v[72:75],a[204:207],a[140:143],v[72:75]
MF v[80:83],a[204:207],a[156:159],v[80:83]
MF v[88:91],a[204:207],a[172:175],v[88:91]
MF v[96:99],a[204:207],a[188:191],v[96:99]
MF v[76:79],a[208:211],a[128:131],0
MF v[84:87],a[208:211],a[144:147],0
MF v[92:95],a[208:211],a[160:163],0
MF v[100:103],a[208:211],a[176:179],0
MF v[76:79],a[212:215],a[132:135],v[76:79]
MF v[84:87],a[212:215],a[148:151],v[84:87]
MF v[92:95],a[212:215],a[164:167],v[92:95]
MF v[100:103],a[212:215],a[180:183],v[100:103]
MF v[76:79],a[216:219],a[136:139],v[76:79]
MF v[84:87],a[216:219],a[152:155],v[84:87]
MF v[92:95],a[216:219],a[168:171],v[92:95]
MF v[100:103],a[216:219],a[184:187],v[100:103]
MF v[76:79],a[220:223],a[140:143],v[76:79]
MF v[84:87],a[220:223],a[156:159],v[84:87]
MF v[92:95],a[220:223],a[172:175],v[92:95]
MF v[100:103],a[220:223],a[188:191],v[100:103]
MF v[104:107],a[224:227],a[128:131],0
MF v[112:115],a[224:227],a[144:147],0
MF v[120:123],a[224:227],a[160:163],0
MF v[128:131],a[224:227],a[176:179],0
MF v[104:107],a[228:231],a[132:135],v[104:107]
MF v[112:115],a[228:231],a[148:151],v[112:115]
MF v[120:123],a[228:231],a[164:167],v[120:123]
MF v[128:131],a[228:231],a[180:183],v[128:131]
MF v[104:107],a[232:235],a[136:139],v[104:107]
MF v[112:115],a[232:235],a[152:155],v[112:115]
MF v[120:123],a[232:235],a[168:171],v[120:123]
MF v[128:131],a[232:235],a[184:187],v[128:131]
MF v[104:107],a[236:239],a[140:143],v[104:107]
MF v[112:115],a[236:239],a[156:159],v[112:115]
MF v[120:123],a[236:239],a[172:175],v[120:123]
MF v[128:131],a[236:239],a[188:191],v[128:131]
MF v[108:111],a[240:243],a[128:131],0
MF v[116:119],a[240:243],a[144:147],0
MF v[124:127],a[240:243],a[160:163],0
MF v[132:135],a[240:243],a[176:179],0
MF v[108:111],a[244:247],a[132:135],v[108:111]
MF v[116:119],a[244:247],a[148:151],v[116:119]
MF v[124:127],a[244:247],a[164:167],v[124:127]
MF v[132:135],a[244:247],a[180:183],v[132:135]
MF v[108:111],a[248:251],a[136:139],v[108:111]
MF v[116:119],a[248:251],a[152:155],v[116:119]
MF v[124:127],a[248:251],a[168:171],v[124:127]
MF v[132:135],a[248:251],a[184:187],v[132:135]
MF v[108:111],a[252:255],a[140:143],v[108:111]
MF v[116:119],a[252:255],a[156:159],v[116:119]
MF v[124:127],a[252:255],a[172:175],v[124:127]
MF v[132:135],a[252:255],a[188:191],v[132:135]
s_nop 15
s_lshl_b32 s77,s58,1
s_sub_u32 s77,s77,s75
s_add_u32 s77,s77,1
s_lshl_b32 s77,s77,6
v_add_u32 v234,s77,v232
v_add_u32 v235,s77,v233
v_cmp_gt_i32_e64 s[82:83],0,v234
v_cmp_gt_i32_e64 s[84:85],1,v234
s_nop 0
v_cndmask_b32_e64 v72,v72,v236,s[82:83]
v_cmp_gt_i32_e64 s[82:83],2,v234
v_cndmask_b32_e64 v73,v73,v236,s[84:85]
v_cmp_gt_i32_e64 s[84:85],3,v234
v_cndmask_b32_e64 v74,v74,v236,s[82:83]
v_cmp_gt_i32_e64 s[82:83],16,v234
v_cndmask_b32_e64 v75,v75,v236,s[84:85]
v_cmp_gt_i32_e64 s[84:85],17,v234
v_cndmask_b32_e64 v76,v76,v236,s[82:83]
v_cmp_gt_i32_e64 s[82:83],18,v234
v_cndmask_b32_e64 v77,v77,v236,s[84:85]
v_cmp_gt_i32_e64 s[84:85],19,v234
v_cndmask_b32_e64 v78,v78,v236,s[82:83]
s_nop 0
v_cndmask_b32_e64 v79,v79,v236,s[84:85]
v_cmp_gt_i32_e64 s[82:83],32,v234
v_cmp_gt_i32_e64 s[84:85],33,v234
s_nop 0
v_cndmask_b32_e64 v104,v104,v236,s[82:83]
v_cmp_gt_i32_e64 s[82:83],34,v234
v_cndmask_b32_e64 v105,v105,v236,s[84:85]
v_cmp_gt_i32_e64 s[84:85],35,v234
v_cndmask_b32_e64 v106,v106,v236,s[82:83]
v_cmp_gt_i32_e64 s[82:83],48,v234
v_cndmask_b32_e64 v107,v107,v236,s[84:85]
v_cmp_gt_i32_e64 s[84:85],49,v234
v_cndmask_b32_e64 v108,v108,v236,s[82:83]
v_cmp_gt_i32_e64 s[82:83],50,v234
v_cndmask_b32_e64 v109,v109,v236,s[84:85]
v_cmp_gt_i32_e64 s[84:85],51,v234
v_cndmask_b32_e64 v110,v110,v236,s[82:83]
s_nop 0
v_cndmask_b32_e64 v111,v111,v236,s[84:85]
v_cmp_gt_i32_e64 s[82:83],0,v235
v_cmp_gt_i32_e64 s[84:85],1,v235
s_nop 0
v_cndmask_b32_e64 v80,v80,v236,s[82:83]
v_cmp_gt_i32_e64 s[82:83],2,v235
v_cndmask_b32_e64 v81,v81,v236,s[84:85]
v_cmp_gt_i32_e64 s[84:85],3,v235
v_cndmask_b32_e64 v82,v82,v236,s[82:83]
v_cmp_gt_i32_e64 s[82:83],16,v235
v_cndmask_b32_e64 v83,v83,v236,s[84:85]
v_cmp_gt_i32_e64 s[84:85],17,v235
v_cndmask_b32_e64 v84,v84,v236,s[82:83]
v_cmp_gt_i32_e64 s[82:83],18,v235
v_cndmask_b32_e64 v85,v85,v236,s[84:85]
v_cmp_gt_i32_e64 s[84:85],19,v235
v_cndmask_b32_e64 v86,v86,v236,s[82:83]
s_nop 0
v_cndmask_b32_e64 v87,v87,v236,s[84:85]
v_cmp_gt_i32_e64 s[82:83],32,v235
v_cmp_gt_i32_e64 s[84:85],33,v235
s_nop 0
v_cndmask_b32_e64 v112,v112,v236,s[82:83]
v_cmp_gt_i32_e64 s[82:83],34,v235
v_cndmask_b32_e64 v113,v113,v236,s[84:85]
v_cmp_gt_i32_e64 s[84:85],35,v235
v_cndmask_b32_e64 v114,v114,v236,s[82:83]
v_cmp_gt_i32_e64 s[82:83],48,v235
v_cndmask_b32_e64 v115,v115,v236,s[84:85]
v_cmp_gt_i32_e64 s[84:85],49,v235
v_cndmask_b32_e64 v116,v116,v236,s[82:83]
v_cmp_gt_i32_e64 s[82:83],50,v235
v_cndmask_b32_e64 v117,v117,v236,s[84:85]
v_cmp_gt_i32_e64 s[84:85],51,v235
v_cndmask_b32_e64 v118,v118,v236,s[82:83]
s_nop 0
v_cndmask_b32_e64 v119,v119,v236,s[84:85]
s_sub_u32 s77,30,s58
s_sub_u32 s77,s77,s58
s_sub_u32 s77,s77,s75
s_add_u32 s77,s77,1
s_lshl_b32 s77,s77,6
v_add_u32 v234,s77,v232
v_add_u32 v235,s77,v233
v_cmp_gt_i32_e64 s[82:83],0,v234
v_cmp_gt_i32_e64 s[84:85],1,v234
s_nop 0
v_cndmask_b32_e64 v88,v88,v236,s[82:83]
v_cmp_gt_i32_e64 s[82:83],2,v234
v_cndmask_b32_e64 v89,v89,v236,s[84:85]
v_cmp_gt_i32_e64 s[84:85],3,v234
v_cndmask_b32_e64 v90,v90,v236,s[82:83]
v_cmp_gt_i32_e64 s[82:83],16,v234
v_cndmask_b32_e64 v91,v91,v236,s[84:85]
v_cmp_gt_i32_e64 s[84:85],17,v234
v_cndmask_b32_e64 v92,v92,v236,s[82:83]
v_cmp_gt_i32_e64 s[82:83],18,v234
v_cndmask_b32_e64 v93,v93,v236,s[84:85]
v_cmp_gt_i32_e64 s[84:85],19,v234
v_cndmask_b32_e64 v94,v94,v236,s[82:83]
s_nop 0
v_cndmask_b32_e64 v95,v95,v236,s[84:85]
v_cmp_gt_i32_e64 s[82:83],32,v234
v_cmp_gt_i32_e64 s[84:85],33,v234
s_nop 0
v_cndmask_b32_e64 v120,v120,v236,s[82:83]
v_cmp_gt_i32_e64 s[82:83],34,v234
v_cndmask_b32_e64 v121,v121,v236,s[84:85]
v_cmp_gt_i32_e64 s[84:85],35,v234
v_cndmask_b32_e64 v122,v122,v236,s[82:83]
v_cmp_gt_i32_e64 s[82:83],48,v234
v_cndmask_b32_e64 v123,v123,v236,s[84:85]
v_cmp_gt_i32_e64 s[84:85],49,v234
v_cndmask_b32_e64 v124,v124,v236,s[82:83]
v_cmp_gt_i32_e64 s[82:83],50,v234
v_cndmask_b32_e64 v125,v125,v236,s[84:85]
v_cmp_gt_i32_e64 s[84:85],51,v234
v_cndmask_b32_e64 v126,v126,v236,s[82:83]
s_nop 0
v_cndmask_b32_e64 v127,v127,v236,s[84:85]
v_cmp_gt_i32_e64 s[82:83],0,v235
v_cmp_gt_i32_e64 s[84:85],1,v235
s_nop 0
v_cndmask_b32_e64 v96,v96,v236,s[82:83]
v_cmp_gt_i32_e64 s[82:83],2,v235
v_cndmask_b32_e64 v97,v97,v236,s[84:85]
v_cmp_gt_i32_e64 s[84:85],3,v235
v_cndmask_b32_e64 v98,v98,v236,s[82:83]
v_cmp_gt_i32_e64 s[82:83],16,v235
v_cndmask_b32_e64 v99,v99,v236,s[84:85]
v_cmp_gt_i32_e64 s[84:85],17,v235
v_cndmask_b32_e64 v100,v100,v236,s[82:83]
v_cmp_gt_i32_e64 s[82:83],18,v235
v_cndmask_b32_e64 v101,v101,v236,s[84:85]
v_cmp_gt_i32_e64 s[84:85],19,v235
v_cndmask_b32_e64 v102,v102,v236,s[82:83]
s_nop 0
v_cndmask_b32_e64 v103,v103,v236,s[84:85]
v_cmp_gt_i32_e64 s[82:83],32,v235
v_cmp_gt_i32_e64 s[84:85],33,v235
s_nop 0
v_cndmask_b32_e64 v128,v128,v236,s[82:83]
v_cmp_gt_i32_e64 s[82:83],34,v235
v_cndmask_b32_e64 v129,v129,v236,s[84:85]
v_cmp_gt_i32_e64 s[84:85],35,v235
v_cndmask_b32_e64 v130,v130,v236,s[82:83]
v_cmp_gt_i32_e64 s[82:83],48,v235
v_cndmask_b32_e64 v131,v131,v236,s[84:85]
v_cmp_gt_i32_e64 s[84:85],49,v235
v_cndmask_b32_e64 v132,v132,v236,s[82:83]
v_cmp_gt_i32_e64 s[82:83],50,v235
v_cndmask_b32_e64 v133,v133,v236,s[84:85]
v_cmp_gt_i32_e64 s[84:85],51,v235
v_cndmask_b32_e64 v134,v134,v236,s[82:83]
s_nop 0
v_cndmask_b32_e64 v135,v135,v236,s[84:85]
v_max3_f32 v216,v72,v73,v74
v_max3_f32 v216,v216,v75,v76
v_max3_f32 v216,v216,v77,v78
v_max3_f32 v216,v216,v79,v104
v_max3_f32 v216,v216,v105,v106
v_max3_f32 v216,v216,v107,v108
v_max3_f32 v216,v216,v109,v110
v_max_f32 v216,v216,v111
v_mov_b32 v217,v216
s_nop 1
v_permlane32_swap_b32 v216,v217
v_max_f32 v216,v216,v217
v_mov_b32 v217,v216
s_nop 1
v_permlane16_swap_b32 v216,v217
v_max_f32 v216,v216,v217
v_max3_f32 v218,v80,v81,v82
v_max3_f32 v218,v218,v83,v84
v_max3_f32 v218,v218,v85,v86
v_max3_f32 v218,v218,v87,v112
v_max3_f32 v218,v218,v113,v114
v_max3_f32 v218,v218,v115,v116
v_max3_f32 v218,v218,v117,v118
v_max_f32 v218,v218,v119
v_mov_b32 v219,v218
s_nop 1
v_permlane32_swap_b32 v218,v219
v_max_f32 v218,v218,v219
v_mov_b32 v219,v218
s_nop 1
v_permlane16_swap_b32 v218,v219
v_max_f32 v218,v218,v219
v_max3_f32 v220,v88,v89,v90
v_max3_f32 v220,v220,v91,v92
v_max3_f32 v220,v220,v93,v94
v_max3_f32 v220,v220,v95,v120
v_max3_f32 v220,v220,v121,v122
v_max3_f32 v220,v220,v123,v124
v_max3_f32 v220,v220,v125,v126
v_max_f32 v220,v220,v127
v_mov_b32 v221,v220
s_nop 1
v_permlane32_swap_b32 v220,v221
v_max_f32 v220,v220,v221
v_mov_b32 v221,v220
s_nop 1
v_permlane16_swap_b32 v220,v221
v_max_f32 v220,v220,v221
v_max3_f32 v222,v96,v97,v98
v_max3_f32 v222,v222,v99,v100
v_max3_f32 v222,v222,v101,v102
v_max3_f32 v222,v222,v103,v128
v_max3_f32 v222,v222,v129,v130
v_max3_f32 v222,v222,v131,v132
v_max3_f32 v222,v222,v133,v134
v_max_f32 v222,v222,v135
v_mov_b32 v223,v222
s_nop 1
v_permlane32_swap_b32 v222,v223
v_max_f32 v222,v222,v223
v_mov_b32 v223,v222
s_nop 1
v_permlane16_swap_b32 v222,v223
v_max_f32 v222,v222,v223
v_add_f32 v216,v216,v200
v_max_f32 v216,0,v216
v_sub_f32 v200,v200,v216
v_sub_f32 v201,v201,v216
v_sub_f32 v202,v202,v216
v_sub_f32 v203,v203,v216
v_sub_f32 v5,0,v216
v_exp_f32 v5,v5
s_nop 0
v_mul_f32 v224,v224,v5
AR v6,a0
v_mul_f32 v6,v6,v5
v_accvgpr_write_b32 a0,v6
AR v6,a1
v_mul_f32 v6,v6,v5
v_accvgpr_write_b32 a1,v6
AR v6,a2
v_mul_f32 v6,v6,v5
v_accvgpr_write_b32 a2,v6
AR v6,a3
v_mul_f32 v6,v6,v5
v_accvgpr_write_b32 a3,v6
AR v6,a4
v_mul_f32 v6,v6,v5
v_accvgpr_write_b32 a4,v6
AR v6,a5
v_mul_f32 v6,v6,v5
v_accvgpr_write_b32 a5,v6
AR v6,a6
v_mul_f32 v6,v6,v5
v_accvgpr_write_b32 a6,v6
AR v6,a7
v_mul_f32 v6,v6,v5
v_accvgpr_write_b32 a7,v6
AR v6,a8
v_mul_f32 v6,v6,v5
v_accvgpr_write_b32 a8,v6
AR v6,a9
v_mul_f32 v6,v6,v5
v_accvgpr_write_b32 a9,v6
AR v6,a10
v_mul_f32 v6,v6,v5
v_accvgpr_write_b32 a10,v6
AR v6,a11
v_mul_f32 v6,v6,v5
v_accvgpr_write_b32 a11,v6
AR v6,a12
v_mul_f32 v6,v6,v5
v_accvgpr_write_b32 a12,v6
AR v6,a13
v_mul_f32 v6,v6,v5
v_accvgpr_write_b32 a13,v6
AR v6,a14
v_mul_f32 v6,v6,v5
v_accvgpr_write_b32 a14,v6
AR v6,a15
v_mul_f32 v6,v6,v5
v_accvgpr_write_b32 a15,v6
AR v6,a16
v_mul_f32 v6,v6,v5
v_accvgpr_write_b32 a16,v6
AR v6,a17
v_mul_f32 v6,v6,v5
v_accvgpr_write_b32 a17,v6
AR v6,a18
v_mul_f32 v6,v6,v5
v_accvgpr_write_b32 a18,v6
AR v6,a19
v_mul_f32 v6,v6,v5
v_accvgpr_write_b32 a19,v6
AR v6,a20
v_mul_f32 v6,v6,v5
v_accvgpr_write_b32 a20,v6
AR v6,a21
v_mul_f32 v6,v6,v5
v_accvgpr_write_b32 a21,v6
AR v6,a22
v_mul_f32 v6,v6,v5
v_accvgpr_write_b32 a22,v6
AR v6,a23
v_mul_f32 v6,v6,v5
v_accvgpr_write_b32 a23,v6
AR v6,a24
v_mul_f32 v6,v6,v5
v_accvgpr_write_b32 a24,v6
AR v6,a25
v_mul_f32 v6,v6,v5
v_accvgpr_write_b32 a25,v6
AR v6,a26
v_mul_f32 v6,v6,v5
v_accvgpr_write_b32 a26,v6
AR v6,a27
v_mul_f32 v6,v6,v5
v_accvgpr_write_b32 a27,v6
AR v6,a28
v_mul_f32 v6,v6,v5
v_accvgpr_write_b32 a28,v6
AR v6,a29
v_mul_f32 v6,v6,v5
v_accvgpr_write_b32 a29,v6
AR v6,a30
v_mul_f32 v6,v6,v5
v_accvgpr_write_b32 a30,v6
AR v6,a31
v_mul_f32 v6,v6,v5
v_accvgpr_write_b32 a31,v6
v_sub_f32 v8,v8,v216
v_add_f32 v72,v72,v200
v_sub_f32 v9,v9,v216
v_add_f32 v73,v73,v200
v_sub_f32 v10,v10,v216
v_add_f32 v74,v74,v200
v_sub_f32 v11,v11,v216
v_add_f32 v75,v75,v200
v_sub_f32 v12,v12,v216
v_add_f32 v76,v76,v200
v_sub_f32 v13,v13,v216
v_add_f32 v77,v77,v200
v_sub_f32 v14,v14,v216
v_add_f32 v78,v78,v200
v_sub_f32 v15,v15,v216
v_add_f32 v79,v79,v200
v_sub_f32 v40,v40,v216
v_add_f32 v104,v104,v200
v_sub_f32 v41,v41,v216
v_add_f32 v105,v105,v200
v_sub_f32 v42,v42,v216
v_add_f32 v106,v106,v200
v_sub_f32 v43,v43,v216
v_add_f32 v107,v107,v200
v_sub_f32 v44,v44,v216
v_add_f32 v108,v108,v200
v_sub_f32 v45,v45,v216
v_add_f32 v109,v109,v200
v_sub_f32 v46,v46,v216
v_add_f32 v110,v110,v200
v_sub_f32 v47,v47,v216
v_add_f32 v111,v111,v200
v_add_f32 v218,v218,v204
v_max_f32 v218,0,v218
v_sub_f32 v204,v204,v218
v_sub_f32 v205,v205,v218
v_sub_f32 v206,v206,v218
v_sub_f32 v207,v207,v218
v_sub_f32 v5,0,v218
v_exp_f32 v5,v5
s_nop 0
v_mul_f32 v225,v225,v5
AR v6,a32
v_mul_f32 v6,v6,v5
v_accvgpr_write_b32 a32,v6
AR v6,a33
v_mul_f32 v6,v6,v5
v_accvgpr_write_b32 a33,v6
AR v6,a34
v_mul_f32 v6,v6,v5
v_accvgpr_write_b32 a34,v6
AR v6,a35
v_mul_f32 v6,v6,v5
v_accvgpr_write_b32 a35,v6
AR v6,a36
v_mul_f32 v6,v6,v5
v_accvgpr_write_b32 a36,v6
AR v6,a37
v_mul_f32 v6,v6,v5
v_accvgpr_write_b32 a37,v6
AR v6,a38
v_mul_f32 v6,v6,v5
v_accvgpr_write_b32 a38,v6
AR v6,a39
v_mul_f32 v6,v6,v5
v_accvgpr_write_b32 a39,v6
AR v6,a40
v_mul_f32 v6,v6,v5
v_accvgpr_write_b32 a40,v6
AR v6,a41
v_mul_f32 v6,v6,v5
v_accvgpr_write_b32 a41,v6
AR v6,a42
v_mul_f32 v6,v6,v5
v_accvgpr_write_b32 a42,v6
AR v6,a43
v_mul_f32 v6,v6,v5
v_accvgpr_write_b32 a43,v6
AR v6,a44
v_mul_f32 v6,v6,v5
v_accvgpr_write_b32 a44,v6
AR v6,a45
v_mul_f32 v6,v6,v5
v_accvgpr_write_b32 a45,v6
AR v6,a46
v_mul_f32 v6,v6,v5
v_accvgpr_write_b32 a46,v6
AR v6,a47
v_mul_f32 v6,v6,v5
v_accvgpr_write_b32 a47,v6
AR v6,a48
v_mul_f32 v6,v6,v5
v_accvgpr_write_b32 a48,v6
AR v6,a49
v_mul_f32 v6,v6,v5
v_accvgpr_write_b32 a49,v6
AR v6,a50
v_mul_f32 v6,v6,v5
v_accvgpr_write_b32 a50,v6
AR v6,a51
v_mul_f32 v6,v6,v5
v_accvgpr_write_b32 a51,v6
AR v6,a52
v_mul_f32 v6,v6,v5
v_accvgpr_write_b32 a52,v6
AR v6,a53
v_mul_f32 v6,v6,v5
v_accvgpr_write_b32 a53,v6
AR v6,a54
v_mul_f32 v6,v6,v5
v_accvgpr_write_b32 a54,v6
AR v6,a55
v_mul_f32 v6,v6,v5
v_accvgpr_write_b32 a55,v6
AR v6,a56
v_mul_f32 v6,v6,v5
v_accvgpr_write_b32 a56,v6
AR v6,a57
v_mul_f32 v6,v6,v5
v_accvgpr_write_b32 a57,v6
AR v6,a58
v_mul_f32 v6,v6,v5
v_accvgpr_write_b32 a58,v6
AR v6,a59
v_mul_f32 v6,v6,v5
v_accvgpr_write_b32 a59,v6
AR v6,a60
v_mul_f32 v6,v6,v5
v_accvgpr_write_b32 a60,v6
AR v6,a61
v_mul_f32 v6,v6,v5
v_accvgpr_write_b32 a61,v6
AR v6,a62
v_mul_f32 v6,v6,v5
v_accvgpr_write_b32 a62,v6
AR v6,a63
v_mul_f32 v6,v6,v5
v_accvgpr_write_b32 a63,v6
v_sub_f32 v16,v16,v218
v_add_f32 v80,v80,v204
v_sub_f32 v17,v17,v218
v_add_f32 v81,v81,v204
v_sub_f32 v18,v18,v218
v_add_f32 v82,v82,v204
v_sub_f32 v19,v19,v218
v_add_f32 v83,v83,v204
v_sub_f32 v20,v20,v218
v_add_f32 v84,v84,v204
v_sub_f32 v21,v21,v218
v_add_f32 v85,v85,v204
v_sub_f32 v22,v22,v218
v_add_f32 v86,v86,v204
v_sub_f32 v23,v23,v218
v_add_f32 v87,v87,v204
v_sub_f32 v48,v48,v218
v_add_f32 v112,v112,v204
v_sub_f32 v49,v49,v218
v_add_f32 v113,v113,v204
v_sub_f32 v50,v50,v218
v_add_f32 v114,v114,v204
v_sub_f32 v51,v51,v218
v_add_f32 v115,v115,v204
v_sub_f32 v52,v52,v218
v_add_f32 v116,v116,v204
v_sub_f32 v53,v53,v218
v_add_f32 v117,v117,v204
v_sub_f32 v54,v54,v218
v_add_f32 v118,v118,v204
v_sub_f32 v55,v55,v218
v_add_f32 v119,v119,v204
v_add_f32 v220,v220,v208
v_max_f32 v220,0,v220
v_sub_f32 v208,v208,v220
v_sub_f32 v209,v209,v220
v_sub_f32 v210,v210,v220
v_sub_f32 v211,v211,v220
v_sub_f32 v5,0,v220
v_exp_f32 v5,v5
s_nop 0
v_mul_f32 v226,v226,v5
AR v6,a64
v_mul_f32 v6,v6,v5
v_accvgpr_write_b32 a64,v6
AR v6,a65
v_mul_f32 v6,v6,v5
v_accvgpr_write_b32 a65,v6
AR v6,a66
v_mul_f32 v6,v6,v5
v_accvgpr_write_b32 a66,v6
AR v6,a67
v_mul_f32 v6,v6,v5
v_accvgpr_write_b32 a67,v6
AR v6,a68
v_mul_f32 v6,v6,v5
v_accvgpr_write_b32 a68,v6
AR v6,a69
v_mul_f32 v6,v6,v5
v_accvgpr_write_b32 a69,v6
AR v6,a70
v_mul_f32 v6,v6,v5
v_accvgpr_write_b32 a70,v6
AR v6,a71
v_mul_f32 v6,v6,v5
v_accvgpr_write_b32 a71,v6
AR v6,a72
v_mul_f32 v6,v6,v5
v_accvgpr_write_b32 a72,v6
AR v6,a73
v_mul_f32 v6,v6,v5
v_accvgpr_write_b32 a73,v6
AR v6,a74
v_mul_f32 v6,v6,v5
v_accvgpr_write_b32 a74,v6
AR v6,a75
v_mul_f32 v6,v6,v5
v_accvgpr_write_b32 a75,v6
AR v6,a76
v_mul_f32 v6,v6,v5
v_accvgpr_write_b32 a76,v6
AR v6,a77
v_mul_f32 v6,v6,v5
v_accvgpr_write_b32 a77,v6
AR v6,a78
v_mul_f32 v6,v6,v5
v_accvgpr_write_b32 a78,v6
AR v6,a79
v_mul_f32 v6,v6,v5
v_accvgpr_write_b32 a79,v6
AR v6,a80
v_mul_f32 v6,v6,v5
v_accvgpr_write_b32 a80,v6
AR v6,a81
v_mul_f32 v6,v6,v5
v_accvgpr_write_b32 a81,v6
AR v6,a82
v_mul_f32 v6,v6,v5
v_accvgpr_write_b32 a82,v6
AR v6,a83
v_mul_f32 v6,v6,v5
v_accvgpr_write_b32 a83,v6
AR v6,a84
v_mul_f32 v6,v6,v5
v_accvgpr_write_b32 a84,v6
AR v6,a85
v_mul_f32 v6,v6,v5
v_accvgpr_write_b32 a85,v6
AR v6,a86
v_mul_f32 v6,v6,v5
v_accvgpr_write_b32 a86,v6
AR v6,a87
v_mul_f32 v6,v6,v5
v_accvgpr_write_b32 a87,v6
AR v6,a88
v_mul_f32 v6,v6,v5
v_accvgpr_write_b32 a88,v6
AR v6,a89
v_mul_f32 v6,v6,v5
v_accvgpr_write_b32 a89,v6
AR v6,a90
v_mul_f32 v6,v6,v5
v_accvgpr_write_b32 a90,v6
AR v6,a91
v_mul_f32 v6,v6,v5
v_accvgpr_write_b32 a91,v6
AR v6,a92
v_mul_f32 v6,v6,v5
v_accvgpr_write_b32 a92,v6
AR v6,a93
v_mul_f32 v6,v6,v5
v_accvgpr_write_b32 a93,v6
AR v6,a94
v_mul_f32 v6,v6,v5
v_accvgpr_write_b32 a94,v6
AR v6,a95
v_mul_f32 v6,v6,v5
v_accvgpr_write_b32 a95,v6
v_sub_f32 v24,v24,v220
v_add_f32 v88,v88,v208
v_sub_f32 v25,v25,v220
v_add_f32 v89,v89,v208
v_sub_f32 v26,v26,v220
v_add_f32 v90,v90,v208
v_sub_f32 v27,v27,v220
v_add_f32 v91,v91,v208
v_sub_f32 v28,v28,v220
v_add_f32 v92,v92,v208
v_sub_f32 v29,v29,v220
v_add_f32 v93,v93,v208
v_sub_f32 v30,v30,v220
v_add_f32 v94,v94,v208
v_sub_f32 v31,v31,v220
v_add_f32 v95,v95,v208
v_sub_f32 v56,v56,v220
v_add_f32 v120,v120,v208
v_sub_f32 v57,v57,v220
v_add_f32 v121,v121,v208
v_sub_f32 v58,v58,v220
v_add_f32 v122,v122,v208
v_sub_f32 v59,v59,v220
v_add_f32 v123,v123,v208
v_sub_f32 v60,v60,v220
v_add_f32 v124,v124,v208
v_sub_f32 v61,v61,v220
v_add_f32 v125,v125,v208
v_sub_f32 v62,v62,v220
v_add_f32 v126,v126,v208
v_sub_f32 v63,v63,v220
v_add_f32 v127,v127,v208
v_add_f32 v222,v222,v212
v_max_f32 v222,0,v222
v_sub_f32 v212,v212,v222
v_sub_f32 v213,v213,v222
v_sub_f32 v214,v214,v222
v_sub_f32 v215,v215,v222
v_sub_f32 v5,0,v222
v_exp_f32 v5,v5
s_nop 0
v_mul_f32 v227,v227,v5
AR v6,a96
v_mul_f32 v6,v6,v5
v_accvgpr_write_b32 a96,v6
AR v6,a97
v_mul_f32 v6,v6,v5
v_accvgpr_write_b32 a97,v6
AR v6,a98
v_mul_f32 v6,v6,v5
v_accvgpr_write_b32 a98,v6
AR v6,a99
v_mul_f32 v6,v6,v5
v_accvgpr_write_b32 a99,v6
AR v6,a100
v_mul_f32 v6,v6,v5
v_accvgpr_write_b32 a100,v6
AR v6,a101
v_mul_f32 v6,v6,v5
v_accvgpr_write_b32 a101,v6
AR v6,a102
v_mul_f32 v6,v6,v5
v_accvgpr_write_b32 a102,v6
AR v6,a103
v_mul_f32 v6,v6,v5
v_accvgpr_write_b32 a103,v6
AR v6,a104
v_mul_f32 v6,v6,v5
v_accvgpr_write_b32 a104,v6
AR v6,a105
v_mul_f32 v6,v6,v5
v_accvgpr_write_b32 a105,v6
AR v6,a106
v_mul_f32 v6,v6,v5
v_accvgpr_write_b32 a106,v6
AR v6,a107
v_mul_f32 v6,v6,v5
v_accvgpr_write_b32 a107,v6
AR v6,a108
v_mul_f32 v6,v6,v5
v_accvgpr_write_b32 a108,v6
AR v6,a109
v_mul_f32 v6,v6,v5
v_accvgpr_write_b32 a109,v6
AR v6,a110
v_mul_f32 v6,v6,v5
v_accvgpr_write_b32 a110,v6
AR v6,a111
v_mul_f32 v6,v6,v5
v_accvgpr_write_b32 a111,v6
AR v6,a112
v_mul_f32 v6,v6,v5
v_accvgpr_write_b32 a112,v6
AR v6,a113
v_mul_f32 v6,v6,v5
v_accvgpr_write_b32 a113,v6
AR v6,a114
v_mul_f32 v6,v6,v5
v_accvgpr_write_b32 a114,v6
AR v6,a115
v_mul_f32 v6,v6,v5
v_accvgpr_write_b32 a115,v6
AR v6,a116
v_mul_f32 v6,v6,v5
v_accvgpr_write_b32 a116,v6
AR v6,a117
v_mul_f32 v6,v6,v5
v_accvgpr_write_b32 a117,v6
AR v6,a118
v_mul_f32 v6,v6,v5
v_accvgpr_write_b32 a118,v6
AR v6,a119
v_mul_f32 v6,v6,v5
v_accvgpr_write_b32 a119,v6
AR v6,a120
v_mul_f32 v6,v6,v5
v_accvgpr_write_b32 a120,v6
AR v6,a121
v_mul_f32 v6,v6,v5
v_accvgpr_write_b32 a121,v6
AR v6,a122
v_mul_f32 v6,v6,v5
v_accvgpr_write_b32 a122,v6
AR v6,a123
v_mul_f32 v6,v6,v5
v_accvgpr_write_b32 a123,v6
AR v6,a124
v_mul_f32 v6,v6,v5
v_accvgpr_write_b32 a124,v6
AR v6,a125
v_mul_f32 v6,v6,v5
v_accvgpr_write_b32 a125,v6
AR v6,a126
v_mul_f32 v6,v6,v5
v_accvgpr_write_b32 a126,v6
AR v6,a127
v_mul_f32 v6,v6,v5
v_accvgpr_write_b32 a127,v6
v_sub_f32 v32,v32,v222
v_add_f32 v96,v96,v212
v_sub_f32 v33,v33,v222
v_add_f32 v97,v97,v212
v_sub_f32 v34,v34,v222
v_add_f32 v98,v98,v212
v_sub_f32 v35,v35,v222
v_add_f32 v99,v99,v212
v_sub_f32 v36,v36,v222
v_add_f32 v100,v100,v212
v_sub_f32 v37,v37,v222
v_add_f32 v101,v101,v212
v_sub_f32 v38,v38,v222
v_add_f32 v102,v102,v212
v_sub_f32 v39,v39,v222
v_add_f32 v103,v103,v212
v_sub_f32 v64,v64,v222
v_add_f32 v128,v128,v212
v_sub_f32 v65,v65,v222
v_add_f32 v129,v129,v212
v_sub_f32 v66,v66,v222
v_add_f32 v130,v130,v212
v_sub_f32 v67,v67,v222
v_add_f32 v131,v131,v212
v_sub_f32 v68,v68,v222
v_add_f32 v132,v132,v212
v_sub_f32 v69,v69,v222
v_add_f32 v133,v133,v212
v_sub_f32 v70,v70,v222
v_add_f32 v134,v134,v212
v_sub_f32 v71,v71,v222
v_add_f32 v135,v135,v212
v_exp_f32 v72,v72
v_exp_f32 v73,v73
v_exp_f32 v74,v74
v_exp_f32 v75,v75
v_exp_f32 v76,v76
v_exp_f32 v77,v77
v_exp_f32 v78,v78
v_exp_f32 v79,v79
v_exp_f32 v80,v80
v_add_f32 v216,v72,v74
v_add_f32 v217,v73,v75
v_exp_f32 v81,v81
CP v72,v72,v73
v_exp_f32 v82,v82
v_add_f32 v216,v76,v216
v_exp_f32 v83,v83
CP v73,v74,v75
v_exp_f32 v84,v84
v_add_f32 v217,v77,v217
v_add_f32 v216,v78,v216
v_exp_f32 v85,v85
CP v74,v76,v77
v_exp_f32 v86,v86
v_add_f32 v217,v79,v217
v_exp_f32 v87,v87
CP v75,v78,v79
v_exp_f32 v88,v88
v_add_f32 v218,v80,v82
v_add_f32 v219,v81,v83
v_exp_f32 v89,v89
CP v80,v80,v81
v_exp_f32 v90,v90
v_add_f32 v218,v84,v218
v_exp_f32 v91,v91
CP v81,v82,v83
v_exp_f32 v92,v92
v_add_f32 v219,v85,v219
v_add_f32 v218,v86,v218
v_exp_f32 v93,v93
CP v82,v84,v85
v_exp_f32 v94,v94
v_add_f32 v219,v87,v219
v_exp_f32 v95,v95
CP v83,v86,v87
v_exp_f32 v96,v96
v_add_f32 v220,v88,v90
v_add_f32 v221,v89,v91
v_exp_f32 v97,v97
CP v88,v88,v89
v_exp_f32 v98,v98
v_add_f32 v220,v92,v220
v_exp_f32 v99,v99
CP v89,v90,v91
v_exp_f32 v100,v100
v_add_f32 v221,v93,v221
v_add_f32 v220,v94,v220
v_exp_f32 v101,v101
CP v90,v92,v93
v_exp_f32 v102,v102
v_add_f32 v221,v95,v221
v_exp_f32 v103,v103
CP v91,v94,v95
v_exp_f32 v104,v104
v_add_f32 v222,v96,v98
v_add_f32 v223,v97,v99
v_exp_f32 v105,v105
CP v96,v96,v97
v_exp_f32 v106,v106
v_add_f32 v222,v100,v222
v_exp_f32 v107,v107
CP v97,v98,v99
v_exp_f32 v108,v108
v_add_f32 v223,v101,v223
v_add_f32 v222,v102,v222
v_exp_f32 v109,v109
CP v98,v100,v101
v_exp_f32 v110,v110
v_add_f32 v223,v103,v223
v_exp_f32 v111,v111
CP v99,v102,v103
v_exp_f32 v112,v112
v_add_f32 v216,v104,v216
v_add_f32 v217,v105,v217
v_exp_f32 v113,v113
CP v104,v104,v105
v_exp_f32 v114,v114
v_add_f32 v216,v106,v216
v_add_f32 v217,v107,v217
v_exp_f32 v115,v115
CP v105,v106,v107
v_exp_f32 v116,v116
v_add_f32 v216,v108,v216
v_add_f32 v217,v109,v217
v_exp_f32 v117,v117
CP v106,v108,v109
v_exp_f32 v118,v118
v_add_f32 v216,v110,v216
v_add_f32 v217,v111,v217
v_exp_f32 v119,v119
CP v107,v110,v111
v_exp_f32 v120,v120
v_add_f32 v218,v112,v218
v_add_f32 v219,v113,v219
v_exp_f32 v121,v121
CP v112,v112,v113
v_exp_f32 v122,v122
v_add_f32 v218,v114,v218
v_add_f32 v219,v115,v219
v_exp_f32 v123,v123
CP v113,v114,v115
v_exp_f32 v124,v124
v_add_f32 v218,v116,v218
v_add_f32 v219,v117,v219
v_exp_f32 v125,v125
CP v114,v116,v117
v_exp_f32 v126,v126
v_add_f32 v218,v118,v218
v_add_f32 v219,v119,v219
v_exp_f32 v127,v127
CP v115,v118,v119
v_exp_f32 v128,v128
v_add_f32 v220,v120,v220
v_add_f32 v221,v121,v221
v_exp_f32 v129,v129
CP v120,v120,v121
v_exp_f32 v130,v130
v_add_f32 v220,v122,v220
v_add_f32 v221,v123,v221
v_exp_f32 v131,v131
CP v121,v122,v123
v_exp_f32 v132,v132
v_add_f32 v220,v124,v220
v_add_f32 v221,v125,v221
v_exp_f32 v133,v133
CP v122,v124,v125
v_exp_f32 v134,v134
v_add_f32 v220,v126,v220
v_add_f32 v221,v127,v221
v_exp_f32 v135,v135
CP v123,v126,v127
v_add_f32 v222,v128,v222
v_add_f32 v223,v129,v223
CP v128,v128,v129
v_add_f32 v222,v130,v222
v_add_f32 v223,v131,v223
CP v129,v130,v131
v_add_f32 v222,v132,v222
v_add_f32 v223,v133,v223
CP v130,v132,v133
v_add_f32 v222,v134,v222
v_add_f32 v223,v135,v223
CP v131,v134,v135
s_nop 3
s_add_u32 s80,s80,4
s_addc_u32 s81,s81,0
s_setpc_b64 s[80:81]
LRARE1S_%=:
s_waitcnt vmcnt(0) lgkmcnt(0)
s_nop 15
s_sub_u32 s100,s75,1
s_lshl_b32 s100,s100,14
s_lshl_b32 s101,s57,19
s_add_u32 s100,s100,s101
buffer_load_dwordx4 a[192:195],v231,s[40:43],s100 offen
buffer_load_dwordx4 a[196:199],v231,s[40:43],s100 offen offset:1024
buffer_load_dwordx4 a[200:203],v231,s[40:43],s100 offen offset:2048
buffer_load_dwordx4 a[204:207],v231,s[40:43],s100 offen offset:3072
s_add_u32 s100,s100,4096
buffer_load_dwordx4 a[208:211],v231,s[40:43],s100 offen
buffer_load_dwordx4 a[212:215],v231,s[40:43],s100 offen offset:1024
buffer_load_dwordx4 a[216:219],v231,s[40:43],s100 offen offset:2048
buffer_load_dwordx4 a[220:223],v231,s[40:43],s100 offen offset:3072
s_add_u32 s100,s100,4096
buffer_load_dwordx4 a[224:227],v231,s[40:43],s100 offen
buffer_load_dwordx4 a[228:231],v231,s[40:43],s100 offen offset:1024
buffer_load_dwordx4 a[232:235],v231,s[40:43],s100 offen offset:2048
buffer_load_dwordx4 a[236:239],v231,s[40:43],s100 offen offset:3072
s_add_u32 s100,s100,4096
buffer_load_dwordx4 a[240:243],v231,s[40:43],s100 offen
buffer_load_dwordx4 a[244:247],v231,s[40:43],s100 offen offset:1024
buffer_load_dwordx4 a[248:251],v231,s[40:43],s100 offen offset:2048
buffer_load_dwordx4 a[252:255],v231,s[40:43],s100 offen offset:3072
s_add_u32 s100,s100,4096
s_waitcnt vmcnt(0)
MF v[88:91],a[192:195],a[160:163],0
MF v[96:99],a[192:195],a[176:179],0
MF v[88:91],a[196:199],a[164:167],v[88:91]
MF v[96:99],a[196:199],a[180:183],v[96:99]
MF v[88:91],a[200:203],a[168:171],v[88:91]
MF v[96:99],a[200:203],a[184:187],v[96:99]
MF v[88:91],a[204:207],a[172:175],v[88:91]
MF v[96:99],a[204:207],a[188:191],v[96:99]
MF v[92:95],a[208:211],a[160:163],0
MF v[100:103],a[208:211],a[176:179],0
MF v[92:95],a[212:215],a[164:167],v[92:95]
MF v[100:103],a[212:215],a[180:183],v[100:103]
MF v[92:95],a[216:219],a[168:171],v[92:95]
MF v[100:103],a[216:219],a[184:187],v[100:103]
MF v[92:95],a[220:223],a[172:175],v[92:95]
MF v[100:103],a[220:223],a[188:191],v[100:103]
MF v[120:123],a[224:227],a[160:163],0
)A"
R"A(MF v[128:131],a[224:227],a[176:179],0
MF v[120:123],a[228:231],a[164:167],v[120:123]
MF v[128:131],a[228:231],a[180:183],v[128:131]
MF v[120:123],a[232:235],a[168:171],v[120:123]
MF v[128:131],a[232:235],a[184:187],v[128:131]
MF v[120:123],a[236:239],a[172:175],v[120:123]
MF v[128:131],a[236:239],a[188:191],v[128:131]
MF v[124:127],a[240:243],a[160:163],0
MF v[132:135],a[240:243],a[176:179],0
MF v[124:127],a[244:247],a[164:167],v[124:127]
MF v[132:135],a[244:247],a[180:183],v[132:135]
MF v[124:127],a[248:251],a[168:171],v[124:127]
MF v[132:135],a[248:251],a[184:187],v[132:135]
MF v[124:127],a[252:255],a[172:175],v[124:127]
MF v[132:135],a[252:255],a[188:191],v[132:135]
s_nop 15
s_sub_u32 s77,30,s58
s_sub_u32 s77,s77,s58
s_sub_u32 s77,s77,s75
s_add_u32 s77,s77,1
s_lshl_b32 s77,s77,6
v_add_u32 v234,s77,v232
v_add_u32 v235,s77,v233
v_cmp_gt_i32_e64 s[82:83],0,v234
v_cmp_gt_i32_e64 s[84:85],1,v234
s_nop 0
v_cndmask_b32_e64 v88,v88,v236,s[82:83]
v_cmp_gt_i32_e64 s[82:83],2,v234
v_cndmask_b32_e64 v89,v89,v236,s[84:85]
v_cmp_gt_i32_e64 s[84:85],3,v234
v_cndmask_b32_e64 v90,v90,v236,s[82:83]
v_cmp_gt_i32_e64 s[82:83],16,v234
v_cndmask_b32_e64 v91,v91,v236,s[84:85]
v_cmp_gt_i32_e64 s[84:85],17,v234
v_cndmask_b32_e64 v92,v92,v236,s[82:83]
v_cmp_gt_i32_e64 s[82:83],18,v234
v_cndmask_b32_e64 v93,v93,v236,s[84:85]
v_cmp_gt_i32_e64 s[84:85],19,v234
v_cndmask_b32_e64 v94,v94,v236,s[82:83]
s_nop 0
v_cndmask_b32_e64 v95,v95,v236,s[84:85]
v_cmp_gt_i32_e64 s[82:83],32,v234
v_cmp_gt_i32_e64 s[84:85],33,v234
s_nop 0
v_cndmask_b32_e64 v120,v120,v236,s[82:83]
v_cmp_gt_i32_e64 s[82:83],34,v234
v_cndmask_b32_e64 v121,v121,v236,s[84:85]
v_cmp_gt_i32_e64 s[84:85],35,v234
v_cndmask_b32_e64 v122,v122,v236,s[82:83]
v_cmp_gt_i32_e64 s[82:83],48,v234
v_cndmask_b32_e64 v123,v123,v236,s[84:85]
v_cmp_gt_i32_e64 s[84:85],49,v234
v_cndmask_b32_e64 v124,v124,v236,s[82:83]
v_cmp_gt_i32_e64 s[82:83],50,v234
v_cndmask_b32_e64 v125,v125,v236,s[84:85]
v_cmp_gt_i32_e64 s[84:85],51,v234
v_cndmask_b32_e64 v126,v126,v236,s[82:83]
s_nop 0
v_cndmask_b32_e64 v127,v127,v236,s[84:85]
v_cmp_gt_i32_e64 s[82:83],0,v235
v_cmp_gt_i32_e64 s[84:85],1,v235
s_nop 0
v_cndmask_b32_e64 v96,v96,v236,s[82:83]
v_cmp_gt_i32_e64 s[82:83],2,v235
v_cndmask_b32_e64 v97,v97,v236,s[84:85]
v_cmp_gt_i32_e64 s[84:85],3,v235
v_cndmask_b32_e64 v98,v98,v236,s[82:83]
v_cmp_gt_i32_e64 s[82:83],16,v235
v_cndmask_b32_e64 v99,v99,v236,s[84:85]
v_cmp_gt_i32_e64 s[84:85],17,v235
v_cndmask_b32_e64 v100,v100,v236,s[82:83]
v_cmp_gt_i32_e64 s[82:83],18,v235
v_cndmask_b32_e64 v101,v101,v236,s[84:85]
v_cmp_gt_i32_e64 s[84:85],19,v235
v_cndmask_b32_e64 v102,v102,v236,s[82:83]
s_nop 0
v_cndmask_b32_e64 v103,v103,v236,s[84:85]
v_cmp_gt_i32_e64 s[82:83],32,v235
v_cmp_gt_i32_e64 s[84:85],33,v235
s_nop 0
v_cndmask_b32_e64 v128,v128,v236,s[82:83]
v_cmp_gt_i32_e64 s[82:83],34,v235
v_cndmask_b32_e64 v129,v129,v236,s[84:85]
v_cmp_gt_i32_e64 s[84:85],35,v235
v_cndmask_b32_e64 v130,v130,v236,s[82:83]
v_cmp_gt_i32_e64 s[82:83],48,v235
v_cndmask_b32_e64 v131,v131,v236,s[84:85]
v_cmp_gt_i32_e64 s[84:85],49,v235
v_cndmask_b32_e64 v132,v132,v236,s[82:83]
v_cmp_gt_i32_e64 s[82:83],50,v235
v_cndmask_b32_e64 v133,v133,v236,s[84:85]
v_cmp_gt_i32_e64 s[84:85],51,v235
v_cndmask_b32_e64 v134,v134,v236,s[82:83]
s_nop 0
v_cndmask_b32_e64 v135,v135,v236,s[84:85]
v_max3_f32 v220,v88,v89,v90
v_max3_f32 v220,v220,v91,v92
v_max3_f32 v220,v220,v93,v94
v_max3_f32 v220,v220,v95,v120
v_max3_f32 v220,v220,v121,v122
v_max3_f32 v220,v220,v123,v124
v_max3_f32 v220,v220,v125,v126
v_max_f32 v220,v220,v127
v_mov_b32 v221,v220
s_nop 1
v_permlane32_swap_b32 v220,v221
v_max_f32 v220,v220,v221
v_mov_b32 v221,v220
s_nop 1
v_permlane16_swap_b32 v220,v221
v_max_f32 v220,v220,v221
v_max3_f32 v222,v96,v97,v98
v_max3_f32 v222,v222,v99,v100
v_max3_f32 v222,v222,v101,v102
v_max3_f32 v222,v222,v103,v128
v_max3_f32 v222,v222,v129,v130
v_max3_f32 v222,v222,v131,v132
v_max3_f32 v222,v222,v133,v134
v_max_f32 v222,v222,v135
v_mov_b32 v223,v222
s_nop 1
v_permlane32_swap_b32 v222,v223
v_max_f32 v222,v222,v223
v_mov_b32 v223,v222
s_nop 1
v_permlane16_swap_b32 v222,v223
v_max_f32 v222,v222,v223
v_add_f32 v220,v220,v208
v_max_f32 v220,0,v220
v_sub_f32 v208,v208,v220
v_sub_f32 v209,v209,v220
v_sub_f32 v210,v210,v220
v_sub_f32 v211,v211,v220
v_sub_f32 v5,0,v220
v_exp_f32 v5,v5
s_nop 0
v_mul_f32 v226,v226,v5
AR v6,a64
v_mul_f32 v6,v6,v5
v_accvgpr_write_b32 a64,v6
AR v6,a65
v_mul_f32 v6,v6,v5
v_accvgpr_write_b32 a65,v6
AR v6,a66
v_mul_f32 v6,v6,v5
v_accvgpr_write_b32 a66,v6
AR v6,a67
v_mul_f32 v6,v6,v5
v_accvgpr_write_b32 a67,v6
AR v6,a68
v_mul_f32 v6,v6,v5
v_accvgpr_write_b32 a68,v6
AR v6,a69
v_mul_f32 v6,v6,v5
v_accvgpr_write_b32 a69,v6
AR v6,a70
v_mul_f32 v6,v6,v5
v_accvgpr_write_b32 a70,v6
AR v6,a71
v_mul_f32 v6,v6,v5
v_accvgpr_write_b32 a71,v6
AR v6,a72
v_mul_f32 v6,v6,v5
v_accvgpr_write_b32 a72,v6
AR v6,a73
v_mul_f32 v6,v6,v5
v_accvgpr_write_b32 a73,v6
AR v6,a74
v_mul_f32 v6,v6,v5
v_accvgpr_write_b32 a74,v6
AR v6,a75
v_mul_f32 v6,v6,v5
v_accvgpr_write_b32 a75,v6
AR v6,a76
v_mul_f32 v6,v6,v5
v_accvgpr_write_b32 a76,v6
AR v6,a77
v_mul_f32 v6,v6,v5
v_accvgpr_write_b32 a77,v6
AR v6,a78
v_mul_f32 v6,v6,v5
v_accvgpr_write_b32 a78,v6
AR v6,a79
v_mul_f32 v6,v6,v5
v_accvgpr_write_b32 a79,v6
AR v6,a80
v_mul_f32 v6,v6,v5
v_accvgpr_write_b32 a80,v6
AR v6,a81
v_mul_f32 v6,v6,v5
v_accvgpr_write_b32 a81,v6
AR v6,a82
v_mul_f32 v6,v6,v5
v_accvgpr_write_b32 a82,v6
AR v6,a83
v_mul_f32 v6,v6,v5
v_accvgpr_write_b32 a83,v6
AR v6,a84
v_mul_f32 v6,v6,v5
v_accvgpr_write_b32 a84,v6
AR v6,a85
v_mul_f32 v6,v6,v5
v_accvgpr_write_b32 a85,v6
AR v6,a86
v_mul_f32 v6,v6,v5
v_accvgpr_write_b32 a86,v6
AR v6,a87
v_mul_f32 v6,v6,v5
v_accvgpr_write_b32 a87,v6
AR v6,a88
v_mul_f32 v6,v6,v5
v_accvgpr_write_b32 a88,v6
AR v6,a89
v_mul_f32 v6,v6,v5
v_accvgpr_write_b32 a89,v6
AR v6,a90
v_mul_f32 v6,v6,v5
v_accvgpr_write_b32 a90,v6
AR v6,a91
v_mul_f32 v6,v6,v5
v_accvgpr_write_b32 a91,v6
AR v6,a92
v_mul_f32 v6,v6,v5
v_accvgpr_write_b32 a92,v6
AR v6,a93
v_mul_f32 v6,v6,v5
v_accvgpr_write_b32 a93,v6
AR v6,a94
v_mul_f32 v6,v6,v5
v_accvgpr_write_b32 a94,v6
AR v6,a95
v_mul_f32 v6,v6,v5
v_accvgpr_write_b32 a95,v6
v_sub_f32 v24,v24,v220
v_add_f32 v88,v88,v208
v_sub_f32 v25,v25,v220
v_add_f32 v89,v89,v208
v_sub_f32 v26,v26,v220
v_add_f32 v90,v90,v208
v_sub_f32 v27,v27,v220
v_add_f32 v91,v91,v208
v_sub_f32 v28,v28,v220
v_add_f32 v92,v92,v208
v_sub_f32 v29,v29,v220
v_add_f32 v93,v93,v208
v_sub_f32 v30,v30,v220
v_add_f32 v94,v94,v208
v_sub_f32 v31,v31,v220
v_add_f32 v95,v95,v208
v_sub_f32 v56,v56,v220
v_add_f32 v120,v120,v208
v_sub_f32 v57,v57,v220
v_add_f32 v121,v121,v208
v_sub_f32 v58,v58,v220
v_add_f32 v122,v122,v208
v_sub_f32 v59,v59,v220
v_add_f32 v123,v123,v208
v_sub_f32 v60,v60,v220
v_add_f32 v124,v124,v208
v_sub_f32 v61,v61,v220
v_add_f32 v125,v125,v208
v_sub_f32 v62,v62,v220
v_add_f32 v126,v126,v208
v_sub_f32 v63,v63,v220
v_add_f32 v127,v127,v208
v_add_f32 v222,v222,v212
v_max_f32 v222,0,v222
v_sub_f32 v212,v212,v222
v_sub_f32 v213,v213,v222
v_sub_f32 v214,v214,v222
v_sub_f32 v215,v215,v222
v_sub_f32 v5,0,v222
v_exp_f32 v5,v5
s_nop 0
v_mul_f32 v227,v227,v5
AR v6,a96
v_mul_f32 v6,v6,v5
v_accvgpr_write_b32 a96,v6
AR v6,a97
v_mul_f32 v6,v6,v5
v_accvgpr_write_b32 a97,v6
AR v6,a98
v_mul_f32 v6,v6,v5
v_accvgpr_write_b32 a98,v6
AR v6,a99
v_mul_f32 v6,v6,v5
v_accvgpr_write_b32 a99,v6
AR v6,a100
v_mul_f32 v6,v6,v5
v_accvgpr_write_b32 a100,v6
AR v6,a101
v_mul_f32 v6,v6,v5
v_accvgpr_write_b32 a101,v6
AR v6,a102
v_mul_f32 v6,v6,v5
v_accvgpr_write_b32 a102,v6
AR v6,a103
v_mul_f32 v6,v6,v5
v_accvgpr_write_b32 a103,v6
AR v6,a104
v_mul_f32 v6,v6,v5
v_accvgpr_write_b32 a104,v6
AR v6,a105
v_mul_f32 v6,v6,v5
v_accvgpr_write_b32 a105,v6
AR v6,a106
v_mul_f32 v6,v6,v5
v_accvgpr_write_b32 a106,v6
AR v6,a107
v_mul_f32 v6,v6,v5
v_accvgpr_write_b32 a107,v6
AR v6,a108
v_mul_f32 v6,v6,v5
v_accvgpr_write_b32 a108,v6
AR v6,a109
v_mul_f32 v6,v6,v5
v_accvgpr_write_b32 a109,v6
AR v6,a110
v_mul_f32 v6,v6,v5
v_accvgpr_write_b32 a110,v6
AR v6,a111
v_mul_f32 v6,v6,v5
v_accvgpr_write_b32 a111,v6
AR v6,a112
v_mul_f32 v6,v6,v5
v_accvgpr_write_b32 a112,v6
AR v6,a113
v_mul_f32 v6,v6,v5
v_accvgpr_write_b32 a113,v6
AR v6,a114
v_mul_f32 v6,v6,v5
v_accvgpr_write_b32 a114,v6
AR v6,a115
v_mul_f32 v6,v6,v5
v_accvgpr_write_b32 a115,v6
AR v6,a116
v_mul_f32 v6,v6,v5
v_accvgpr_write_b32 a116,v6
AR v6,a117
v_mul_f32 v6,v6,v5
v_accvgpr_write_b32 a117,v6
AR v6,a118
v_mul_f32 v6,v6,v5
v_accvgpr_write_b32 a118,v6
AR v6,a119
v_mul_f32 v6,v6,v5
v_accvgpr_write_b32 a119,v6
AR v6,a120
v_mul_f32 v6,v6,v5
v_accvgpr_write_b32 a120,v6
AR v6,a121
v_mul_f32 v6,v6,v5
v_accvgpr_write_b32 a121,v6
AR v6,a122
v_mul_f32 v6,v6,v5
v_accvgpr_write_b32 a122,v6
AR v6,a123
v_mul_f32 v6,v6,v5
v_accvgpr_write_b32 a123,v6
AR v6,a124
v_mul_f32 v6,v6,v5
v_accvgpr_write_b32 a124,v6
AR v6,a125
v_mul_f32 v6,v6,v5
v_accvgpr_write_b32 a125,v6
AR v6,a126
v_mul_f32 v6,v6,v5
v_accvgpr_write_b32 a126,v6
AR v6,a127
v_mul_f32 v6,v6,v5
v_accvgpr_write_b32 a127,v6
v_sub_f32 v32,v32,v222
v_add_f32 v96,v96,v212
v_sub_f32 v33,v33,v222
v_add_f32 v97,v97,v212
v_sub_f32 v34,v34,v222
v_add_f32 v98,v98,v212
v_sub_f32 v35,v35,v222
v_add_f32 v99,v99,v212
v_sub_f32 v36,v36,v222
v_add_f32 v100,v100,v212
v_sub_f32 v37,v37,v222
v_add_f32 v101,v101,v212
v_sub_f32 v38,v38,v222
v_add_f32 v102,v102,v212
v_sub_f32 v39,v39,v222
v_add_f32 v103,v103,v212
v_sub_f32 v64,v64,v222
v_add_f32 v128,v128,v212
v_sub_f32 v65,v65,v222
v_add_f32 v129,v129,v212
v_sub_f32 v66,v66,v222
v_add_f32 v130,v130,v212
v_sub_f32 v67,v67,v222
v_add_f32 v131,v131,v212
v_sub_f32 v68,v68,v222
v_add_f32 v132,v132,v212
v_sub_f32 v69,v69,v222
v_add_f32 v133,v133,v212
v_sub_f32 v70,v70,v222
v_add_f32 v134,v134,v212
v_sub_f32 v71,v71,v222
v_add_f32 v135,v135,v212
v_exp_f32 v88,v88
v_exp_f32 v89,v89
v_exp_f32 v90,v90
v_exp_f32 v91,v91
v_exp_f32 v92,v92
v_exp_f32 v93,v93
v_exp_f32 v94,v94
v_exp_f32 v95,v95
v_exp_f32 v96,v96
v_add_f32 v220,v88,v90
v_add_f32 v221,v89,v91
v_exp_f32 v97,v97
CP v88,v88,v89
v_exp_f32 v98,v98
v_add_f32 v220,v92,v220
v_exp_f32 v99,v99
CP v89,v90,v91
v_exp_f32 v100,v100
v_add_f32 v221,v93,v221
v_add_f32 v220,v94,v220
v_exp_f32 v101,v101
CP v90,v92,v93
v_exp_f32 v102,v102
v_add_f32 v221,v95,v221
v_exp_f32 v103,v103
CP v91,v94,v95
v_exp_f32 v120,v120
v_add_f32 v222,v96,v98
v_add_f32 v223,v97,v99
v_exp_f32 v121,v121
CP v96,v96,v97
v_exp_f32 v122,v122
v_add_f32 v222,v100,v222
v_exp_f32 v123,v123
CP v97,v98,v99
v_exp_f32 v124,v124
v_add_f32 v223,v101,v223
v_add_f32 v222,v102,v222
v_exp_f32 v125,v125
CP v98,v100,v101
v_exp_f32 v126,v126
v_add_f32 v223,v103,v223
v_exp_f32 v127,v127
CP v99,v102,v103
v_exp_f32 v128,v128
v_add_f32 v220,v120,v220
v_add_f32 v221,v121,v221
v_exp_f32 v129,v129
CP v120,v120,v121
v_exp_f32 v130,v130
v_add_f32 v220,v122,v220
v_add_f32 v221,v123,v221
v_exp_f32 v131,v131
CP v121,v122,v123
v_exp_f32 v132,v132
v_add_f32 v220,v124,v220
v_add_f32 v221,v125,v221
v_exp_f32 v133,v133
CP v122,v124,v125
v_exp_f32 v134,v134
v_add_f32 v220,v126,v220
v_add_f32 v221,v127,v221
v_exp_f32 v135,v135
CP v123,v126,v127
v_add_f32 v222,v128,v222
v_add_f32 v223,v129,v223
CP v128,v128,v129
v_add_f32 v222,v130,v222
v_add_f32 v223,v131,v223
CP v129,v130,v131
v_add_f32 v222,v132,v222
v_add_f32 v223,v133,v223
CP v130,v132,v133
v_add_f32 v222,v134,v222
v_add_f32 v223,v135,v223
CP v131,v134,v135
s_nop 3
s_add_u32 s80,s80,4
s_addc_u32 s81,s81,0
s_setpc_b64 s[80:81]
.purgem MF
.purgem AR
.purgem CP
)A"
    :: "{s[4:5]}"(ka), "{s6}"(blockIdx.x), "{v0}"(threadIdx.x) : "memory", "vcc", "s8", "s9", "s10", "s11", "s12", "s13", "s14", "s15", "s16", "s17", "s18", "s19", "s20", "s21", "s22", "s23", "s24", "s25", "s26", "s27", "s28", "s29", "s30", "s31", "s32", "s33", "s34", "s35", "s36", "s37", "s38", "s39", "s40", "s41", "s42", "s43", "s44", "s45", "s46", "s47", "s48", "s49", "s50", "s51", "s52", "s53", "s54", "s55", "s56", "s57", "s58", "s59", "s60", "s61", "s62", "s63", "s64", "s65", "s66", "s67", "s68", "s69", "s70", "s71", "s72", "s73", "s74", "s75", "s76", "s77", "s78", "s79", "s80", "s81", "s82", "s83", "s84", "s85", "s86", "s87", "s88", "s89", "s90", "s91", "s92", "s93", "s94", "s95", "s96", "s97", "s98", "s99", "s100", "s101", "v1", "v2", "v3", "v4", "v5", "v6", "v7", "v8", "v9", "v10", "v11", "v12", "v13", "v14", "v15", "v16", "v17", "v18", "v19", "v20", "v21", "v22", "v23", "v24", "v25", "v26", "v27", "v28", "v29", "v30", "v31", "v32", "v33", "v34", "v35", "v36", "v37", "v38", "v39", "v40", "v41", "v42", "v43", "v44", "v45", "v46", "v47", "v48", "v49", "v50", "v51", "v52", "v53", "v54", "v55", "v56", "v57", "v58", "v59", "v60", "v61", "v62", "v63", "v64", "v65", "v66", "v67", "v68", "v69", "v70", "v71", "v72", "v73", "v74", "v75", "v76", "v77", "v78", "v79", "v80", "v81", "v82", "v83", "v84", "v85", "v86", "v87", "v88", "v89", "v90", "v91", "v92", "v93", "v94", "v95", "v96", "v97", "v98", "v99", "v100", "v101", "v102", "v103", "v104", "v105", "v106", "v107", "v108", "v109", "v110", "v111", "v112", "v113", "v114", "v115", "v116", "v117", "v118", "v119", "v120", "v121", "v122", "v123", "v124", "v125", "v126", "v127", "v128", "v129", "v130", "v131", "v132", "v133", "v134", "v135", "v136", "v137", "v138", "v139", "v140", "v141", "v142", "v143", "v144", "v145", "v146", "v147", "v148", "v149", "v150", "v151", "v152", "v153", "v154", "v155", "v156", "v157", "v158", "v159", "v160", "v161", "v162", "v163", "v164", "v165", "v166", "v167", "v168", "v169", "v170", "v171", "v172", "v173", "v174", "v175", "v176", "v177", "v178", "v179", "v180", "v181", "v182", "v183", "v184", "v185", "v186", "v187", "v188", "v189", "v190", "v191", "v192", "v193", "v194", "v195", "v196", "v197", "v198", "v199", "v200", "v201", "v202", "v203", "v204", "v205", "v206", "v207", "v208", "v209", "v210", "v211", "v212", "v213", "v214", "v215", "v216", "v217", "v218", "v219", "v220", "v221", "v222", "v223", "v224", "v225", "v226", "v227", "v228", "v229", "v230", "v231", "v232", "v233", "v234", "v235", "v236", "v237", "v238", "v239", "v240", "v241", "v242", "v243", "v244", "v245", "v246", "v247", "v248", "v249", "v250", "v251", "v252", "v253", "v254", "v255", "a0", "a1", "a2", "a3", "a4", "a5", "a6", "a7", "a8", "a9", "a10", "a11", "a12", "a13", "a14", "a15", "a16", "a17", "a18", "a19", "a20", "a21", "a22", "a23", "a24", "a25", "a26", "a27", "a28", "a29", "a30", "a31", "a32", "a33", "a34", "a35", "a36", "a37", "a38", "a39", "a40", "a41", "a42", "a43", "a44", "a45", "a46", "a47", "a48", "a49", "a50", "a51", "a52", "a53", "a54", "a55", "a56", "a57", "a58", "a59", "a60", "a61", "a62", "a63", "a64", "a65", "a66", "a67", "a68", "a69", "a70", "a71", "a72", "a73", "a74", "a75", "a76", "a77", "a78", "a79", "a80", "a81", "a82", "a83", "a84", "a85", "a86", "a87", "a88", "a89", "a90", "a91", "a92", "a93", "a94", "a95", "a96", "a97", "a98", "a99", "a100", "a101", "a102", "a103", "a104", "a105", "a106", "a107", "a108", "a109", "a110", "a111", "a112", "a113", "a114", "a115", "a116", "a117", "a118", "a119", "a120", "a121", "a122", "a123", "a124", "a125", "a126", "a127", "a128", "a129", "a130", "a131", "a132", "a133", "a134", "a135", "a136", "a137", "a138", "a139", "a140", "a141", "a142", "a143", "a144", "a145", "a146", "a147", "a148", "a149", "a150", "a151", "a152", "a153", "a154", "a155", "a156", "a157", "a158", "a159", "a160", "a161", "a162", "a163", "a164", "a165", "a166", "a167", "a168", "a169", "a170", "a171", "a172", "a173", "a174", "a175", "a176", "a177", "a178", "a179", "a180", "a181", "a182", "a183", "a184", "a185", "a186", "a187", "a188", "a189", "a190", "a191", "a192", "a193", "a194", "a195", "a196", "a197", "a198", "a199", "a200", "a201", "a202", "a203", "a204", "a205", "a206", "a207", "a208", "a209", "a210", "a211", "a212", "a213", "a214", "a215", "a216", "a217", "a218", "a219", "a220", "a221", "a222", "a223", "a224", "a225", "a226", "a227", "a228", "a229", "a230", "a231", "a232", "a233", "a234", "a235", "a236", "a237", "a238", "a239", "a240", "a241", "a242", "a243", "a244", "a245", "a246", "a247", "a248", "a249", "a250", "a251", "a252", "a253", "a254", "a255");
}
extern "C" __global__ __launch_bounds__(256, 1) void k3(K3Args p) {
  auto ka = __builtin_amdgcn_kernarg_segment_ptr();
  asm volatile(
R"A(.macro MF d,a,b,c
v_mfma_f32_16x16x32_bf16 \d,\a,\b,\c
.endm
.macro AR d,a
v_accvgpr_read_b32 \d,\a
.endm
.macro CP d,a,b
v_cvt_pk_bf16_f32 \d,\a,\b
.endm
s_load_dwordx16 s[8:23],s[4:5],0x0
s_load_dwordx8 s[24:31],s[4:5],0x40
s_load_dwordx2 s[32:33],s[4:5],0x60
v_and_b32 v1,63,v0
v_lshrrev_b32 v2,6,v0
s_nop 0
v_readfirstlane_b32 s60,v2
v_lshlrev_b32 v10,4,v1
s_and_b32 s100,s6,7
s_lshr_b32 s74,s100,2
s_and_b32 s100,s100,3
s_lshl_b32 s72,s100,2
s_lshr_b32 s101,s6,3
s_mul_i32 s100,s101,37
s_lshr_b32 s100,s100,8
s_add_u32 s72,s72,s100
s_mul_i32 s100,s100,7
s_sub_u32 s73,s101,s100
s_cmp_ge_u32 s6,224
s_cselect_b32 s80,1,0
s_xor_b32 s79,s60,s74
v_lshrrev_b32 v2,3,v1
v_and_b32 v3,7,v1
v_lshrrev_b32 v4,4,v1
s_mul_i32 s100,s79,32
s_mov_b32 s101,4096
v_add_u32 v6,s100,v2
v_mul_lo_u32 v6,v6,s101
v_add_u32 v5,0,v4
v_xor_b32 v5,v5,v3
v_lshl_add_u32 v74,v5,4,v6
v_add_u32 v5,4,v4
v_xor_b32 v5,v5,v3
v_lshl_add_u32 v75,v5,4,v6
v_add_u32 v75,32768,v75
v_add_u32 v76,65536,v74
v_add_u32 v77,65536,v75
s_mul_i32 s63,s60,4096
v_lshrrev_b32 v2,2,v1
v_and_b32 v2,7,v2
v_lshrrev_b32 v3,5,v1
v_and_b32 v4,3,v1
s_lshl_b32 s100,s60,4
s_mov_b32 s101,1792
v_add_u32 v5,s100,v2
v_mul_lo_u32 v5,v5,s101
v_xor_b32 v6,0,v4
v_lshl_add_u32 v6,v3,2,v6
v_lshl_add_u32 v11,v6,4,v5
v_add_u32 v12,128,v11
v_add_u32 v5,s100,v2
v_add_u32 v5,8,v5
v_mul_lo_u32 v5,v5,s101
v_xor_b32 v6,2,v4
v_lshl_add_u32 v6,v3,2,v6
v_lshl_add_u32 v13,v6,4,v5
v_add_u32 v14,128,v13
s_lshl_b32 s64,s60,12
s_waitcnt lgkmcnt(0)
s_mov_b32 s36,s8
s_and_b32 s37,s9,0xffff
s_mov_b32 s38,8388608
s_mov_b32 s39,0x20000
s_mov_b32 s40,s10
s_and_b32 s41,s11,0xffff
s_mov_b32 s42,3670016
s_mov_b32 s43,0x20000
s_mul_i32 s62,s72,524288
s_lshl_b32 s100,s74,11
s_add_u32 s62,s62,s100
s_mul_i32 s61,s74,1835008
s_lshl_b32 s100,s73,8
s_add_u32 s61,s61,s100
s_cmp_eq_u32 s80,1
s_cbranch_scc1 LNOSP0_%=
s_add_u32 m0,s63,0
s_nop 0
buffer_load_dwordx4 v74,s[36:39],s62 offen lds
s_add_u32 m0,s63,1024
s_nop 0
buffer_load_dwordx4 v75,s[36:39],s62 offen lds
s_add_u32 m0,s63,2048
s_nop 0
buffer_load_dwordx4 v76,s[36:39],s62 offen lds
s_add_u32 m0,s63,3072
s_nop 0
buffer_load_dwordx4 v77,s[36:39],s62 offen lds
s_add_u32 m0,s64,16384
s_nop 0
buffer_load_dwordx4 v11,s[40:43],s61 offen lds
s_add_u32 m0,s64,17408
s_nop 0
buffer_load_dwordx4 v12,s[40:43],s61 offen lds
s_add_u32 m0,s64,18432
s_nop 0
buffer_load_dwordx4 v13,s[40:43],s61 offen lds
s_add_u32 m0,s64,19456
s_nop 0
buffer_load_dwordx4 v14,s[40:43],s61 offen lds
s_add_u32 s62,s62,128
s_add_u32 s61,s61,114688
s_add_u32 m0,s63,32768
s_nop 0
buffer_load_dwordx4 v74,s[36:39],s62 offen lds
s_add_u32 m0,s63,33792
s_nop 0
buffer_load_dwordx4 v75,s[36:39],s62 offen lds
s_add_u32 m0,s63,34816
s_nop 0
buffer_load_dwordx4 v76,s[36:39],s62 offen lds
s_add_u32 m0,s63,35840
s_nop 0
buffer_load_dwordx4 v77,s[36:39],s62 offen lds
s_add_u32 m0,s64,49152
s_nop 0
buffer_load_dwordx4 v11,s[40:43],s61 offen lds
s_add_u32 m0,s64,50176
s_nop 0
buffer_load_dwordx4 v12,s[40:43],s61 offen lds
s_add_u32 m0,s64,51200
s_nop 0
buffer_load_dwordx4 v13,s[40:43],s61 offen lds
s_add_u32 m0,s64,52224
s_nop 0
buffer_load_dwordx4 v14,s[40:43],s61 offen lds
s_add_u32 s62,s62,128
s_add_u32 s61,s61,114688
s_add_u32 m0,s63,65536
s_nop 0
buffer_load_dwordx4 v74,s[36:39],s62 offen lds
s_add_u32 m0,s63,66560
s_nop 0
buffer_load_dwordx4 v75,s[36:39],s62 offen lds
s_add_u32 m0,s63,67584
s_nop 0
buffer_load_dwordx4 v76,s[36:39],s62 offen lds
s_add_u32 m0,s63,68608
s_nop 0
buffer_load_dwordx4 v77,s[36:39],s62 offen lds
s_add_u32 m0,s64,81920
s_nop 0
buffer_load_dwordx4 v11,s[40:43],s61 offen lds
s_add_u32 m0,s64,82944
s_nop 0
buffer_load_dwordx4 v12,s[40:43],s61 offen lds
s_add_u32 m0,s64,83968
s_nop 0
buffer_load_dwordx4 v13,s[40:43],s61 offen lds
s_add_u32 m0,s64,84992
s_nop 0
buffer_load_dwordx4 v14,s[40:43],s61 offen lds
s_add_u32 s62,s62,128
s_add_u32 s61,s61,114688
s_add_u32 m0,s63,98304
s_nop 0
buffer_load_dwordx4 v74,s[36:39],s62 offen lds
s_add_u32 m0,s63,99328
s_nop 0
buffer_load_dwordx4 v75,s[36:39],s62 offen lds
s_add_u32 m0,s63,100352
s_nop 0
buffer_load_dwordx4 v76,s[36:39],s62 offen lds
s_add_u32 m0,s63,101376
s_nop 0
buffer_load_dwordx4 v77,s[36:39],s62 offen lds
s_add_u32 m0,s64,114688
s_nop 0
buffer_load_dwordx4 v11,s[40:43],s61 offen lds
s_add_u32 m0,s64,115712
s_nop 0
buffer_load_dwordx4 v12,s[40:43],s61 offen lds
s_add_u32 m0,s64,116736
s_nop 0
buffer_load_dwordx4 v13,s[40:43],s61 offen lds
s_add_u32 m0,s64,117760
s_nop 0
buffer_load_dwordx4 v14,s[40:43],s61 offen lds
s_add_u32 s62,s62,128
s_add_u32 s61,s61,114688
LNOSP0_%=:
s_and_b32 s100,s6,7
s_mul_i32 s100,s100,30
s_lshr_b32 s101,s6,3
s_add_u32 s100,s100,s101
s_mul_i32 s71,s100,205
s_lshr_b32 s71,s71,14
s_mul_i32 s101,s71,80
s_sub_u32 s100,s100,s101
s_mul_i32 s69,s100,205
s_lshr_b32 s69,s69,10
s_mul_i32 s101,s69,5
s_sub_u32 s70,s100,s101
v_lshrrev_b32 v2,2,v1
v_and_b32 v2,7,v2
v_lshrrev_b32 v3,5,v1
v_and_b32 v4,3,v1
s_lshl_b32 s100,s60,4
s_mov_b32 s101,1280
v_add_u32 v5,s100,v2
v_bfe_u32 v7,v5,2,1
v_lshlrev_b32 v7,4,v7
v_bfe_u32 v8,v5,3,2
v_lshl_add_u32 v7,v8,2,v7
v_and_b32 v5,35,v5
v_or_b32 v5,v5,v7
v_mul_lo_u32 v5,v5,s101
v_xor_b32 v6,0,v4
v_lshl_add_u32 v6,v3,2,v6
v_lshl_add_u32 v88,v6,4,v5
v_add_u32 v89,128,v88
v_add_u32 v5,s100,v2
v_add_u32 v5,8,v5
v_bfe_u32 v7,v5,2,1
v_lshlrev_b32 v7,4,v7
v_bfe_u32 v8,v5,3,2
v_lshl_add_u32 v7,v8,2,v7
v_and_b32 v5,35,v5
v_or_b32 v5,v5,v7
v_mul_lo_u32 v5,v5,s101
v_xor_b32 v6,2,v4
v_lshl_add_u32 v6,v3,2,v6
v_lshl_add_u32 v90,v6,4,v5
v_add_u32 v91,128,v90
s_lshl_b32 s68,s60,12
s_mov_b32 s44,s16
s_and_b32 s45,s17,0xffff
s_mov_b32 s46,20447232
s_mov_b32 s47,0x20000
s_mov_b32 s48,s18
s_and_b32 s49,s19,0xffff
s_mov_b32 s50,5242880
s_mov_b32 s51,0x20000
s_mov_b32 s52,s20
s_and_b32 s53,s21,0xffff
s_mov_b32 s54,1105920
s_mov_b32 s55,0x20000
s_mul_i32 s76,s71,21
s_min_u32 s100,s71,1
s_add_u32 s76,s76,s100
s_mul_i32 s78,s71,5
s_sub_u32 s78,s78,s100
s_lshl_b32 s77,s69,3
s_lshl_b32 s101,s60,1
s_add_u32 s77,s77,s101
s_mul_i32 s66,s77,159744
s_lshl_b32 s101,s76,11
s_add_u32 s66,s66,s101
s_add_u32 s67,s66,159744
s_mul_i32 s65,s76,81920
s_lshl_b32 s101,s70,8
s_add_u32 s65,s65,s101
s_lshr_b32 s98,s60,0
s_and_b32 s99,s60,0
v_lshrrev_b32 v2,4,v1
v_lshrrev_b32 v3,2,v1
v_and_b32 v3,3,v3
v_and_b32 v4,3,v1
v_lshlrev_b32 v5,11,v2
v_lshl_add_u32 v5,v3,6,v5
v_lshl_add_u32 v5,v4,3,v5
v_and_b32 v6,1,v2
v_xor_b32 v7,0,v6
v_lshl_add_u32 v7,v7,5,v5
v_add_u32 v172,32768,v7
v_add_u32 v174,65536,v7
v_add_u32 v176,98304,v7
v_add_u32 v178,131072,v7
v_add_u32 v180,0,v7
v_xor_b32 v7,1,v6
v_lshl_add_u32 v7,v7,5,v5
v_add_u32 v173,32768,v7
v_add_u32 v175,65536,v7
v_add_u32 v177,98304,v7
v_add_u32 v179,131072,v7
v_add_u32 v181,0,v7
s_lshr_b32 s98,s60,1
s_and_b32 s99,s60,1
v_and_b32 v2,15,v1
v_lshrrev_b32 v3,4,v1
v_lshrrev_b32 v4,1,v2
s_mul_i32 s100,s98,8192
v_add_u32 v5,0,v3
v_xor_b32 v5,v5,v4
v_lshlrev_b32 v5,4,v5
v_lshl_add_u32 v5,v2,7,v5
v_add_u32 v5,s100,v5
v_add_u32 v64,0,v5
v_add_u32 v66,32768,v5
v_add_u32 v68,65536,v5
v_add_u32 v70,98304,v5
v_add_u32 v72,131072,v5
v_add_u32 v5,4,v3
v_xor_b32 v5,v5,v4
v_lshlrev_b32 v5,4,v5
v_lshl_add_u32 v5,v2,7,v5
v_add_u32 v5,s100,v5
v_add_u32 v65,0,v5
v_add_u32 v67,32768,v5
v_add_u32 v69,65536,v5
v_add_u32 v71,98304,v5
v_add_u32 v73,131072,v5
v_lshrrev_b32 v2,4,v1
v_lshrrev_b32 v3,2,v1
v_and_b32 v3,3,v3
v_and_b32 v4,3,v1
v_lshlrev_b32 v5,11,v2
v_lshl_add_u32 v5,v3,6,v5
v_lshl_add_u32 v5,v4,3,v5
v_and_b32 v6,1,v2
s_mul_i32 s100,s99,1024
v_add_u32 v5,s100,v5
v_xor_b32 v7,0,v6
v_lshl_add_u32 v7,v7,5,v5
v_add_u32 v78,16384,v7
v_add_u32 v80,49152,v7
v_add_u32 v82,81920,v7
v_add_u32 v84,114688,v7
v_add_u32 v86,147456,v7
v_xor_b32 v7,1,v6
v_lshl_add_u32 v7,v7,5,v5
v_add_u32 v79,16384,v7
v_add_u32 v81,49152,v7
v_add_u32 v83,81920,v7
v_add_u32 v85,114688,v7
v_add_u32 v87,147456,v7
s_lshr_b32 s81,s60,1
s_and_b32 s82,s60,1
s_lshl_b32 s83,s72,3
s_lshl_b32 s100,s81,2
s_add_u32 s83,s83,s100
s_xor_b32 s100,s74,1
s_lshl_b32 s100,s100,1
s_add_u32 s83,s83,s100
s_lshl_b32 s84,s73,2
s_lshl_b32 s100,s82,1
s_add_u32 s84,s84,s100
s_mul_i32 s100,s72,7
s_add_u32 s100,s100,s73
s_lshl_b32 s101,s100,1
s_add_u32 s85,s101,s74
s_xor_b32 s86,s74,1
s_add_u32 s86,s86,s101
s_lshl_b32 s85,s85,2
s_add_u32 s85,s85,s60
s_lshl_b32 s85,s85,12
s_lshl_b32 s86,s86,2
s_add_u32 s86,s86,s60
s_lshl_b32 s86,s86,12
s_mul_i32 s87,s83,156
s_add_u32 s87,s87,s84
s_add_u32 s87,s87,128
s_lshl_b32 s87,s87,10
s_cmp_eq_u32 s80,1
s_cselect_b32 s85,0x7ffff000,s85
s_cselect_b32 s87,0x7ffff000,s87
s_mov_b32 s56,s32
s_and_b32 s57,s33,0xffff
s_mov_b32 s58,3670016
s_mov_b32 s59,0x20000
s_cmp_eq_u32 s80,1
s_cbranch_scc1 LOUT_%=
s_waitcnt vmcnt(24)
s_barrier
ds_read_b64_tr_b16 a[128:129],v78
ds_read_b64_tr_b16 a[130:131],v78 offset:256
ds_read_b128 v[16:19],v64
ds_read_b64_tr_b16 a[132:133],v79
ds_read_b64_tr_b16 a[134:135],v79 offset:256
ds_read_b64_tr_b16 a[136:137],v78 offset:512
ds_read_b64_tr_b16 a[138:139],v78 offset:768
ds_read_b64_tr_b16 a[140:141],v79 offset:512
ds_read_b64_tr_b16 a[142:143],v79 offset:768
ds_read_b128 v[20:23],v64 offset:2048
ds_read_b128 v[24:27],v64 offset:4096
ds_read_b128 v[28:31],v64 offset:6144
ds_read_b64_tr_b16 a[160:161],v78 offset:8192
ds_read_b64_tr_b16 a[162:163],v78 offset:8448
ds_read_b128 v[32:35],v65
ds_read_b64_tr_b16 a[164:165],v79 offset:8192
ds_read_b64_tr_b16 a[166:167],v79 offset:8448
ds_read_b64_tr_b16 a[168:169],v78 offset:8704
ds_read_b64_tr_b16 a[170:171],v78 offset:8960
ds_read_b64_tr_b16 a[172:173],v79 offset:8704
ds_read_b64_tr_b16 a[174:175],v79 offset:8960
ds_read_b128 v[36:39],v65 offset:2048
ds_read_b128 v[40:43],v65 offset:4096
ds_read_b128 v[44:47],v65 offset:6144
s_waitcnt vmcnt(16)
s_barrier
s_waitcnt lgkmcnt(15)
MF a[0:3],a[128:131],v[16:19],0
ds_read_b64_tr_b16 a[192:193],v80
MF a[4:7],a[132:135],v[16:19],0
ds_read_b64_tr_b16 a[194:195],v80 offset:256
s_add_u32 m0,s63,131072
MF a[8:11],a[136:139],v[16:19],0
buffer_load_dwordx4 v74,s[36:39],s62 offen lds
MF a[12:15],a[140:143],v[16:19],0
ds_read_b128 v[48:51],v66
s_waitcnt lgkmcnt(15)
MF a[16:19],a[128:131],v[20:23],0
ds_read_b64_tr_b16 a[196:197],v81
MF a[20:23],a[132:135],v[20:23],0
ds_read_b64_tr_b16 a[198:199],v81 offset:256
s_add_u32 m0,s63,132096
MF a[24:27],a[136:139],v[20:23],0
buffer_load_dwordx4 v75,s[36:39],s62 offen lds
MF a[28:31],a[140:143],v[20:23],0
ds_read_b64_tr_b16 a[200:201],v80 offset:512
MF a[32:35],a[128:131],v[24:27],0
ds_read_b64_tr_b16 a[202:203],v80 offset:768
MF a[36:39],a[132:135],v[24:27],0
ds_read_b64_tr_b16 a[204:205],v81 offset:512
s_add_u32 m0,s63,133120
MF a[40:43],a[136:139],v[24:27],0
buffer_load_dwordx4 v76,s[36:39],s62 offen lds
MF a[44:47],a[140:143],v[24:27],0
ds_read_b64_tr_b16 a[206:207],v81 offset:768
MF a[48:51],a[128:131],v[28:31],0
ds_read_b128 v[52:55],v66 offset:2048
MF a[52:55],a[132:135],v[28:31],0
ds_read_b128 v[56:59],v66 offset:4096
s_add_u32 m0,s63,134144
MF a[56:59],a[136:139],v[28:31],0
buffer_load_dwordx4 v77,s[36:39],s62 offen lds
MF a[60:63],a[140:143],v[28:31],0
ds_read_b128 v[60:63],v66 offset:6144
s_waitcnt lgkmcnt(15)
MF a[0:3],a[160:163],v[32:35],a[0:3]
ds_read_b64_tr_b16 a[128:129],v80 offset:8192
MF a[4:7],a[164:167],v[32:35],a[4:7]
ds_read_b64_tr_b16 a[130:131],v80 offset:8448
s_add_u32 m0,s64,147456
MF a[8:11],a[168:171],v[32:35],a[8:11]
buffer_load_dwordx4 v11,s[40:43],s61 offen lds
MF a[12:15],a[172:175],v[32:35],a[12:15]
ds_read_b128 v[16:19],v67
s_waitcnt lgkmcnt(15)
MF a[16:19],a[160:163],v[36:39],a[16:19]
ds_read_b64_tr_b16 a[132:133],v81 offset:8192
MF a[20:23],a[164:167],v[36:39],a[20:23]
ds_read_b64_tr_b16 a[134:135],v81 offset:8448
s_add_u32 m0,s64,148480
MF a[24:27],a[168:171],v[36:39],a[24:27]
buffer_load_dwordx4 v12,s[40:43],s61 offen lds
MF a[28:31],a[172:175],v[36:39],a[28:31]
ds_read_b64_tr_b16 a[136:137],v80 offset:8704
MF a[32:35],a[160:163],v[40:43],a[32:35]
ds_read_b64_tr_b16 a[138:139],v80 offset:8960
MF a[36:39],a[164:167],v[40:43],a[36:39]
ds_read_b64_tr_b16 a[140:141],v81 offset:8704
s_add_u32 m0,s64,149504
MF a[40:43],a[168:171],v[40:43],a[40:43]
buffer_load_dwordx4 v13,s[40:43],s61 offen lds
MF a[44:47],a[172:175],v[40:43],a[44:47]
ds_read_b64_tr_b16 a[142:143],v81 offset:8960
MF a[48:51],a[160:163],v[44:47],a[48:51]
ds_read_b128 v[20:23],v67 offset:2048
MF a[52:55],a[164:167],v[44:47],a[52:55]
ds_read_b128 v[24:27],v67 offset:4096
s_add_u32 m0,s64,150528
MF a[56:59],a[168:171],v[44:47],a[56:59]
buffer_load_dwordx4 v14,s[40:43],s61 offen lds
MF a[60:63],a[172:175],v[44:47],a[60:63]
ds_read_b128 v[28:31],v67 offset:6144
s_add_u32 s62,s62,128
s_add_u32 s61,s61,114688
s_waitcnt vmcnt(16)
s_barrier
s_waitcnt lgkmcnt(15)
MF a[0:3],a[192:195],v[48:51],a[0:3]
ds_read_b64_tr_b16 a[160:161],v82
MF a[4:7],a[196:199],v[48:51],a[4:7]
ds_read_b64_tr_b16 a[162:163],v82 offset:256
s_add_u32 m0,s63,0
MF a[8:11],a[200:203],v[48:51],a[8:11]
buffer_load_dwordx4 v74,s[36:39],s62 offen lds
MF a[12:15],a[204:207],v[48:51],a[12:15]
ds_read_b128 v[32:35],v68
s_waitcnt lgkmcnt(15)
MF a[16:19],a[192:195],v[52:55],a[16:19]
ds_read_b64_tr_b16 a[164:165],v83
MF a[20:23],a[196:199],v[52:55],a[20:23]
ds_read_b64_tr_b16 a[166:167],v83 offset:256
s_add_u32 m0,s63,1024
MF a[24:27],a[200:203],v[52:55],a[24:27]
buffer_load_dwordx4 v75,s[36:39],s62 offen lds
MF a[28:31],a[204:207],v[52:55],a[28:31]
ds_read_b64_tr_b16 a[168:169],v82 offset:512
MF a[32:35],a[192:195],v[56:59],a[32:35]
ds_read_b64_tr_b16 a[170:171],v82 offset:768
MF a[36:39],a[196:199],v[56:59],a[36:39]
ds_read_b64_tr_b16 a[172:173],v83 offset:512
s_add_u32 m0,s63,2048
MF a[40:43],a[200:203],v[56:59],a[40:43]
buffer_load_dwordx4 v76,s[36:39],s62 offen lds
MF a[44:47],a[204:207],v[56:59],a[44:47]
ds_read_b64_tr_b16 a[174:175],v83 offset:768
MF a[48:51],a[192:195],v[60:63],a[48:51]
ds_read_b128 v[36:39],v68 offset:2048
MF a[52:55],a[196:199],v[60:63],a[52:55]
ds_read_b128 v[40:43],v68 offset:4096
s_add_u32 m0,s63,3072
MF a[56:59],a[200:203],v[60:63],a[56:59]
buffer_load_dwordx4 v77,s[36:39],s62 offen lds
MF a[60:63],a[204:207],v[60:63],a[60:63]
ds_read_b128 v[44:47],v68 offset:6144
s_waitcnt lgkmcnt(15)
MF a[0:3],a[128:131],v[16:19],a[0:3]
ds_read_b64_tr_b16 a[192:193],v82 offset:8192
MF a[4:7],a[132:135],v[16:19],a[4:7]
ds_read_b64_tr_b16 a[194:195],v82 offset:8448
s_add_u32 m0,s64,16384
MF a[8:11],a[136:139],v[16:19],a[8:11]
buffer_load_dwordx4 v11,s[40:43],s61 offen lds
MF a[12:15],a[140:143],v[16:19],a[12:15]
ds_read_b128 v[48:51],v69
s_waitcnt lgkmcnt(15)
MF a[16:19],a[128:131],v[20:23],a[16:19]
ds_read_b64_tr_b16 a[196:197],v83 offset:8192
MF a[20:23],a[132:135],v[20:23],a[20:23]
ds_read_b64_tr_b16 a[198:199],v83 offset:8448
s_add_u32 m0,s64,17408
MF a[24:27],a[136:139],v[20:23],a[24:27]
buffer_load_dwordx4 v12,s[40:43],s61 offen lds
MF a[28:31],a[140:143],v[20:23],a[28:31]
ds_read_b64_tr_b16 a[200:201],v82 offset:8704
MF a[32:35],a[128:131],v[24:27],a[32:35]
ds_read_b64_tr_b16 a[202:203],v82 offset:8960
MF a[36:39],a[132:135],v[24:27],a[36:39]
ds_read_b64_tr_b16 a[204:205],v83 offset:8704
s_add_u32 m0,s64,18432
MF a[40:43],a[136:139],v[24:27],a[40:43]
buffer_load_dwordx4 v13,s[40:43],s61 offen lds
MF a[44:47],a[140:143],v[24:27],a[44:47]
ds_read_b64_tr_b16 a[206:207],v83 offset:8960
MF a[48:51],a[128:131],v[28:31],a[48:51]
ds_read_b128 v[52:55],v69 offset:2048
MF a[52:55],a[132:135],v[28:31],a[52:55]
ds_read_b128 v[56:59],v69 offset:4096
s_add_u32 m0,s64,19456
MF a[56:59],a[136:139],v[28:31],a[56:59]
buffer_load_dwordx4 v14,s[40:43],s61 offen lds
MF a[60:63],a[140:143],v[28:31],a[60:63]
ds_read_b128 v[60:63],v69 offset:6144
s_add_u32 s62,s62,128
s_add_u32 s61,s61,114688
s_waitcnt vmcnt(16)
s_barrier
s_waitcnt lgkmcnt(15)
MF a[0:3],a[160:163],v[32:35],a[0:3]
ds_read_b64_tr_b16 a[128:129],v84
MF a[4:7],a[164:167],v[32:35],a[4:7]
ds_read_b64_tr_b16 a[130:131],v84 offset:256
s_add_u32 m0,s63,32768
MF a[8:11],a[168:171],v[32:35],a[8:11]
buffer_load_dwordx4 v74,s[36:39],s62 offen lds
MF a[12:15],a[172:175],v[32:35],a[12:15]
ds_read_b128 v[16:19],v70
s_waitcnt lgkmcnt(15)
MF a[16:19],a[160:163],v[36:39],a[16:19]
ds_read_b64_tr_b16 a[132:133],v85
MF a[20:23],a[164:167],v[36:39],a[20:23]
ds_read_b64_tr_b16 a[134:135],v85 offset:256
s_add_u32 m0,s63,33792
MF a[24:27],a[168:171],v[36:39],a[24:27]
buffer_load_dwordx4 v75,s[36:39],s62 offen lds
MF a[28:31],a[172:175],v[36:39],a[28:31]
ds_read_b64_tr_b16 a[136:137],v84 offset:512
MF a[32:35],a[160:163],v[40:43],a[32:35]
ds_read_b64_tr_b16 a[138:139],v84 offset:768
MF a[36:39],a[164:167],v[40:43],a[36:39]
ds_read_b64_tr_b16 a[140:141],v85 offset:512
s_add_u32 m0,s63,34816
MF a[40:43],a[168:171],v[40:43],a[40:43]
buffer_load_dwordx4 v76,s[36:39],s62 offen lds
MF a[44:47],a[172:175],v[40:43],a[44:47]
ds_read_b64_tr_b16 a[142:143],v85 offset:768
MF a[48:51],a[160:163],v[44:47],a[48:51]
ds_read_b128 v[20:23],v70 offset:2048
MF a[52:55],a[164:167],v[44:47],a[52:55]
ds_read_b128 v[24:27],v70 offset:4096
s_add_u32 m0,s63,35840
MF a[56:59],a[168:171],v[44:47],a[56:59]
buffer_load_dwordx4 v77,s[36:39],s62 offen lds
MF a[60:63],a[172:175],v[44:47],a[60:63]
ds_read_b128 v[28:31],v70 offset:6144
s_waitcnt lgkmcnt(15)
MF a[0:3],a[192:195],v[48:51],a[0:3]
ds_read_b64_tr_b16 a[160:161],v84 offset:8192
MF a[4:7],a[196:199],v[48:51],a[4:7]
ds_read_b64_tr_b16 a[162:163],v84 offset:8448
s_add_u32 m0,s64,49152
MF a[8:11],a[200:203],v[48:51],a[8:11]
buffer_load_dwordx4 v11,s[40:43],s61 offen lds
MF a[12:15],a[204:207],v[48:51],a[12:15]
ds_read_b128 v[32:35],v71
s_waitcnt lgkmcnt(15)
MF a[16:19],a[192:195],v[52:55],a[16:19]
ds_read_b64_tr_b16 a[164:165],v85 offset:8192
MF a[20:23],a[196:199],v[52:55],a[20:23]
ds_read_b64_tr_b16 a[166:167],v85 offset:8448
s_add_u32 m0,s64,50176
MF a[24:27],a[200:203],v[52:55],a[24:27]
buffer_load_dwordx4 v12,s[40:43],s61 offen lds
MF a[28:31],a[204:207],v[52:55],a[28:31]
ds_read_b64_tr_b16 a[168:169],v84 offset:8704
MF a[32:35],a[192:195],v[56:59],a[32:35]
ds_read_b64_tr_b16 a[170:171],v84 offset:8960
MF a[36:39],a[196:199],v[56:59],a[36:39]
ds_read_b64_tr_b16 a[172:173],v85 offset:8704
s_add_u32 m0,s64,51200
MF a[40:43],a[200:203],v[56:59],a[40:43]
buffer_load_dwordx4 v13,s[40:43],s61 offen lds
MF a[44:47],a[204:207],v[56:59],a[44:47]
ds_read_b64_tr_b16 a[174:175],v85 offset:8960
MF a[48:51],a[192:195],v[60:63],a[48:51]
ds_read_b128 v[36:39],v71 offset:2048
MF a[52:55],a[196:199],v[60:63],a[52:55]
ds_read_b128 v[40:43],v71 offset:4096
s_add_u32 m0,s64,52224
MF a[56:59],a[200:203],v[60:63],a[56:59]
buffer_load_dwordx4 v14,s[40:43],s61 offen lds
MF a[60:63],a[204:207],v[60:63],a[60:63]
ds_read_b128 v[44:47],v71 offset:6144
s_add_u32 s62,s62,128
s_add_u32 s61,s61,114688
s_waitcnt vmcnt(16)
s_barrier
s_waitcnt lgkmcnt(15)
MF a[0:3],a[128:131],v[16:19],a[0:3]
ds_read_b64_tr_b16 a[192:193],v86
MF a[4:7],a[132:135],v[16:19],a[4:7]
ds_read_b64_tr_b16 a[194:195],v86 offset:256
s_add_u32 m0,s63,65536
MF a[8:11],a[136:139],v[16:19],a[8:11]
buffer_load_dwordx4 v74,s[36:39],s62 offen lds
MF a[12:15],a[140:143],v[16:19],a[12:15]
ds_read_b128 v[48:51],v72
s_waitcnt lgkmcnt(15)
MF a[16:19],a[128:131],v[20:23],a[16:19]
ds_read_b64_tr_b16 a[196:197],v87
MF a[20:23],a[132:135],v[20:23],a[20:23]
ds_read_b64_tr_b16 a[198:199],v87 offset:256
s_add_u32 m0,s63,66560
MF a[24:27],a[136:139],v[20:23],a[24:27]
buffer_load_dwordx4 v75,s[36:39],s62 offen lds
MF a[28:31],a[140:143],v[20:23],a[28:31]
ds_read_b64_tr_b16 a[200:201],v86 offset:512
MF a[32:35],a[128:131],v[24:27],a[32:35]
ds_read_b64_tr_b16 a[202:203],v86 offset:768
MF a[36:39],a[132:135],v[24:27],a[36:39]
ds_read_b64_tr_b16 a[204:205],v87 offset:512
s_add_u32 m0,s63,67584
MF a[40:43],a[136:139],v[24:27],a[40:43]
buffer_load_dwordx4 v76,s[36:39],s62 offen lds
MF a[44:47],a[140:143],v[24:27],a[44:47]
ds_read_b64_tr_b16 a[206:207],v87 offset:768
MF a[48:51],a[128:131],v[28:31],a[48:51]
ds_read_b128 v[52:55],v72 offset:2048
MF a[52:55],a[132:135],v[28:31],a[52:55]
ds_read_b128 v[56:59],v72 offset:4096
s_add_u32 m0,s63,68608
MF a[56:59],a[136:139],v[28:31],a[56:59]
buffer_load_dwordx4 v77,s[36:39],s62 offen lds
MF a[60:63],a[140:143],v[28:31],a[60:63]
ds_read_b128 v[60:63],v72 offset:6144
s_waitcnt lgkmcnt(15)
MF a[0:3],a[160:163],v[32:35],a[0:3]
ds_read_b64_tr_b16 a[128:129],v86 offset:8192
MF a[4:7],a[164:167],v[32:35],a[4:7]
ds_read_b64_tr_b16 a[130:131],v86 offset:8448
s_add_u32 m0,s64,81920
MF a[8:11],a[168:171],v[32:35],a[8:11]
buffer_load_dwordx4 v11,s[40:43],s61 offen lds
MF a[12:15],a[172:175],v[32:35],a[12:15]
ds_read_b128 v[16:19],v73
s_waitcnt lgkmcnt(15)
MF a[16:19],a[160:163],v[36:39],a[16:19]
ds_read_b64_tr_b16 a[132:133],v87 offset:8192
MF a[20:23],a[164:167],v[36:39],a[20:23]
ds_read_b64_tr_b16 a[134:135],v87 offset:8448
s_add_u32 m0,s64,82944
MF a[24:27],a[168:171],v[36:39],a[24:27]
buffer_load_dwordx4 v12,s[40:43],s61 offen lds
MF a[28:31],a[172:175],v[36:39],a[28:31]
ds_read_b64_tr_b16 a[136:137],v86 offset:8704
MF a[32:35],a[160:163],v[40:43],a[32:35]
ds_read_b64_tr_b16 a[138:139],v86 offset:8960
MF a[36:39],a[164:167],v[40:43],a[36:39]
ds_read_b64_tr_b16 a[140:141],v87 offset:8704
s_add_u32 m0,s64,83968
MF a[40:43],a[168:171],v[40:43],a[40:43]
buffer_load_dwordx4 v13,s[40:43],s61 offen lds
MF a[44:47],a[172:175],v[40:43],a[44:47]
ds_read_b64_tr_b16 a[142:143],v87 offset:8960
MF a[48:51],a[160:163],v[44:47],a[48:51]
ds_read_b128 v[20:23],v73 offset:2048
MF a[52:55],a[164:167],v[44:47],a[52:55]
ds_read_b128 v[24:27],v73 offset:4096
s_add_u32 m0,s64,84992
MF a[56:59],a[168:171],v[44:47],a[56:59]
buffer_load_dwordx4 v14,s[40:43],s61 offen lds
MF a[60:63],a[172:175],v[44:47],a[60:63]
ds_read_b128 v[28:31],v73 offset:6144
s_add_u32 s62,s62,128
s_add_u32 s61,s61,114688
s_waitcnt vmcnt(16)
s_barrier
s_waitcnt lgkmcnt(15)
MF a[0:3],a[192:195],v[48:51],a[0:3]
ds_read_b64_tr_b16 a[160:161],v78
MF a[4:7],a[196:199],v[48:51],a[4:7]
ds_read_b64_tr_b16 a[162:163],v78 offset:256
s_add_u32 m0,s63,98304
MF a[8:11],a[200:203],v[48:51],a[8:11]
buffer_load_dwordx4 v74,s[36:39],s62 offen lds
MF a[12:15],a[204:207],v[48:51],a[12:15]
ds_read_b128 v[32:35],v64
s_waitcnt lgkmcnt(15)
MF a[16:19],a[192:195],v[52:55],a[16:19]
ds_read_b64_tr_b16 a[164:165],v79
MF a[20:23],a[196:199],v[52:55],a[20:23]
ds_read_b64_tr_b16 a[166:167],v79 offset:256
s_add_u32 m0,s63,99328
MF a[24:27],a[200:203],v[52:55],a[24:27]
buffer_load_dwordx4 v75,s[36:39],s62 offen lds
MF a[28:31],a[204:207],v[52:55],a[28:31]
ds_read_b64_tr_b16 a[168:169],v78 offset:512
MF a[32:35],a[192:195],v[56:59],a[32:35]
ds_read_b64_tr_b16 a[170:171],v78 offset:768
MF a[36:39],a[196:199],v[56:59],a[36:39]
ds_read_b64_tr_b16 a[172:173],v79 offset:512
s_add_u32 m0,s63,100352
MF a[40:43],a[200:203],v[56:59],a[40:43]
buffer_load_dwordx4 v76,s[36:39],s62 offen lds
MF a[44:47],a[204:207],v[56:59],a[44:47]
ds_read_b64_tr_b16 a[174:175],v79 offset:768
MF a[48:51],a[192:195],v[60:63],a[48:51]
ds_read_b128 v[36:39],v64 offset:2048
MF a[52:55],a[196:199],v[60:63],a[52:55]
ds_read_b128 v[40:43],v64 offset:4096
s_add_u32 m0,s63,101376
MF a[56:59],a[200:203],v[60:63],a[56:59]
buffer_load_dwordx4 v77,s[36:39],s62 offen lds
MF a[60:63],a[204:207],v[60:63],a[60:63]
ds_read_b128 v[44:47],v64 offset:6144
s_waitcnt lgkmcnt(15)
MF a[0:3],a[128:131],v[16:19],a[0:3]
ds_read_b64_tr_b16 a[192:193],v78 offset:8192
MF a[4:7],a[132:135],v[16:19],a[4:7]
ds_read_b64_tr_b16 a[194:195],v78 offset:8448
s_add_u32 m0,s64,114688
MF a[8:11],a[136:139],v[16:19],a[8:11]
buffer_load_dwordx4 v11,s[40:43],s61 offen lds
MF a[12:15],a[140:143],v[16:19],a[12:15]
ds_read_b128 v[48:51],v65
s_waitcnt lgkmcnt(15)
MF a[16:19],a[128:131],v[20:23],a[16:19]
ds_read_b64_tr_b16 a[196:197],v79 offset:8192
MF a[20:23],a[132:135],v[20:23],a[20:23]
ds_read_b64_tr_b16 a[198:199],v79 offset:8448
s_add_u32 m0,s64,115712
MF a[24:27],a[136:139],v[20:23],a[24:27]
buffer_load_dwordx4 v12,s[40:43],s61 offen lds
MF a[28:31],a[140:143],v[20:23],a[28:31]
ds_read_b64_tr_b16 a[200:201],v78 offset:8704
MF a[32:35],a[128:131],v[24:27],a[32:35]
ds_read_b64_tr_b16 a[202:203],v78 offset:8960
MF a[36:39],a[132:135],v[24:27],a[36:39]
ds_read_b64_tr_b16 a[204:205],v79 offset:8704
s_add_u32 m0,s64,116736
MF a[40:43],a[136:139],v[24:27],a[40:43]
buffer_load_dwordx4 v13,s[40:43],s61 offen lds
MF a[44:47],a[140:143],v[24:27],a[44:47]
ds_read_b64_tr_b16 a[206:207],v79 offset:8960
MF a[48:51],a[128:131],v[28:31],a[48:51]
ds_read_b128 v[52:55],v65 offset:2048
MF a[52:55],a[132:135],v[28:31],a[52:55]
ds_read_b128 v[56:59],v65 offset:4096
s_add_u32 m0,s64,117760
MF a[56:59],a[136:139],v[28:31],a[56:59]
buffer_load_dwordx4 v14,s[40:43],s61 offen lds
MF a[60:63],a[140:143],v[28:31],a[60:63]
ds_read_b128 v[60:63],v65 offset:6144
s_add_u32 s62,s62,128
s_add_u32 s61,s61,114688
s_waitcnt vmcnt(16)
s_barrier
s_waitcnt lgkmcnt(15)
MF a[0:3],a[160:163],v[32:35],a[0:3]
ds_read_b64_tr_b16 a[128:129],v80
MF a[4:7],a[164:167],v[32:35],a[4:7]
ds_read_b64_tr_b16 a[130:131],v80 offset:256
s_add_u32 m0,s63,131072
MF a[8:11],a[168:171],v[32:35],a[8:11]
buffer_load_dwordx4 v74,s[36:39],s62 offen lds
MF a[12:15],a[172:175],v[32:35],a[12:15]
ds_read_b128 v[16:19],v66
s_waitcnt lgkmcnt(15)
MF a[16:19],a[160:163],v[36:39],a[16:19]
ds_read_b64_tr_b16 a[132:133],v81
MF a[20:23],a[164:167],v[36:39],a[20:23]
ds_read_b64_tr_b16 a[134:135],v81 offset:256
s_add_u32 m0,s63,132096
MF a[24:27],a[168:171],v[36:39],a[24:27]
buffer_load_dwordx4 v75,s[36:39],s62 offen lds
MF a[28:31],a[172:175],v[36:39],a[28:31]
ds_read_b64_tr_b16 a[136:137],v80 offset:512
MF a[32:35],a[160:163],v[40:43],a[32:35]
ds_read_b64_tr_b16 a[138:139],v80 offset:768
MF a[36:39],a[164:167],v[40:43],a[36:39]
ds_read_b64_tr_b16 a[140:141],v81 offset:512
s_add_u32 m0,s63,133120
MF a[40:43],a[168:171],v[40:43],a[40:43]
buffer_load_dwordx4 v76,s[36:39],s62 offen lds
MF a[44:47],a[172:175],v[40:43],a[44:47]
ds_read_b64_tr_b16 a[142:143],v81 offset:768
MF a[48:51],a[160:163],v[44:47],a[48:51]
ds_read_b128 v[20:23],v66 offset:2048
MF a[52:55],a[164:167],v[44:47],a[52:55]
ds_read_b128 v[24:27],v66 offset:4096
s_add_u32 m0,s63,134144
MF a[56:59],a[168:171],v[44:47],a[56:59]
buffer_load_dwordx4 v77,s[36:39],s62 offen lds
MF a[60:63],a[172:175],v[44:47],a[60:63]
ds_read_b128 v[28:31],v66 offset:6144
s_waitcnt lgkmcnt(15)
MF a[0:3],a[192:195],v[48:51],a[0:3]
ds_read_b64_tr_b16 a[160:161],v80 offset:8192
MF a[4:7],a[196:199],v[48:51],a[4:7]
ds_read_b64_tr_b16 a[162:163],v80 offset:8448
s_add_u32 m0,s64,147456
MF a[8:11],a[200:203],v[48:51],a[8:11]
buffer_load_dwordx4 v11,s[40:43],s61 offen lds
MF a[12:15],a[204:207],v[48:51],a[12:15]
ds_read_b128 v[32:35],v67
s_waitcnt lgkmcnt(15)
MF a[16:19],a[192:195],v[52:55],a[16:19]
ds_read_b64_tr_b16 a[164:165],v81 offset:8192
MF a[20:23],a[196:199],v[52:55],a[20:23]
ds_read_b64_tr_b16 a[166:167],v81 offset:8448
s_add_u32 m0,s64,148480
MF a[24:27],a[200:203],v[52:55],a[24:27]
buffer_load_dwordx4 v12,s[40:43],s61 offen lds
MF a[28:31],a[204:207],v[52:55],a[28:31]
ds_read_b64_tr_b16 a[168:169],v80 offset:8704
MF a[32:35],a[192:195],v[56:59],a[32:35]
ds_read_b64_tr_b16 a[170:171],v80 offset:8960
MF a[36:39],a[196:199],v[56:59],a[36:39]
ds_read_b64_tr_b16 a[172:173],v81 offset:8704
s_add_u32 m0,s64,149504
MF a[40:43],a[200:203],v[56:59],a[40:43]
buffer_load_dwordx4 v13,s[40:43],s61 offen lds
MF a[44:47],a[204:207],v[56:59],a[44:47]
ds_read_b64_tr_b16 a[174:175],v81 offset:8960
MF a[48:51],a[192:195],v[60:63],a[48:51]
ds_read_b128 v[36:39],v67 offset:2048
MF a[52:55],a[196:199],v[60:63],a[52:55]
ds_read_b128 v[40:43],v67 offset:4096
s_add_u32 m0,s64,150528
MF a[56:59],a[200:203],v[60:63],a[56:59]
buffer_load_dwordx4 v14,s[40:43],s61 offen lds
MF a[60:63],a[204:207],v[60:63],a[60:63]
ds_read_b128 v[44:47],v67 offset:6144
s_add_u32 s62,s62,128
s_add_u32 s61,s61,114688
s_waitcnt vmcnt(16)
s_barrier
s_waitcnt lgkmcnt(15)
MF a[0:3],a[128:131],v[16:19],a[0:3]
ds_read_b64_tr_b16 a[192:193],v82
MF a[4:7],a[132:135],v[16:19],a[4:7]
ds_read_b64_tr_b16 a[194:195],v82 offset:256
s_add_u32 m0,s63,0
MF a[8:11],a[136:139],v[16:19],a[8:11]
buffer_load_dwordx4 v74,s[36:39],s62 offen lds
MF a[12:15],a[140:143],v[16:19],a[12:15]
ds_read_b128 v[48:51],v68
s_waitcnt lgkmcnt(15)
MF a[16:19],a[128:131],v[20:23],a[16:19]
ds_read_b64_tr_b16 a[196:197],v83
MF a[20:23],a[132:135],v[20:23],a[20:23]
ds_read_b64_tr_b16 a[198:199],v83 offset:256
s_add_u32 m0,s63,1024
MF a[24:27],a[136:139],v[20:23],a[24:27]
buffer_load_dwordx4 v75,s[36:39],s62 offen lds
MF a[28:31],a[140:143],v[20:23],a[28:31]
ds_read_b64_tr_b16 a[200:201],v82 offset:512
MF a[32:35],a[128:131],v[24:27],a[32:35]
ds_read_b64_tr_b16 a[202:203],v82 offset:768
MF a[36:39],a[132:135],v[24:27],a[36:39]
ds_read_b64_tr_b16 a[204:205],v83 offset:512
s_add_u32 m0,s63,2048
MF a[40:43],a[136:139],v[24:27],a[40:43]
buffer_load_dwordx4 v76,s[36:39],s62 offen lds
MF a[44:47],a[140:143],v[24:27],a[44:47]
ds_read_b64_tr_b16 a[206:207],v83 offset:768
MF a[48:51],a[128:131],v[28:31],a[48:51]
ds_read_b128 v[52:55],v68 offset:2048
MF a[52:55],a[132:135],v[28:31],a[52:55]
ds_read_b128 v[56:59],v68 offset:4096
s_add_u32 m0,s63,3072
MF a[56:59],a[136:139],v[28:31],a[56:59]
buffer_load_dwordx4 v77,s[36:39],s62 offen lds
MF a[60:63],a[140:143],v[28:31],a[60:63]
ds_read_b128 v[60:63],v68 offset:6144
s_waitcnt lgkmcnt(15)
MF a[0:3],a[160:163],v[32:35],a[0:3]
ds_read_b64_tr_b16 a[128:129],v82 offset:8192
MF a[4:7],a[164:167],v[32:35],a[4:7]
ds_read_b64_tr_b16 a[130:131],v82 offset:8448
s_add_u32 m0,s64,16384
MF a[8:11],a[168:171],v[32:35],a[8:11]
buffer_load_dwordx4 v11,s[40:43],s61 offen lds
MF a[12:15],a[172:175],v[32:35],a[12:15]
ds_read_b128 v[16:19],v69
s_waitcnt lgkmcnt(15)
MF a[16:19],a[160:163],v[36:39],a[16:19]
ds_read_b64_tr_b16 a[132:133],v83 offset:8192
MF a[20:23],a[164:167],v[36:39],a[20:23]
ds_read_b64_tr_b16 a[134:135],v83 offset:8448
s_add_u32 m0,s64,17408
MF a[24:27],a[168:171],v[36:39],a[24:27]
buffer_load_dwordx4 v12,s[40:43],s61 offen lds
MF a[28:31],a[172:175],v[36:39],a[28:31]
ds_read_b64_tr_b16 a[136:137],v82 offset:8704
MF a[32:35],a[160:163],v[40:43],a[32:35]
ds_read_b64_tr_b16 a[138:139],v82 offset:8960
MF a[36:39],a[164:167],v[40:43],a[36:39]
ds_read_b64_tr_b16 a[140:141],v83 offset:8704
s_add_u32 m0,s64,18432
MF a[40:43],a[168:171],v[40:43],a[40:43]
buffer_load_dwordx4 v13,s[40:43],s61 offen lds
MF a[44:47],a[172:175],v[40:43],a[44:47]
)A"
R"A(ds_read_b64_tr_b16 a[142:143],v83 offset:8960
MF a[48:51],a[160:163],v[44:47],a[48:51]
ds_read_b128 v[20:23],v69 offset:2048
MF a[52:55],a[164:167],v[44:47],a[52:55]
ds_read_b128 v[24:27],v69 offset:4096
s_add_u32 m0,s64,19456
MF a[56:59],a[168:171],v[44:47],a[56:59]
buffer_load_dwordx4 v14,s[40:43],s61 offen lds
MF a[60:63],a[172:175],v[44:47],a[60:63]
ds_read_b128 v[28:31],v69 offset:6144
s_add_u32 s62,s62,128
s_add_u32 s61,s61,114688
s_waitcnt vmcnt(16)
s_barrier
s_waitcnt lgkmcnt(15)
MF a[0:3],a[192:195],v[48:51],a[0:3]
ds_read_b64_tr_b16 a[160:161],v84
MF a[4:7],a[196:199],v[48:51],a[4:7]
ds_read_b64_tr_b16 a[162:163],v84 offset:256
s_add_u32 m0,s63,32768
MF a[8:11],a[200:203],v[48:51],a[8:11]
buffer_load_dwordx4 v74,s[36:39],s62 offen lds
MF a[12:15],a[204:207],v[48:51],a[12:15]
ds_read_b128 v[32:35],v70
s_waitcnt lgkmcnt(15)
MF a[16:19],a[192:195],v[52:55],a[16:19]
ds_read_b64_tr_b16 a[164:165],v85
MF a[20:23],a[196:199],v[52:55],a[20:23]
ds_read_b64_tr_b16 a[166:167],v85 offset:256
s_add_u32 m0,s63,33792
MF a[24:27],a[200:203],v[52:55],a[24:27]
buffer_load_dwordx4 v75,s[36:39],s62 offen lds
MF a[28:31],a[204:207],v[52:55],a[28:31]
ds_read_b64_tr_b16 a[168:169],v84 offset:512
MF a[32:35],a[192:195],v[56:59],a[32:35]
ds_read_b64_tr_b16 a[170:171],v84 offset:768
MF a[36:39],a[196:199],v[56:59],a[36:39]
ds_read_b64_tr_b16 a[172:173],v85 offset:512
s_add_u32 m0,s63,34816
MF a[40:43],a[200:203],v[56:59],a[40:43]
buffer_load_dwordx4 v76,s[36:39],s62 offen lds
MF a[44:47],a[204:207],v[56:59],a[44:47]
ds_read_b64_tr_b16 a[174:175],v85 offset:768
MF a[48:51],a[192:195],v[60:63],a[48:51]
ds_read_b128 v[36:39],v70 offset:2048
MF a[52:55],a[196:199],v[60:63],a[52:55]
ds_read_b128 v[40:43],v70 offset:4096
s_add_u32 m0,s63,35840
MF a[56:59],a[200:203],v[60:63],a[56:59]
buffer_load_dwordx4 v77,s[36:39],s62 offen lds
MF a[60:63],a[204:207],v[60:63],a[60:63]
ds_read_b128 v[44:47],v70 offset:6144
s_waitcnt lgkmcnt(15)
MF a[0:3],a[128:131],v[16:19],a[0:3]
ds_read_b64_tr_b16 a[192:193],v84 offset:8192
MF a[4:7],a[132:135],v[16:19],a[4:7]
ds_read_b64_tr_b16 a[194:195],v84 offset:8448
s_add_u32 m0,s64,49152
MF a[8:11],a[136:139],v[16:19],a[8:11]
buffer_load_dwordx4 v11,s[40:43],s61 offen lds
MF a[12:15],a[140:143],v[16:19],a[12:15]
ds_read_b128 v[48:51],v71
s_waitcnt lgkmcnt(15)
MF a[16:19],a[128:131],v[20:23],a[16:19]
ds_read_b64_tr_b16 a[196:197],v85 offset:8192
MF a[20:23],a[132:135],v[20:23],a[20:23]
ds_read_b64_tr_b16 a[198:199],v85 offset:8448
s_add_u32 m0,s64,50176
MF a[24:27],a[136:139],v[20:23],a[24:27]
buffer_load_dwordx4 v12,s[40:43],s61 offen lds
MF a[28:31],a[140:143],v[20:23],a[28:31]
ds_read_b64_tr_b16 a[200:201],v84 offset:8704
MF a[32:35],a[128:131],v[24:27],a[32:35]
ds_read_b64_tr_b16 a[202:203],v84 offset:8960
MF a[36:39],a[132:135],v[24:27],a[36:39]
ds_read_b64_tr_b16 a[204:205],v85 offset:8704
s_add_u32 m0,s64,51200
MF a[40:43],a[136:139],v[24:27],a[40:43]
buffer_load_dwordx4 v13,s[40:43],s61 offen lds
MF a[44:47],a[140:143],v[24:27],a[44:47]
ds_read_b64_tr_b16 a[206:207],v85 offset:8960
MF a[48:51],a[128:131],v[28:31],a[48:51]
ds_read_b128 v[52:55],v71 offset:2048
MF a[52:55],a[132:135],v[28:31],a[52:55]
ds_read_b128 v[56:59],v71 offset:4096
s_add_u32 m0,s64,52224
MF a[56:59],a[136:139],v[28:31],a[56:59]
buffer_load_dwordx4 v14,s[40:43],s61 offen lds
MF a[60:63],a[140:143],v[28:31],a[60:63]
ds_read_b128 v[60:63],v71 offset:6144
s_add_u32 s62,s62,128
s_add_u32 s61,s61,114688
s_mov_b32 s52,s14
s_and_b32 s53,s15,0xffff
s_mov_b32 s54,3538944
s_mov_b32 s55,0x20000
s_mov_b32 s56,s12
s_and_b32 s57,s13,0xffff
s_mov_b32 s58,4096
s_mov_b32 s59,0x20000
v_and_b32 v232,15,v1
v_lshlrev_b32 v232,1,v232
s_add_u32 s100,s83,0
s_lshl_b32 s101,s100,5
buffer_load_ushort v182,v232,s[56:59],s101 offen
s_mul_i32 s100,s100,27
s_add_u32 s100,s100,s84
s_lshl_b32 s100,s100,10
buffer_load_dwordx4 v[184:187],v10,s[52:55],s100 offen
buffer_load_dwordx4 v[188:191],v10,s[52:55],s100 offen offset:1024
s_add_u32 s100,s83,1
s_lshl_b32 s101,s100,5
buffer_load_ushort v183,v232,s[56:59],s101 offen
s_mul_i32 s100,s100,27
s_add_u32 s100,s100,s84
s_lshl_b32 s100,s100,10
buffer_load_dwordx4 v[192:195],v10,s[52:55],s100 offen
buffer_load_dwordx4 v[196:199],v10,s[52:55],s100 offen offset:1024
s_mov_b32 s52,s20
s_and_b32 s53,s21,0xffff
s_mov_b32 s54,1105920
s_mov_b32 s55,0x20000
s_mov_b32 s56,s32
s_and_b32 s57,s33,0xffff
s_mov_b32 s58,3670016
s_mov_b32 s59,0x20000
s_waitcnt vmcnt(16)
s_barrier
s_waitcnt lgkmcnt(15)
MF a[0:3],a[160:163],v[32:35],a[0:3]
ds_read_b64_tr_b16 a[128:129],v86
MF a[4:7],a[164:167],v[32:35],a[4:7]
ds_read_b64_tr_b16 a[130:131],v86 offset:256
s_add_u32 m0,s63,65536
MF a[8:11],a[168:171],v[32:35],a[8:11]
buffer_load_dwordx4 v74,s[36:39],s62 offen lds
MF a[12:15],a[172:175],v[32:35],a[12:15]
ds_read_b128 v[16:19],v72
s_waitcnt lgkmcnt(15)
MF a[16:19],a[160:163],v[36:39],a[16:19]
ds_read_b64_tr_b16 a[132:133],v87
MF a[20:23],a[164:167],v[36:39],a[20:23]
ds_read_b64_tr_b16 a[134:135],v87 offset:256
s_add_u32 m0,s63,66560
MF a[24:27],a[168:171],v[36:39],a[24:27]
buffer_load_dwordx4 v75,s[36:39],s62 offen lds
MF a[28:31],a[172:175],v[36:39],a[28:31]
ds_read_b64_tr_b16 a[136:137],v86 offset:512
MF a[32:35],a[160:163],v[40:43],a[32:35]
ds_read_b64_tr_b16 a[138:139],v86 offset:768
MF a[36:39],a[164:167],v[40:43],a[36:39]
ds_read_b64_tr_b16 a[140:141],v87 offset:512
s_add_u32 m0,s63,67584
MF a[40:43],a[168:171],v[40:43],a[40:43]
buffer_load_dwordx4 v76,s[36:39],s62 offen lds
MF a[44:47],a[172:175],v[40:43],a[44:47]
ds_read_b64_tr_b16 a[142:143],v87 offset:768
MF a[48:51],a[160:163],v[44:47],a[48:51]
ds_read_b128 v[20:23],v72 offset:2048
MF a[52:55],a[164:167],v[44:47],a[52:55]
ds_read_b128 v[24:27],v72 offset:4096
s_add_u32 m0,s63,68608
MF a[56:59],a[168:171],v[44:47],a[56:59]
buffer_load_dwordx4 v77,s[36:39],s62 offen lds
MF a[60:63],a[172:175],v[44:47],a[60:63]
ds_read_b128 v[28:31],v72 offset:6144
s_waitcnt lgkmcnt(15)
MF a[0:3],a[192:195],v[48:51],a[0:3]
ds_read_b64_tr_b16 a[160:161],v86 offset:8192
MF a[4:7],a[196:199],v[48:51],a[4:7]
ds_read_b64_tr_b16 a[162:163],v86 offset:8448
s_add_u32 m0,s64,81920
MF a[8:11],a[200:203],v[48:51],a[8:11]
buffer_load_dwordx4 v11,s[40:43],s61 offen lds
MF a[12:15],a[204:207],v[48:51],a[12:15]
ds_read_b128 v[32:35],v73
s_waitcnt lgkmcnt(15)
MF a[16:19],a[192:195],v[52:55],a[16:19]
ds_read_b64_tr_b16 a[164:165],v87 offset:8192
MF a[20:23],a[196:199],v[52:55],a[20:23]
ds_read_b64_tr_b16 a[166:167],v87 offset:8448
s_add_u32 m0,s64,82944
MF a[24:27],a[200:203],v[52:55],a[24:27]
buffer_load_dwordx4 v12,s[40:43],s61 offen lds
MF a[28:31],a[204:207],v[52:55],a[28:31]
ds_read_b64_tr_b16 a[168:169],v86 offset:8704
MF a[32:35],a[192:195],v[56:59],a[32:35]
ds_read_b64_tr_b16 a[170:171],v86 offset:8960
MF a[36:39],a[196:199],v[56:59],a[36:39]
ds_read_b64_tr_b16 a[172:173],v87 offset:8704
s_add_u32 m0,s64,83968
MF a[40:43],a[200:203],v[56:59],a[40:43]
buffer_load_dwordx4 v13,s[40:43],s61 offen lds
MF a[44:47],a[204:207],v[56:59],a[44:47]
ds_read_b64_tr_b16 a[174:175],v87 offset:8960
MF a[48:51],a[192:195],v[60:63],a[48:51]
ds_read_b128 v[36:39],v73 offset:2048
MF a[52:55],a[196:199],v[60:63],a[52:55]
ds_read_b128 v[40:43],v73 offset:4096
s_add_u32 m0,s64,84992
MF a[56:59],a[200:203],v[60:63],a[56:59]
buffer_load_dwordx4 v14,s[40:43],s61 offen lds
MF a[60:63],a[204:207],v[60:63],a[60:63]
ds_read_b128 v[44:47],v73 offset:6144
s_add_u32 s62,s62,128
s_add_u32 s61,s61,114688
s_waitcnt vmcnt(16)
s_barrier
s_waitcnt lgkmcnt(15)
MF a[0:3],a[128:131],v[16:19],a[0:3]
ds_read_b64_tr_b16 a[192:193],v78
MF a[4:7],a[132:135],v[16:19],a[4:7]
ds_read_b64_tr_b16 a[194:195],v78 offset:256
s_add_u32 m0,s63,98304
MF a[8:11],a[136:139],v[16:19],a[8:11]
buffer_load_dwordx4 v74,s[36:39],s62 offen lds
MF a[12:15],a[140:143],v[16:19],a[12:15]
ds_read_b128 v[48:51],v64
s_waitcnt lgkmcnt(15)
MF a[16:19],a[128:131],v[20:23],a[16:19]
ds_read_b64_tr_b16 a[196:197],v79
MF a[20:23],a[132:135],v[20:23],a[20:23]
ds_read_b64_tr_b16 a[198:199],v79 offset:256
s_add_u32 m0,s63,99328
MF a[24:27],a[136:139],v[20:23],a[24:27]
buffer_load_dwordx4 v75,s[36:39],s62 offen lds
MF a[28:31],a[140:143],v[20:23],a[28:31]
ds_read_b64_tr_b16 a[200:201],v78 offset:512
MF a[32:35],a[128:131],v[24:27],a[32:35]
ds_read_b64_tr_b16 a[202:203],v78 offset:768
MF a[36:39],a[132:135],v[24:27],a[36:39]
ds_read_b64_tr_b16 a[204:205],v79 offset:512
s_add_u32 m0,s63,100352
MF a[40:43],a[136:139],v[24:27],a[40:43]
buffer_load_dwordx4 v76,s[36:39],s62 offen lds
MF a[44:47],a[140:143],v[24:27],a[44:47]
ds_read_b64_tr_b16 a[206:207],v79 offset:768
MF a[48:51],a[128:131],v[28:31],a[48:51]
ds_read_b128 v[52:55],v64 offset:2048
MF a[52:55],a[132:135],v[28:31],a[52:55]
ds_read_b128 v[56:59],v64 offset:4096
s_add_u32 m0,s63,101376
MF a[56:59],a[136:139],v[28:31],a[56:59]
buffer_load_dwordx4 v77,s[36:39],s62 offen lds
MF a[60:63],a[140:143],v[28:31],a[60:63]
ds_read_b128 v[60:63],v64 offset:6144
s_waitcnt lgkmcnt(15)
MF a[0:3],a[160:163],v[32:35],a[0:3]
ds_read_b64_tr_b16 a[128:129],v78 offset:8192
MF a[4:7],a[164:167],v[32:35],a[4:7]
ds_read_b64_tr_b16 a[130:131],v78 offset:8448
s_add_u32 m0,s64,114688
MF a[8:11],a[168:171],v[32:35],a[8:11]
buffer_load_dwordx4 v11,s[40:43],s61 offen lds
MF a[12:15],a[172:175],v[32:35],a[12:15]
ds_read_b128 v[16:19],v65
s_waitcnt lgkmcnt(15)
MF a[16:19],a[160:163],v[36:39],a[16:19]
ds_read_b64_tr_b16 a[132:133],v79 offset:8192
MF a[20:23],a[164:167],v[36:39],a[20:23]
ds_read_b64_tr_b16 a[134:135],v79 offset:8448
s_add_u32 m0,s64,115712
MF a[24:27],a[168:171],v[36:39],a[24:27]
buffer_load_dwordx4 v12,s[40:43],s61 offen lds
MF a[28:31],a[172:175],v[36:39],a[28:31]
ds_read_b64_tr_b16 a[136:137],v78 offset:8704
MF a[32:35],a[160:163],v[40:43],a[32:35]
ds_read_b64_tr_b16 a[138:139],v78 offset:8960
MF a[36:39],a[164:167],v[40:43],a[36:39]
ds_read_b64_tr_b16 a[140:141],v79 offset:8704
s_add_u32 m0,s64,116736
MF a[40:43],a[168:171],v[40:43],a[40:43]
buffer_load_dwordx4 v13,s[40:43],s61 offen lds
MF a[44:47],a[172:175],v[40:43],a[44:47]
ds_read_b64_tr_b16 a[142:143],v79 offset:8960
MF a[48:51],a[160:163],v[44:47],a[48:51]
ds_read_b128 v[20:23],v65 offset:2048
MF a[52:55],a[164:167],v[44:47],a[52:55]
ds_read_b128 v[24:27],v65 offset:4096
s_add_u32 m0,s64,117760
MF a[56:59],a[168:171],v[44:47],a[56:59]
buffer_load_dwordx4 v14,s[40:43],s61 offen lds
MF a[60:63],a[172:175],v[44:47],a[60:63]
ds_read_b128 v[28:31],v65 offset:6144
s_add_u32 s62,s62,128
s_add_u32 s61,s61,114688
s_waitcnt vmcnt(16)
s_barrier
s_waitcnt lgkmcnt(15)
MF a[0:3],a[192:195],v[48:51],a[0:3]
ds_read_b64_tr_b16 a[160:161],v80
MF a[4:7],a[196:199],v[48:51],a[4:7]
ds_read_b64_tr_b16 a[162:163],v80 offset:256
s_add_u32 m0,s63,131072
MF a[8:11],a[200:203],v[48:51],a[8:11]
buffer_load_dwordx4 v74,s[36:39],s62 offen lds
MF a[12:15],a[204:207],v[48:51],a[12:15]
ds_read_b128 v[32:35],v66
s_waitcnt lgkmcnt(15)
MF a[16:19],a[192:195],v[52:55],a[16:19]
ds_read_b64_tr_b16 a[164:165],v81
MF a[20:23],a[196:199],v[52:55],a[20:23]
ds_read_b64_tr_b16 a[166:167],v81 offset:256
s_add_u32 m0,s63,132096
MF a[24:27],a[200:203],v[52:55],a[24:27]
buffer_load_dwordx4 v75,s[36:39],s62 offen lds
MF a[28:31],a[204:207],v[52:55],a[28:31]
ds_read_b64_tr_b16 a[168:169],v80 offset:512
MF a[32:35],a[192:195],v[56:59],a[32:35]
ds_read_b64_tr_b16 a[170:171],v80 offset:768
MF a[36:39],a[196:199],v[56:59],a[36:39]
ds_read_b64_tr_b16 a[172:173],v81 offset:512
s_add_u32 m0,s63,133120
MF a[40:43],a[200:203],v[56:59],a[40:43]
buffer_load_dwordx4 v76,s[36:39],s62 offen lds
MF a[44:47],a[204:207],v[56:59],a[44:47]
ds_read_b64_tr_b16 a[174:175],v81 offset:768
MF a[48:51],a[192:195],v[60:63],a[48:51]
ds_read_b128 v[36:39],v66 offset:2048
MF a[52:55],a[196:199],v[60:63],a[52:55]
ds_read_b128 v[40:43],v66 offset:4096
s_add_u32 m0,s63,134144
MF a[56:59],a[200:203],v[60:63],a[56:59]
buffer_load_dwordx4 v77,s[36:39],s62 offen lds
MF a[60:63],a[204:207],v[60:63],a[60:63]
ds_read_b128 v[44:47],v66 offset:6144
s_waitcnt lgkmcnt(15)
MF a[0:3],a[128:131],v[16:19],a[0:3]
ds_read_b64_tr_b16 a[192:193],v80 offset:8192
MF a[4:7],a[132:135],v[16:19],a[4:7]
ds_read_b64_tr_b16 a[194:195],v80 offset:8448
s_add_u32 m0,s64,147456
MF a[8:11],a[136:139],v[16:19],a[8:11]
buffer_load_dwordx4 v11,s[40:43],s61 offen lds
MF a[12:15],a[140:143],v[16:19],a[12:15]
ds_read_b128 v[48:51],v67
s_waitcnt lgkmcnt(15)
MF a[16:19],a[128:131],v[20:23],a[16:19]
ds_read_b64_tr_b16 a[196:197],v81 offset:8192
MF a[20:23],a[132:135],v[20:23],a[20:23]
ds_read_b64_tr_b16 a[198:199],v81 offset:8448
s_add_u32 m0,s64,148480
MF a[24:27],a[136:139],v[20:23],a[24:27]
buffer_load_dwordx4 v12,s[40:43],s61 offen lds
MF a[28:31],a[140:143],v[20:23],a[28:31]
ds_read_b64_tr_b16 a[200:201],v80 offset:8704
MF a[32:35],a[128:131],v[24:27],a[32:35]
ds_read_b64_tr_b16 a[202:203],v80 offset:8960
MF a[36:39],a[132:135],v[24:27],a[36:39]
ds_read_b64_tr_b16 a[204:205],v81 offset:8704
s_add_u32 m0,s64,149504
MF a[40:43],a[136:139],v[24:27],a[40:43]
buffer_load_dwordx4 v13,s[40:43],s61 offen lds
MF a[44:47],a[140:143],v[24:27],a[44:47]
ds_read_b64_tr_b16 a[206:207],v81 offset:8960
MF a[48:51],a[128:131],v[28:31],a[48:51]
ds_read_b128 v[52:55],v67 offset:2048
MF a[52:55],a[132:135],v[28:31],a[52:55]
ds_read_b128 v[56:59],v67 offset:4096
s_add_u32 m0,s64,150528
MF a[56:59],a[136:139],v[28:31],a[56:59]
buffer_load_dwordx4 v14,s[40:43],s61 offen lds
MF a[60:63],a[140:143],v[28:31],a[60:63]
ds_read_b128 v[60:63],v67 offset:6144
s_add_u32 s62,s62,128
s_add_u32 s61,s61,114688
s_waitcnt vmcnt(16)
s_barrier
s_waitcnt lgkmcnt(15)
MF a[0:3],a[160:163],v[32:35],a[0:3]
ds_read_b64_tr_b16 a[128:129],v82
MF a[4:7],a[164:167],v[32:35],a[4:7]
ds_read_b64_tr_b16 a[130:131],v82 offset:256
s_add_u32 m0,s63,0
MF a[8:11],a[168:171],v[32:35],a[8:11]
buffer_load_dwordx4 v74,s[36:39],s62 offen lds
MF a[12:15],a[172:175],v[32:35],a[12:15]
ds_read_b128 v[16:19],v68
s_waitcnt lgkmcnt(15)
MF a[16:19],a[160:163],v[36:39],a[16:19]
ds_read_b64_tr_b16 a[132:133],v83
MF a[20:23],a[164:167],v[36:39],a[20:23]
ds_read_b64_tr_b16 a[134:135],v83 offset:256
s_add_u32 m0,s63,1024
MF a[24:27],a[168:171],v[36:39],a[24:27]
buffer_load_dwordx4 v75,s[36:39],s62 offen lds
MF a[28:31],a[172:175],v[36:39],a[28:31]
ds_read_b64_tr_b16 a[136:137],v82 offset:512
MF a[32:35],a[160:163],v[40:43],a[32:35]
ds_read_b64_tr_b16 a[138:139],v82 offset:768
MF a[36:39],a[164:167],v[40:43],a[36:39]
ds_read_b64_tr_b16 a[140:141],v83 offset:512
s_add_u32 m0,s63,2048
MF a[40:43],a[168:171],v[40:43],a[40:43]
buffer_load_dwordx4 v76,s[36:39],s62 offen lds
MF a[44:47],a[172:175],v[40:43],a[44:47]
ds_read_b64_tr_b16 a[142:143],v83 offset:768
MF a[48:51],a[160:163],v[44:47],a[48:51]
ds_read_b128 v[20:23],v68 offset:2048
MF a[52:55],a[164:167],v[44:47],a[52:55]
ds_read_b128 v[24:27],v68 offset:4096
s_add_u32 m0,s63,3072
MF a[56:59],a[168:171],v[44:47],a[56:59]
buffer_load_dwordx4 v77,s[36:39],s62 offen lds
MF a[60:63],a[172:175],v[44:47],a[60:63]
ds_read_b128 v[28:31],v68 offset:6144
s_waitcnt lgkmcnt(15)
MF a[0:3],a[192:195],v[48:51],a[0:3]
ds_read_b64_tr_b16 a[160:161],v82 offset:8192
MF a[4:7],a[196:199],v[48:51],a[4:7]
ds_read_b64_tr_b16 a[162:163],v82 offset:8448
s_add_u32 m0,s64,16384
MF a[8:11],a[200:203],v[48:51],a[8:11]
buffer_load_dwordx4 v11,s[40:43],s61 offen lds
MF a[12:15],a[204:207],v[48:51],a[12:15]
ds_read_b128 v[32:35],v69
s_waitcnt lgkmcnt(15)
MF a[16:19],a[192:195],v[52:55],a[16:19]
ds_read_b64_tr_b16 a[164:165],v83 offset:8192
MF a[20:23],a[196:199],v[52:55],a[20:23]
ds_read_b64_tr_b16 a[166:167],v83 offset:8448
s_add_u32 m0,s64,17408
MF a[24:27],a[200:203],v[52:55],a[24:27]
buffer_load_dwordx4 v12,s[40:43],s61 offen lds
MF a[28:31],a[204:207],v[52:55],a[28:31]
ds_read_b64_tr_b16 a[168:169],v82 offset:8704
MF a[32:35],a[192:195],v[56:59],a[32:35]
ds_read_b64_tr_b16 a[170:171],v82 offset:8960
MF a[36:39],a[196:199],v[56:59],a[36:39]
ds_read_b64_tr_b16 a[172:173],v83 offset:8704
s_add_u32 m0,s64,18432
MF a[40:43],a[200:203],v[56:59],a[40:43]
buffer_load_dwordx4 v13,s[40:43],s61 offen lds
MF a[44:47],a[204:207],v[56:59],a[44:47]
ds_read_b64_tr_b16 a[174:175],v83 offset:8960
MF a[48:51],a[192:195],v[60:63],a[48:51]
ds_read_b128 v[36:39],v69 offset:2048
MF a[52:55],a[196:199],v[60:63],a[52:55]
ds_read_b128 v[40:43],v69 offset:4096
s_add_u32 m0,s64,19456
MF a[56:59],a[200:203],v[60:63],a[56:59]
buffer_load_dwordx4 v14,s[40:43],s61 offen lds
MF a[60:63],a[204:207],v[60:63],a[60:63]
ds_read_b128 v[44:47],v69 offset:6144
s_add_u32 s62,s62,128
s_add_u32 s61,s61,114688
s_waitcnt vmcnt(16)
s_barrier
s_waitcnt lgkmcnt(15)
MF a[0:3],a[128:131],v[16:19],a[0:3]
ds_read_b64_tr_b16 a[192:193],v84
MF a[4:7],a[132:135],v[16:19],a[4:7]
ds_read_b64_tr_b16 a[194:195],v84 offset:256
MF a[8:11],a[136:139],v[16:19],a[8:11]
buffer_load_dwordx4 v[92:95],v10,s[44:47],s66 offen
MF a[12:15],a[140:143],v[16:19],a[12:15]
ds_read_b128 v[48:51],v70
s_waitcnt lgkmcnt(15)
MF a[16:19],a[128:131],v[20:23],a[16:19]
ds_read_b64_tr_b16 a[196:197],v85
MF a[20:23],a[132:135],v[20:23],a[20:23]
ds_read_b64_tr_b16 a[198:199],v85 offset:256
MF a[24:27],a[136:139],v[20:23],a[24:27]
buffer_load_dwordx4 v[96:99],v10,s[44:47],s66 offen offset:1024
MF a[28:31],a[140:143],v[20:23],a[28:31]
ds_read_b64_tr_b16 a[200:201],v84 offset:512
MF a[32:35],a[128:131],v[24:27],a[32:35]
ds_read_b64_tr_b16 a[202:203],v84 offset:768
MF a[36:39],a[132:135],v[24:27],a[36:39]
ds_read_b64_tr_b16 a[204:205],v85 offset:512
MF a[40:43],a[136:139],v[24:27],a[40:43]
buffer_load_dwordx4 v[100:103],v10,s[44:47],s67 offen
MF a[44:47],a[140:143],v[24:27],a[44:47]
ds_read_b64_tr_b16 a[206:207],v85 offset:768
MF a[48:51],a[128:131],v[28:31],a[48:51]
ds_read_b128 v[52:55],v70 offset:2048
MF a[52:55],a[132:135],v[28:31],a[52:55]
ds_read_b128 v[56:59],v70 offset:4096
MF a[56:59],a[136:139],v[28:31],a[56:59]
buffer_load_dwordx4 v[104:107],v10,s[44:47],s67 offen offset:1024
MF a[60:63],a[140:143],v[28:31],a[60:63]
ds_read_b128 v[60:63],v70 offset:6144
s_waitcnt lgkmcnt(15)
MF a[0:3],a[160:163],v[32:35],a[0:3]
ds_read_b64_tr_b16 a[128:129],v84 offset:8192
MF a[4:7],a[164:167],v[32:35],a[4:7]
ds_read_b64_tr_b16 a[130:131],v84 offset:8448
s_add_u32 m0,s68,32768
MF a[8:11],a[168:171],v[32:35],a[8:11]
buffer_load_dwordx4 v88,s[48:51],s65 offen lds
MF a[12:15],a[172:175],v[32:35],a[12:15]
ds_read_b128 v[16:19],v71
s_waitcnt lgkmcnt(15)
MF a[16:19],a[160:163],v[36:39],a[16:19]
ds_read_b64_tr_b16 a[132:133],v85 offset:8192
MF a[20:23],a[164:167],v[36:39],a[20:23]
ds_read_b64_tr_b16 a[134:135],v85 offset:8448
s_add_u32 m0,s68,33792
MF a[24:27],a[168:171],v[36:39],a[24:27]
buffer_load_dwordx4 v89,s[48:51],s65 offen lds
MF a[28:31],a[172:175],v[36:39],a[28:31]
ds_read_b64_tr_b16 a[136:137],v84 offset:8704
MF a[32:35],a[160:163],v[40:43],a[32:35]
ds_read_b64_tr_b16 a[138:139],v84 offset:8960
MF a[36:39],a[164:167],v[40:43],a[36:39]
ds_read_b64_tr_b16 a[140:141],v85 offset:8704
s_add_u32 m0,s68,34816
MF a[40:43],a[168:171],v[40:43],a[40:43]
buffer_load_dwordx4 v90,s[48:51],s65 offen lds
MF a[44:47],a[172:175],v[40:43],a[44:47]
ds_read_b64_tr_b16 a[142:143],v85 offset:8960
MF a[48:51],a[160:163],v[44:47],a[48:51]
ds_read_b128 v[20:23],v71 offset:2048
MF a[52:55],a[164:167],v[44:47],a[52:55]
ds_read_b128 v[24:27],v71 offset:4096
s_add_u32 m0,s68,35840
MF a[56:59],a[168:171],v[44:47],a[56:59]
buffer_load_dwordx4 v91,s[48:51],s65 offen lds
MF a[60:63],a[172:175],v[44:47],a[60:63]
ds_read_b128 v[28:31],v71 offset:6144
s_add_u32 s65,s65,81920
s_add_u32 s66,s66,2048
s_add_u32 s67,s67,2048
s_waitcnt vmcnt(12)
s_barrier
s_waitcnt lgkmcnt(15)
MF a[0:3],a[192:195],v[48:51],a[0:3]
ds_read_b64_tr_b16 a[160:161],v86
MF a[4:7],a[196:199],v[48:51],a[4:7]
ds_read_b64_tr_b16 a[162:163],v86 offset:256
MF a[8:11],a[200:203],v[48:51],a[8:11]
buffer_load_dwordx4 v[108:111],v10,s[44:47],s66 offen
MF a[12:15],a[204:207],v[48:51],a[12:15]
ds_read_b128 v[32:35],v72
s_waitcnt lgkmcnt(15)
MF a[16:19],a[192:195],v[52:55],a[16:19]
ds_read_b64_tr_b16 a[164:165],v87
MF a[20:23],a[196:199],v[52:55],a[20:23]
ds_read_b64_tr_b16 a[166:167],v87 offset:256
MF a[24:27],a[200:203],v[52:55],a[24:27]
buffer_load_dwordx4 v[112:115],v10,s[44:47],s66 offen offset:1024
MF a[28:31],a[204:207],v[52:55],a[28:31]
ds_read_b64_tr_b16 a[168:169],v86 offset:512
MF a[32:35],a[192:195],v[56:59],a[32:35]
ds_read_b64_tr_b16 a[170:171],v86 offset:768
MF a[36:39],a[196:199],v[56:59],a[36:39]
ds_read_b64_tr_b16 a[172:173],v87 offset:512
MF a[40:43],a[200:203],v[56:59],a[40:43]
buffer_load_dwordx4 v[116:119],v10,s[44:47],s67 offen
MF a[44:47],a[204:207],v[56:59],a[44:47]
ds_read_b64_tr_b16 a[174:175],v87 offset:768
MF a[48:51],a[192:195],v[60:63],a[48:51]
ds_read_b128 v[36:39],v72 offset:2048
MF a[52:55],a[196:199],v[60:63],a[52:55]
ds_read_b128 v[40:43],v72 offset:4096
MF a[56:59],a[200:203],v[60:63],a[56:59]
buffer_load_dwordx4 v[120:123],v10,s[44:47],s67 offen offset:1024
MF a[60:63],a[204:207],v[60:63],a[60:63]
ds_read_b128 v[44:47],v72 offset:6144
s_waitcnt lgkmcnt(15)
MF a[0:3],a[128:131],v[16:19],a[0:3]
ds_read_b64_tr_b16 a[192:193],v86 offset:8192
MF a[4:7],a[132:135],v[16:19],a[4:7]
ds_read_b64_tr_b16 a[194:195],v86 offset:8448
s_add_u32 m0,s68,65536
MF a[8:11],a[136:139],v[16:19],a[8:11]
buffer_load_dwordx4 v88,s[48:51],s65 offen lds
MF a[12:15],a[140:143],v[16:19],a[12:15]
ds_read_b128 v[48:51],v73
s_waitcnt lgkmcnt(15)
MF a[16:19],a[128:131],v[20:23],a[16:19]
ds_read_b64_tr_b16 a[196:197],v87 offset:8192
MF a[20:23],a[132:135],v[20:23],a[20:23]
ds_read_b64_tr_b16 a[198:199],v87 offset:8448
s_add_u32 m0,s68,66560
MF a[24:27],a[136:139],v[20:23],a[24:27]
buffer_load_dwordx4 v89,s[48:51],s65 offen lds
MF a[28:31],a[140:143],v[20:23],a[28:31]
ds_read_b64_tr_b16 a[200:201],v86 offset:8704
MF a[32:35],a[128:131],v[24:27],a[32:35]
ds_read_b64_tr_b16 a[202:203],v86 offset:8960
MF a[36:39],a[132:135],v[24:27],a[36:39]
ds_read_b64_tr_b16 a[204:205],v87 offset:8704
s_add_u32 m0,s68,67584
MF a[40:43],a[136:139],v[24:27],a[40:43]
buffer_load_dwordx4 v90,s[48:51],s65 offen lds
MF a[44:47],a[140:143],v[24:27],a[44:47]
ds_read_b64_tr_b16 a[206:207],v87 offset:8960
MF a[48:51],a[128:131],v[28:31],a[48:51]
ds_read_b128 v[52:55],v73 offset:2048
MF a[52:55],a[132:135],v[28:31],a[52:55]
ds_read_b128 v[56:59],v73 offset:4096
s_add_u32 m0,s68,68608
MF a[56:59],a[136:139],v[28:31],a[56:59]
buffer_load_dwordx4 v91,s[48:51],s65 offen lds
MF a[60:63],a[140:143],v[28:31],a[60:63]
ds_read_b128 v[60:63],v73 offset:6144
s_add_u32 s65,s65,81920
s_add_u32 s66,s66,2048
s_add_u32 s67,s67,2048
s_waitcnt vmcnt(8)
s_barrier
s_waitcnt lgkmcnt(15)
MF a[0:3],a[160:163],v[32:35],a[0:3]
ds_read_b64_tr_b16 a[128:129],v78
MF a[4:7],a[164:167],v[32:35],a[4:7]
ds_read_b64_tr_b16 a[130:131],v78 offset:256
MF a[8:11],a[168:171],v[32:35],a[8:11]
buffer_load_dwordx4 v[124:127],v10,s[44:47],s66 offen
MF a[12:15],a[172:175],v[32:35],a[12:15]
ds_read_b128 v[16:19],v64
s_waitcnt lgkmcnt(15)
MF a[16:19],a[160:163],v[36:39],a[16:19]
ds_read_b64_tr_b16 a[132:133],v79
MF a[20:23],a[164:167],v[36:39],a[20:23]
ds_read_b64_tr_b16 a[134:135],v79 offset:256
MF a[24:27],a[168:171],v[36:39],a[24:27]
buffer_load_dwordx4 v[128:131],v10,s[44:47],s66 offen offset:1024
MF a[28:31],a[172:175],v[36:39],a[28:31]
ds_read_b64_tr_b16 a[136:137],v78 offset:512
MF a[32:35],a[160:163],v[40:43],a[32:35]
ds_read_b64_tr_b16 a[138:139],v78 offset:768
MF a[36:39],a[164:167],v[40:43],a[36:39]
ds_read_b64_tr_b16 a[140:141],v79 offset:512
MF a[40:43],a[168:171],v[40:43],a[40:43]
buffer_load_dwordx4 v[132:135],v10,s[44:47],s67 offen
MF a[44:47],a[172:175],v[40:43],a[44:47]
ds_read_b64_tr_b16 a[142:143],v79 offset:768
MF a[48:51],a[160:163],v[44:47],a[48:51]
ds_read_b128 v[20:23],v64 offset:2048
MF a[52:55],a[164:167],v[44:47],a[52:55]
ds_read_b128 v[24:27],v64 offset:4096
MF a[56:59],a[168:171],v[44:47],a[56:59]
buffer_load_dwordx4 v[136:139],v10,s[44:47],s67 offen offset:1024
MF a[60:63],a[172:175],v[44:47],a[60:63]
ds_read_b128 v[28:31],v64 offset:6144
s_waitcnt lgkmcnt(15)
MF a[0:3],a[192:195],v[48:51],a[0:3]
ds_read_b64_tr_b16 a[160:161],v78 offset:8192
MF a[4:7],a[196:199],v[48:51],a[4:7]
ds_read_b64_tr_b16 a[162:163],v78 offset:8448
s_add_u32 m0,s68,98304
MF a[8:11],a[200:203],v[48:51],a[8:11]
buffer_load_dwordx4 v88,s[48:51],s65 offen lds
MF a[12:15],a[204:207],v[48:51],a[12:15]
ds_read_b128 v[32:35],v65
s_waitcnt lgkmcnt(15)
MF a[16:19],a[192:195],v[52:55],a[16:19]
ds_read_b64_tr_b16 a[164:165],v79 offset:8192
MF a[20:23],a[196:199],v[52:55],a[20:23]
ds_read_b64_tr_b16 a[166:167],v79 offset:8448
s_add_u32 m0,s68,99328
MF a[24:27],a[200:203],v[52:55],a[24:27]
buffer_load_dwordx4 v89,s[48:51],s65 offen lds
MF a[28:31],a[204:207],v[52:55],a[28:31]
ds_read_b64_tr_b16 a[168:169],v78 offset:8704
MF a[32:35],a[192:195],v[56:59],a[32:35]
ds_read_b64_tr_b16 a[170:171],v78 offset:8960
MF a[36:39],a[196:199],v[56:59],a[36:39]
ds_read_b64_tr_b16 a[172:173],v79 offset:8704
s_add_u32 m0,s68,100352
MF a[40:43],a[200:203],v[56:59],a[40:43]
buffer_load_dwordx4 v90,s[48:51],s65 offen lds
MF a[44:47],a[204:207],v[56:59],a[44:47]
ds_read_b64_tr_b16 a[174:175],v79 offset:8960
MF a[48:51],a[192:195],v[60:63],a[48:51]
ds_read_b128 v[36:39],v65 offset:2048
MF a[52:55],a[196:199],v[60:63],a[52:55]
ds_read_b128 v[40:43],v65 offset:4096
s_add_u32 m0,s68,101376
MF a[56:59],a[200:203],v[60:63],a[56:59]
buffer_load_dwordx4 v91,s[48:51],s65 offen lds
MF a[60:63],a[204:207],v[60:63],a[60:63]
ds_read_b128 v[44:47],v65 offset:6144
s_add_u32 s65,s65,81920
s_add_u32 s66,s66,2048
s_add_u32 s67,s67,2048
s_barrier
s_waitcnt lgkmcnt(15)
MF a[0:3],a[128:131],v[16:19],a[0:3]
buffer_load_dwordx4 v[140:143],v10,s[44:47],s66 offen
MF a[4:7],a[132:135],v[16:19],a[4:7]
MF a[8:11],a[136:139],v[16:19],a[8:11]
MF a[12:15],a[140:143],v[16:19],a[12:15]
s_waitcnt lgkmcnt(14)
MF a[16:19],a[128:131],v[20:23],a[16:19]
buffer_load_dwordx4 v[144:147],v10,s[44:47],s66 offen offset:1024
MF a[20:23],a[132:135],v[20:23],a[20:23]
MF a[24:27],a[136:139],v[20:23],a[24:27]
MF a[28:31],a[140:143],v[20:23],a[28:31]
s_waitcnt lgkmcnt(13)
MF a[32:35],a[128:131],v[24:27],a[32:35]
buffer_load_dwordx4 v[148:151],v10,s[44:47],s67 offen
MF a[36:39],a[132:135],v[24:27],a[36:39]
MF a[40:43],a[136:139],v[24:27],a[40:43]
MF a[44:47],a[140:143],v[24:27],a[44:47]
s_waitcnt lgkmcnt(12)
MF a[48:51],a[128:131],v[28:31],a[48:51]
buffer_load_dwordx4 v[152:155],v10,s[44:47],s67 offen offset:1024
MF a[52:55],a[132:135],v[28:31],a[52:55]
MF a[56:59],a[136:139],v[28:31],a[56:59]
MF a[60:63],a[140:143],v[28:31],a[60:63]
s_add_u32 m0,s68,131072
s_waitcnt lgkmcnt(9)
MF a[0:3],a[160:163],v[32:35],a[0:3]
buffer_load_dwordx4 v88,s[48:51],s65 offen lds
s_waitcnt lgkmcnt(7)
MF a[4:7],a[164:167],v[32:35],a[4:7]
s_waitcnt lgkmcnt(5)
MF a[8:11],a[168:171],v[32:35],a[8:11]
s_waitcnt lgkmcnt(3)
MF a[12:15],a[172:175],v[32:35],a[12:15]
s_add_u32 m0,s68,132096
s_waitcnt lgkmcnt(2)
MF a[16:19],a[160:163],v[36:39],a[16:19]
buffer_load_dwordx4 v89,s[48:51],s65 offen lds
MF a[20:23],a[164:167],v[36:39],a[20:23]
MF a[24:27],a[168:171],v[36:39],a[24:27]
MF a[28:31],a[172:175],v[36:39],a[28:31]
s_add_u32 m0,s68,133120
s_waitcnt lgkmcnt(1)
MF a[32:35],a[160:163],v[40:43],a[32:35]
buffer_load_dwordx4 v90,s[48:51],s65 offen lds
MF a[36:39],a[164:167],v[40:43],a[36:39]
MF a[40:43],a[168:171],v[40:43],a[40:43]
MF a[44:47],a[172:175],v[40:43],a[44:47]
s_add_u32 m0,s68,134144
s_waitcnt lgkmcnt(0)
MF a[48:51],a[160:163],v[44:47],a[48:51]
buffer_load_dwordx4 v91,s[48:51],s65 offen lds
MF a[52:55],a[164:167],v[44:47],a[52:55]
MF a[56:59],a[168:171],v[44:47],a[56:59]
MF a[60:63],a[172:175],v[44:47],a[60:63]
s_add_u32 s65,s65,81920
s_add_u32 s66,s66,2048
s_add_u32 s67,s67,2048
s_branch LOUT2_%=
LOUT_%=:
buffer_load_dwordx4 v[92:95],v10,s[44:47],s66 offen
buffer_load_dwordx4 v[96:99],v10,s[44:47],s66 offen offset:1024
buffer_load_dwordx4 v[100:103],v10,s[44:47],s67 offen
buffer_load_dwordx4 v[104:107],v10,s[44:47],s67 offen offset:1024
s_add_u32 s66,s66,2048
s_add_u32 s67,s67,2048
s_add_u32 m0,s68,32768
s_nop 0
buffer_load_dwordx4 v88,s[48:51],s65 offen lds
s_add_u32 m0,s68,33792
s_nop 0
buffer_load_dwordx4 v89,s[48:51],s65 offen lds
s_add_u32 m0,s68,34816
s_nop 0
buffer_load_dwordx4 v90,s[48:51],s65 offen lds
s_add_u32 m0,s68,35840
s_nop 0
buffer_load_dwordx4 v91,s[48:51],s65 offen lds
s_add_u32 s65,s65,81920
buffer_load_dwordx4 v[108:111],v10,s[44:47],s66 offen
buffer_load_dwordx4 v[112:115],v10,s[44:47],s66 offen offset:1024
buffer_load_dwordx4 v[116:119],v10,s[44:47],s67 offen
buffer_load_dwordx4 v[120:123],v10,s[44:47],s67 offen offset:1024
s_add_u32 s66,s66,2048
s_add_u32 s67,s67,2048
s_add_u32 m0,s68,65536
s_nop 0
buffer_load_dwordx4 v88,s[48:51],s65 offen lds
s_add_u32 m0,s68,66560
s_nop 0
buffer_load_dwordx4 v89,s[48:51],s65 offen lds
s_add_u32 m0,s68,67584
s_nop 0
buffer_load_dwordx4 v90,s[48:51],s65 offen lds
s_add_u32 m0,s68,68608
s_nop 0
buffer_load_dwordx4 v91,s[48:51],s65 offen lds
s_add_u32 s65,s65,81920
buffer_load_dwordx4 v[124:127],v10,s[44:47],s66 offen
buffer_load_dwordx4 v[128:131],v10,s[44:47],s66 offen offset:1024
buffer_load_dwordx4 v[132:135],v10,s[44:47],s67 offen
buffer_load_dwordx4 v[136:139],v10,s[44:47],s67 offen offset:1024
s_add_u32 s66,s66,2048
s_add_u32 s67,s67,2048
s_add_u32 m0,s68,98304
s_nop 0
buffer_load_dwordx4 v88,s[48:51],s65 offen lds
s_add_u32 m0,s68,99328
s_nop 0
buffer_load_dwordx4 v89,s[48:51],s65 offen lds
s_add_u32 m0,s68,100352
s_nop 0
buffer_load_dwordx4 v90,s[48:51],s65 offen lds
s_add_u32 m0,s68,101376
s_nop 0
buffer_load_dwordx4 v91,s[48:51],s65 offen lds
s_add_u32 s65,s65,81920
buffer_load_dwordx4 v[140:143],v10,s[44:47],s66 offen
buffer_load_dwordx4 v[144:147],v10,s[44:47],s66 offen offset:1024
buffer_load_dwordx4 v[148:151],v10,s[44:47],s67 offen
buffer_load_dwordx4 v[152:155],v10,s[44:47],s67 offen offset:1024
s_add_u32 s66,s66,2048
s_add_u32 s67,s67,2048
s_add_u32 m0,s68,131072
s_nop 0
buffer_load_dwordx4 v88,s[48:51],s65 offen lds
s_add_u32 m0,s68,132096
s_nop 0
buffer_load_dwordx4 v89,s[48:51],s65 offen lds
s_add_u32 m0,s68,133120
s_nop 0
buffer_load_dwordx4 v90,s[48:51],s65 offen lds
s_add_u32 m0,s68,134144
s_nop 0
buffer_load_dwordx4 v91,s[48:51],s65 offen lds
)A"
R"A(s_add_u32 s65,s65,81920
LOUT2_%=:
s_add_u32 s90,s87,159744
s_waitcnt vmcnt(12)
s_barrier
ds_read_b64_tr_b16 a[128:129],v172
ds_read_b64_tr_b16 a[130:131],v172 offset:256
ds_read_b64_tr_b16 a[132:133],v173
ds_read_b64_tr_b16 a[134:135],v173 offset:256
ds_read_b64_tr_b16 a[136:137],v172 offset:512
ds_read_b64_tr_b16 a[138:139],v172 offset:768
ds_read_b64_tr_b16 a[140:141],v173 offset:512
ds_read_b64_tr_b16 a[142:143],v173 offset:768
ds_read_b64_tr_b16 a[144:145],v172 offset:1024
ds_read_b64_tr_b16 a[146:147],v172 offset:1280
ds_read_b64_tr_b16 a[148:149],v173 offset:1024
ds_read_b64_tr_b16 a[150:151],v173 offset:1280
ds_read_b64_tr_b16 a[152:153],v172 offset:1536
ds_read_b64_tr_b16 a[154:155],v172 offset:1792
ds_read_b64_tr_b16 a[156:157],v173 offset:1536
ds_read_b64_tr_b16 a[158:159],v173 offset:1792
ds_read_b64_tr_b16 a[160:161],v172 offset:8192
ds_read_b64_tr_b16 a[162:163],v172 offset:8448
ds_read_b64_tr_b16 a[164:165],v173 offset:8192
ds_read_b64_tr_b16 a[166:167],v173 offset:8448
ds_read_b64_tr_b16 a[168:169],v172 offset:8704
ds_read_b64_tr_b16 a[170:171],v172 offset:8960
ds_read_b64_tr_b16 a[172:173],v173 offset:8704
ds_read_b64_tr_b16 a[174:175],v173 offset:8960
ds_read_b64_tr_b16 a[176:177],v172 offset:9216
ds_read_b64_tr_b16 a[178:179],v172 offset:9472
ds_read_b64_tr_b16 a[180:181],v173 offset:9216
ds_read_b64_tr_b16 a[182:183],v173 offset:9472
ds_read_b64_tr_b16 a[184:185],v172 offset:9728
ds_read_b64_tr_b16 a[186:187],v172 offset:9984
ds_read_b64_tr_b16 a[188:189],v173 offset:9728
ds_read_b64_tr_b16 a[190:191],v173 offset:9984
s_waitcnt vmcnt(8)
s_barrier
s_waitcnt lgkmcnt(15)
MF a[64:67],a[128:131],v[92:95],0
ds_read_b64_tr_b16 a[192:193],v174
ds_read_b64_tr_b16 a[194:195],v174 offset:256
AR v236,a0
MF a[96:99],a[128:131],v[100:103],0
buffer_load_dwordx4 v[156:159],v10,s[44:47],s66 offen
AR v237,a1
MF a[68:71],a[132:135],v[92:95],0
ds_read_b64_tr_b16 a[196:197],v175
ds_read_b64_tr_b16 a[198:199],v175 offset:256
AR v238,a2
MF a[100:103],a[132:135],v[100:103],0
buffer_load_dwordx4 v[160:163],v10,s[44:47],s66 offen offset:1024
AR v239,a3
MF a[72:75],a[136:139],v[92:95],0
ds_read_b64_tr_b16 a[200:201],v174 offset:512
AR v240,a4
AR v241,a5
MF a[104:107],a[136:139],v[100:103],0
ds_read_b64_tr_b16 a[202:203],v174 offset:768
ds_read_b64_tr_b16 a[204:205],v175 offset:512
AR v242,a6
MF a[76:79],a[140:143],v[92:95],0
buffer_load_dwordx4 v[164:167],v10,s[44:47],s67 offen
AR v243,a7
MF a[108:111],a[140:143],v[100:103],0
ds_read_b64_tr_b16 a[206:207],v175 offset:768
CP v200,v236,v237
CP v201,v238,v239
MF a[80:83],a[144:147],v[92:95],0
ds_read_b64_tr_b16 a[208:209],v174 offset:1024
ds_read_b64_tr_b16 a[210:211],v174 offset:1280
CP v202,v240,v241
MF a[112:115],a[144:147],v[100:103],0
buffer_load_dwordx4 v[168:171],v10,s[44:47],s67 offen offset:1024
CP v203,v242,v243
MF a[84:87],a[148:151],v[92:95],0
ds_read_b64_tr_b16 a[212:213],v175 offset:1024
ds_read_b64_tr_b16 a[214:215],v175 offset:1280
s_add_u32 m0,s68,0
MF a[116:119],a[148:151],v[100:103],0
buffer_load_dwordx4 v88,s[48:51],s65 offen lds
AR v236,a8
MF a[88:91],a[152:155],v[92:95],0
ds_read_b64_tr_b16 a[216:217],v174 offset:1536
AR v237,a9
AR v238,a10
MF a[120:123],a[152:155],v[100:103],0
ds_read_b64_tr_b16 a[218:219],v174 offset:1792
ds_read_b64_tr_b16 a[220:221],v175 offset:1536
s_add_u32 m0,s68,1024
MF a[92:95],a[156:159],v[92:95],0
buffer_load_dwordx4 v89,s[48:51],s65 offen lds
AR v239,a11
MF a[124:127],a[156:159],v[100:103],0
ds_read_b64_tr_b16 a[222:223],v175 offset:1792
AR v240,a12
AR v241,a13
AR v242,a14
AR v243,a15
CP v204,v236,v237
CP v205,v238,v239
CP v206,v240,v241
CP v207,v242,v243
AR v236,a16
AR v237,a17
AR v238,a18
AR v239,a19
AR v240,a20
AR v241,a21
AR v242,a22
AR v243,a23
CP v208,v236,v237
CP v209,v238,v239
CP v210,v240,v241
CP v211,v242,v243
AR v236,a24
AR v237,a25
AR v238,a26
AR v239,a27
AR v240,a28
AR v241,a29
AR v242,a30
AR v243,a31
CP v212,v236,v237
CP v213,v238,v239
CP v214,v240,v241
CP v215,v242,v243
s_waitcnt lgkmcnt(15)
MF a[64:67],a[160:163],v[96:99],a[64:67]
ds_read_b64_tr_b16 a[128:129],v174 offset:8192
ds_read_b64_tr_b16 a[130:131],v174 offset:8448
s_add_u32 m0,s68,2048
MF a[96:99],a[160:163],v[104:107],a[96:99]
buffer_load_dwordx4 v90,s[48:51],s65 offen lds
MF a[68:71],a[164:167],v[96:99],a[68:71]
ds_read_b64_tr_b16 a[132:133],v175 offset:8192
ds_read_b64_tr_b16 a[134:135],v175 offset:8448
s_add_u32 m0,s68,3072
MF a[100:103],a[164:167],v[104:107],a[100:103]
buffer_load_dwordx4 v91,s[48:51],s65 offen lds
MF a[72:75],a[168:171],v[96:99],a[72:75]
ds_read_b64_tr_b16 a[136:137],v174 offset:8704
MF a[104:107],a[168:171],v[104:107],a[104:107]
ds_read_b64_tr_b16 a[138:139],v174 offset:8960
ds_read_b64_tr_b16 a[140:141],v175 offset:8704
MF a[76:79],a[172:175],v[96:99],a[76:79]
buffer_store_dwordx4 v[200:203],v10,s[56:59],s85 offen sc1
MF a[108:111],a[172:175],v[104:107],a[108:111]
ds_read_b64_tr_b16 a[142:143],v175 offset:8960
MF a[80:83],a[176:179],v[96:99],a[80:83]
ds_read_b64_tr_b16 a[144:145],v174 offset:9216
ds_read_b64_tr_b16 a[146:147],v174 offset:9472
MF a[112:115],a[176:179],v[104:107],a[112:115]
buffer_store_dwordx4 v[204:207],v10,s[56:59],s85 offen offset:1024 sc1
MF a[84:87],a[180:183],v[96:99],a[84:87]
ds_read_b64_tr_b16 a[148:149],v175 offset:9216
ds_read_b64_tr_b16 a[150:151],v175 offset:9472
MF a[116:119],a[180:183],v[104:107],a[116:119]
buffer_store_dwordx4 v[208:211],v10,s[56:59],s85 offen offset:2048 sc1
MF a[88:91],a[184:187],v[96:99],a[88:91]
ds_read_b64_tr_b16 a[152:153],v174 offset:9728
MF a[120:123],a[184:187],v[104:107],a[120:123]
ds_read_b64_tr_b16 a[154:155],v174 offset:9984
ds_read_b64_tr_b16 a[156:157],v175 offset:9728
MF a[92:95],a[188:191],v[96:99],a[92:95]
buffer_store_dwordx4 v[212:215],v10,s[56:59],s85 offen offset:3072 sc1
MF a[124:127],a[188:191],v[104:107],a[124:127]
ds_read_b64_tr_b16 a[158:159],v175 offset:9984
s_add_u32 s65,s65,81920
s_add_u32 s66,s66,2048
s_add_u32 s67,s67,2048
s_waitcnt vmcnt(8)
s_barrier
s_waitcnt lgkmcnt(15)
MF a[64:67],a[192:195],v[108:111],a[64:67]
ds_read_b64_tr_b16 a[160:161],v176
ds_read_b64_tr_b16 a[162:163],v176 offset:256
MF a[96:99],a[192:195],v[116:119],a[96:99]
buffer_load_dwordx4 v[92:95],v10,s[44:47],s66 offen
MF a[68:71],a[196:199],v[108:111],a[68:71]
ds_read_b64_tr_b16 a[164:165],v177
MF a[100:103],a[196:199],v[116:119],a[100:103]
ds_read_b64_tr_b16 a[166:167],v177 offset:256
MF a[72:75],a[200:203],v[108:111],a[72:75]
ds_read_b64_tr_b16 a[168:169],v176 offset:512
ds_read_b64_tr_b16 a[170:171],v176 offset:768
MF a[104:107],a[200:203],v[116:119],a[104:107]
buffer_load_dwordx4 v[96:99],v10,s[44:47],s66 offen offset:1024
MF a[76:79],a[204:207],v[108:111],a[76:79]
ds_read_b64_tr_b16 a[172:173],v177 offset:512
MF a[108:111],a[204:207],v[116:119],a[108:111]
ds_read_b64_tr_b16 a[174:175],v177 offset:768
MF a[80:83],a[208:211],v[108:111],a[80:83]
ds_read_b64_tr_b16 a[176:177],v176 offset:1024
ds_read_b64_tr_b16 a[178:179],v176 offset:1280
MF a[112:115],a[208:211],v[116:119],a[112:115]
buffer_load_dwordx4 v[100:103],v10,s[44:47],s67 offen
MF a[84:87],a[212:215],v[108:111],a[84:87]
ds_read_b64_tr_b16 a[180:181],v177 offset:1024
MF a[116:119],a[212:215],v[116:119],a[116:119]
ds_read_b64_tr_b16 a[182:183],v177 offset:1280
MF a[88:91],a[216:219],v[108:111],a[88:91]
ds_read_b64_tr_b16 a[184:185],v176 offset:1536
ds_read_b64_tr_b16 a[186:187],v176 offset:1792
MF a[120:123],a[216:219],v[116:119],a[120:123]
buffer_load_dwordx4 v[104:107],v10,s[44:47],s67 offen offset:1024
MF a[92:95],a[220:223],v[108:111],a[92:95]
ds_read_b64_tr_b16 a[188:189],v177 offset:1536
MF a[124:127],a[220:223],v[116:119],a[124:127]
ds_read_b64_tr_b16 a[190:191],v177 offset:1792
s_waitcnt lgkmcnt(15)
MF a[64:67],a[128:131],v[112:115],a[64:67]
ds_read_b64_tr_b16 a[192:193],v176 offset:8192
ds_read_b64_tr_b16 a[194:195],v176 offset:8448
s_add_u32 m0,s68,32768
MF a[96:99],a[128:131],v[120:123],a[96:99]
buffer_load_dwordx4 v88,s[48:51],s65 offen lds
MF a[68:71],a[132:135],v[112:115],a[68:71]
ds_read_b64_tr_b16 a[196:197],v177 offset:8192
MF a[100:103],a[132:135],v[120:123],a[100:103]
ds_read_b64_tr_b16 a[198:199],v177 offset:8448
MF a[72:75],a[136:139],v[112:115],a[72:75]
ds_read_b64_tr_b16 a[200:201],v176 offset:8704
ds_read_b64_tr_b16 a[202:203],v176 offset:8960
s_add_u32 m0,s68,33792
MF a[104:107],a[136:139],v[120:123],a[104:107]
buffer_load_dwordx4 v89,s[48:51],s65 offen lds
MF a[76:79],a[140:143],v[112:115],a[76:79]
ds_read_b64_tr_b16 a[204:205],v177 offset:8704
MF a[108:111],a[140:143],v[120:123],a[108:111]
ds_read_b64_tr_b16 a[206:207],v177 offset:8960
MF a[80:83],a[144:147],v[112:115],a[80:83]
ds_read_b64_tr_b16 a[208:209],v176 offset:9216
ds_read_b64_tr_b16 a[210:211],v176 offset:9472
s_add_u32 m0,s68,34816
MF a[112:115],a[144:147],v[120:123],a[112:115]
buffer_load_dwordx4 v90,s[48:51],s65 offen lds
MF a[84:87],a[148:151],v[112:115],a[84:87]
ds_read_b64_tr_b16 a[212:213],v177 offset:9216
MF a[116:119],a[148:151],v[120:123],a[116:119]
ds_read_b64_tr_b16 a[214:215],v177 offset:9472
MF a[88:91],a[152:155],v[112:115],a[88:91]
ds_read_b64_tr_b16 a[216:217],v176 offset:9728
ds_read_b64_tr_b16 a[218:219],v176 offset:9984
s_add_u32 m0,s68,35840
MF a[120:123],a[152:155],v[120:123],a[120:123]
buffer_load_dwordx4 v91,s[48:51],s65 offen lds
MF a[92:95],a[156:159],v[112:115],a[92:95]
ds_read_b64_tr_b16 a[220:221],v177 offset:9728
MF a[124:127],a[156:159],v[120:123],a[124:127]
ds_read_b64_tr_b16 a[222:223],v177 offset:9984
s_add_u32 s65,s65,81920
s_add_u32 s66,s66,2048
s_add_u32 s67,s67,2048
s_waitcnt vmcnt(8)
s_barrier
s_waitcnt lgkmcnt(15)
MF a[64:67],a[160:163],v[124:127],a[64:67]
ds_read_b64_tr_b16 a[128:129],v178
ds_read_b64_tr_b16 a[130:131],v178 offset:256
MF a[96:99],a[160:163],v[132:135],a[96:99]
buffer_load_dwordx4 v[108:111],v10,s[44:47],s66 offen
MF a[68:71],a[164:167],v[124:127],a[68:71]
ds_read_b64_tr_b16 a[132:133],v179
MF a[100:103],a[164:167],v[132:135],a[100:103]
ds_read_b64_tr_b16 a[134:135],v179 offset:256
MF a[72:75],a[168:171],v[124:127],a[72:75]
ds_read_b64_tr_b16 a[136:137],v178 offset:512
ds_read_b64_tr_b16 a[138:139],v178 offset:768
MF a[104:107],a[168:171],v[132:135],a[104:107]
buffer_load_dwordx4 v[112:115],v10,s[44:47],s66 offen offset:1024
MF a[76:79],a[172:175],v[124:127],a[76:79]
ds_read_b64_tr_b16 a[140:141],v179 offset:512
MF a[108:111],a[172:175],v[132:135],a[108:111]
ds_read_b64_tr_b16 a[142:143],v179 offset:768
MF a[80:83],a[176:179],v[124:127],a[80:83]
ds_read_b64_tr_b16 a[144:145],v178 offset:1024
ds_read_b64_tr_b16 a[146:147],v178 offset:1280
MF a[112:115],a[176:179],v[132:135],a[112:115]
buffer_load_dwordx4 v[116:119],v10,s[44:47],s67 offen
MF a[84:87],a[180:183],v[124:127],a[84:87]
ds_read_b64_tr_b16 a[148:149],v179 offset:1024
MF a[116:119],a[180:183],v[132:135],a[116:119]
ds_read_b64_tr_b16 a[150:151],v179 offset:1280
MF a[88:91],a[184:187],v[124:127],a[88:91]
ds_read_b64_tr_b16 a[152:153],v178 offset:1536
ds_read_b64_tr_b16 a[154:155],v178 offset:1792
MF a[120:123],a[184:187],v[132:135],a[120:123]
buffer_load_dwordx4 v[120:123],v10,s[44:47],s67 offen offset:1024
MF a[92:95],a[188:191],v[124:127],a[92:95]
ds_read_b64_tr_b16 a[156:157],v179 offset:1536
MF a[124:127],a[188:191],v[132:135],a[124:127]
ds_read_b64_tr_b16 a[158:159],v179 offset:1792
s_waitcnt lgkmcnt(15)
MF a[64:67],a[192:195],v[128:131],a[64:67]
ds_read_b64_tr_b16 a[160:161],v178 offset:8192
ds_read_b64_tr_b16 a[162:163],v178 offset:8448
s_add_u32 m0,s68,65536
MF a[96:99],a[192:195],v[136:139],a[96:99]
buffer_load_dwordx4 v88,s[48:51],s65 offen lds
MF a[68:71],a[196:199],v[128:131],a[68:71]
ds_read_b64_tr_b16 a[164:165],v179 offset:8192
MF a[100:103],a[196:199],v[136:139],a[100:103]
ds_read_b64_tr_b16 a[166:167],v179 offset:8448
MF a[72:75],a[200:203],v[128:131],a[72:75]
ds_read_b64_tr_b16 a[168:169],v178 offset:8704
ds_read_b64_tr_b16 a[170:171],v178 offset:8960
s_add_u32 m0,s68,66560
MF a[104:107],a[200:203],v[136:139],a[104:107]
buffer_load_dwordx4 v89,s[48:51],s65 offen lds
MF a[76:79],a[204:207],v[128:131],a[76:79]
ds_read_b64_tr_b16 a[172:173],v179 offset:8704
MF a[108:111],a[204:207],v[136:139],a[108:111]
ds_read_b64_tr_b16 a[174:175],v179 offset:8960
MF a[80:83],a[208:211],v[128:131],a[80:83]
ds_read_b64_tr_b16 a[176:177],v178 offset:9216
ds_read_b64_tr_b16 a[178:179],v178 offset:9472
s_add_u32 m0,s68,67584
MF a[112:115],a[208:211],v[136:139],a[112:115]
buffer_load_dwordx4 v90,s[48:51],s65 offen lds
MF a[84:87],a[212:215],v[128:131],a[84:87]
ds_read_b64_tr_b16 a[180:181],v179 offset:9216
MF a[116:119],a[212:215],v[136:139],a[116:119]
ds_read_b64_tr_b16 a[182:183],v179 offset:9472
MF a[88:91],a[216:219],v[128:131],a[88:91]
ds_read_b64_tr_b16 a[184:185],v178 offset:9728
ds_read_b64_tr_b16 a[186:187],v178 offset:9984
s_add_u32 m0,s68,68608
MF a[120:123],a[216:219],v[136:139],a[120:123]
buffer_load_dwordx4 v91,s[48:51],s65 offen lds
MF a[92:95],a[220:223],v[128:131],a[92:95]
ds_read_b64_tr_b16 a[188:189],v179 offset:9728
MF a[124:127],a[220:223],v[136:139],a[124:127]
ds_read_b64_tr_b16 a[190:191],v179 offset:9984
s_add_u32 s65,s65,81920
s_add_u32 s66,s66,2048
s_add_u32 s67,s67,2048
s_waitcnt vmcnt(0)
s_barrier
s_cmp_eq_u32 s80,1
s_cbranch_scc1 LSA_%=
s_cmp_lg_u32 s60,0
s_cbranch_scc1 LSA_%=
s_mul_i32 s100,s72,7
s_add_u32 s100,s100,s73
s_lshl_b32 s100,s100,6
s_add_u32 s100,s100,8192
v_mov_b32 v244,s100
v_mov_b32 v245,1
s_mov_b64 exec,1
global_atomic_add v244,v245,s[24:25]
s_mov_b64 exec,-1
LSA_%=:
s_waitcnt lgkmcnt(15)
MF a[64:67],a[128:131],v[140:143],a[64:67]
ds_read_b64_tr_b16 a[192:193],v180
ds_read_b64_tr_b16 a[194:195],v180 offset:256
MF a[96:99],a[128:131],v[148:151],a[96:99]
buffer_load_dwordx4 v[124:127],v10,s[44:47],s66 offen
MF a[68:71],a[132:135],v[140:143],a[68:71]
ds_read_b64_tr_b16 a[196:197],v181
MF a[100:103],a[132:135],v[148:151],a[100:103]
ds_read_b64_tr_b16 a[198:199],v181 offset:256
MF a[72:75],a[136:139],v[140:143],a[72:75]
ds_read_b64_tr_b16 a[200:201],v180 offset:512
ds_read_b64_tr_b16 a[202:203],v180 offset:768
MF a[104:107],a[136:139],v[148:151],a[104:107]
buffer_load_dwordx4 v[128:131],v10,s[44:47],s66 offen offset:1024
MF a[76:79],a[140:143],v[140:143],a[76:79]
ds_read_b64_tr_b16 a[204:205],v181 offset:512
MF a[108:111],a[140:143],v[148:151],a[108:111]
ds_read_b64_tr_b16 a[206:207],v181 offset:768
MF a[80:83],a[144:147],v[140:143],a[80:83]
ds_read_b64_tr_b16 a[208:209],v180 offset:1024
ds_read_b64_tr_b16 a[210:211],v180 offset:1280
MF a[112:115],a[144:147],v[148:151],a[112:115]
buffer_load_dwordx4 v[132:135],v10,s[44:47],s67 offen
MF a[84:87],a[148:151],v[140:143],a[84:87]
ds_read_b64_tr_b16 a[212:213],v181 offset:1024
MF a[116:119],a[148:151],v[148:151],a[116:119]
ds_read_b64_tr_b16 a[214:215],v181 offset:1280
MF a[88:91],a[152:155],v[140:143],a[88:91]
ds_read_b64_tr_b16 a[216:217],v180 offset:1536
ds_read_b64_tr_b16 a[218:219],v180 offset:1792
MF a[120:123],a[152:155],v[148:151],a[120:123]
buffer_load_dwordx4 v[136:139],v10,s[44:47],s67 offen offset:1024
MF a[92:95],a[156:159],v[140:143],a[92:95]
ds_read_b64_tr_b16 a[220:221],v181 offset:1536
MF a[124:127],a[156:159],v[148:151],a[124:127]
ds_read_b64_tr_b16 a[222:223],v181 offset:1792
s_waitcnt lgkmcnt(15)
MF a[64:67],a[160:163],v[144:147],a[64:67]
ds_read_b64_tr_b16 a[128:129],v180 offset:8192
ds_read_b64_tr_b16 a[130:131],v180 offset:8448
s_add_u32 m0,s68,98304
MF a[96:99],a[160:163],v[152:155],a[96:99]
buffer_load_dwordx4 v88,s[48:51],s65 offen lds
MF a[68:71],a[164:167],v[144:147],a[68:71]
ds_read_b64_tr_b16 a[132:133],v181 offset:8192
MF a[100:103],a[164:167],v[152:155],a[100:103]
ds_read_b64_tr_b16 a[134:135],v181 offset:8448
MF a[72:75],a[168:171],v[144:147],a[72:75]
ds_read_b64_tr_b16 a[136:137],v180 offset:8704
ds_read_b64_tr_b16 a[138:139],v180 offset:8960
s_add_u32 m0,s68,99328
MF a[104:107],a[168:171],v[152:155],a[104:107]
buffer_load_dwordx4 v89,s[48:51],s65 offen lds
MF a[76:79],a[172:175],v[144:147],a[76:79]
ds_read_b64_tr_b16 a[140:141],v181 offset:8704
MF a[108:111],a[172:175],v[152:155],a[108:111]
ds_read_b64_tr_b16 a[142:143],v181 offset:8960
MF a[80:83],a[176:179],v[144:147],a[80:83]
ds_read_b64_tr_b16 a[144:145],v180 offset:9216
ds_read_b64_tr_b16 a[146:147],v180 offset:9472
s_add_u32 m0,s68,100352
MF a[112:115],a[176:179],v[152:155],a[112:115]
buffer_load_dwordx4 v90,s[48:51],s65 offen lds
MF a[84:87],a[180:183],v[144:147],a[84:87]
ds_read_b64_tr_b16 a[148:149],v181 offset:9216
MF a[116:119],a[180:183],v[152:155],a[116:119]
ds_read_b64_tr_b16 a[150:151],v181 offset:9472
MF a[88:91],a[184:187],v[144:147],a[88:91]
ds_read_b64_tr_b16 a[152:153],v180 offset:9728
ds_read_b64_tr_b16 a[154:155],v180 offset:9984
s_add_u32 m0,s68,101376
MF a[120:123],a[184:187],v[152:155],a[120:123]
buffer_load_dwordx4 v91,s[48:51],s65 offen lds
MF a[92:95],a[188:191],v[144:147],a[92:95]
ds_read_b64_tr_b16 a[156:157],v181 offset:9728
MF a[124:127],a[188:191],v[152:155],a[124:127]
ds_read_b64_tr_b16 a[158:159],v181 offset:9984
s_add_u32 s65,s65,81920
s_add_u32 s66,s66,2048
s_add_u32 s67,s67,2048
s_barrier
s_waitcnt lgkmcnt(15)
MF a[64:67],a[192:195],v[156:159],a[64:67]
ds_read_b64_tr_b16 a[160:161],v172
ds_read_b64_tr_b16 a[162:163],v172 offset:256
MF a[96:99],a[192:195],v[164:167],a[96:99]
buffer_load_dwordx4 v[140:143],v10,s[44:47],s66 offen
MF a[68:71],a[196:199],v[156:159],a[68:71]
ds_read_b64_tr_b16 a[164:165],v173
MF a[100:103],a[196:199],v[164:167],a[100:103]
ds_read_b64_tr_b16 a[166:167],v173 offset:256
MF a[72:75],a[200:203],v[156:159],a[72:75]
ds_read_b64_tr_b16 a[168:169],v172 offset:512
ds_read_b64_tr_b16 a[170:171],v172 offset:768
MF a[104:107],a[200:203],v[164:167],a[104:107]
buffer_load_dwordx4 v[144:147],v10,s[44:47],s66 offen offset:1024
MF a[76:79],a[204:207],v[156:159],a[76:79]
ds_read_b64_tr_b16 a[172:173],v173 offset:512
MF a[108:111],a[204:207],v[164:167],a[108:111]
ds_read_b64_tr_b16 a[174:175],v173 offset:768
MF a[80:83],a[208:211],v[156:159],a[80:83]
ds_read_b64_tr_b16 a[176:177],v172 offset:1024
ds_read_b64_tr_b16 a[178:179],v172 offset:1280
MF a[112:115],a[208:211],v[164:167],a[112:115]
buffer_load_dwordx4 v[148:151],v10,s[44:47],s67 offen
MF a[84:87],a[212:215],v[156:159],a[84:87]
ds_read_b64_tr_b16 a[180:181],v173 offset:1024
MF a[116:119],a[212:215],v[164:167],a[116:119]
ds_read_b64_tr_b16 a[182:183],v173 offset:1280
MF a[88:91],a[216:219],v[156:159],a[88:91]
ds_read_b64_tr_b16 a[184:185],v172 offset:1536
ds_read_b64_tr_b16 a[186:187],v172 offset:1792
MF a[120:123],a[216:219],v[164:167],a[120:123]
buffer_load_dwordx4 v[152:155],v10,s[44:47],s67 offen offset:1024
MF a[92:95],a[220:223],v[156:159],a[92:95]
ds_read_b64_tr_b16 a[188:189],v173 offset:1536
MF a[124:127],a[220:223],v[164:167],a[124:127]
ds_read_b64_tr_b16 a[190:191],v173 offset:1792
s_waitcnt lgkmcnt(15)
MF a[64:67],a[128:131],v[160:163],a[64:67]
ds_read_b64_tr_b16 a[192:193],v172 offset:8192
ds_read_b64_tr_b16 a[194:195],v172 offset:8448
s_add_u32 m0,s68,131072
MF a[96:99],a[128:131],v[168:171],a[96:99]
buffer_load_dwordx4 v88,s[48:51],s65 offen lds
MF a[68:71],a[132:135],v[160:163],a[68:71]
ds_read_b64_tr_b16 a[196:197],v173 offset:8192
MF a[100:103],a[132:135],v[168:171],a[100:103]
ds_read_b64_tr_b16 a[198:199],v173 offset:8448
MF a[72:75],a[136:139],v[160:163],a[72:75]
ds_read_b64_tr_b16 a[200:201],v172 offset:8704
ds_read_b64_tr_b16 a[202:203],v172 offset:8960
s_add_u32 m0,s68,132096
MF a[104:107],a[136:139],v[168:171],a[104:107]
buffer_load_dwordx4 v89,s[48:51],s65 offen lds
MF a[76:79],a[140:143],v[160:163],a[76:79]
ds_read_b64_tr_b16 a[204:205],v173 offset:8704
MF a[108:111],a[140:143],v[168:171],a[108:111]
ds_read_b64_tr_b16 a[206:207],v173 offset:8960
MF a[80:83],a[144:147],v[160:163],a[80:83]
ds_read_b64_tr_b16 a[208:209],v172 offset:9216
ds_read_b64_tr_b16 a[210:211],v172 offset:9472
s_add_u32 m0,s68,133120
MF a[112:115],a[144:147],v[168:171],a[112:115]
buffer_load_dwordx4 v90,s[48:51],s65 offen lds
MF a[84:87],a[148:151],v[160:163],a[84:87]
ds_read_b64_tr_b16 a[212:213],v173 offset:9216
MF a[116:119],a[148:151],v[168:171],a[116:119]
ds_read_b64_tr_b16 a[214:215],v173 offset:9472
MF a[88:91],a[152:155],v[160:163],a[88:91]
ds_read_b64_tr_b16 a[216:217],v172 offset:9728
ds_read_b64_tr_b16 a[218:219],v172 offset:9984
s_add_u32 m0,s68,134144
MF a[120:123],a[152:155],v[168:171],a[120:123]
buffer_load_dwordx4 v91,s[48:51],s65 offen lds
MF a[92:95],a[156:159],v[160:163],a[92:95]
ds_read_b64_tr_b16 a[220:221],v173 offset:9728
MF a[124:127],a[156:159],v[168:171],a[124:127]
ds_read_b64_tr_b16 a[222:223],v173 offset:9984
s_add_u32 s65,s65,81920
s_add_u32 s66,s66,2048
s_add_u32 s67,s67,2048
s_barrier
s_waitcnt lgkmcnt(15)
MF a[64:67],a[160:163],v[92:95],a[64:67]
ds_read_b64_tr_b16 a[128:129],v174
ds_read_b64_tr_b16 a[130:131],v174 offset:256
MF a[96:99],a[160:163],v[100:103],a[96:99]
buffer_load_dwordx4 v[156:159],v10,s[44:47],s66 offen
MF a[68:71],a[164:167],v[92:95],a[68:71]
ds_read_b64_tr_b16 a[132:133],v175
MF a[100:103],a[164:167],v[100:103],a[100:103]
ds_read_b64_tr_b16 a[134:135],v175 offset:256
MF a[72:75],a[168:171],v[92:95],a[72:75]
ds_read_b64_tr_b16 a[136:137],v174 offset:512
ds_read_b64_tr_b16 a[138:139],v174 offset:768
MF a[104:107],a[168:171],v[100:103],a[104:107]
buffer_load_dwordx4 v[160:163],v10,s[44:47],s66 offen offset:1024
MF a[76:79],a[172:175],v[92:95],a[76:79]
ds_read_b64_tr_b16 a[140:141],v175 offset:512
MF a[108:111],a[172:175],v[100:103],a[108:111]
ds_read_b64_tr_b16 a[142:143],v175 offset:768
MF a[80:83],a[176:179],v[92:95],a[80:83]
ds_read_b64_tr_b16 a[144:145],v174 offset:1024
ds_read_b64_tr_b16 a[146:147],v174 offset:1280
MF a[112:115],a[176:179],v[100:103],a[112:115]
buffer_load_dwordx4 v[164:167],v10,s[44:47],s67 offen
MF a[84:87],a[180:183],v[92:95],a[84:87]
ds_read_b64_tr_b16 a[148:149],v175 offset:1024
MF a[116:119],a[180:183],v[100:103],a[116:119]
ds_read_b64_tr_b16 a[150:151],v175 offset:1280
MF a[88:91],a[184:187],v[92:95],a[88:91]
ds_read_b64_tr_b16 a[152:153],v174 offset:1536
ds_read_b64_tr_b16 a[154:155],v174 offset:1792
MF a[120:123],a[184:187],v[100:103],a[120:123]
buffer_load_dwordx4 v[168:171],v10,s[44:47],s67 offen offset:1024
MF a[92:95],a[188:191],v[92:95],a[92:95]
ds_read_b64_tr_b16 a[156:157],v175 offset:1536
MF a[124:127],a[188:191],v[100:103],a[124:127]
ds_read_b64_tr_b16 a[158:159],v175 offset:1792
s_waitcnt lgkmcnt(15)
MF a[64:67],a[192:195],v[96:99],a[64:67]
ds_read_b64_tr_b16 a[160:161],v174 offset:8192
ds_read_b64_tr_b16 a[162:163],v174 offset:8448
s_add_u32 m0,s68,0
MF a[96:99],a[192:195],v[104:107],a[96:99]
buffer_load_dwordx4 v88,s[48:51],s65 offen lds
MF a[68:71],a[196:199],v[96:99],a[68:71]
ds_read_b64_tr_b16 a[164:165],v175 offset:8192
MF a[100:103],a[196:199],v[104:107],a[100:103]
ds_read_b64_tr_b16 a[166:167],v175 offset:8448
MF a[72:75],a[200:203],v[96:99],a[72:75]
ds_read_b64_tr_b16 a[168:169],v174 offset:8704
ds_read_b64_tr_b16 a[170:171],v174 offset:8960
s_add_u32 m0,s68,1024
MF a[104:107],a[200:203],v[104:107],a[104:107]
buffer_load_dwordx4 v89,s[48:51],s65 offen lds
MF a[76:79],a[204:207],v[96:99],a[76:79]
ds_read_b64_tr_b16 a[172:173],v175 offset:8704
MF a[108:111],a[204:207],v[104:107],a[108:111]
ds_read_b64_tr_b16 a[174:175],v175 offset:8960
MF a[80:83],a[208:211],v[96:99],a[80:83]
ds_read_b64_tr_b16 a[176:177],v174 offset:9216
ds_read_b64_tr_b16 a[178:179],v174 offset:9472
s_add_u32 m0,s68,2048
MF a[112:115],a[208:211],v[104:107],a[112:115]
buffer_load_dwordx4 v90,s[48:51],s65 offen lds
MF a[84:87],a[212:215],v[96:99],a[84:87]
ds_read_b64_tr_b16 a[180:181],v175 offset:9216
MF a[116:119],a[212:215],v[104:107],a[116:119]
ds_read_b64_tr_b16 a[182:183],v175 offset:9472
MF a[88:91],a[216:219],v[96:99],a[88:91]
ds_read_b64_tr_b16 a[184:185],v174 offset:9728
ds_read_b64_tr_b16 a[186:187],v174 offset:9984
s_add_u32 m0,s68,3072
MF a[120:123],a[216:219],v[104:107],a[120:123]
buffer_load_dwordx4 v91,s[48:51],s65 offen lds
MF a[92:95],a[220:223],v[96:99],a[92:95]
ds_read_b64_tr_b16 a[188:189],v175 offset:9728
MF a[124:127],a[220:223],v[104:107],a[124:127]
ds_read_b64_tr_b16 a[190:191],v175 offset:9984
s_add_u32 s65,s65,81920
s_add_u32 s66,s66,2048
s_add_u32 s67,s67,2048
s_mul_i32 s100,s72,7
s_add_u32 s100,s100,s73
s_lshl_b32 s100,s100,6
s_add_u32 s100,s100,8192
v_mov_b32 v247,s100
s_waitcnt vmcnt(8)
s_barrier
s_waitcnt lgkmcnt(15)
MF a[64:67],a[128:131],v[108:111],a[64:67]
ds_read_b64_tr_b16 a[192:193],v176
ds_read_b64_tr_b16 a[194:195],v176 offset:256
MF a[96:99],a[128:131],v[116:119],a[96:99]
global_load_dword v246,v247,s[24:25] sc1
MF a[68:71],a[132:135],v[108:111],a[68:71]
ds_read_b64_tr_b16 a[196:197],v177
MF a[100:103],a[132:135],v[116:119],a[100:103]
ds_read_b64_tr_b16 a[198:199],v177 offset:256
ds_read_b64_tr_b16 a[200:201],v176 offset:512
MF a[72:75],a[136:139],v[108:111],a[72:75]
buffer_load_dwordx4 v[92:95],v10,s[44:47],s66 offen
MF a[104:107],a[136:139],v[116:119],a[104:107]
ds_read_b64_tr_b16 a[202:203],v176 offset:768
MF a[76:79],a[140:143],v[108:111],a[76:79]
ds_read_b64_tr_b16 a[204:205],v177 offset:512
ds_read_b64_tr_b16 a[206:207],v177 offset:768
MF a[108:111],a[140:143],v[116:119],a[108:111]
buffer_load_dwordx4 v[96:99],v10,s[44:47],s66 offen offset:1024
MF a[80:83],a[144:147],v[108:111],a[80:83]
ds_read_b64_tr_b16 a[208:209],v176 offset:1024
MF a[112:115],a[144:147],v[116:119],a[112:115]
ds_read_b64_tr_b16 a[210:211],v176 offset:1280
ds_read_b64_tr_b16 a[212:213],v177 offset:1024
MF a[84:87],a[148:151],v[108:111],a[84:87]
ds_read_b64_tr_b16 a[214:215],v177 offset:1280
MF a[116:119],a[148:151],v[116:119],a[116:119]
buffer_load_dwordx4 v[100:103],v10,s[44:47],s67 offen
MF a[88:91],a[152:155],v[108:111],a[88:91]
ds_read_b64_tr_b16 a[216:217],v176 offset:1536
ds_read_b64_tr_b16 a[218:219],v176 offset:1792
MF a[120:123],a[152:155],v[116:119],a[120:123]
ds_read_b64_tr_b16 a[220:221],v177 offset:1536
MF a[92:95],a[156:159],v[108:111],a[92:95]
buffer_load_dwordx4 v[104:107],v10,s[44:47],s67 offen offset:1024
MF a[124:127],a[156:159],v[116:119],a[124:127]
ds_read_b64_tr_b16 a[222:223],v177 offset:1792
s_waitcnt lgkmcnt(15)
MF a[64:67],a[160:163],v[112:115],a[64:67]
ds_read_b64_tr_b16 a[128:129],v176 offset:8192
ds_read_b64_tr_b16 a[130:131],v176 offset:8448
s_add_u32 m0,s68,32768
MF a[96:99],a[160:163],v[120:123],a[96:99]
buffer_load_dwordx4 v88,s[48:51],s65 offen lds
MF a[68:71],a[164:167],v[112:115],a[68:71]
ds_read_b64_tr_b16 a[132:133],v177 offset:8192
MF a[100:103],a[164:167],v[120:123],a[100:103]
ds_read_b64_tr_b16 a[134:135],v177 offset:8448
MF a[72:75],a[168:171],v[112:115],a[72:75]
ds_read_b64_tr_b16 a[136:137],v176 offset:8704
ds_read_b64_tr_b16 a[138:139],v176 offset:8960
s_add_u32 m0,s68,33792
MF a[104:107],a[168:171],v[120:123],a[104:107]
buffer_load_dwordx4 v89,s[48:51],s65 offen lds
MF a[76:79],a[172:175],v[112:115],a[76:79]
ds_read_b64_tr_b16 a[140:141],v177 offset:8704
MF a[108:111],a[172:175],v[120:123],a[108:111]
ds_read_b64_tr_b16 a[142:143],v177 offset:8960
MF a[80:83],a[176:179],v[112:115],a[80:83]
ds_read_b64_tr_b16 a[144:145],v176 offset:9216
ds_read_b64_tr_b16 a[146:147],v176 offset:9472
s_add_u32 m0,s68,34816
MF a[112:115],a[176:179],v[120:123],a[112:115]
buffer_load_dwordx4 v90,s[48:51],s65 offen lds
MF a[84:87],a[180:183],v[112:115],a[84:87]
ds_read_b64_tr_b16 a[148:149],v177 offset:9216
MF a[116:119],a[180:183],v[120:123],a[116:119]
ds_read_b64_tr_b16 a[150:151],v177 offset:9472
MF a[88:91],a[184:187],v[112:115],a[88:91]
ds_read_b64_tr_b16 a[152:153],v176 offset:9728
ds_read_b64_tr_b16 a[154:155],v176 offset:9984
s_add_u32 m0,s68,35840
MF a[120:123],a[184:187],v[120:123],a[120:123]
buffer_load_dwordx4 v91,s[48:51],s65 offen lds
MF a[92:95],a[188:191],v[112:115],a[92:95]
ds_read_b64_tr_b16 a[156:157],v177 offset:9728
MF a[124:127],a[188:191],v[120:123],a[124:127]
ds_read_b64_tr_b16 a[158:159],v177 offset:9984
s_add_u32 s65,s65,81920
s_add_u32 s66,s66,2048
s_add_u32 s67,s67,2048
s_waitcnt vmcnt(8)
s_barrier
s_waitcnt lgkmcnt(15)
MF a[64:67],a[192:195],v[124:127],a[64:67]
ds_read_b64_tr_b16 a[160:161],v178
ds_read_b64_tr_b16 a[162:163],v178 offset:256
MF a[96:99],a[192:195],v[132:135],a[96:99]
buffer_load_dwordx4 v[108:111],v10,s[44:47],s66 offen
MF a[68:71],a[196:199],v[124:127],a[68:71]
ds_read_b64_tr_b16 a[164:165],v179
MF a[100:103],a[196:199],v[132:135],a[100:103]
ds_read_b64_tr_b16 a[166:167],v179 offset:256
MF a[72:75],a[200:203],v[124:127],a[72:75]
ds_read_b64_tr_b16 a[168:169],v178 offset:512
ds_read_b64_tr_b16 a[170:171],v178 offset:768
MF a[104:107],a[200:203],v[132:135],a[104:107]
buffer_load_dwordx4 v[112:115],v10,s[44:47],s66 offen offset:1024
MF a[76:79],a[204:207],v[124:127],a[76:79]
ds_read_b64_tr_b16 a[172:173],v179 offset:512
MF a[108:111],a[204:207],v[132:135],a[108:111]
ds_read_b64_tr_b16 a[174:175],v179 offset:768
MF a[80:83],a[208:211],v[124:127],a[80:83]
ds_read_b64_tr_b16 a[176:177],v178 offset:1024
ds_read_b64_tr_b16 a[178:179],v178 offset:1280
MF a[112:115],a[208:211],v[132:135],a[112:115]
buffer_load_dwordx4 v[116:119],v10,s[44:47],s67 offen
MF a[84:87],a[212:215],v[124:127],a[84:87]
ds_read_b64_tr_b16 a[180:181],v179 offset:1024
MF a[116:119],a[212:215],v[132:135],a[116:119]
ds_read_b64_tr_b16 a[182:183],v179 offset:1280
MF a[88:91],a[216:219],v[124:127],a[88:91]
ds_read_b64_tr_b16 a[184:185],v178 offset:1536
ds_read_b64_tr_b16 a[186:187],v178 offset:1792
MF a[120:123],a[216:219],v[132:135],a[120:123]
buffer_load_dwordx4 v[120:123],v10,s[44:47],s67 offen offset:1024
MF a[92:95],a[220:223],v[124:127],a[92:95]
ds_read_b64_tr_b16 a[188:189],v179 offset:1536
MF a[124:127],a[220:223],v[132:135],a[124:127]
ds_read_b64_tr_b16 a[190:191],v179 offset:1792
s_waitcnt lgkmcnt(15)
MF a[64:67],a[128:131],v[128:131],a[64:67]
ds_read_b64_tr_b16 a[192:193],v178 offset:8192
ds_read_b64_tr_b16 a[194:195],v178 offset:8448
s_add_u32 m0,s68,65536
MF a[96:99],a[128:131],v[136:139],a[96:99]
buffer_load_dwordx4 v88,s[48:51],s65 offen lds
MF a[68:71],a[132:135],v[128:131],a[68:71]
ds_read_b64_tr_b16 a[196:197],v179 offset:8192
MF a[100:103],a[132:135],v[136:139],a[100:103]
ds_read_b64_tr_b16 a[198:199],v179 offset:8448
MF a[72:75],a[136:139],v[128:131],a[72:75]
ds_read_b64_tr_b16 a[200:201],v178 offset:8704
ds_read_b64_tr_b16 a[202:203],v178 offset:8960
)A"
R"A(s_add_u32 m0,s68,66560
MF a[104:107],a[136:139],v[136:139],a[104:107]
buffer_load_dwordx4 v89,s[48:51],s65 offen lds
MF a[76:79],a[140:143],v[128:131],a[76:79]
ds_read_b64_tr_b16 a[204:205],v179 offset:8704
MF a[108:111],a[140:143],v[136:139],a[108:111]
ds_read_b64_tr_b16 a[206:207],v179 offset:8960
MF a[80:83],a[144:147],v[128:131],a[80:83]
ds_read_b64_tr_b16 a[208:209],v178 offset:9216
ds_read_b64_tr_b16 a[210:211],v178 offset:9472
s_add_u32 m0,s68,67584
MF a[112:115],a[144:147],v[136:139],a[112:115]
buffer_load_dwordx4 v90,s[48:51],s65 offen lds
MF a[84:87],a[148:151],v[128:131],a[84:87]
ds_read_b64_tr_b16 a[212:213],v179 offset:9216
MF a[116:119],a[148:151],v[136:139],a[116:119]
ds_read_b64_tr_b16 a[214:215],v179 offset:9472
MF a[88:91],a[152:155],v[128:131],a[88:91]
ds_read_b64_tr_b16 a[216:217],v178 offset:9728
ds_read_b64_tr_b16 a[218:219],v178 offset:9984
s_add_u32 m0,s68,68608
MF a[120:123],a[152:155],v[136:139],a[120:123]
buffer_load_dwordx4 v91,s[48:51],s65 offen lds
MF a[92:95],a[156:159],v[128:131],a[92:95]
ds_read_b64_tr_b16 a[220:221],v179 offset:9728
MF a[124:127],a[156:159],v[136:139],a[124:127]
ds_read_b64_tr_b16 a[222:223],v179 offset:9984
s_add_u32 s65,s65,81920
s_add_u32 s66,s66,2048
s_add_u32 s67,s67,2048
s_waitcnt vmcnt(8)
s_barrier
s_waitcnt lgkmcnt(15)
MF a[64:67],a[160:163],v[140:143],a[64:67]
ds_read_b64_tr_b16 a[128:129],v180
ds_read_b64_tr_b16 a[130:131],v180 offset:256
MF a[96:99],a[160:163],v[148:151],a[96:99]
buffer_load_dwordx4 v[124:127],v10,s[44:47],s66 offen
MF a[68:71],a[164:167],v[140:143],a[68:71]
ds_read_b64_tr_b16 a[132:133],v181
MF a[100:103],a[164:167],v[148:151],a[100:103]
ds_read_b64_tr_b16 a[134:135],v181 offset:256
MF a[72:75],a[168:171],v[140:143],a[72:75]
ds_read_b64_tr_b16 a[136:137],v180 offset:512
ds_read_b64_tr_b16 a[138:139],v180 offset:768
MF a[104:107],a[168:171],v[148:151],a[104:107]
buffer_load_dwordx4 v[128:131],v10,s[44:47],s66 offen offset:1024
MF a[76:79],a[172:175],v[140:143],a[76:79]
ds_read_b64_tr_b16 a[140:141],v181 offset:512
MF a[108:111],a[172:175],v[148:151],a[108:111]
ds_read_b64_tr_b16 a[142:143],v181 offset:768
MF a[80:83],a[176:179],v[140:143],a[80:83]
ds_read_b64_tr_b16 a[144:145],v180 offset:1024
ds_read_b64_tr_b16 a[146:147],v180 offset:1280
MF a[112:115],a[176:179],v[148:151],a[112:115]
buffer_load_dwordx4 v[132:135],v10,s[44:47],s67 offen
MF a[84:87],a[180:183],v[140:143],a[84:87]
ds_read_b64_tr_b16 a[148:149],v181 offset:1024
MF a[116:119],a[180:183],v[148:151],a[116:119]
ds_read_b64_tr_b16 a[150:151],v181 offset:1280
MF a[88:91],a[184:187],v[140:143],a[88:91]
ds_read_b64_tr_b16 a[152:153],v180 offset:1536
ds_read_b64_tr_b16 a[154:155],v180 offset:1792
MF a[120:123],a[184:187],v[148:151],a[120:123]
buffer_load_dwordx4 v[136:139],v10,s[44:47],s67 offen offset:1024
MF a[92:95],a[188:191],v[140:143],a[92:95]
ds_read_b64_tr_b16 a[156:157],v181 offset:1536
MF a[124:127],a[188:191],v[148:151],a[124:127]
ds_read_b64_tr_b16 a[158:159],v181 offset:1792
s_waitcnt lgkmcnt(15)
MF a[64:67],a[192:195],v[144:147],a[64:67]
ds_read_b64_tr_b16 a[160:161],v180 offset:8192
ds_read_b64_tr_b16 a[162:163],v180 offset:8448
s_add_u32 m0,s68,98304
MF a[96:99],a[192:195],v[152:155],a[96:99]
buffer_load_dwordx4 v88,s[48:51],s65 offen lds
MF a[68:71],a[196:199],v[144:147],a[68:71]
ds_read_b64_tr_b16 a[164:165],v181 offset:8192
MF a[100:103],a[196:199],v[152:155],a[100:103]
ds_read_b64_tr_b16 a[166:167],v181 offset:8448
MF a[72:75],a[200:203],v[144:147],a[72:75]
ds_read_b64_tr_b16 a[168:169],v180 offset:8704
ds_read_b64_tr_b16 a[170:171],v180 offset:8960
s_add_u32 m0,s68,99328
MF a[104:107],a[200:203],v[152:155],a[104:107]
buffer_load_dwordx4 v89,s[48:51],s65 offen lds
MF a[76:79],a[204:207],v[144:147],a[76:79]
ds_read_b64_tr_b16 a[172:173],v181 offset:8704
MF a[108:111],a[204:207],v[152:155],a[108:111]
ds_read_b64_tr_b16 a[174:175],v181 offset:8960
MF a[80:83],a[208:211],v[144:147],a[80:83]
ds_read_b64_tr_b16 a[176:177],v180 offset:9216
ds_read_b64_tr_b16 a[178:179],v180 offset:9472
s_add_u32 m0,s68,100352
MF a[112:115],a[208:211],v[152:155],a[112:115]
buffer_load_dwordx4 v90,s[48:51],s65 offen lds
MF a[84:87],a[212:215],v[144:147],a[84:87]
ds_read_b64_tr_b16 a[180:181],v181 offset:9216
MF a[116:119],a[212:215],v[152:155],a[116:119]
ds_read_b64_tr_b16 a[182:183],v181 offset:9472
MF a[88:91],a[216:219],v[144:147],a[88:91]
ds_read_b64_tr_b16 a[184:185],v180 offset:9728
ds_read_b64_tr_b16 a[186:187],v180 offset:9984
s_add_u32 m0,s68,101376
MF a[120:123],a[216:219],v[152:155],a[120:123]
buffer_load_dwordx4 v91,s[48:51],s65 offen lds
MF a[92:95],a[220:223],v[144:147],a[92:95]
ds_read_b64_tr_b16 a[188:189],v181 offset:9728
MF a[124:127],a[220:223],v[152:155],a[124:127]
ds_read_b64_tr_b16 a[190:191],v181 offset:9984
s_add_u32 s65,s65,81920
s_add_u32 s66,s66,2048
s_add_u32 s67,s67,2048
s_waitcnt vmcnt(8)
s_barrier
s_cmp_eq_u32 s80,1
s_cbranch_scc1 LSSOK_%=
v_readfirstlane_b32 s100,v246
s_cmp_ge_u32 s100,2
s_cbranch_scc1 LSSOK_%=
s_mov_b32 s96,0
LSS_%=:
s_sleep 2
global_load_dword v246,v247,s[24:25] sc1
s_waitcnt vmcnt(0)
v_readfirstlane_b32 s100,v246
s_add_u32 s96,s96,1
s_cmp_ge_u32 s100,2
s_cbranch_scc1 LSSOK_%=
s_cmp_lt_u32 s96,0x10000
s_cbranch_scc1 LSS_%=
LSSOK_%=:
s_waitcnt lgkmcnt(15)
MF a[64:67],a[128:131],v[156:159],a[64:67]
ds_read_b64_tr_b16 a[192:193],v172
ds_read_b64_tr_b16 a[194:195],v172 offset:256
MF a[96:99],a[128:131],v[164:167],a[96:99]
buffer_load_dwordx4 v[216:219],v10,s[56:59],s86 offen sc1
MF a[68:71],a[132:135],v[156:159],a[68:71]
ds_read_b64_tr_b16 a[196:197],v173
ds_read_b64_tr_b16 a[198:199],v173 offset:256
MF a[100:103],a[132:135],v[164:167],a[100:103]
buffer_load_dwordx4 v[220:223],v10,s[56:59],s86 offen offset:1024 sc1
MF a[72:75],a[136:139],v[156:159],a[72:75]
ds_read_b64_tr_b16 a[200:201],v172 offset:512
MF a[104:107],a[136:139],v[164:167],a[104:107]
ds_read_b64_tr_b16 a[202:203],v172 offset:768
ds_read_b64_tr_b16 a[204:205],v173 offset:512
MF a[76:79],a[140:143],v[156:159],a[76:79]
buffer_load_dwordx4 v[224:227],v10,s[56:59],s86 offen offset:2048 sc1
MF a[108:111],a[140:143],v[164:167],a[108:111]
ds_read_b64_tr_b16 a[206:207],v173 offset:768
MF a[80:83],a[144:147],v[156:159],a[80:83]
ds_read_b64_tr_b16 a[208:209],v172 offset:1024
ds_read_b64_tr_b16 a[210:211],v172 offset:1280
MF a[112:115],a[144:147],v[164:167],a[112:115]
buffer_load_dwordx4 v[228:231],v10,s[56:59],s86 offen offset:3072 sc1
MF a[84:87],a[148:151],v[156:159],a[84:87]
ds_read_b64_tr_b16 a[212:213],v173 offset:1024
ds_read_b64_tr_b16 a[214:215],v173 offset:1280
MF a[116:119],a[148:151],v[164:167],a[116:119]
buffer_load_dwordx4 v[140:143],v10,s[44:47],s66 offen
MF a[88:91],a[152:155],v[156:159],a[88:91]
ds_read_b64_tr_b16 a[216:217],v172 offset:1536
MF a[120:123],a[152:155],v[164:167],a[120:123]
ds_read_b64_tr_b16 a[218:219],v172 offset:1792
ds_read_b64_tr_b16 a[220:221],v173 offset:1536
MF a[92:95],a[156:159],v[156:159],a[92:95]
buffer_load_dwordx4 v[144:147],v10,s[44:47],s66 offen offset:1024
MF a[124:127],a[156:159],v[164:167],a[124:127]
ds_read_b64_tr_b16 a[222:223],v173 offset:1792
s_waitcnt lgkmcnt(15)
MF a[64:67],a[160:163],v[160:163],a[64:67]
ds_read_b64_tr_b16 a[128:129],v172 offset:8192
ds_read_b64_tr_b16 a[130:131],v172 offset:8448
MF a[96:99],a[160:163],v[168:171],a[96:99]
buffer_load_dwordx4 v[148:151],v10,s[44:47],s67 offen
MF a[68:71],a[164:167],v[160:163],a[68:71]
ds_read_b64_tr_b16 a[132:133],v173 offset:8192
ds_read_b64_tr_b16 a[134:135],v173 offset:8448
MF a[100:103],a[164:167],v[168:171],a[100:103]
buffer_load_dwordx4 v[152:155],v10,s[44:47],s67 offen offset:1024
MF a[72:75],a[168:171],v[160:163],a[72:75]
ds_read_b64_tr_b16 a[136:137],v172 offset:8704
MF a[104:107],a[168:171],v[168:171],a[104:107]
ds_read_b64_tr_b16 a[138:139],v172 offset:8960
ds_read_b64_tr_b16 a[140:141],v173 offset:8704
s_add_u32 m0,s68,131072
MF a[76:79],a[172:175],v[160:163],a[76:79]
buffer_load_dwordx4 v88,s[48:51],s65 offen lds
MF a[108:111],a[172:175],v[168:171],a[108:111]
ds_read_b64_tr_b16 a[142:143],v173 offset:8960
MF a[80:83],a[176:179],v[160:163],a[80:83]
ds_read_b64_tr_b16 a[144:145],v172 offset:9216
ds_read_b64_tr_b16 a[146:147],v172 offset:9472
s_add_u32 m0,s68,132096
MF a[112:115],a[176:179],v[168:171],a[112:115]
buffer_load_dwordx4 v89,s[48:51],s65 offen lds
MF a[84:87],a[180:183],v[160:163],a[84:87]
ds_read_b64_tr_b16 a[148:149],v173 offset:9216
ds_read_b64_tr_b16 a[150:151],v173 offset:9472
s_add_u32 m0,s68,133120
MF a[116:119],a[180:183],v[168:171],a[116:119]
buffer_load_dwordx4 v90,s[48:51],s65 offen lds
MF a[88:91],a[184:187],v[160:163],a[88:91]
ds_read_b64_tr_b16 a[152:153],v172 offset:9728
MF a[120:123],a[184:187],v[168:171],a[120:123]
ds_read_b64_tr_b16 a[154:155],v172 offset:9984
ds_read_b64_tr_b16 a[156:157],v173 offset:9728
s_add_u32 m0,s68,134144
MF a[92:95],a[188:191],v[160:163],a[92:95]
buffer_load_dwordx4 v91,s[48:51],s65 offen lds
MF a[124:127],a[188:191],v[168:171],a[124:127]
ds_read_b64_tr_b16 a[158:159],v173 offset:9984
s_add_u32 s65,s65,81920
s_add_u32 s66,s66,2048
s_add_u32 s67,s67,2048
s_waitcnt vmcnt(8)
s_barrier
s_waitcnt lgkmcnt(15)
MF a[64:67],a[192:195],v[92:95],a[64:67]
ds_read_b64_tr_b16 a[160:161],v174
ds_read_b64_tr_b16 a[162:163],v174 offset:256
v_lshlrev_b32 v182,16,v182
MF a[96:99],a[192:195],v[100:103],a[96:99]
buffer_load_dwordx4 v[156:159],v10,s[44:47],s66 offen
AR v236,a32
MF a[68:71],a[196:199],v[92:95],a[68:71]
ds_read_b64_tr_b16 a[164:165],v175
AR v237,a33
AR v238,a34
MF a[100:103],a[196:199],v[100:103],a[100:103]
ds_read_b64_tr_b16 a[166:167],v175 offset:256
AR v239,a35
s_waitcnt vmcnt(8)
v_lshlrev_b32 v2,16,v216
MF a[72:75],a[200:203],v[92:95],a[72:75]
ds_read_b64_tr_b16 a[168:169],v174 offset:512
ds_read_b64_tr_b16 a[170:171],v174 offset:768
v_and_b32 v3,0xffff0000,v216
MF a[104:107],a[200:203],v[100:103],a[104:107]
buffer_load_dwordx4 v[160:163],v10,s[44:47],s66 offen offset:1024
v_lshlrev_b32 v4,16,v217
MF a[76:79],a[204:207],v[92:95],a[76:79]
ds_read_b64_tr_b16 a[172:173],v175 offset:512
v_and_b32 v5,0xffff0000,v217
v_add_f32 v236,v236,v2
MF a[108:111],a[204:207],v[100:103],a[108:111]
ds_read_b64_tr_b16 a[174:175],v175 offset:768
v_add_f32 v237,v237,v3
v_add_f32 v238,v238,v4
MF a[80:83],a[208:211],v[92:95],a[80:83]
ds_read_b64_tr_b16 a[176:177],v174 offset:1024
ds_read_b64_tr_b16 a[178:179],v174 offset:1280
v_add_f32 v239,v239,v5
MF a[112:115],a[208:211],v[100:103],a[112:115]
buffer_load_dwordx4 v[164:167],v10,s[44:47],s67 offen
v_lshlrev_b32 v2,16,v184
MF a[84:87],a[212:215],v[92:95],a[84:87]
ds_read_b64_tr_b16 a[180:181],v175 offset:1024
v_and_b32 v3,0xffff0000,v184
v_lshlrev_b32 v4,16,v185
MF a[116:119],a[212:215],v[100:103],a[116:119]
ds_read_b64_tr_b16 a[182:183],v175 offset:1280
v_and_b32 v5,0xffff0000,v185
v_add_f32 v236,v236,v182
MF a[88:91],a[216:219],v[92:95],a[88:91]
ds_read_b64_tr_b16 a[184:185],v174 offset:1536
ds_read_b64_tr_b16 a[186:187],v174 offset:1792
v_add_f32 v237,v237,v182
MF a[120:123],a[216:219],v[100:103],a[120:123]
buffer_load_dwordx4 v[168:171],v10,s[44:47],s67 offen offset:1024
v_add_f32 v238,v238,v182
MF a[92:95],a[220:223],v[92:95],a[92:95]
ds_read_b64_tr_b16 a[188:189],v175 offset:1536
v_add_f32 v239,v239,v182
v_mul_f32 v236,v236,v2
MF a[124:127],a[220:223],v[100:103],a[124:127]
ds_read_b64_tr_b16 a[190:191],v175 offset:1792
v_mul_f32 v237,v237,v3
v_mul_f32 v238,v238,v4
v_mul_f32 v239,v239,v5
AR v240,a36
AR v241,a37
AR v242,a38
AR v243,a39
v_lshlrev_b32 v2,16,v218
v_and_b32 v3,0xffff0000,v218
v_lshlrev_b32 v4,16,v219
v_and_b32 v5,0xffff0000,v219
v_add_f32 v240,v240,v2
v_add_f32 v241,v241,v3
v_add_f32 v242,v242,v4
v_add_f32 v243,v243,v5
v_lshlrev_b32 v2,16,v186
v_and_b32 v3,0xffff0000,v186
v_lshlrev_b32 v4,16,v187
v_and_b32 v5,0xffff0000,v187
v_add_f32 v240,v240,v182
v_add_f32 v241,v241,v182
v_add_f32 v242,v242,v182
v_add_f32 v243,v243,v182
v_mul_f32 v240,v240,v2
v_mul_f32 v241,v241,v3
v_mul_f32 v242,v242,v4
v_mul_f32 v243,v243,v5
CP v200,v236,v237
CP v201,v238,v239
CP v202,v240,v241
CP v203,v242,v243
s_add_u32 s99,s84,0
s_cmp_lt_u32 s99,27
s_cselect_b64 s[88:89],-1,0
v_cndmask_b32_e64 v200,0,v200,s[88:89]
v_cndmask_b32_e64 v201,0,v201,s[88:89]
v_cndmask_b32_e64 v202,0,v202,s[88:89]
s_waitcnt lgkmcnt(15)
MF a[64:67],a[128:131],v[96:99],a[64:67]
ds_read_b64_tr_b16 a[192:193],v174 offset:8192
ds_read_b64_tr_b16 a[194:195],v174 offset:8448
s_add_u32 m0,s68,0
MF a[96:99],a[128:131],v[104:107],a[96:99]
buffer_load_dwordx4 v88,s[48:51],s65 offen lds
v_cndmask_b32_e64 v203,0,v203,s[88:89]
MF a[68:71],a[132:135],v[96:99],a[68:71]
ds_read_b64_tr_b16 a[196:197],v175 offset:8192
AR v236,a40
AR v237,a41
MF a[100:103],a[132:135],v[104:107],a[100:103]
ds_read_b64_tr_b16 a[198:199],v175 offset:8448
AR v238,a42
AR v239,a43
MF a[72:75],a[136:139],v[96:99],a[72:75]
ds_read_b64_tr_b16 a[200:201],v174 offset:8704
ds_read_b64_tr_b16 a[202:203],v174 offset:8960
s_add_u32 m0,s68,1024
MF a[104:107],a[136:139],v[104:107],a[104:107]
buffer_load_dwordx4 v89,s[48:51],s65 offen lds
s_waitcnt vmcnt(10)
v_lshlrev_b32 v2,16,v220
MF a[76:79],a[140:143],v[96:99],a[76:79]
ds_read_b64_tr_b16 a[204:205],v175 offset:8704
v_and_b32 v3,0xffff0000,v220
v_lshlrev_b32 v4,16,v221
MF a[108:111],a[140:143],v[104:107],a[108:111]
ds_read_b64_tr_b16 a[206:207],v175 offset:8960
v_and_b32 v5,0xffff0000,v221
v_add_f32 v236,v236,v2
MF a[80:83],a[144:147],v[96:99],a[80:83]
ds_read_b64_tr_b16 a[208:209],v174 offset:9216
ds_read_b64_tr_b16 a[210:211],v174 offset:9472
s_add_u32 m0,s68,2048
MF a[112:115],a[144:147],v[104:107],a[112:115]
buffer_load_dwordx4 v90,s[48:51],s65 offen lds
v_add_f32 v237,v237,v3
MF a[84:87],a[148:151],v[96:99],a[84:87]
ds_read_b64_tr_b16 a[212:213],v175 offset:9216
v_add_f32 v238,v238,v4
v_add_f32 v239,v239,v5
MF a[116:119],a[148:151],v[104:107],a[116:119]
ds_read_b64_tr_b16 a[214:215],v175 offset:9472
v_lshlrev_b32 v2,16,v188
v_and_b32 v3,0xffff0000,v188
MF a[88:91],a[152:155],v[96:99],a[88:91]
ds_read_b64_tr_b16 a[216:217],v174 offset:9728
ds_read_b64_tr_b16 a[218:219],v174 offset:9984
s_add_u32 m0,s68,3072
MF a[120:123],a[152:155],v[104:107],a[120:123]
buffer_load_dwordx4 v91,s[48:51],s65 offen lds
v_lshlrev_b32 v4,16,v189
MF a[92:95],a[156:159],v[96:99],a[92:95]
ds_read_b64_tr_b16 a[220:221],v175 offset:9728
v_and_b32 v5,0xffff0000,v189
v_add_f32 v236,v236,v182
MF a[124:127],a[156:159],v[104:107],a[124:127]
ds_read_b64_tr_b16 a[222:223],v175 offset:9984
v_add_f32 v237,v237,v182
v_add_f32 v238,v238,v182
v_add_f32 v239,v239,v182
v_mul_f32 v236,v236,v2
v_mul_f32 v237,v237,v3
v_mul_f32 v238,v238,v4
s_add_u32 s65,s65,81920
s_add_u32 s66,s66,2048
s_add_u32 s67,s67,2048
v_mul_f32 v239,v239,v5
AR v240,a44
AR v241,a45
AR v242,a46
AR v243,a47
v_lshlrev_b32 v2,16,v222
v_and_b32 v3,0xffff0000,v222
v_lshlrev_b32 v4,16,v223
v_and_b32 v5,0xffff0000,v223
v_add_f32 v240,v240,v2
v_add_f32 v241,v241,v3
v_add_f32 v242,v242,v4
v_add_f32 v243,v243,v5
v_lshlrev_b32 v2,16,v190
v_and_b32 v3,0xffff0000,v190
v_lshlrev_b32 v4,16,v191
v_and_b32 v5,0xffff0000,v191
v_add_f32 v240,v240,v182
v_add_f32 v241,v241,v182
v_add_f32 v242,v242,v182
v_add_f32 v243,v243,v182
v_mul_f32 v240,v240,v2
v_mul_f32 v241,v241,v3
v_mul_f32 v242,v242,v4
v_mul_f32 v243,v243,v5
CP v204,v236,v237
CP v205,v238,v239
CP v206,v240,v241
CP v207,v242,v243
s_add_u32 s99,s84,1
s_cmp_lt_u32 s99,27
s_cselect_b64 s[88:89],-1,0
v_cndmask_b32_e64 v204,0,v204,s[88:89]
v_cndmask_b32_e64 v205,0,v205,s[88:89]
v_cndmask_b32_e64 v206,0,v206,s[88:89]
v_cndmask_b32_e64 v207,0,v207,s[88:89]
s_waitcnt vmcnt(8)
s_barrier
s_waitcnt lgkmcnt(15)
MF a[64:67],a[160:163],v[108:111],a[64:67]
ds_read_b64_tr_b16 a[128:129],v176
ds_read_b64_tr_b16 a[130:131],v176 offset:256
v_lshlrev_b32 v183,16,v183
MF a[96:99],a[160:163],v[116:119],a[96:99]
buffer_load_dwordx4 v[92:95],v10,s[44:47],s66 offen
AR v236,a48
MF a[68:71],a[164:167],v[108:111],a[68:71]
ds_read_b64_tr_b16 a[132:133],v177
AR v237,a49
AR v238,a50
MF a[100:103],a[164:167],v[116:119],a[100:103]
ds_read_b64_tr_b16 a[134:135],v177 offset:256
AR v239,a51
v_lshlrev_b32 v2,16,v224
MF a[72:75],a[168:171],v[108:111],a[72:75]
ds_read_b64_tr_b16 a[136:137],v176 offset:512
ds_read_b64_tr_b16 a[138:139],v176 offset:768
v_and_b32 v3,0xffff0000,v224
MF a[104:107],a[168:171],v[116:119],a[104:107]
buffer_load_dwordx4 v[96:99],v10,s[44:47],s66 offen offset:1024
v_lshlrev_b32 v4,16,v225
MF a[76:79],a[172:175],v[108:111],a[76:79]
ds_read_b64_tr_b16 a[140:141],v177 offset:512
v_and_b32 v5,0xffff0000,v225
v_add_f32 v236,v236,v2
MF a[108:111],a[172:175],v[116:119],a[108:111]
ds_read_b64_tr_b16 a[142:143],v177 offset:768
v_add_f32 v237,v237,v3
v_add_f32 v238,v238,v4
MF a[80:83],a[176:179],v[108:111],a[80:83]
ds_read_b64_tr_b16 a[144:145],v176 offset:1024
ds_read_b64_tr_b16 a[146:147],v176 offset:1280
v_add_f32 v239,v239,v5
MF a[112:115],a[176:179],v[116:119],a[112:115]
buffer_load_dwordx4 v[100:103],v10,s[44:47],s67 offen
v_lshlrev_b32 v2,16,v192
MF a[84:87],a[180:183],v[108:111],a[84:87]
ds_read_b64_tr_b16 a[148:149],v177 offset:1024
v_and_b32 v3,0xffff0000,v192
v_lshlrev_b32 v4,16,v193
MF a[116:119],a[180:183],v[116:119],a[116:119]
ds_read_b64_tr_b16 a[150:151],v177 offset:1280
v_and_b32 v5,0xffff0000,v193
v_add_f32 v236,v236,v183
MF a[88:91],a[184:187],v[108:111],a[88:91]
ds_read_b64_tr_b16 a[152:153],v176 offset:1536
ds_read_b64_tr_b16 a[154:155],v176 offset:1792
v_add_f32 v237,v237,v183
MF a[120:123],a[184:187],v[116:119],a[120:123]
buffer_load_dwordx4 v[104:107],v10,s[44:47],s67 offen offset:1024
v_add_f32 v238,v238,v183
MF a[92:95],a[188:191],v[108:111],a[92:95]
ds_read_b64_tr_b16 a[156:157],v177 offset:1536
v_add_f32 v239,v239,v183
v_mul_f32 v236,v236,v2
MF a[124:127],a[188:191],v[116:119],a[124:127]
ds_read_b64_tr_b16 a[158:159],v177 offset:1792
v_mul_f32 v237,v237,v3
v_mul_f32 v238,v238,v4
v_mul_f32 v239,v239,v5
AR v240,a52
AR v241,a53
AR v242,a54
AR v243,a55
v_lshlrev_b32 v2,16,v226
v_and_b32 v3,0xffff0000,v226
v_lshlrev_b32 v4,16,v227
v_and_b32 v5,0xffff0000,v227
v_add_f32 v240,v240,v2
v_add_f32 v241,v241,v3
v_add_f32 v242,v242,v4
v_add_f32 v243,v243,v5
v_lshlrev_b32 v2,16,v194
v_and_b32 v3,0xffff0000,v194
v_lshlrev_b32 v4,16,v195
v_and_b32 v5,0xffff0000,v195
v_add_f32 v240,v240,v183
v_add_f32 v241,v241,v183
v_add_f32 v242,v242,v183
v_add_f32 v243,v243,v183
v_mul_f32 v240,v240,v2
v_mul_f32 v241,v241,v3
v_mul_f32 v242,v242,v4
v_mul_f32 v243,v243,v5
CP v208,v236,v237
CP v209,v238,v239
CP v210,v240,v241
CP v211,v242,v243
s_add_u32 s99,s84,0
s_cmp_lt_u32 s99,27
s_cselect_b64 s[88:89],-1,0
v_cndmask_b32_e64 v208,0,v208,s[88:89]
v_cndmask_b32_e64 v209,0,v209,s[88:89]
v_cndmask_b32_e64 v210,0,v210,s[88:89]
s_waitcnt lgkmcnt(15)
MF a[64:67],a[192:195],v[112:115],a[64:67]
ds_read_b64_tr_b16 a[160:161],v176 offset:8192
ds_read_b64_tr_b16 a[162:163],v176 offset:8448
s_add_u32 m0,s68,32768
MF a[96:99],a[192:195],v[120:123],a[96:99]
buffer_load_dwordx4 v88,s[48:51],s65 offen lds
v_cndmask_b32_e64 v211,0,v211,s[88:89]
MF a[68:71],a[196:199],v[112:115],a[68:71]
ds_read_b64_tr_b16 a[164:165],v177 offset:8192
AR v236,a56
AR v237,a57
MF a[100:103],a[196:199],v[120:123],a[100:103]
ds_read_b64_tr_b16 a[166:167],v177 offset:8448
AR v238,a58
AR v239,a59
MF a[72:75],a[200:203],v[112:115],a[72:75]
ds_read_b64_tr_b16 a[168:169],v176 offset:8704
ds_read_b64_tr_b16 a[170:171],v176 offset:8960
s_add_u32 m0,s68,33792
MF a[104:107],a[200:203],v[120:123],a[104:107]
buffer_load_dwordx4 v89,s[48:51],s65 offen lds
v_lshlrev_b32 v2,16,v228
MF a[76:79],a[204:207],v[112:115],a[76:79]
ds_read_b64_tr_b16 a[172:173],v177 offset:8704
v_and_b32 v3,0xffff0000,v228
v_lshlrev_b32 v4,16,v229
MF a[108:111],a[204:207],v[120:123],a[108:111]
ds_read_b64_tr_b16 a[174:175],v177 offset:8960
v_and_b32 v5,0xffff0000,v229
v_add_f32 v236,v236,v2
MF a[80:83],a[208:211],v[112:115],a[80:83]
ds_read_b64_tr_b16 a[176:177],v176 offset:9216
ds_read_b64_tr_b16 a[178:179],v176 offset:9472
s_add_u32 m0,s68,34816
MF a[112:115],a[208:211],v[120:123],a[112:115]
buffer_load_dwordx4 v90,s[48:51],s65 offen lds
v_add_f32 v237,v237,v3
MF a[84:87],a[212:215],v[112:115],a[84:87]
ds_read_b64_tr_b16 a[180:181],v177 offset:9216
v_add_f32 v238,v238,v4
v_add_f32 v239,v239,v5
MF a[116:119],a[212:215],v[120:123],a[116:119]
ds_read_b64_tr_b16 a[182:183],v177 offset:9472
v_lshlrev_b32 v2,16,v196
v_and_b32 v3,0xffff0000,v196
MF a[88:91],a[216:219],v[112:115],a[88:91]
ds_read_b64_tr_b16 a[184:185],v176 offset:9728
ds_read_b64_tr_b16 a[186:187],v176 offset:9984
s_add_u32 m0,s68,35840
MF a[120:123],a[216:219],v[120:123],a[120:123]
buffer_load_dwordx4 v91,s[48:51],s65 offen lds
v_lshlrev_b32 v4,16,v197
MF a[92:95],a[220:223],v[112:115],a[92:95]
ds_read_b64_tr_b16 a[188:189],v177 offset:9728
v_and_b32 v5,0xffff0000,v197
v_add_f32 v236,v236,v183
MF a[124:127],a[220:223],v[120:123],a[124:127]
ds_read_b64_tr_b16 a[190:191],v177 offset:9984
v_add_f32 v237,v237,v183
v_add_f32 v238,v238,v183
v_add_f32 v239,v239,v183
v_mul_f32 v236,v236,v2
v_mul_f32 v237,v237,v3
v_mul_f32 v238,v238,v4
s_add_u32 s65,s65,81920
s_add_u32 s66,s66,2048
s_add_u32 s67,s67,2048
v_mul_f32 v239,v239,v5
AR v240,a60
AR v241,a61
AR v242,a62
AR v243,a63
v_lshlrev_b32 v2,16,v230
v_and_b32 v3,0xffff0000,v230
v_lshlrev_b32 v4,16,v231
v_and_b32 v5,0xffff0000,v231
v_add_f32 v240,v240,v2
v_add_f32 v241,v241,v3
v_add_f32 v242,v242,v4
v_add_f32 v243,v243,v5
v_lshlrev_b32 v2,16,v198
v_and_b32 v3,0xffff0000,v198
v_lshlrev_b32 v4,16,v199
v_and_b32 v5,0xffff0000,v199
v_add_f32 v240,v240,v183
v_add_f32 v241,v241,v183
v_add_f32 v242,v242,v183
v_add_f32 v243,v243,v183
v_mul_f32 v240,v240,v2
v_mul_f32 v241,v241,v3
v_mul_f32 v242,v242,v4
v_mul_f32 v243,v243,v5
CP v212,v236,v237
CP v213,v238,v239
CP v214,v240,v241
CP v215,v242,v243
s_add_u32 s99,s84,1
s_cmp_lt_u32 s99,27
s_cselect_b64 s[88:89],-1,0
v_cndmask_b32_e64 v212,0,v212,s[88:89]
v_cndmask_b32_e64 v213,0,v213,s[88:89]
v_cndmask_b32_e64 v214,0,v214,s[88:89]
v_cndmask_b32_e64 v215,0,v215,s[88:89]
buffer_store_dwordx4 v[200:203],v10,s[44:47],s87 offen sc1
buffer_store_dwordx4 v[204:207],v10,s[44:47],s87 offen offset:1024 sc1
buffer_store_dwordx4 v[208:211],v10,s[44:47],s90 offen sc1
buffer_store_dwordx4 v[212:215],v10,s[44:47],s90 offen offset:1024 sc1
s_waitcnt vmcnt(8)
s_barrier
s_waitcnt lgkmcnt(15)
MF a[64:67],a[128:131],v[124:127],a[64:67]
ds_read_b64_tr_b16 a[192:193],v178
ds_read_b64_tr_b16 a[194:195],v178 offset:256
MF a[96:99],a[128:131],v[132:135],a[96:99]
buffer_load_dwordx4 v[108:111],v10,s[44:47],s66 offen
MF a[68:71],a[132:135],v[124:127],a[68:71]
ds_read_b64_tr_b16 a[196:197],v179
MF a[100:103],a[132:135],v[132:135],a[100:103]
ds_read_b64_tr_b16 a[198:199],v179 offset:256
MF a[72:75],a[136:139],v[124:127],a[72:75]
ds_read_b64_tr_b16 a[200:201],v178 offset:512
ds_read_b64_tr_b16 a[202:203],v178 offset:768
MF a[104:107],a[136:139],v[132:135],a[104:107]
buffer_load_dwordx4 v[112:115],v10,s[44:47],s66 offen offset:1024
MF a[76:79],a[140:143],v[124:127],a[76:79]
ds_read_b64_tr_b16 a[204:205],v179 offset:512
MF a[108:111],a[140:143],v[132:135],a[108:111]
ds_read_b64_tr_b16 a[206:207],v179 offset:768
MF a[80:83],a[144:147],v[124:127],a[80:83]
ds_read_b64_tr_b16 a[208:209],v178 offset:1024
ds_read_b64_tr_b16 a[210:211],v178 offset:1280
MF a[112:115],a[144:147],v[132:135],a[112:115]
buffer_load_dwordx4 v[116:119],v10,s[44:47],s67 offen
MF a[84:87],a[148:151],v[124:127],a[84:87]
ds_read_b64_tr_b16 a[212:213],v179 offset:1024
MF a[116:119],a[148:151],v[132:135],a[116:119]
ds_read_b64_tr_b16 a[214:215],v179 offset:1280
MF a[88:91],a[152:155],v[124:127],a[88:91]
ds_read_b64_tr_b16 a[216:217],v178 offset:1536
ds_read_b64_tr_b16 a[218:219],v178 offset:1792
MF a[120:123],a[152:155],v[132:135],a[120:123]
buffer_load_dwordx4 v[120:123],v10,s[44:47],s67 offen offset:1024
MF a[92:95],a[156:159],v[124:127],a[92:95]
ds_read_b64_tr_b16 a[220:221],v179 offset:1536
MF a[124:127],a[156:159],v[132:135],a[124:127]
ds_read_b64_tr_b16 a[222:223],v179 offset:1792
s_waitcnt lgkmcnt(15)
MF a[64:67],a[160:163],v[128:131],a[64:67]
ds_read_b64_tr_b16 a[128:129],v178 offset:8192
ds_read_b64_tr_b16 a[130:131],v178 offset:8448
s_add_u32 m0,s68,65536
MF a[96:99],a[160:163],v[136:139],a[96:99]
buffer_load_dwordx4 v88,s[48:51],s65 offen lds
MF a[68:71],a[164:167],v[128:131],a[68:71]
ds_read_b64_tr_b16 a[132:133],v179 offset:8192
MF a[100:103],a[164:167],v[136:139],a[100:103]
ds_read_b64_tr_b16 a[134:135],v179 offset:8448
MF a[72:75],a[168:171],v[128:131],a[72:75]
ds_read_b64_tr_b16 a[136:137],v178 offset:8704
ds_read_b64_tr_b16 a[138:139],v178 offset:8960
s_add_u32 m0,s68,66560
MF a[104:107],a[168:171],v[136:139],a[104:107]
buffer_load_dwordx4 v89,s[48:51],s65 offen lds
MF a[76:79],a[172:175],v[128:131],a[76:79]
ds_read_b64_tr_b16 a[140:141],v179 offset:8704
MF a[108:111],a[172:175],v[136:139],a[108:111]
ds_read_b64_tr_b16 a[142:143],v179 offset:8960
MF a[80:83],a[176:179],v[128:131],a[80:83]
ds_read_b64_tr_b16 a[144:145],v178 offset:9216
ds_read_b64_tr_b16 a[146:147],v178 offset:9472
s_add_u32 m0,s68,67584
MF a[112:115],a[176:179],v[136:139],a[112:115]
buffer_load_dwordx4 v90,s[48:51],s65 offen lds
MF a[84:87],a[180:183],v[128:131],a[84:87]
ds_read_b64_tr_b16 a[148:149],v179 offset:9216
MF a[116:119],a[180:183],v[136:139],a[116:119]
ds_read_b64_tr_b16 a[150:151],v179 offset:9472
MF a[88:91],a[184:187],v[128:131],a[88:91]
ds_read_b64_tr_b16 a[152:153],v178 offset:9728
ds_read_b64_tr_b16 a[154:155],v178 offset:9984
s_add_u32 m0,s68,68608
MF a[120:123],a[184:187],v[136:139],a[120:123]
buffer_load_dwordx4 v91,s[48:51],s65 offen lds
MF a[92:95],a[188:191],v[128:131],a[92:95]
ds_read_b64_tr_b16 a[156:157],v179 offset:9728
MF a[124:127],a[188:191],v[136:139],a[124:127]
ds_read_b64_tr_b16 a[158:159],v179 offset:9984
s_add_u32 s65,s65,81920
s_add_u32 s66,s66,2048
s_add_u32 s67,s67,2048
s_waitcnt vmcnt(0)
s_barrier
s_cmp_eq_u32 s80,1
s_cbranch_scc1 LYA_%=
s_cmp_lg_u32 s60,0
s_cbranch_scc1 LYA_%=
s_lshl_b32 s100,s72,6
s_add_u32 s100,s100,1024
v_mov_b32 v244,s100
v_mov_b32 v245,1
s_mov_b64 exec,1
global_atomic_add v244,v245,s[24:25]
s_mov_b64 exec,-1
LYA_%=:
s_waitcnt lgkmcnt(15)
MF a[64:67],a[192:195],v[140:143],a[64:67]
ds_read_b64_tr_b16 a[160:161],v180
ds_read_b64_tr_b16 a[162:163],v180 offset:256
MF a[96:99],a[192:195],v[148:151],a[96:99]
buffer_load_dwordx4 v[124:127],v10,s[44:47],s66 offen
MF a[68:71],a[196:199],v[140:143],a[68:71]
ds_read_b64_tr_b16 a[164:165],v181
MF a[100:103],a[196:199],v[148:151],a[100:103]
ds_read_b64_tr_b16 a[166:167],v181 offset:256
MF a[72:75],a[200:203],v[140:143],a[72:75]
ds_read_b64_tr_b16 a[168:169],v180 offset:512
ds_read_b64_tr_b16 a[170:171],v180 offset:768
MF a[104:107],a[200:203],v[148:151],a[104:107]
buffer_load_dwordx4 v[128:131],v10,s[44:47],s66 offen offset:1024
MF a[76:79],a[204:207],v[140:143],a[76:79]
ds_read_b64_tr_b16 a[172:173],v181 offset:512
MF a[108:111],a[204:207],v[148:151],a[108:111]
ds_read_b64_tr_b16 a[174:175],v181 offset:768
MF a[80:83],a[208:211],v[140:143],a[80:83]
ds_read_b64_tr_b16 a[176:177],v180 offset:1024
ds_read_b64_tr_b16 a[178:179],v180 offset:1280
MF a[112:115],a[208:211],v[148:151],a[112:115]
buffer_load_dwordx4 v[132:135],v10,s[44:47],s67 offen
MF a[84:87],a[212:215],v[140:143],a[84:87]
ds_read_b64_tr_b16 a[180:181],v181 offset:1024
MF a[116:119],a[212:215],v[148:151],a[116:119]
ds_read_b64_tr_b16 a[182:183],v181 offset:1280
MF a[88:91],a[216:219],v[140:143],a[88:91]
ds_read_b64_tr_b16 a[184:185],v180 offset:1536
ds_read_b64_tr_b16 a[186:187],v180 offset:1792
MF a[120:123],a[216:219],v[148:151],a[120:123]
buffer_load_dwordx4 v[136:139],v10,s[44:47],s67 offen offset:1024
MF a[92:95],a[220:223],v[140:143],a[92:95]
ds_read_b64_tr_b16 a[188:189],v181 offset:1536
MF a[124:127],a[220:223],v[148:151],a[124:127]
ds_read_b64_tr_b16 a[190:191],v181 offset:1792
s_waitcnt lgkmcnt(15)
MF a[64:67],a[128:131],v[144:147],a[64:67]
ds_read_b64_tr_b16 a[192:193],v180 offset:8192
ds_read_b64_tr_b16 a[194:195],v180 offset:8448
s_add_u32 m0,s68,98304
MF a[96:99],a[128:131],v[152:155],a[96:99]
buffer_load_dwordx4 v88,s[48:51],s65 offen lds
MF a[68:71],a[132:135],v[144:147],a[68:71]
ds_read_b64_tr_b16 a[196:197],v181 offset:8192
MF a[100:103],a[132:135],v[152:155],a[100:103]
ds_read_b64_tr_b16 a[198:199],v181 offset:8448
MF a[72:75],a[136:139],v[144:147],a[72:75]
ds_read_b64_tr_b16 a[200:201],v180 offset:8704
ds_read_b64_tr_b16 a[202:203],v180 offset:8960
s_add_u32 m0,s68,99328
MF a[104:107],a[136:139],v[152:155],a[104:107]
buffer_load_dwordx4 v89,s[48:51],s65 offen lds
MF a[76:79],a[140:143],v[144:147],a[76:79]
ds_read_b64_tr_b16 a[204:205],v181 offset:8704
MF a[108:111],a[140:143],v[152:155],a[108:111]
ds_read_b64_tr_b16 a[206:207],v181 offset:8960
MF a[80:83],a[144:147],v[144:147],a[80:83]
ds_read_b64_tr_b16 a[208:209],v180 offset:9216
ds_read_b64_tr_b16 a[210:211],v180 offset:9472
s_add_u32 m0,s68,100352
MF a[112:115],a[144:147],v[152:155],a[112:115]
buffer_load_dwordx4 v90,s[48:51],s65 offen lds
MF a[84:87],a[148:151],v[144:147],a[84:87]
ds_read_b64_tr_b16 a[212:213],v181 offset:9216
MF a[116:119],a[148:151],v[152:155],a[116:119]
ds_read_b64_tr_b16 a[214:215],v181 offset:9472
MF a[88:91],a[152:155],v[144:147],a[88:91]
ds_read_b64_tr_b16 a[216:217],v180 offset:9728
ds_read_b64_tr_b16 a[218:219],v180 offset:9984
s_add_u32 m0,s68,101376
MF a[120:123],a[152:155],v[152:155],a[120:123]
buffer_load_dwordx4 v91,s[48:51],s65 offen lds
MF a[92:95],a[156:159],v[144:147],a[92:95]
ds_read_b64_tr_b16 a[220:221],v181 offset:9728
MF a[124:127],a[156:159],v[152:155],a[124:127]
ds_read_b64_tr_b16 a[222:223],v181 offset:9984
s_add_u32 s65,s65,81920
s_add_u32 s66,s66,2048
s_add_u32 s67,s67,2048
s_barrier
s_waitcnt lgkmcnt(15)
MF a[64:67],a[160:163],v[156:159],a[64:67]
ds_read_b64_tr_b16 a[128:129],v172
ds_read_b64_tr_b16 a[130:131],v172 offset:256
MF a[96:99],a[160:163],v[164:167],a[96:99]
buffer_load_dwordx4 v[140:143],v10,s[44:47],s66 offen
MF a[68:71],a[164:167],v[156:159],a[68:71]
ds_read_b64_tr_b16 a[132:133],v173
MF a[100:103],a[164:167],v[164:167],a[100:103]
ds_read_b64_tr_b16 a[134:135],v173 offset:256
MF a[72:75],a[168:171],v[156:159],a[72:75]
ds_read_b64_tr_b16 a[136:137],v172 offset:512
ds_read_b64_tr_b16 a[138:139],v172 offset:768
MF a[104:107],a[168:171],v[164:167],a[104:107]
)A"
R"A(buffer_load_dwordx4 v[144:147],v10,s[44:47],s66 offen offset:1024
MF a[76:79],a[172:175],v[156:159],a[76:79]
ds_read_b64_tr_b16 a[140:141],v173 offset:512
MF a[108:111],a[172:175],v[164:167],a[108:111]
ds_read_b64_tr_b16 a[142:143],v173 offset:768
MF a[80:83],a[176:179],v[156:159],a[80:83]
ds_read_b64_tr_b16 a[144:145],v172 offset:1024
ds_read_b64_tr_b16 a[146:147],v172 offset:1280
MF a[112:115],a[176:179],v[164:167],a[112:115]
buffer_load_dwordx4 v[148:151],v10,s[44:47],s67 offen
MF a[84:87],a[180:183],v[156:159],a[84:87]
ds_read_b64_tr_b16 a[148:149],v173 offset:1024
MF a[116:119],a[180:183],v[164:167],a[116:119]
ds_read_b64_tr_b16 a[150:151],v173 offset:1280
MF a[88:91],a[184:187],v[156:159],a[88:91]
ds_read_b64_tr_b16 a[152:153],v172 offset:1536
ds_read_b64_tr_b16 a[154:155],v172 offset:1792
MF a[120:123],a[184:187],v[164:167],a[120:123]
buffer_load_dwordx4 v[152:155],v10,s[44:47],s67 offen offset:1024
MF a[92:95],a[188:191],v[156:159],a[92:95]
ds_read_b64_tr_b16 a[156:157],v173 offset:1536
MF a[124:127],a[188:191],v[164:167],a[124:127]
ds_read_b64_tr_b16 a[158:159],v173 offset:1792
s_waitcnt lgkmcnt(15)
MF a[64:67],a[192:195],v[160:163],a[64:67]
ds_read_b64_tr_b16 a[160:161],v172 offset:8192
ds_read_b64_tr_b16 a[162:163],v172 offset:8448
s_add_u32 m0,s68,131072
MF a[96:99],a[192:195],v[168:171],a[96:99]
buffer_load_dwordx4 v88,s[48:51],s65 offen lds
MF a[68:71],a[196:199],v[160:163],a[68:71]
ds_read_b64_tr_b16 a[164:165],v173 offset:8192
MF a[100:103],a[196:199],v[168:171],a[100:103]
ds_read_b64_tr_b16 a[166:167],v173 offset:8448
MF a[72:75],a[200:203],v[160:163],a[72:75]
ds_read_b64_tr_b16 a[168:169],v172 offset:8704
ds_read_b64_tr_b16 a[170:171],v172 offset:8960
s_add_u32 m0,s68,132096
MF a[104:107],a[200:203],v[168:171],a[104:107]
buffer_load_dwordx4 v89,s[48:51],s65 offen lds
MF a[76:79],a[204:207],v[160:163],a[76:79]
ds_read_b64_tr_b16 a[172:173],v173 offset:8704
MF a[108:111],a[204:207],v[168:171],a[108:111]
ds_read_b64_tr_b16 a[174:175],v173 offset:8960
MF a[80:83],a[208:211],v[160:163],a[80:83]
ds_read_b64_tr_b16 a[176:177],v172 offset:9216
ds_read_b64_tr_b16 a[178:179],v172 offset:9472
s_add_u32 m0,s68,133120
MF a[112:115],a[208:211],v[168:171],a[112:115]
buffer_load_dwordx4 v90,s[48:51],s65 offen lds
MF a[84:87],a[212:215],v[160:163],a[84:87]
ds_read_b64_tr_b16 a[180:181],v173 offset:9216
MF a[116:119],a[212:215],v[168:171],a[116:119]
ds_read_b64_tr_b16 a[182:183],v173 offset:9472
MF a[88:91],a[216:219],v[160:163],a[88:91]
ds_read_b64_tr_b16 a[184:185],v172 offset:9728
ds_read_b64_tr_b16 a[186:187],v172 offset:9984
s_add_u32 m0,s68,134144
MF a[120:123],a[216:219],v[168:171],a[120:123]
buffer_load_dwordx4 v91,s[48:51],s65 offen lds
MF a[92:95],a[220:223],v[160:163],a[92:95]
ds_read_b64_tr_b16 a[188:189],v173 offset:9728
MF a[124:127],a[220:223],v[168:171],a[124:127]
ds_read_b64_tr_b16 a[190:191],v173 offset:9984
s_add_u32 s65,s65,81920
s_add_u32 s66,s66,2048
s_add_u32 s67,s67,2048
s_lshl_b32 s100,s69,6
s_add_u32 s100,s100,1024
v_mov_b32 v247,s100
s_barrier
s_waitcnt lgkmcnt(15)
MF a[64:67],a[128:131],v[92:95],a[64:67]
ds_read_b64_tr_b16 a[192:193],v174
ds_read_b64_tr_b16 a[194:195],v174 offset:256
MF a[96:99],a[128:131],v[100:103],a[96:99]
global_load_dword v246,v247,s[24:25] sc1
MF a[68:71],a[132:135],v[92:95],a[68:71]
ds_read_b64_tr_b16 a[196:197],v175
MF a[100:103],a[132:135],v[100:103],a[100:103]
ds_read_b64_tr_b16 a[198:199],v175 offset:256
ds_read_b64_tr_b16 a[200:201],v174 offset:512
MF a[72:75],a[136:139],v[92:95],a[72:75]
buffer_load_dwordx4 v[156:159],v10,s[44:47],s66 offen
MF a[104:107],a[136:139],v[100:103],a[104:107]
ds_read_b64_tr_b16 a[202:203],v174 offset:768
MF a[76:79],a[140:143],v[92:95],a[76:79]
ds_read_b64_tr_b16 a[204:205],v175 offset:512
ds_read_b64_tr_b16 a[206:207],v175 offset:768
MF a[108:111],a[140:143],v[100:103],a[108:111]
buffer_load_dwordx4 v[160:163],v10,s[44:47],s66 offen offset:1024
MF a[80:83],a[144:147],v[92:95],a[80:83]
ds_read_b64_tr_b16 a[208:209],v174 offset:1024
MF a[112:115],a[144:147],v[100:103],a[112:115]
ds_read_b64_tr_b16 a[210:211],v174 offset:1280
ds_read_b64_tr_b16 a[212:213],v175 offset:1024
MF a[84:87],a[148:151],v[92:95],a[84:87]
ds_read_b64_tr_b16 a[214:215],v175 offset:1280
MF a[116:119],a[148:151],v[100:103],a[116:119]
buffer_load_dwordx4 v[164:167],v10,s[44:47],s67 offen
MF a[88:91],a[152:155],v[92:95],a[88:91]
ds_read_b64_tr_b16 a[216:217],v174 offset:1536
ds_read_b64_tr_b16 a[218:219],v174 offset:1792
MF a[120:123],a[152:155],v[100:103],a[120:123]
ds_read_b64_tr_b16 a[220:221],v175 offset:1536
MF a[92:95],a[156:159],v[92:95],a[92:95]
buffer_load_dwordx4 v[168:171],v10,s[44:47],s67 offen offset:1024
MF a[124:127],a[156:159],v[100:103],a[124:127]
ds_read_b64_tr_b16 a[222:223],v175 offset:1792
s_waitcnt lgkmcnt(15)
MF a[64:67],a[160:163],v[96:99],a[64:67]
ds_read_b64_tr_b16 a[128:129],v174 offset:8192
ds_read_b64_tr_b16 a[130:131],v174 offset:8448
s_add_u32 m0,s68,0
MF a[96:99],a[160:163],v[104:107],a[96:99]
buffer_load_dwordx4 v88,s[48:51],s65 offen lds
MF a[68:71],a[164:167],v[96:99],a[68:71]
ds_read_b64_tr_b16 a[132:133],v175 offset:8192
MF a[100:103],a[164:167],v[104:107],a[100:103]
ds_read_b64_tr_b16 a[134:135],v175 offset:8448
MF a[72:75],a[168:171],v[96:99],a[72:75]
ds_read_b64_tr_b16 a[136:137],v174 offset:8704
ds_read_b64_tr_b16 a[138:139],v174 offset:8960
s_add_u32 m0,s68,1024
MF a[104:107],a[168:171],v[104:107],a[104:107]
buffer_load_dwordx4 v89,s[48:51],s65 offen lds
MF a[76:79],a[172:175],v[96:99],a[76:79]
ds_read_b64_tr_b16 a[140:141],v175 offset:8704
MF a[108:111],a[172:175],v[104:107],a[108:111]
ds_read_b64_tr_b16 a[142:143],v175 offset:8960
MF a[80:83],a[176:179],v[96:99],a[80:83]
ds_read_b64_tr_b16 a[144:145],v174 offset:9216
ds_read_b64_tr_b16 a[146:147],v174 offset:9472
s_add_u32 m0,s68,2048
MF a[112:115],a[176:179],v[104:107],a[112:115]
buffer_load_dwordx4 v90,s[48:51],s65 offen lds
MF a[84:87],a[180:183],v[96:99],a[84:87]
ds_read_b64_tr_b16 a[148:149],v175 offset:9216
MF a[116:119],a[180:183],v[104:107],a[116:119]
ds_read_b64_tr_b16 a[150:151],v175 offset:9472
MF a[88:91],a[184:187],v[96:99],a[88:91]
ds_read_b64_tr_b16 a[152:153],v174 offset:9728
ds_read_b64_tr_b16 a[154:155],v174 offset:9984
s_add_u32 m0,s68,3072
MF a[120:123],a[184:187],v[104:107],a[120:123]
buffer_load_dwordx4 v91,s[48:51],s65 offen lds
MF a[92:95],a[188:191],v[96:99],a[92:95]
ds_read_b64_tr_b16 a[156:157],v175 offset:9728
MF a[124:127],a[188:191],v[104:107],a[124:127]
ds_read_b64_tr_b16 a[158:159],v175 offset:9984
s_add_u32 s65,s65,81920
s_add_u32 s66,s66,2048
s_add_u32 s67,s67,2048
s_waitcnt vmcnt(8)
s_barrier
s_waitcnt lgkmcnt(15)
MF a[64:67],a[192:195],v[108:111],a[64:67]
ds_read_b64_tr_b16 a[160:161],v176
ds_read_b64_tr_b16 a[162:163],v176 offset:256
MF a[96:99],a[192:195],v[116:119],a[96:99]
buffer_load_dwordx4 v[92:95],v10,s[44:47],s66 offen
MF a[68:71],a[196:199],v[108:111],a[68:71]
ds_read_b64_tr_b16 a[164:165],v177
MF a[100:103],a[196:199],v[116:119],a[100:103]
ds_read_b64_tr_b16 a[166:167],v177 offset:256
MF a[72:75],a[200:203],v[108:111],a[72:75]
ds_read_b64_tr_b16 a[168:169],v176 offset:512
ds_read_b64_tr_b16 a[170:171],v176 offset:768
MF a[104:107],a[200:203],v[116:119],a[104:107]
buffer_load_dwordx4 v[96:99],v10,s[44:47],s66 offen offset:1024
MF a[76:79],a[204:207],v[108:111],a[76:79]
ds_read_b64_tr_b16 a[172:173],v177 offset:512
MF a[108:111],a[204:207],v[116:119],a[108:111]
ds_read_b64_tr_b16 a[174:175],v177 offset:768
MF a[80:83],a[208:211],v[108:111],a[80:83]
ds_read_b64_tr_b16 a[176:177],v176 offset:1024
ds_read_b64_tr_b16 a[178:179],v176 offset:1280
MF a[112:115],a[208:211],v[116:119],a[112:115]
buffer_load_dwordx4 v[100:103],v10,s[44:47],s67 offen
MF a[84:87],a[212:215],v[108:111],a[84:87]
ds_read_b64_tr_b16 a[180:181],v177 offset:1024
MF a[116:119],a[212:215],v[116:119],a[116:119]
ds_read_b64_tr_b16 a[182:183],v177 offset:1280
MF a[88:91],a[216:219],v[108:111],a[88:91]
ds_read_b64_tr_b16 a[184:185],v176 offset:1536
ds_read_b64_tr_b16 a[186:187],v176 offset:1792
MF a[120:123],a[216:219],v[116:119],a[120:123]
buffer_load_dwordx4 v[104:107],v10,s[44:47],s67 offen offset:1024
MF a[92:95],a[220:223],v[108:111],a[92:95]
ds_read_b64_tr_b16 a[188:189],v177 offset:1536
MF a[124:127],a[220:223],v[116:119],a[124:127]
ds_read_b64_tr_b16 a[190:191],v177 offset:1792
s_waitcnt lgkmcnt(15)
MF a[64:67],a[128:131],v[112:115],a[64:67]
ds_read_b64_tr_b16 a[192:193],v176 offset:8192
ds_read_b64_tr_b16 a[194:195],v176 offset:8448
s_add_u32 m0,s68,32768
MF a[96:99],a[128:131],v[120:123],a[96:99]
buffer_load_dwordx4 v88,s[48:51],s65 offen lds
MF a[68:71],a[132:135],v[112:115],a[68:71]
ds_read_b64_tr_b16 a[196:197],v177 offset:8192
MF a[100:103],a[132:135],v[120:123],a[100:103]
ds_read_b64_tr_b16 a[198:199],v177 offset:8448
MF a[72:75],a[136:139],v[112:115],a[72:75]
ds_read_b64_tr_b16 a[200:201],v176 offset:8704
ds_read_b64_tr_b16 a[202:203],v176 offset:8960
s_add_u32 m0,s68,33792
MF a[104:107],a[136:139],v[120:123],a[104:107]
buffer_load_dwordx4 v89,s[48:51],s65 offen lds
MF a[76:79],a[140:143],v[112:115],a[76:79]
ds_read_b64_tr_b16 a[204:205],v177 offset:8704
MF a[108:111],a[140:143],v[120:123],a[108:111]
ds_read_b64_tr_b16 a[206:207],v177 offset:8960
MF a[80:83],a[144:147],v[112:115],a[80:83]
ds_read_b64_tr_b16 a[208:209],v176 offset:9216
ds_read_b64_tr_b16 a[210:211],v176 offset:9472
s_add_u32 m0,s68,34816
MF a[112:115],a[144:147],v[120:123],a[112:115]
buffer_load_dwordx4 v90,s[48:51],s65 offen lds
MF a[84:87],a[148:151],v[112:115],a[84:87]
ds_read_b64_tr_b16 a[212:213],v177 offset:9216
MF a[116:119],a[148:151],v[120:123],a[116:119]
ds_read_b64_tr_b16 a[214:215],v177 offset:9472
MF a[88:91],a[152:155],v[112:115],a[88:91]
ds_read_b64_tr_b16 a[216:217],v176 offset:9728
ds_read_b64_tr_b16 a[218:219],v176 offset:9984
s_add_u32 m0,s68,35840
MF a[120:123],a[152:155],v[120:123],a[120:123]
buffer_load_dwordx4 v91,s[48:51],s65 offen lds
MF a[92:95],a[156:159],v[112:115],a[92:95]
ds_read_b64_tr_b16 a[220:221],v177 offset:9728
MF a[124:127],a[156:159],v[120:123],a[124:127]
ds_read_b64_tr_b16 a[222:223],v177 offset:9984
s_add_u32 s65,s65,81920
s_add_u32 s66,s66,2048
s_add_u32 s67,s67,2048
s_waitcnt vmcnt(8)
s_barrier
s_waitcnt lgkmcnt(15)
MF a[64:67],a[160:163],v[124:127],a[64:67]
ds_read_b64_tr_b16 a[128:129],v178
MF a[96:99],a[160:163],v[132:135],a[96:99]
ds_read_b64_tr_b16 a[130:131],v178 offset:256
MF a[68:71],a[164:167],v[124:127],a[68:71]
ds_read_b64_tr_b16 a[132:133],v179
MF a[100:103],a[164:167],v[132:135],a[100:103]
ds_read_b64_tr_b16 a[134:135],v179 offset:256
MF a[72:75],a[168:171],v[124:127],a[72:75]
ds_read_b64_tr_b16 a[136:137],v178 offset:512
MF a[104:107],a[168:171],v[132:135],a[104:107]
ds_read_b64_tr_b16 a[138:139],v178 offset:768
MF a[76:79],a[172:175],v[124:127],a[76:79]
ds_read_b64_tr_b16 a[140:141],v179 offset:512
MF a[108:111],a[172:175],v[132:135],a[108:111]
ds_read_b64_tr_b16 a[142:143],v179 offset:768
MF a[80:83],a[176:179],v[124:127],a[80:83]
ds_read_b64_tr_b16 a[144:145],v178 offset:1024
MF a[112:115],a[176:179],v[132:135],a[112:115]
ds_read_b64_tr_b16 a[146:147],v178 offset:1280
MF a[84:87],a[180:183],v[124:127],a[84:87]
ds_read_b64_tr_b16 a[148:149],v179 offset:1024
MF a[116:119],a[180:183],v[132:135],a[116:119]
ds_read_b64_tr_b16 a[150:151],v179 offset:1280
MF a[88:91],a[184:187],v[124:127],a[88:91]
ds_read_b64_tr_b16 a[152:153],v178 offset:1536
MF a[120:123],a[184:187],v[132:135],a[120:123]
ds_read_b64_tr_b16 a[154:155],v178 offset:1792
MF a[92:95],a[188:191],v[124:127],a[92:95]
ds_read_b64_tr_b16 a[156:157],v179 offset:1536
MF a[124:127],a[188:191],v[132:135],a[124:127]
ds_read_b64_tr_b16 a[158:159],v179 offset:1792
s_waitcnt lgkmcnt(15)
MF a[64:67],a[192:195],v[128:131],a[64:67]
ds_read_b64_tr_b16 a[160:161],v178 offset:8192
MF a[96:99],a[192:195],v[136:139],a[96:99]
ds_read_b64_tr_b16 a[162:163],v178 offset:8448
MF a[68:71],a[196:199],v[128:131],a[68:71]
ds_read_b64_tr_b16 a[164:165],v179 offset:8192
MF a[100:103],a[196:199],v[136:139],a[100:103]
ds_read_b64_tr_b16 a[166:167],v179 offset:8448
MF a[72:75],a[200:203],v[128:131],a[72:75]
ds_read_b64_tr_b16 a[168:169],v178 offset:8704
MF a[104:107],a[200:203],v[136:139],a[104:107]
ds_read_b64_tr_b16 a[170:171],v178 offset:8960
MF a[76:79],a[204:207],v[128:131],a[76:79]
ds_read_b64_tr_b16 a[172:173],v179 offset:8704
MF a[108:111],a[204:207],v[136:139],a[108:111]
ds_read_b64_tr_b16 a[174:175],v179 offset:8960
MF a[80:83],a[208:211],v[128:131],a[80:83]
ds_read_b64_tr_b16 a[176:177],v178 offset:9216
MF a[112:115],a[208:211],v[136:139],a[112:115]
ds_read_b64_tr_b16 a[178:179],v178 offset:9472
MF a[84:87],a[212:215],v[128:131],a[84:87]
ds_read_b64_tr_b16 a[180:181],v179 offset:9216
MF a[116:119],a[212:215],v[136:139],a[116:119]
ds_read_b64_tr_b16 a[182:183],v179 offset:9472
MF a[88:91],a[216:219],v[128:131],a[88:91]
ds_read_b64_tr_b16 a[184:185],v178 offset:9728
MF a[120:123],a[216:219],v[136:139],a[120:123]
ds_read_b64_tr_b16 a[186:187],v178 offset:9984
MF a[92:95],a[220:223],v[128:131],a[92:95]
ds_read_b64_tr_b16 a[188:189],v179 offset:9728
MF a[124:127],a[220:223],v[136:139],a[124:127]
ds_read_b64_tr_b16 a[190:191],v179 offset:9984
s_waitcnt vmcnt(4)
s_barrier
v_readfirstlane_b32 s100,v246
s_cmp_ge_u32 s100,14
s_cbranch_scc1 LYSOK_%=
s_mov_b32 s96,0
LYS_%=:
s_sleep 2
global_load_dword v246,v247,s[24:25] sc1
s_waitcnt vmcnt(0)
v_readfirstlane_b32 s100,v246
s_add_u32 s96,s96,1
s_cmp_ge_u32 s100,14
s_cbranch_scc1 LYSOK_%=
s_cmp_lt_u32 s96,0x10000
s_cbranch_scc1 LYS_%=
LYSOK_%=:
s_mul_i32 s100,s77,159744
s_lshl_b32 s101,s78,11
s_add_u32 s100,s100,s101
s_add_u32 s100,s100,131072
s_add_u32 s97,s100,159744
s_mul_i32 s101,s78,81920
s_lshl_b32 s96,s70,8
s_add_u32 s101,s101,s96
s_cmp_lg_u32 s71,0
s_cselect_b32 s66,s100,s66
s_cselect_b32 s67,s97,s67
s_cselect_b32 s65,s101,s65
s_cselect_b32 s48,s52,s48
s_cselect_b32 s49,s53,s49
s_cselect_b32 s50,s54,s50
s_waitcnt lgkmcnt(15)
MF a[64:67],a[128:131],v[140:143],a[64:67]
ds_read_b64_tr_b16 a[192:193],v180
ds_read_b64_tr_b16 a[194:195],v180 offset:256
MF a[96:99],a[128:131],v[148:151],a[96:99]
buffer_load_dwordx4 v[108:111],v10,s[44:47],s66 offen sc1
MF a[68:71],a[132:135],v[140:143],a[68:71]
ds_read_b64_tr_b16 a[196:197],v181
MF a[100:103],a[132:135],v[148:151],a[100:103]
ds_read_b64_tr_b16 a[198:199],v181 offset:256
MF a[72:75],a[136:139],v[140:143],a[72:75]
ds_read_b64_tr_b16 a[200:201],v180 offset:512
ds_read_b64_tr_b16 a[202:203],v180 offset:768
MF a[104:107],a[136:139],v[148:151],a[104:107]
buffer_load_dwordx4 v[112:115],v10,s[44:47],s66 offen offset:1024 sc1
MF a[76:79],a[140:143],v[140:143],a[76:79]
ds_read_b64_tr_b16 a[204:205],v181 offset:512
MF a[108:111],a[140:143],v[148:151],a[108:111]
ds_read_b64_tr_b16 a[206:207],v181 offset:768
MF a[80:83],a[144:147],v[140:143],a[80:83]
ds_read_b64_tr_b16 a[208:209],v180 offset:1024
ds_read_b64_tr_b16 a[210:211],v180 offset:1280
MF a[112:115],a[144:147],v[148:151],a[112:115]
buffer_load_dwordx4 v[116:119],v10,s[44:47],s67 offen sc1
MF a[84:87],a[148:151],v[140:143],a[84:87]
ds_read_b64_tr_b16 a[212:213],v181 offset:1024
MF a[116:119],a[148:151],v[148:151],a[116:119]
ds_read_b64_tr_b16 a[214:215],v181 offset:1280
MF a[88:91],a[152:155],v[140:143],a[88:91]
ds_read_b64_tr_b16 a[216:217],v180 offset:1536
ds_read_b64_tr_b16 a[218:219],v180 offset:1792
MF a[120:123],a[152:155],v[148:151],a[120:123]
buffer_load_dwordx4 v[120:123],v10,s[44:47],s67 offen offset:1024 sc1
MF a[92:95],a[156:159],v[140:143],a[92:95]
ds_read_b64_tr_b16 a[220:221],v181 offset:1536
MF a[124:127],a[156:159],v[148:151],a[124:127]
ds_read_b64_tr_b16 a[222:223],v181 offset:1792
s_waitcnt lgkmcnt(15)
MF a[64:67],a[160:163],v[144:147],a[64:67]
ds_read_b64_tr_b16 a[128:129],v180 offset:8192
ds_read_b64_tr_b16 a[130:131],v180 offset:8448
s_add_u32 m0,s68,65536
MF a[96:99],a[160:163],v[152:155],a[96:99]
buffer_load_dwordx4 v88,s[48:51],s65 offen lds
MF a[68:71],a[164:167],v[144:147],a[68:71]
ds_read_b64_tr_b16 a[132:133],v181 offset:8192
MF a[100:103],a[164:167],v[152:155],a[100:103]
ds_read_b64_tr_b16 a[134:135],v181 offset:8448
MF a[72:75],a[168:171],v[144:147],a[72:75]
ds_read_b64_tr_b16 a[136:137],v180 offset:8704
ds_read_b64_tr_b16 a[138:139],v180 offset:8960
s_add_u32 m0,s68,66560
MF a[104:107],a[168:171],v[152:155],a[104:107]
buffer_load_dwordx4 v89,s[48:51],s65 offen lds
MF a[76:79],a[172:175],v[144:147],a[76:79]
ds_read_b64_tr_b16 a[140:141],v181 offset:8704
MF a[108:111],a[172:175],v[152:155],a[108:111]
ds_read_b64_tr_b16 a[142:143],v181 offset:8960
MF a[80:83],a[176:179],v[144:147],a[80:83]
ds_read_b64_tr_b16 a[144:145],v180 offset:9216
ds_read_b64_tr_b16 a[146:147],v180 offset:9472
s_add_u32 m0,s68,67584
MF a[112:115],a[176:179],v[152:155],a[112:115]
buffer_load_dwordx4 v90,s[48:51],s65 offen lds
MF a[84:87],a[180:183],v[144:147],a[84:87]
ds_read_b64_tr_b16 a[148:149],v181 offset:9216
MF a[116:119],a[180:183],v[152:155],a[116:119]
ds_read_b64_tr_b16 a[150:151],v181 offset:9472
MF a[88:91],a[184:187],v[144:147],a[88:91]
ds_read_b64_tr_b16 a[152:153],v180 offset:9728
ds_read_b64_tr_b16 a[154:155],v180 offset:9984
s_add_u32 m0,s68,68608
MF a[120:123],a[184:187],v[152:155],a[120:123]
buffer_load_dwordx4 v91,s[48:51],s65 offen lds
MF a[92:95],a[188:191],v[144:147],a[92:95]
ds_read_b64_tr_b16 a[156:157],v181 offset:9728
MF a[124:127],a[188:191],v[152:155],a[124:127]
ds_read_b64_tr_b16 a[158:159],v181 offset:9984
s_add_u32 s65,s65,81920
s_add_u32 s66,s66,2048
s_add_u32 s67,s67,2048
s_waitcnt vmcnt(4)
s_barrier
s_mul_i32 s100,s77,159744
s_lshl_b32 s101,s78,11
s_add_u32 s100,s100,s101
s_add_u32 s100,s100,131072
s_add_u32 s97,s100,159744
s_mul_i32 s101,s78,81920
s_lshl_b32 s96,s70,8
s_add_u32 s101,s101,s96
s_cmp_eq_u32 s71,0
s_cselect_b32 s66,s100,s66
s_cselect_b32 s67,s97,s67
s_cselect_b32 s65,s101,s65
s_cselect_b32 s48,s52,s48
s_cselect_b32 s49,s53,s49
s_cselect_b32 s50,s54,s50
s_waitcnt lgkmcnt(15)
MF a[64:67],a[192:195],v[156:159],a[64:67]
ds_read_b64_tr_b16 a[160:161],v172
ds_read_b64_tr_b16 a[162:163],v172 offset:256
MF a[96:99],a[192:195],v[164:167],a[96:99]
buffer_load_dwordx4 v[124:127],v10,s[44:47],s66 offen sc1
MF a[68:71],a[196:199],v[156:159],a[68:71]
ds_read_b64_tr_b16 a[164:165],v173
MF a[100:103],a[196:199],v[164:167],a[100:103]
ds_read_b64_tr_b16 a[166:167],v173 offset:256
MF a[72:75],a[200:203],v[156:159],a[72:75]
ds_read_b64_tr_b16 a[168:169],v172 offset:512
ds_read_b64_tr_b16 a[170:171],v172 offset:768
MF a[104:107],a[200:203],v[164:167],a[104:107]
buffer_load_dwordx4 v[128:131],v10,s[44:47],s66 offen offset:1024 sc1
MF a[76:79],a[204:207],v[156:159],a[76:79]
ds_read_b64_tr_b16 a[172:173],v173 offset:512
MF a[108:111],a[204:207],v[164:167],a[108:111]
ds_read_b64_tr_b16 a[174:175],v173 offset:768
MF a[80:83],a[208:211],v[156:159],a[80:83]
ds_read_b64_tr_b16 a[176:177],v172 offset:1024
ds_read_b64_tr_b16 a[178:179],v172 offset:1280
MF a[112:115],a[208:211],v[164:167],a[112:115]
buffer_load_dwordx4 v[132:135],v10,s[44:47],s67 offen sc1
MF a[84:87],a[212:215],v[156:159],a[84:87]
ds_read_b64_tr_b16 a[180:181],v173 offset:1024
MF a[116:119],a[212:215],v[164:167],a[116:119]
ds_read_b64_tr_b16 a[182:183],v173 offset:1280
MF a[88:91],a[216:219],v[156:159],a[88:91]
ds_read_b64_tr_b16 a[184:185],v172 offset:1536
ds_read_b64_tr_b16 a[186:187],v172 offset:1792
MF a[120:123],a[216:219],v[164:167],a[120:123]
buffer_load_dwordx4 v[136:139],v10,s[44:47],s67 offen offset:1024 sc1
MF a[92:95],a[220:223],v[156:159],a[92:95]
ds_read_b64_tr_b16 a[188:189],v173 offset:1536
MF a[124:127],a[220:223],v[164:167],a[124:127]
ds_read_b64_tr_b16 a[190:191],v173 offset:1792
s_waitcnt lgkmcnt(15)
MF a[64:67],a[128:131],v[160:163],a[64:67]
ds_read_b64_tr_b16 a[192:193],v172 offset:8192
ds_read_b64_tr_b16 a[194:195],v172 offset:8448
s_add_u32 m0,s68,98304
MF a[96:99],a[128:131],v[168:171],a[96:99]
buffer_load_dwordx4 v88,s[48:51],s65 offen lds
MF a[68:71],a[132:135],v[160:163],a[68:71]
ds_read_b64_tr_b16 a[196:197],v173 offset:8192
MF a[100:103],a[132:135],v[168:171],a[100:103]
ds_read_b64_tr_b16 a[198:199],v173 offset:8448
MF a[72:75],a[136:139],v[160:163],a[72:75]
ds_read_b64_tr_b16 a[200:201],v172 offset:8704
ds_read_b64_tr_b16 a[202:203],v172 offset:8960
s_add_u32 m0,s68,99328
MF a[104:107],a[136:139],v[168:171],a[104:107]
buffer_load_dwordx4 v89,s[48:51],s65 offen lds
MF a[76:79],a[140:143],v[160:163],a[76:79]
ds_read_b64_tr_b16 a[204:205],v173 offset:8704
MF a[108:111],a[140:143],v[168:171],a[108:111]
ds_read_b64_tr_b16 a[206:207],v173 offset:8960
MF a[80:83],a[144:147],v[160:163],a[80:83]
ds_read_b64_tr_b16 a[208:209],v172 offset:9216
ds_read_b64_tr_b16 a[210:211],v172 offset:9472
s_add_u32 m0,s68,100352
MF a[112:115],a[144:147],v[168:171],a[112:115]
buffer_load_dwordx4 v90,s[48:51],s65 offen lds
MF a[84:87],a[148:151],v[160:163],a[84:87]
ds_read_b64_tr_b16 a[212:213],v173 offset:9216
MF a[116:119],a[148:151],v[168:171],a[116:119]
ds_read_b64_tr_b16 a[214:215],v173 offset:9472
MF a[88:91],a[152:155],v[160:163],a[88:91]
ds_read_b64_tr_b16 a[216:217],v172 offset:9728
ds_read_b64_tr_b16 a[218:219],v172 offset:9984
s_add_u32 m0,s68,101376
MF a[120:123],a[152:155],v[168:171],a[120:123]
buffer_load_dwordx4 v91,s[48:51],s65 offen lds
MF a[92:95],a[156:159],v[160:163],a[92:95]
ds_read_b64_tr_b16 a[220:221],v173 offset:9728
MF a[124:127],a[156:159],v[168:171],a[124:127]
ds_read_b64_tr_b16 a[222:223],v173 offset:9984
s_add_u32 s65,s65,81920
s_add_u32 s66,s66,2048
s_add_u32 s67,s67,2048
s_waitcnt vmcnt(4)
s_barrier
s_waitcnt lgkmcnt(15)
MF a[64:67],a[160:163],v[92:95],a[64:67]
ds_read_b64_tr_b16 a[128:129],v174
ds_read_b64_tr_b16 a[130:131],v174 offset:256
MF a[96:99],a[160:163],v[100:103],a[96:99]
buffer_load_dwordx4 v[140:143],v10,s[44:47],s66 offen sc1
MF a[68:71],a[164:167],v[92:95],a[68:71]
ds_read_b64_tr_b16 a[132:133],v175
MF a[100:103],a[164:167],v[100:103],a[100:103]
ds_read_b64_tr_b16 a[134:135],v175 offset:256
MF a[72:75],a[168:171],v[92:95],a[72:75]
ds_read_b64_tr_b16 a[136:137],v174 offset:512
ds_read_b64_tr_b16 a[138:139],v174 offset:768
MF a[104:107],a[168:171],v[100:103],a[104:107]
buffer_load_dwordx4 v[144:147],v10,s[44:47],s66 offen offset:1024 sc1
MF a[76:79],a[172:175],v[92:95],a[76:79]
ds_read_b64_tr_b16 a[140:141],v175 offset:512
MF a[108:111],a[172:175],v[100:103],a[108:111]
ds_read_b64_tr_b16 a[142:143],v175 offset:768
MF a[80:83],a[176:179],v[92:95],a[80:83]
ds_read_b64_tr_b16 a[144:145],v174 offset:1024
ds_read_b64_tr_b16 a[146:147],v174 offset:1280
MF a[112:115],a[176:179],v[100:103],a[112:115]
buffer_load_dwordx4 v[148:151],v10,s[44:47],s67 offen sc1
MF a[84:87],a[180:183],v[92:95],a[84:87]
ds_read_b64_tr_b16 a[148:149],v175 offset:1024
MF a[116:119],a[180:183],v[100:103],a[116:119]
ds_read_b64_tr_b16 a[150:151],v175 offset:1280
MF a[88:91],a[184:187],v[92:95],a[88:91]
ds_read_b64_tr_b16 a[152:153],v174 offset:1536
ds_read_b64_tr_b16 a[154:155],v174 offset:1792
MF a[120:123],a[184:187],v[100:103],a[120:123]
buffer_load_dwordx4 v[152:155],v10,s[44:47],s67 offen offset:1024 sc1
MF a[92:95],a[188:191],v[92:95],a[92:95]
ds_read_b64_tr_b16 a[156:157],v175 offset:1536
MF a[124:127],a[188:191],v[100:103],a[124:127]
ds_read_b64_tr_b16 a[158:159],v175 offset:1792
s_waitcnt lgkmcnt(15)
MF a[64:67],a[192:195],v[96:99],a[64:67]
ds_read_b64_tr_b16 a[160:161],v174 offset:8192
ds_read_b64_tr_b16 a[162:163],v174 offset:8448
s_add_u32 m0,s68,131072
MF a[96:99],a[192:195],v[104:107],a[96:99]
buffer_load_dwordx4 v88,s[48:51],s65 offen lds
MF a[68:71],a[196:199],v[96:99],a[68:71]
ds_read_b64_tr_b16 a[164:165],v175 offset:8192
MF a[100:103],a[196:199],v[104:107],a[100:103]
ds_read_b64_tr_b16 a[166:167],v175 offset:8448
MF a[72:75],a[200:203],v[96:99],a[72:75]
ds_read_b64_tr_b16 a[168:169],v174 offset:8704
ds_read_b64_tr_b16 a[170:171],v174 offset:8960
s_add_u32 m0,s68,132096
MF a[104:107],a[200:203],v[104:107],a[104:107]
buffer_load_dwordx4 v89,s[48:51],s65 offen lds
MF a[76:79],a[204:207],v[96:99],a[76:79]
ds_read_b64_tr_b16 a[172:173],v175 offset:8704
MF a[108:111],a[204:207],v[104:107],a[108:111]
ds_read_b64_tr_b16 a[174:175],v175 offset:8960
MF a[80:83],a[208:211],v[96:99],a[80:83]
ds_read_b64_tr_b16 a[176:177],v174 offset:9216
ds_read_b64_tr_b16 a[178:179],v174 offset:9472
s_add_u32 m0,s68,133120
MF a[112:115],a[208:211],v[104:107],a[112:115]
buffer_load_dwordx4 v90,s[48:51],s65 offen lds
MF a[84:87],a[212:215],v[96:99],a[84:87]
ds_read_b64_tr_b16 a[180:181],v175 offset:9216
MF a[116:119],a[212:215],v[104:107],a[116:119]
ds_read_b64_tr_b16 a[182:183],v175 offset:9472
MF a[88:91],a[216:219],v[96:99],a[88:91]
ds_read_b64_tr_b16 a[184:185],v174 offset:9728
ds_read_b64_tr_b16 a[186:187],v174 offset:9984
s_add_u32 m0,s68,134144
MF a[120:123],a[216:219],v[104:107],a[120:123]
buffer_load_dwordx4 v91,s[48:51],s65 offen lds
MF a[92:95],a[220:223],v[96:99],a[92:95]
ds_read_b64_tr_b16 a[188:189],v175 offset:9728
MF a[124:127],a[220:223],v[104:107],a[124:127]
ds_read_b64_tr_b16 a[190:191],v175 offset:9984
s_add_u32 s65,s65,81920
s_add_u32 s66,s66,2048
s_add_u32 s67,s67,2048
s_waitcnt vmcnt(4)
s_barrier
s_waitcnt lgkmcnt(15)
MF a[64:67],a[128:131],v[108:111],a[64:67]
ds_read_b64_tr_b16 a[192:193],v176
ds_read_b64_tr_b16 a[194:195],v176 offset:256
MF a[96:99],a[128:131],v[116:119],a[96:99]
buffer_load_dwordx4 v[156:159],v10,s[44:47],s66 offen sc1
MF a[68:71],a[132:135],v[108:111],a[68:71]
ds_read_b64_tr_b16 a[196:197],v177
MF a[100:103],a[132:135],v[116:119],a[100:103]
ds_read_b64_tr_b16 a[198:199],v177 offset:256
MF a[72:75],a[136:139],v[108:111],a[72:75]
ds_read_b64_tr_b16 a[200:201],v176 offset:512
ds_read_b64_tr_b16 a[202:203],v176 offset:768
MF a[104:107],a[136:139],v[116:119],a[104:107]
buffer_load_dwordx4 v[160:163],v10,s[44:47],s66 offen offset:1024 sc1
MF a[76:79],a[140:143],v[108:111],a[76:79]
ds_read_b64_tr_b16 a[204:205],v177 offset:512
MF a[108:111],a[140:143],v[116:119],a[108:111]
ds_read_b64_tr_b16 a[206:207],v177 offset:768
MF a[80:83],a[144:147],v[108:111],a[80:83]
ds_read_b64_tr_b16 a[208:209],v176 offset:1024
ds_read_b64_tr_b16 a[210:211],v176 offset:1280
MF a[112:115],a[144:147],v[116:119],a[112:115]
buffer_load_dwordx4 v[164:167],v10,s[44:47],s67 offen sc1
MF a[84:87],a[148:151],v[108:111],a[84:87]
ds_read_b64_tr_b16 a[212:213],v177 offset:1024
MF a[116:119],a[148:151],v[116:119],a[116:119]
ds_read_b64_tr_b16 a[214:215],v177 offset:1280
MF a[88:91],a[152:155],v[108:111],a[88:91]
ds_read_b64_tr_b16 a[216:217],v176 offset:1536
ds_read_b64_tr_b16 a[218:219],v176 offset:1792
MF a[120:123],a[152:155],v[116:119],a[120:123]
buffer_load_dwordx4 v[168:171],v10,s[44:47],s67 offen offset:1024 sc1
MF a[92:95],a[156:159],v[108:111],a[92:95]
ds_read_b64_tr_b16 a[220:221],v177 offset:1536
MF a[124:127],a[156:159],v[116:119],a[124:127]
ds_read_b64_tr_b16 a[222:223],v177 offset:1792
s_waitcnt lgkmcnt(15)
MF a[64:67],a[160:163],v[112:115],a[64:67]
ds_read_b64_tr_b16 a[128:129],v176 offset:8192
ds_read_b64_tr_b16 a[130:131],v176 offset:8448
s_add_u32 m0,s68,0
MF a[96:99],a[160:163],v[120:123],a[96:99]
buffer_load_dwordx4 v88,s[48:51],s65 offen lds
MF a[68:71],a[164:167],v[112:115],a[68:71]
ds_read_b64_tr_b16 a[132:133],v177 offset:8192
MF a[100:103],a[164:167],v[120:123],a[100:103]
ds_read_b64_tr_b16 a[134:135],v177 offset:8448
MF a[72:75],a[168:171],v[112:115],a[72:75]
ds_read_b64_tr_b16 a[136:137],v176 offset:8704
ds_read_b64_tr_b16 a[138:139],v176 offset:8960
s_add_u32 m0,s68,1024
MF a[104:107],a[168:171],v[120:123],a[104:107]
buffer_load_dwordx4 v89,s[48:51],s65 offen lds
MF a[76:79],a[172:175],v[112:115],a[76:79]
ds_read_b64_tr_b16 a[140:141],v177 offset:8704
MF a[108:111],a[172:175],v[120:123],a[108:111]
ds_read_b64_tr_b16 a[142:143],v177 offset:8960
MF a[80:83],a[176:179],v[112:115],a[80:83]
ds_read_b64_tr_b16 a[144:145],v176 offset:9216
ds_read_b64_tr_b16 a[146:147],v176 offset:9472
s_add_u32 m0,s68,2048
MF a[112:115],a[176:179],v[120:123],a[112:115]
buffer_load_dwordx4 v90,s[48:51],s65 offen lds
MF a[84:87],a[180:183],v[112:115],a[84:87]
ds_read_b64_tr_b16 a[148:149],v177 offset:9216
MF a[116:119],a[180:183],v[120:123],a[116:119]
ds_read_b64_tr_b16 a[150:151],v177 offset:9472
MF a[88:91],a[184:187],v[112:115],a[88:91]
ds_read_b64_tr_b16 a[152:153],v176 offset:9728
ds_read_b64_tr_b16 a[154:155],v176 offset:9984
s_add_u32 m0,s68,3072
MF a[120:123],a[184:187],v[120:123],a[120:123]
buffer_load_dwordx4 v91,s[48:51],s65 offen lds
MF a[92:95],a[188:191],v[112:115],a[92:95]
ds_read_b64_tr_b16 a[156:157],v177 offset:9728
MF a[124:127],a[188:191],v[120:123],a[124:127]
ds_read_b64_tr_b16 a[158:159],v177 offset:9984
s_add_u32 s65,s65,81920
s_add_u32 s66,s66,2048
s_add_u32 s67,s67,2048
s_waitcnt vmcnt(4)
s_barrier
s_waitcnt lgkmcnt(15)
MF a[64:67],a[192:195],v[124:127],a[64:67]
ds_read_b64_tr_b16 a[160:161],v178
ds_read_b64_tr_b16 a[162:163],v178 offset:256
MF a[96:99],a[192:195],v[132:135],a[96:99]
buffer_load_dwordx4 v[92:95],v10,s[44:47],s66 offen sc1
MF a[68:71],a[196:199],v[124:127],a[68:71]
ds_read_b64_tr_b16 a[164:165],v179
MF a[100:103],a[196:199],v[132:135],a[100:103]
ds_read_b64_tr_b16 a[166:167],v179 offset:256
MF a[72:75],a[200:203],v[124:127],a[72:75]
ds_read_b64_tr_b16 a[168:169],v178 offset:512
ds_read_b64_tr_b16 a[170:171],v178 offset:768
MF a[104:107],a[200:203],v[132:135],a[104:107]
buffer_load_dwordx4 v[96:99],v10,s[44:47],s66 offen offset:1024 sc1
MF a[76:79],a[204:207],v[124:127],a[76:79]
ds_read_b64_tr_b16 a[172:173],v179 offset:512
MF a[108:111],a[204:207],v[132:135],a[108:111]
ds_read_b64_tr_b16 a[174:175],v179 offset:768
MF a[80:83],a[208:211],v[124:127],a[80:83]
ds_read_b64_tr_b16 a[176:177],v178 offset:1024
ds_read_b64_tr_b16 a[178:179],v178 offset:1280
MF a[112:115],a[208:211],v[132:135],a[112:115]
buffer_load_dwordx4 v[100:103],v10,s[44:47],s67 offen sc1
MF a[84:87],a[212:215],v[124:127],a[84:87]
ds_read_b64_tr_b16 a[180:181],v179 offset:1024
MF a[116:119],a[212:215],v[132:135],a[116:119]
ds_read_b64_tr_b16 a[182:183],v179 offset:1280
MF a[88:91],a[216:219],v[124:127],a[88:91]
ds_read_b64_tr_b16 a[184:185],v178 offset:1536
ds_read_b64_tr_b16 a[186:187],v178 offset:1792
MF a[120:123],a[216:219],v[132:135],a[120:123]
buffer_load_dwordx4 v[104:107],v10,s[44:47],s67 offen offset:1024 sc1
)A"
R"A(MF a[92:95],a[220:223],v[124:127],a[92:95]
ds_read_b64_tr_b16 a[188:189],v179 offset:1536
MF a[124:127],a[220:223],v[132:135],a[124:127]
ds_read_b64_tr_b16 a[190:191],v179 offset:1792
s_waitcnt lgkmcnt(15)
MF a[64:67],a[128:131],v[128:131],a[64:67]
ds_read_b64_tr_b16 a[192:193],v178 offset:8192
ds_read_b64_tr_b16 a[194:195],v178 offset:8448
s_add_u32 m0,s68,32768
MF a[96:99],a[128:131],v[136:139],a[96:99]
buffer_load_dwordx4 v88,s[48:51],s65 offen lds
MF a[68:71],a[132:135],v[128:131],a[68:71]
ds_read_b64_tr_b16 a[196:197],v179 offset:8192
MF a[100:103],a[132:135],v[136:139],a[100:103]
ds_read_b64_tr_b16 a[198:199],v179 offset:8448
MF a[72:75],a[136:139],v[128:131],a[72:75]
ds_read_b64_tr_b16 a[200:201],v178 offset:8704
ds_read_b64_tr_b16 a[202:203],v178 offset:8960
s_add_u32 m0,s68,33792
MF a[104:107],a[136:139],v[136:139],a[104:107]
buffer_load_dwordx4 v89,s[48:51],s65 offen lds
MF a[76:79],a[140:143],v[128:131],a[76:79]
ds_read_b64_tr_b16 a[204:205],v179 offset:8704
MF a[108:111],a[140:143],v[136:139],a[108:111]
ds_read_b64_tr_b16 a[206:207],v179 offset:8960
MF a[80:83],a[144:147],v[128:131],a[80:83]
ds_read_b64_tr_b16 a[208:209],v178 offset:9216
ds_read_b64_tr_b16 a[210:211],v178 offset:9472
s_add_u32 m0,s68,34816
MF a[112:115],a[144:147],v[136:139],a[112:115]
buffer_load_dwordx4 v90,s[48:51],s65 offen lds
MF a[84:87],a[148:151],v[128:131],a[84:87]
ds_read_b64_tr_b16 a[212:213],v179 offset:9216
MF a[116:119],a[148:151],v[136:139],a[116:119]
ds_read_b64_tr_b16 a[214:215],v179 offset:9472
MF a[88:91],a[152:155],v[128:131],a[88:91]
ds_read_b64_tr_b16 a[216:217],v178 offset:9728
ds_read_b64_tr_b16 a[218:219],v178 offset:9984
s_add_u32 m0,s68,35840
MF a[120:123],a[152:155],v[136:139],a[120:123]
buffer_load_dwordx4 v91,s[48:51],s65 offen lds
MF a[92:95],a[156:159],v[128:131],a[92:95]
ds_read_b64_tr_b16 a[220:221],v179 offset:9728
MF a[124:127],a[156:159],v[136:139],a[124:127]
ds_read_b64_tr_b16 a[222:223],v179 offset:9984
s_add_u32 s65,s65,81920
s_add_u32 s66,s66,2048
s_add_u32 s67,s67,2048
s_waitcnt vmcnt(4)
s_barrier
s_waitcnt lgkmcnt(15)
MF a[64:67],a[160:163],v[140:143],a[64:67]
ds_read_b64_tr_b16 a[128:129],v180
MF a[96:99],a[160:163],v[148:151],a[96:99]
ds_read_b64_tr_b16 a[130:131],v180 offset:256
MF a[68:71],a[164:167],v[140:143],a[68:71]
ds_read_b64_tr_b16 a[132:133],v181
MF a[100:103],a[164:167],v[148:151],a[100:103]
ds_read_b64_tr_b16 a[134:135],v181 offset:256
MF a[72:75],a[168:171],v[140:143],a[72:75]
ds_read_b64_tr_b16 a[136:137],v180 offset:512
MF a[104:107],a[168:171],v[148:151],a[104:107]
ds_read_b64_tr_b16 a[138:139],v180 offset:768
MF a[76:79],a[172:175],v[140:143],a[76:79]
ds_read_b64_tr_b16 a[140:141],v181 offset:512
MF a[108:111],a[172:175],v[148:151],a[108:111]
ds_read_b64_tr_b16 a[142:143],v181 offset:768
MF a[80:83],a[176:179],v[140:143],a[80:83]
ds_read_b64_tr_b16 a[144:145],v180 offset:1024
MF a[112:115],a[176:179],v[148:151],a[112:115]
ds_read_b64_tr_b16 a[146:147],v180 offset:1280
MF a[84:87],a[180:183],v[140:143],a[84:87]
ds_read_b64_tr_b16 a[148:149],v181 offset:1024
MF a[116:119],a[180:183],v[148:151],a[116:119]
ds_read_b64_tr_b16 a[150:151],v181 offset:1280
MF a[88:91],a[184:187],v[140:143],a[88:91]
ds_read_b64_tr_b16 a[152:153],v180 offset:1536
MF a[120:123],a[184:187],v[148:151],a[120:123]
ds_read_b64_tr_b16 a[154:155],v180 offset:1792
MF a[92:95],a[188:191],v[140:143],a[92:95]
ds_read_b64_tr_b16 a[156:157],v181 offset:1536
MF a[124:127],a[188:191],v[148:151],a[124:127]
ds_read_b64_tr_b16 a[158:159],v181 offset:1792
s_waitcnt lgkmcnt(15)
MF a[64:67],a[192:195],v[144:147],a[64:67]
ds_read_b64_tr_b16 a[160:161],v180 offset:8192
MF a[96:99],a[192:195],v[152:155],a[96:99]
ds_read_b64_tr_b16 a[162:163],v180 offset:8448
MF a[68:71],a[196:199],v[144:147],a[68:71]
ds_read_b64_tr_b16 a[164:165],v181 offset:8192
MF a[100:103],a[196:199],v[152:155],a[100:103]
ds_read_b64_tr_b16 a[166:167],v181 offset:8448
MF a[72:75],a[200:203],v[144:147],a[72:75]
ds_read_b64_tr_b16 a[168:169],v180 offset:8704
MF a[104:107],a[200:203],v[152:155],a[104:107]
ds_read_b64_tr_b16 a[170:171],v180 offset:8960
MF a[76:79],a[204:207],v[144:147],a[76:79]
ds_read_b64_tr_b16 a[172:173],v181 offset:8704
MF a[108:111],a[204:207],v[152:155],a[108:111]
ds_read_b64_tr_b16 a[174:175],v181 offset:8960
MF a[80:83],a[208:211],v[144:147],a[80:83]
ds_read_b64_tr_b16 a[176:177],v180 offset:9216
MF a[112:115],a[208:211],v[152:155],a[112:115]
ds_read_b64_tr_b16 a[178:179],v180 offset:9472
MF a[84:87],a[212:215],v[144:147],a[84:87]
ds_read_b64_tr_b16 a[180:181],v181 offset:9216
MF a[116:119],a[212:215],v[152:155],a[116:119]
ds_read_b64_tr_b16 a[182:183],v181 offset:9472
MF a[88:91],a[216:219],v[144:147],a[88:91]
ds_read_b64_tr_b16 a[184:185],v180 offset:9728
MF a[120:123],a[216:219],v[152:155],a[120:123]
ds_read_b64_tr_b16 a[186:187],v180 offset:9984
MF a[92:95],a[220:223],v[144:147],a[92:95]
ds_read_b64_tr_b16 a[188:189],v181 offset:9728
MF a[124:127],a[220:223],v[152:155],a[124:127]
ds_read_b64_tr_b16 a[190:191],v181 offset:9984
s_waitcnt vmcnt(0)
s_barrier
s_waitcnt lgkmcnt(15)
MF a[64:67],a[128:131],v[156:159],a[64:67]
ds_read_b64_tr_b16 a[192:193],v172
MF a[96:99],a[128:131],v[164:167],a[96:99]
ds_read_b64_tr_b16 a[194:195],v172 offset:256
MF a[68:71],a[132:135],v[156:159],a[68:71]
ds_read_b64_tr_b16 a[196:197],v173
MF a[100:103],a[132:135],v[164:167],a[100:103]
ds_read_b64_tr_b16 a[198:199],v173 offset:256
MF a[72:75],a[136:139],v[156:159],a[72:75]
ds_read_b64_tr_b16 a[200:201],v172 offset:512
MF a[104:107],a[136:139],v[164:167],a[104:107]
ds_read_b64_tr_b16 a[202:203],v172 offset:768
MF a[76:79],a[140:143],v[156:159],a[76:79]
ds_read_b64_tr_b16 a[204:205],v173 offset:512
MF a[108:111],a[140:143],v[164:167],a[108:111]
ds_read_b64_tr_b16 a[206:207],v173 offset:768
MF a[80:83],a[144:147],v[156:159],a[80:83]
ds_read_b64_tr_b16 a[208:209],v172 offset:1024
MF a[112:115],a[144:147],v[164:167],a[112:115]
ds_read_b64_tr_b16 a[210:211],v172 offset:1280
MF a[84:87],a[148:151],v[156:159],a[84:87]
ds_read_b64_tr_b16 a[212:213],v173 offset:1024
MF a[116:119],a[148:151],v[164:167],a[116:119]
ds_read_b64_tr_b16 a[214:215],v173 offset:1280
MF a[88:91],a[152:155],v[156:159],a[88:91]
ds_read_b64_tr_b16 a[216:217],v172 offset:1536
MF a[120:123],a[152:155],v[164:167],a[120:123]
ds_read_b64_tr_b16 a[218:219],v172 offset:1792
MF a[92:95],a[156:159],v[156:159],a[92:95]
ds_read_b64_tr_b16 a[220:221],v173 offset:1536
MF a[124:127],a[156:159],v[164:167],a[124:127]
ds_read_b64_tr_b16 a[222:223],v173 offset:1792
s_waitcnt lgkmcnt(15)
MF a[64:67],a[160:163],v[160:163],a[64:67]
ds_read_b64_tr_b16 a[128:129],v172 offset:8192
MF a[96:99],a[160:163],v[168:171],a[96:99]
ds_read_b64_tr_b16 a[130:131],v172 offset:8448
MF a[68:71],a[164:167],v[160:163],a[68:71]
ds_read_b64_tr_b16 a[132:133],v173 offset:8192
MF a[100:103],a[164:167],v[168:171],a[100:103]
ds_read_b64_tr_b16 a[134:135],v173 offset:8448
MF a[72:75],a[168:171],v[160:163],a[72:75]
ds_read_b64_tr_b16 a[136:137],v172 offset:8704
MF a[104:107],a[168:171],v[168:171],a[104:107]
ds_read_b64_tr_b16 a[138:139],v172 offset:8960
MF a[76:79],a[172:175],v[160:163],a[76:79]
ds_read_b64_tr_b16 a[140:141],v173 offset:8704
MF a[108:111],a[172:175],v[168:171],a[108:111]
ds_read_b64_tr_b16 a[142:143],v173 offset:8960
MF a[80:83],a[176:179],v[160:163],a[80:83]
ds_read_b64_tr_b16 a[144:145],v172 offset:9216
MF a[112:115],a[176:179],v[168:171],a[112:115]
ds_read_b64_tr_b16 a[146:147],v172 offset:9472
MF a[84:87],a[180:183],v[160:163],a[84:87]
ds_read_b64_tr_b16 a[148:149],v173 offset:9216
MF a[116:119],a[180:183],v[168:171],a[116:119]
ds_read_b64_tr_b16 a[150:151],v173 offset:9472
MF a[88:91],a[184:187],v[160:163],a[88:91]
ds_read_b64_tr_b16 a[152:153],v172 offset:9728
MF a[120:123],a[184:187],v[168:171],a[120:123]
ds_read_b64_tr_b16 a[154:155],v172 offset:9984
MF a[92:95],a[188:191],v[160:163],a[92:95]
ds_read_b64_tr_b16 a[156:157],v173 offset:9728
MF a[124:127],a[188:191],v[168:171],a[124:127]
ds_read_b64_tr_b16 a[158:159],v173 offset:9984
s_waitcnt lgkmcnt(15)
MF a[64:67],a[192:195],v[92:95],a[64:67]
MF a[96:99],a[192:195],v[100:103],a[96:99]
MF a[68:71],a[196:199],v[92:95],a[68:71]
MF a[100:103],a[196:199],v[100:103],a[100:103]
MF a[72:75],a[200:203],v[92:95],a[72:75]
MF a[104:107],a[200:203],v[100:103],a[104:107]
MF a[76:79],a[204:207],v[92:95],a[76:79]
MF a[108:111],a[204:207],v[100:103],a[108:111]
MF a[80:83],a[208:211],v[92:95],a[80:83]
MF a[112:115],a[208:211],v[100:103],a[112:115]
MF a[84:87],a[212:215],v[92:95],a[84:87]
MF a[116:119],a[212:215],v[100:103],a[116:119]
MF a[88:91],a[216:219],v[92:95],a[88:91]
MF a[120:123],a[216:219],v[100:103],a[120:123]
MF a[92:95],a[220:223],v[92:95],a[92:95]
MF a[124:127],a[220:223],v[100:103],a[124:127]
s_waitcnt lgkmcnt(14)
MF a[64:67],a[128:131],v[96:99],a[64:67]
MF a[96:99],a[128:131],v[104:107],a[96:99]
s_waitcnt lgkmcnt(12)
MF a[68:71],a[132:135],v[96:99],a[68:71]
MF a[100:103],a[132:135],v[104:107],a[100:103]
s_waitcnt lgkmcnt(10)
MF a[72:75],a[136:139],v[96:99],a[72:75]
MF a[104:107],a[136:139],v[104:107],a[104:107]
s_waitcnt lgkmcnt(8)
MF a[76:79],a[140:143],v[96:99],a[76:79]
MF a[108:111],a[140:143],v[104:107],a[108:111]
s_waitcnt lgkmcnt(6)
MF a[80:83],a[144:147],v[96:99],a[80:83]
MF a[112:115],a[144:147],v[104:107],a[112:115]
s_waitcnt lgkmcnt(4)
MF a[84:87],a[148:151],v[96:99],a[84:87]
MF a[116:119],a[148:151],v[104:107],a[116:119]
s_waitcnt lgkmcnt(2)
MF a[88:91],a[152:155],v[96:99],a[88:91]
MF a[120:123],a[152:155],v[104:107],a[120:123]
s_waitcnt lgkmcnt(0)
MF a[92:95],a[156:159],v[96:99],a[92:95]
MF a[124:127],a[156:159],v[104:107],a[124:127]
s_mov_b32 s36,s22
s_and_b32 s37,s23,0xffff
s_mov_b32 s38,7864320
s_mov_b32 s39,0x20000
s_mov_b32 s40,s26
s_and_b32 s41,s27,0xffff
s_mov_b32 s42,2621440
s_mov_b32 s43,0x20000
s_mov_b32 s52,s28
s_and_b32 s53,s29,0xffff
s_mov_b32 s54,2621440
s_mov_b32 s55,0x20000
s_mul_i32 s76,s69,5
s_add_u32 s76,s76,s70
s_mul_i32 s75,s76,12
s_add_u32 s75,s75,s60
s_lshl_b32 s75,s75,13
v_and_b32 v96,15,v1
v_mul_u32_u24 v84,1280,v96
v_lshrrev_b32 v96,4,v1
v_lshl_add_u32 v85,v96,4,v84
v_lshl_add_u32 v84,v96,3,v84
s_mul_i32 s78,s77,20480
s_lshl_b32 s101,s70,8
s_add_u32 s78,s78,s101
s_cmp_eq_u32 s71,1
s_cbranch_scc1 LT1_%=
s_cmp_eq_u32 s71,2
s_cbranch_scc1 LT2_%=
s_add_u32 s96,s78,0
buffer_load_dwordx2 v[64:65],v84,s[40:43],s96 offen
buffer_load_dwordx2 v[66:67],v84,s[40:43],s96 offen offset:32
s_add_u32 s96,s78,0
buffer_load_dwordx2 v[68:69],v84,s[40:43],s96 offen offset:192
buffer_load_dwordx2 v[70:71],v84,s[40:43],s96 offen offset:224
s_add_u32 s96,s78,20480
buffer_load_dwordx2 v[72:73],v84,s[40:43],s96 offen offset:128
buffer_load_dwordx2 v[74:75],v84,s[40:43],s96 offen offset:160
s_add_u32 s97,s75,0
s_nop 3
AR v40,a72
AR v41,a73
AR v42,a74
AR v43,a75
AR v44,a76
AR v45,a77
AR v46,a78
AR v47,a79
CP v56,v40,v41
CP v57,v42,v43
CP v58,v44,v45
CP v59,v46,v47
buffer_store_dwordx4 v[56:59],v10,s[36:39],s97 offen offset:1024 sc1
AR v48,a80
AR v49,a81
AR v50,a82
AR v51,a83
AR v52,a84
AR v53,a85
AR v54,a86
AR v55,a87
CP v60,v48,v49
CP v61,v50,v51
CP v62,v52,v53
CP v63,v54,v55
buffer_store_dwordx4 v[60:63],v10,s[36:39],s97 offen offset:2048 sc1
AR v40,a96
AR v41,a97
AR v42,a98
AR v43,a99
AR v44,a100
AR v45,a101
AR v46,a102
AR v47,a103
CP v56,v40,v41
CP v57,v42,v43
CP v58,v44,v45
CP v59,v46,v47
s_add_u32 s100,s97,4096
buffer_store_dwordx4 v[56:59],v10,s[36:39],s100 offen sc1
AR v48,a104
AR v49,a105
AR v50,a106
AR v51,a107
AR v52,a108
AR v53,a109
AR v54,a110
AR v55,a111
CP v60,v48,v49
CP v61,v50,v51
CP v62,v52,v53
CP v63,v54,v55
s_add_u32 s100,s97,4096
buffer_store_dwordx4 v[60:63],v10,s[36:39],s100 offen offset:1024 sc1
AR v40,a120
AR v41,a121
AR v42,a122
AR v43,a123
AR v44,a124
AR v45,a125
AR v46,a126
AR v47,a127
CP v56,v40,v41
CP v57,v42,v43
CP v58,v44,v45
CP v59,v46,v47
s_add_u32 s100,s97,4096
buffer_store_dwordx4 v[56:59],v10,s[36:39],s100 offen offset:3072 sc1
s_waitcnt vmcnt(0)
s_barrier
s_branch LX_%=
LT1_%=:
s_add_u32 s96,s78,0
buffer_load_dwordx2 v[64:65],v84,s[40:43],s96 offen offset:64
buffer_load_dwordx2 v[66:67],v84,s[40:43],s96 offen offset:96
s_add_u32 s96,s78,20480
buffer_load_dwordx2 v[68:69],v84,s[40:43],s96 offen
buffer_load_dwordx2 v[70:71],v84,s[40:43],s96 offen offset:32
s_add_u32 s96,s78,20480
buffer_load_dwordx2 v[72:73],v84,s[40:43],s96 offen offset:192
buffer_load_dwordx2 v[74:75],v84,s[40:43],s96 offen offset:224
s_add_u32 s97,s75,32768
s_nop 3
AR v40,a64
AR v41,a65
AR v42,a66
AR v43,a67
AR v44,a68
AR v45,a69
AR v46,a70
AR v47,a71
CP v56,v40,v41
CP v57,v42,v43
CP v58,v44,v45
CP v59,v46,v47
buffer_store_dwordx4 v[56:59],v10,s[36:39],s97 offen sc1
AR v48,a80
AR v49,a81
AR v50,a82
AR v51,a83
AR v52,a84
AR v53,a85
AR v54,a86
AR v55,a87
CP v60,v48,v49
CP v61,v50,v51
CP v62,v52,v53
CP v63,v54,v55
buffer_store_dwordx4 v[60:63],v10,s[36:39],s97 offen offset:2048 sc1
AR v40,a88
AR v41,a89
AR v42,a90
AR v43,a91
AR v44,a92
AR v45,a93
AR v46,a94
AR v47,a95
CP v56,v40,v41
CP v57,v42,v43
CP v58,v44,v45
CP v59,v46,v47
buffer_store_dwordx4 v[56:59],v10,s[36:39],s97 offen offset:3072 sc1
AR v48,a104
AR v49,a105
AR v50,a106
AR v51,a107
AR v52,a108
AR v53,a109
AR v54,a110
AR v55,a111
CP v60,v48,v49
CP v61,v50,v51
CP v62,v52,v53
CP v63,v54,v55
s_add_u32 s100,s97,4096
buffer_store_dwordx4 v[60:63],v10,s[36:39],s100 offen offset:1024 sc1
AR v40,a112
AR v41,a113
AR v42,a114
AR v43,a115
AR v44,a116
AR v45,a117
AR v46,a118
AR v47,a119
CP v56,v40,v41
CP v57,v42,v43
CP v58,v44,v45
CP v59,v46,v47
s_add_u32 s100,s97,4096
buffer_store_dwordx4 v[56:59],v10,s[36:39],s100 offen offset:2048 sc1
s_waitcnt vmcnt(0)
s_barrier
s_branch LX_%=
LT2_%=:
s_add_u32 s96,s78,0
buffer_load_dwordx2 v[64:65],v84,s[40:43],s96 offen offset:128
buffer_load_dwordx2 v[66:67],v84,s[40:43],s96 offen offset:160
s_add_u32 s96,s78,20480
buffer_load_dwordx2 v[68:69],v84,s[40:43],s96 offen offset:64
buffer_load_dwordx2 v[70:71],v84,s[40:43],s96 offen offset:96
s_add_u32 s97,s75,65536
s_nop 3
AR v40,a64
AR v41,a65
AR v42,a66
AR v43,a67
AR v44,a68
AR v45,a69
AR v46,a70
AR v47,a71
CP v56,v40,v41
CP v57,v42,v43
CP v58,v44,v45
CP v59,v46,v47
buffer_store_dwordx4 v[56:59],v10,s[36:39],s97 offen sc1
AR v48,a72
AR v49,a73
AR v50,a74
AR v51,a75
AR v52,a76
AR v53,a77
AR v54,a78
AR v55,a79
CP v60,v48,v49
CP v61,v50,v51
CP v62,v52,v53
CP v63,v54,v55
buffer_store_dwordx4 v[60:63],v10,s[36:39],s97 offen offset:1024 sc1
AR v40,a88
AR v41,a89
AR v42,a90
AR v43,a91
AR v44,a92
AR v45,a93
AR v46,a94
AR v47,a95
CP v56,v40,v41
CP v57,v42,v43
CP v58,v44,v45
CP v59,v46,v47
buffer_store_dwordx4 v[56:59],v10,s[36:39],s97 offen offset:3072 sc1
AR v48,a96
AR v49,a97
AR v50,a98
AR v51,a99
AR v52,a100
AR v53,a101
AR v54,a102
AR v55,a103
CP v60,v48,v49
CP v61,v50,v51
CP v62,v52,v53
CP v63,v54,v55
s_add_u32 s100,s97,4096
buffer_store_dwordx4 v[60:63],v10,s[36:39],s100 offen sc1
AR v40,a112
AR v41,a113
AR v42,a114
AR v43,a115
AR v44,a116
AR v45,a117
AR v46,a118
AR v47,a119
CP v56,v40,v41
CP v57,v42,v43
CP v58,v44,v45
CP v59,v46,v47
s_add_u32 s100,s97,4096
buffer_store_dwordx4 v[56:59],v10,s[36:39],s100 offen offset:2048 sc1
AR v48,a120
AR v49,a121
AR v50,a122
AR v51,a123
AR v52,a124
AR v53,a125
AR v54,a126
AR v55,a127
CP v60,v48,v49
CP v61,v50,v51
CP v62,v52,v53
CP v63,v54,v55
s_add_u32 s100,s97,4096
buffer_store_dwordx4 v[60:63],v10,s[36:39],s100 offen offset:3072 sc1
s_waitcnt vmcnt(0)
s_barrier
LX_%=:
s_cmp_lg_u32 s60,0
s_cbranch_scc1 LPA_%=
s_lshl_b32 s100,s76,6
s_add_u32 s100,s100,2048
v_mov_b32 v244,s100
v_mov_b32 v245,1
s_mov_b64 exec,1
global_atomic_add v244,v245,s[24:25]
s_mov_b64 exec,-1
LPA_%=:
s_lshl_b32 s100,s76,6
s_add_u32 s100,s100,2048
v_mov_b32 v247,s100
global_load_dword v246,v247,s[24:25] sc1
s_waitcnt vmcnt(0)
v_readfirstlane_b32 s100,v246
s_cmp_ge_u32 s100,3
s_cbranch_scc1 LPSOK_%=
s_mov_b32 s96,0
LPS_%=:
s_sleep 2
global_load_dword v246,v247,s[24:25] sc1
s_waitcnt vmcnt(0)
v_readfirstlane_b32 s100,v246
s_add_u32 s96,s96,1
s_cmp_ge_u32 s100,3
s_cbranch_scc1 LPSOK_%=
s_cmp_lt_u32 s96,0x10000
s_cbranch_scc1 LPS_%=
LPSOK_%=:
s_cmp_eq_u32 s71,1
s_cbranch_scc1 LU1_%=
s_cmp_eq_u32 s71,2
s_cbranch_scc1 LU2_%=
s_add_u32 s100,s75,32768
buffer_load_dwordx4 v[16:19],v10,s[36:39],s100 offen sc1
s_add_u32 s100,s75,65536
buffer_load_dwordx4 v[28:31],v10,s[36:39],s100 offen sc1
s_add_u32 s100,s75,35840
buffer_load_dwordx4 v[20:23],v10,s[36:39],s100 offen sc1
s_add_u32 s100,s75,68608
buffer_load_dwordx4 v[32:35],v10,s[36:39],s100 offen sc1
s_add_u32 s100,s75,38912
buffer_load_dwordx4 v[24:27],v10,s[36:39],s100 offen sc1
s_add_u32 s100,s75,71680
buffer_load_dwordx4 v[36:39],v10,s[36:39],s100 offen sc1
AR v76,a64
AR v77,a65
AR v78,a66
AR v79,a67
AR v80,a68
AR v81,a69
AR v82,a70
AR v83,a71
v_lshlrev_b32 v96,16,v64
v_and_b32 v97,0xffff0000,v64
v_lshlrev_b32 v98,16,v65
v_and_b32 v99,0xffff0000,v65
v_add_f32 v76,v76,v96
v_add_f32 v77,v77,v97
v_add_f32 v78,v78,v98
v_add_f32 v79,v79,v99
v_lshlrev_b32 v96,16,v66
v_and_b32 v97,0xffff0000,v66
v_lshlrev_b32 v98,16,v67
v_and_b32 v99,0xffff0000,v67
v_add_f32 v80,v80,v96
v_add_f32 v81,v81,v97
v_add_f32 v82,v82,v98
v_add_f32 v83,v83,v99
s_waitcnt vmcnt(5)
v_lshlrev_b32 v96,16,v16
v_and_b32 v97,0xffff0000,v16
v_lshlrev_b32 v98,16,v17
v_and_b32 v99,0xffff0000,v17
v_add_f32 v76,v76,v96
v_add_f32 v77,v77,v97
v_add_f32 v78,v78,v98
v_add_f32 v79,v79,v99
v_lshlrev_b32 v96,16,v18
v_and_b32 v97,0xffff0000,v18
v_lshlrev_b32 v98,16,v19
v_and_b32 v99,0xffff0000,v19
v_add_f32 v80,v80,v96
v_add_f32 v81,v81,v97
v_add_f32 v82,v82,v98
v_add_f32 v83,v83,v99
s_waitcnt vmcnt(4)
v_lshlrev_b32 v96,16,v28
v_and_b32 v97,0xffff0000,v28
v_lshlrev_b32 v98,16,v29
v_and_b32 v99,0xffff0000,v29
v_add_f32 v76,v76,v96
v_add_f32 v77,v77,v97
v_add_f32 v78,v78,v98
v_add_f32 v79,v79,v99
v_lshlrev_b32 v96,16,v30
v_and_b32 v97,0xffff0000,v30
v_lshlrev_b32 v98,16,v31
v_and_b32 v99,0xffff0000,v31
v_add_f32 v80,v80,v96
v_add_f32 v81,v81,v97
v_add_f32 v82,v82,v98
v_add_f32 v83,v83,v99
CP v88,v76,v77
CP v89,v78,v79
CP v90,v80,v81
CP v91,v82,v83
s_nop 0
v_permlane32_swap_b32 v88,v90
v_permlane32_swap_b32 v89,v91
s_nop 0
v_permlane16_swap_b32 v88,v90
v_permlane16_swap_b32 v89,v91
s_add_u32 s96,s78,0
buffer_store_dwordx4 v[88:91],v85,s[52:55],s96 offen
AR v76,a88
AR v77,a89
AR v78,a90
AR v79,a91
AR v80,a92
AR v81,a93
AR v82,a94
AR v83,a95
v_lshlrev_b32 v96,16,v68
v_and_b32 v97,0xffff0000,v68
v_lshlrev_b32 v98,16,v69
v_and_b32 v99,0xffff0000,v69
v_add_f32 v76,v76,v96
v_add_f32 v77,v77,v97
v_add_f32 v78,v78,v98
v_add_f32 v79,v79,v99
v_lshlrev_b32 v96,16,v70
v_and_b32 v97,0xffff0000,v70
v_lshlrev_b32 v98,16,v71
v_and_b32 v99,0xffff0000,v71
v_add_f32 v80,v80,v96
v_add_f32 v81,v81,v97
v_add_f32 v82,v82,v98
v_add_f32 v83,v83,v99
s_waitcnt vmcnt(3)
v_lshlrev_b32 v96,16,v20
v_and_b32 v97,0xffff0000,v20
v_lshlrev_b32 v98,16,v21
v_and_b32 v99,0xffff0000,v21
v_add_f32 v76,v76,v96
v_add_f32 v77,v77,v97
v_add_f32 v78,v78,v98
v_add_f32 v79,v79,v99
v_lshlrev_b32 v96,16,v22
v_and_b32 v97,0xffff0000,v22
v_lshlrev_b32 v98,16,v23
v_and_b32 v99,0xffff0000,v23
v_add_f32 v80,v80,v96
v_add_f32 v81,v81,v97
v_add_f32 v82,v82,v98
v_add_f32 v83,v83,v99
s_waitcnt vmcnt(2)
v_lshlrev_b32 v96,16,v32
v_and_b32 v97,0xffff0000,v32
v_lshlrev_b32 v98,16,v33
v_and_b32 v99,0xffff0000,v33
v_add_f32 v76,v76,v96
v_add_f32 v77,v77,v97
v_add_f32 v78,v78,v98
v_add_f32 v79,v79,v99
v_lshlrev_b32 v96,16,v34
v_and_b32 v97,0xffff0000,v34
v_lshlrev_b32 v98,16,v35
v_and_b32 v99,0xffff0000,v35
v_add_f32 v80,v80,v96
v_add_f32 v81,v81,v97
v_add_f32 v82,v82,v98
v_add_f32 v83,v83,v99
CP v92,v76,v77
CP v93,v78,v79
CP v94,v80,v81
CP v95,v82,v83
s_nop 0
v_permlane32_swap_b32 v92,v94
v_permlane32_swap_b32 v93,v95
s_nop 0
v_permlane16_swap_b32 v92,v94
v_permlane16_swap_b32 v93,v95
s_add_u32 s96,s78,0
buffer_store_dwordx4 v[92:95],v85,s[52:55],s96 offen offset:192
AR v76,a112
AR v77,a113
AR v78,a114
AR v79,a115
AR v80,a116
AR v81,a117
AR v82,a118
AR v83,a119
v_lshlrev_b32 v96,16,v72
v_and_b32 v97,0xffff0000,v72
v_lshlrev_b32 v98,16,v73
v_and_b32 v99,0xffff0000,v73
v_add_f32 v76,v76,v96
v_add_f32 v77,v77,v97
v_add_f32 v78,v78,v98
v_add_f32 v79,v79,v99
v_lshlrev_b32 v96,16,v74
v_and_b32 v97,0xffff0000,v74
v_lshlrev_b32 v98,16,v75
v_and_b32 v99,0xffff0000,v75
v_add_f32 v80,v80,v96
v_add_f32 v81,v81,v97
v_add_f32 v82,v82,v98
v_add_f32 v83,v83,v99
s_waitcnt vmcnt(1)
v_lshlrev_b32 v96,16,v24
v_and_b32 v97,0xffff0000,v24
v_lshlrev_b32 v98,16,v25
v_and_b32 v99,0xffff0000,v25
v_add_f32 v76,v76,v96
v_add_f32 v77,v77,v97
v_add_f32 v78,v78,v98
v_add_f32 v79,v79,v99
v_lshlrev_b32 v96,16,v26
v_and_b32 v97,0xffff0000,v26
v_lshlrev_b32 v98,16,v27
v_and_b32 v99,0xffff0000,v27
v_add_f32 v80,v80,v96
v_add_f32 v81,v81,v97
v_add_f32 v82,v82,v98
v_add_f32 v83,v83,v99
s_waitcnt vmcnt(0)
v_lshlrev_b32 v96,16,v36
v_and_b32 v97,0xffff0000,v36
v_lshlrev_b32 v98,16,v37
v_and_b32 v99,0xffff0000,v37
v_add_f32 v76,v76,v96
v_add_f32 v77,v77,v97
v_add_f32 v78,v78,v98
v_add_f32 v79,v79,v99
v_lshlrev_b32 v96,16,v38
v_and_b32 v97,0xffff0000,v38
v_lshlrev_b32 v98,16,v39
v_and_b32 v99,0xffff0000,v39
v_add_f32 v80,v80,v96
v_add_f32 v81,v81,v97
v_add_f32 v82,v82,v98
v_add_f32 v83,v83,v99
CP v88,v76,v77
CP v89,v78,v79
CP v90,v80,v81
CP v91,v82,v83
s_nop 0
v_permlane32_swap_b32 v88,v90
v_permlane32_swap_b32 v89,v91
s_nop 0
v_permlane16_swap_b32 v88,v90
v_permlane16_swap_b32 v89,v91
s_add_u32 s96,s78,20480
buffer_store_dwordx4 v[88:91],v85,s[52:55],s96 offen offset:128
s_branch LFIN_%=
LU1_%=:
s_add_u32 s100,s75,1024
buffer_load_dwordx4 v[16:19],v10,s[36:39],s100 offen sc1
s_add_u32 s100,s75,66560
buffer_load_dwordx4 v[28:31],v10,s[36:39],s100 offen sc1
s_add_u32 s100,s75,4096
buffer_load_dwordx4 v[20:23],v10,s[36:39],s100 offen sc1
s_add_u32 s100,s75,69632
buffer_load_dwordx4 v[32:35],v10,s[36:39],s100 offen sc1
s_add_u32 s100,s75,7168
buffer_load_dwordx4 v[24:27],v10,s[36:39],s100 offen sc1
s_add_u32 s100,s75,72704
buffer_load_dwordx4 v[36:39],v10,s[36:39],s100 offen sc1
AR v76,a72
AR v77,a73
AR v78,a74
AR v79,a75
AR v80,a76
AR v81,a77
AR v82,a78
AR v83,a79
v_lshlrev_b32 v96,16,v64
v_and_b32 v97,0xffff0000,v64
v_lshlrev_b32 v98,16,v65
v_and_b32 v99,0xffff0000,v65
v_add_f32 v76,v76,v96
v_add_f32 v77,v77,v97
v_add_f32 v78,v78,v98
v_add_f32 v79,v79,v99
v_lshlrev_b32 v96,16,v66
v_and_b32 v97,0xffff0000,v66
v_lshlrev_b32 v98,16,v67
v_and_b32 v99,0xffff0000,v67
v_add_f32 v80,v80,v96
v_add_f32 v81,v81,v97
v_add_f32 v82,v82,v98
v_add_f32 v83,v83,v99
s_waitcnt vmcnt(5)
v_lshlrev_b32 v96,16,v16
v_and_b32 v97,0xffff0000,v16
v_lshlrev_b32 v98,16,v17
v_and_b32 v99,0xffff0000,v17
v_add_f32 v76,v76,v96
v_add_f32 v77,v77,v97
v_add_f32 v78,v78,v98
v_add_f32 v79,v79,v99
v_lshlrev_b32 v96,16,v18
v_and_b32 v97,0xffff0000,v18
v_lshlrev_b32 v98,16,v19
v_and_b32 v99,0xffff0000,v19
v_add_f32 v80,v80,v96
v_add_f32 v81,v81,v97
v_add_f32 v82,v82,v98
v_add_f32 v83,v83,v99
s_waitcnt vmcnt(4)
v_lshlrev_b32 v96,16,v28
v_and_b32 v97,0xffff0000,v28
v_lshlrev_b32 v98,16,v29
v_and_b32 v99,0xffff0000,v29
v_add_f32 v76,v76,v96
v_add_f32 v77,v77,v97
v_add_f32 v78,v78,v98
v_add_f32 v79,v79,v99
v_lshlrev_b32 v96,16,v30
v_and_b32 v97,0xffff0000,v30
v_lshlrev_b32 v98,16,v31
v_and_b32 v99,0xffff0000,v31
v_add_f32 v80,v80,v96
v_add_f32 v81,v81,v97
v_add_f32 v82,v82,v98
v_add_f32 v83,v83,v99
CP v88,v76,v77
CP v89,v78,v79
CP v90,v80,v81
CP v91,v82,v83
s_nop 0
v_permlane32_swap_b32 v88,v90
v_permlane32_swap_b32 v89,v91
s_nop 0
v_permlane16_swap_b32 v88,v90
v_permlane16_swap_b32 v89,v91
s_add_u32 s96,s78,0
buffer_store_dwordx4 v[88:91],v85,s[52:55],s96 offen offset:64
AR v76,a96
AR v77,a97
AR v78,a98
AR v79,a99
AR v80,a100
AR v81,a101
AR v82,a102
AR v83,a103
v_lshlrev_b32 v96,16,v68
v_and_b32 v97,0xffff0000,v68
v_lshlrev_b32 v98,16,v69
v_and_b32 v99,0xffff0000,v69
v_add_f32 v76,v76,v96
v_add_f32 v77,v77,v97
v_add_f32 v78,v78,v98
v_add_f32 v79,v79,v99
v_lshlrev_b32 v96,16,v70
v_and_b32 v97,0xffff0000,v70
v_lshlrev_b32 v98,16,v71
v_and_b32 v99,0xffff0000,v71
v_add_f32 v80,v80,v96
v_add_f32 v81,v81,v97
v_add_f32 v82,v82,v98
v_add_f32 v83,v83,v99
s_waitcnt vmcnt(3)
v_lshlrev_b32 v96,16,v20
v_and_b32 v97,0xffff0000,v20
v_lshlrev_b32 v98,16,v21
v_and_b32 v99,0xffff0000,v21
v_add_f32 v76,v76,v96
v_add_f32 v77,v77,v97
v_add_f32 v78,v78,v98
v_add_f32 v79,v79,v99
v_lshlrev_b32 v96,16,v22
v_and_b32 v97,0xffff0000,v22
v_lshlrev_b32 v98,16,v23
v_and_b32 v99,0xffff0000,v23
v_add_f32 v80,v80,v96
v_add_f32 v81,v81,v97
v_add_f32 v82,v82,v98
v_add_f32 v83,v83,v99
s_waitcnt vmcnt(2)
v_lshlrev_b32 v96,16,v32
v_and_b32 v97,0xffff0000,v32
v_lshlrev_b32 v98,16,v33
v_and_b32 v99,0xffff0000,v33
v_add_f32 v76,v76,v96
v_add_f32 v77,v77,v97
v_add_f32 v78,v78,v98
v_add_f32 v79,v79,v99
v_lshlrev_b32 v96,16,v34
v_and_b32 v97,0xffff0000,v34
v_lshlrev_b32 v98,16,v35
v_and_b32 v99,0xffff0000,v35
v_add_f32 v80,v80,v96
v_add_f32 v81,v81,v97
v_add_f32 v82,v82,v98
v_add_f32 v83,v83,v99
CP v92,v76,v77
CP v93,v78,v79
CP v94,v80,v81
CP v95,v82,v83
s_nop 0
v_permlane32_swap_b32 v92,v94
v_permlane32_swap_b32 v93,v95
s_nop 0
v_permlane16_swap_b32 v92,v94
v_permlane16_swap_b32 v93,v95
s_add_u32 s96,s78,20480
buffer_store_dwordx4 v[92:95],v85,s[52:55],s96 offen
AR v76,a120
AR v77,a121
AR v78,a122
AR v79,a123
AR v80,a124
AR v81,a125
AR v82,a126
AR v83,a127
v_lshlrev_b32 v96,16,v72
v_and_b32 v97,0xffff0000,v72
v_lshlrev_b32 v98,16,v73
v_and_b32 v99,0xffff0000,v73
v_add_f32 v76,v76,v96
v_add_f32 v77,v77,v97
v_add_f32 v78,v78,v98
v_add_f32 v79,v79,v99
v_lshlrev_b32 v96,16,v74
v_and_b32 v97,0xffff0000,v74
v_lshlrev_b32 v98,16,v75
v_and_b32 v99,0xffff0000,v75
v_add_f32 v80,v80,v96
v_add_f32 v81,v81,v97
v_add_f32 v82,v82,v98
v_add_f32 v83,v83,v99
s_waitcnt vmcnt(1)
v_lshlrev_b32 v96,16,v24
v_and_b32 v97,0xffff0000,v24
v_lshlrev_b32 v98,16,v25
v_and_b32 v99,0xffff0000,v25
v_add_f32 v76,v76,v96
v_add_f32 v77,v77,v97
v_add_f32 v78,v78,v98
v_add_f32 v79,v79,v99
v_lshlrev_b32 v96,16,v26
v_and_b32 v97,0xffff0000,v26
v_lshlrev_b32 v98,16,v27
v_and_b32 v99,0xffff0000,v27
v_add_f32 v80,v80,v96
v_add_f32 v81,v81,v97
v_add_f32 v82,v82,v98
v_add_f32 v83,v83,v99
s_waitcnt vmcnt(0)
v_lshlrev_b32 v96,16,v36
v_and_b32 v97,0xffff0000,v36
v_lshlrev_b32 v98,16,v37
v_and_b32 v99,0xffff0000,v37
v_add_f32 v76,v76,v96
v_add_f32 v77,v77,v97
v_add_f32 v78,v78,v98
v_add_f32 v79,v79,v99
v_lshlrev_b32 v96,16,v38
v_and_b32 v97,0xffff0000,v38
v_lshlrev_b32 v98,16,v39
v_and_b32 v99,0xffff0000,v39
v_add_f32 v80,v80,v96
v_add_f32 v81,v81,v97
v_add_f32 v82,v82,v98
v_add_f32 v83,v83,v99
CP v88,v76,v77
CP v89,v78,v79
CP v90,v80,v81
CP v91,v82,v83
s_nop 0
v_permlane32_swap_b32 v88,v90
v_permlane32_swap_b32 v89,v91
s_nop 0
v_permlane16_swap_b32 v88,v90
v_permlane16_swap_b32 v89,v91
s_add_u32 s96,s78,20480
buffer_store_dwordx4 v[88:91],v85,s[52:55],s96 offen offset:192
s_branch LFIN_%=
LU2_%=:
s_add_u32 s100,s75,2048
buffer_load_dwordx4 v[16:19],v10,s[36:39],s100 offen sc1
s_add_u32 s100,s75,34816
buffer_load_dwordx4 v[28:31],v10,s[36:39],s100 offen sc1
s_add_u32 s100,s75,5120
buffer_load_dwordx4 v[20:23],v10,s[36:39],s100 offen sc1
s_add_u32 s100,s75,37888
buffer_load_dwordx4 v[32:35],v10,s[36:39],s100 offen sc1
AR v76,a80
AR v77,a81
AR v78,a82
AR v79,a83
AR v80,a84
AR v81,a85
AR v82,a86
AR v83,a87
v_lshlrev_b32 v96,16,v64
v_and_b32 v97,0xffff0000,v64
v_lshlrev_b32 v98,16,v65
v_and_b32 v99,0xffff0000,v65
v_add_f32 v76,v76,v96
v_add_f32 v77,v77,v97
v_add_f32 v78,v78,v98
v_add_f32 v79,v79,v99
v_lshlrev_b32 v96,16,v66
v_and_b32 v97,0xffff0000,v66
v_lshlrev_b32 v98,16,v67
v_and_b32 v99,0xffff0000,v67
v_add_f32 v80,v80,v96
v_add_f32 v81,v81,v97
v_add_f32 v82,v82,v98
v_add_f32 v83,v83,v99
s_waitcnt vmcnt(3)
v_lshlrev_b32 v96,16,v16
v_and_b32 v97,0xffff0000,v16
v_lshlrev_b32 v98,16,v17
v_and_b32 v99,0xffff0000,v17
v_add_f32 v76,v76,v96
v_add_f32 v77,v77,v97
v_add_f32 v78,v78,v98
v_add_f32 v79,v79,v99
v_lshlrev_b32 v96,16,v18
v_and_b32 v97,0xffff0000,v18
v_lshlrev_b32 v98,16,v19
v_and_b32 v99,0xffff0000,v19
v_add_f32 v80,v80,v96
v_add_f32 v81,v81,v97
v_add_f32 v82,v82,v98
v_add_f32 v83,v83,v99
s_waitcnt vmcnt(2)
v_lshlrev_b32 v96,16,v28
v_and_b32 v97,0xffff0000,v28
v_lshlrev_b32 v98,16,v29
v_and_b32 v99,0xffff0000,v29
v_add_f32 v76,v76,v96
v_add_f32 v77,v77,v97
v_add_f32 v78,v78,v98
v_add_f32 v79,v79,v99
v_lshlrev_b32 v96,16,v30
v_and_b32 v97,0xffff0000,v30
v_lshlrev_b32 v98,16,v31
v_and_b32 v99,0xffff0000,v31
v_add_f32 v80,v80,v96
v_add_f32 v81,v81,v97
v_add_f32 v82,v82,v98
v_add_f32 v83,v83,v99
CP v88,v76,v77
CP v89,v78,v79
CP v90,v80,v81
CP v91,v82,v83
s_nop 0
v_permlane32_swap_b32 v88,v90
v_permlane32_swap_b32 v89,v91
s_nop 0
v_permlane16_swap_b32 v88,v90
v_permlane16_swap_b32 v89,v91
s_add_u32 s96,s78,0
buffer_store_dwordx4 v[88:91],v85,s[52:55],s96 offen offset:128
AR v76,a104
AR v77,a105
AR v78,a106
AR v79,a107
AR v80,a108
AR v81,a109
AR v82,a110
AR v83,a111
v_lshlrev_b32 v96,16,v68
v_and_b32 v97,0xffff0000,v68
v_lshlrev_b32 v98,16,v69
v_and_b32 v99,0xffff0000,v69
v_add_f32 v76,v76,v96
v_add_f32 v77,v77,v97
v_add_f32 v78,v78,v98
v_add_f32 v79,v79,v99
v_lshlrev_b32 v96,16,v70
v_and_b32 v97,0xffff0000,v70
v_lshlrev_b32 v98,16,v71
v_and_b32 v99,0xffff0000,v71
v_add_f32 v80,v80,v96
v_add_f32 v81,v81,v97
v_add_f32 v82,v82,v98
v_add_f32 v83,v83,v99
s_waitcnt vmcnt(1)
v_lshlrev_b32 v96,16,v20
v_and_b32 v97,0xffff0000,v20
v_lshlrev_b32 v98,16,v21
v_and_b32 v99,0xffff0000,v21
v_add_f32 v76,v76,v96
v_add_f32 v77,v77,v97
v_add_f32 v78,v78,v98
v_add_f32 v79,v79,v99
v_lshlrev_b32 v96,16,v22
v_and_b32 v97,0xffff0000,v22
v_lshlrev_b32 v98,16,v23
v_and_b32 v99,0xffff0000,v23
v_add_f32 v80,v80,v96
v_add_f32 v81,v81,v97
v_add_f32 v82,v82,v98
v_add_f32 v83,v83,v99
s_waitcnt vmcnt(0)
v_lshlrev_b32 v96,16,v32
v_and_b32 v97,0xffff0000,v32
v_lshlrev_b32 v98,16,v33
v_and_b32 v99,0xffff0000,v33
v_add_f32 v76,v76,v96
v_add_f32 v77,v77,v97
v_add_f32 v78,v78,v98
v_add_f32 v79,v79,v99
v_lshlrev_b32 v96,16,v34
v_and_b32 v97,0xffff0000,v34
v_lshlrev_b32 v98,16,v35
v_and_b32 v99,0xffff0000,v35
v_add_f32 v80,v80,v96
v_add_f32 v81,v81,v97
v_add_f32 v82,v82,v98
v_add_f32 v83,v83,v99
CP v92,v76,v77
CP v93,v78,v79
CP v94,v80,v81
CP v95,v82,v83
s_nop 0
v_permlane32_swap_b32 v92,v94
v_permlane32_swap_b32 v93,v95
s_nop 0
v_permlane16_swap_b32 v92,v94
v_permlane16_swap_b32 v93,v95
s_add_u32 s96,s78,20480
buffer_store_dwordx4 v[92:95],v85,s[52:55],s96 offen offset:64
)A"
R"A(s_branch LFIN_%=
LFIN_%=:
s_endpgm
LK3END:
.purgem MF
.purgem AR
.purgem CP
)A"
    :: "{s[4:5]}"(ka), "{s6}"(blockIdx.x), "{v0}"(threadIdx.x) : "memory", "vcc", "s8", "s9", "s10", "s11", "s12", "s13", "s14", "s15", "s16", "s17", "s18", "s19", "s20", "s21", "s22", "s23", "s24", "s25", "s26", "s27", "s28", "s29", "s30", "s31", "s32", "s33", "s34", "s35", "s36", "s37", "s38", "s39", "s40", "s41", "s42", "s43", "s44", "s45", "s46", "s47", "s48", "s49", "s50", "s51", "s52", "s53", "s54", "s55", "s56", "s57", "s58", "s59", "s60", "s61", "s62", "s63", "s64", "s65", "s66", "s67", "s68", "s69", "s70", "s71", "s72", "s73", "s74", "s75", "s76", "s77", "s78", "s79", "s80", "s81", "s82", "s83", "s84", "s85", "s86", "s87", "s88", "s89", "s90", "s91", "s92", "s93", "s94", "s95", "s96", "s97", "s98", "s99", "s100", "s101", "v1", "v2", "v3", "v4", "v5", "v6", "v7", "v8", "v9", "v10", "v11", "v12", "v13", "v14", "v15", "v16", "v17", "v18", "v19", "v20", "v21", "v22", "v23", "v24", "v25", "v26", "v27", "v28", "v29", "v30", "v31", "v32", "v33", "v34", "v35", "v36", "v37", "v38", "v39", "v40", "v41", "v42", "v43", "v44", "v45", "v46", "v47", "v48", "v49", "v50", "v51", "v52", "v53", "v54", "v55", "v56", "v57", "v58", "v59", "v60", "v61", "v62", "v63", "v64", "v65", "v66", "v67", "v68", "v69", "v70", "v71", "v72", "v73", "v74", "v75", "v76", "v77", "v78", "v79", "v80", "v81", "v82", "v83", "v84", "v85", "v86", "v87", "v88", "v89", "v90", "v91", "v92", "v93", "v94", "v95", "v96", "v97", "v98", "v99", "v100", "v101", "v102", "v103", "v104", "v105", "v106", "v107", "v108", "v109", "v110", "v111", "v112", "v113", "v114", "v115", "v116", "v117", "v118", "v119", "v120", "v121", "v122", "v123", "v124", "v125", "v126", "v127", "v128", "v129", "v130", "v131", "v132", "v133", "v134", "v135", "v136", "v137", "v138", "v139", "v140", "v141", "v142", "v143", "v144", "v145", "v146", "v147", "v148", "v149", "v150", "v151", "v152", "v153", "v154", "v155", "v156", "v157", "v158", "v159", "v160", "v161", "v162", "v163", "v164", "v165", "v166", "v167", "v168", "v169", "v170", "v171", "v172", "v173", "v174", "v175", "v176", "v177", "v178", "v179", "v180", "v181", "v182", "v183", "v184", "v185", "v186", "v187", "v188", "v189", "v190", "v191", "v192", "v193", "v194", "v195", "v196", "v197", "v198", "v199", "v200", "v201", "v202", "v203", "v204", "v205", "v206", "v207", "v208", "v209", "v210", "v211", "v212", "v213", "v214", "v215", "v216", "v217", "v218", "v219", "v220", "v221", "v222", "v223", "v224", "v225", "v226", "v227", "v228", "v229", "v230", "v231", "v232", "v233", "v234", "v235", "v236", "v237", "v238", "v239", "v240", "v241", "v242", "v243", "v244", "v245", "v246", "v247", "v248", "v249", "v250", "v251", "v252", "v253", "v254", "v255", "a0", "a1", "a2", "a3", "a4", "a5", "a6", "a7", "a8", "a9", "a10", "a11", "a12", "a13", "a14", "a15", "a16", "a17", "a18", "a19", "a20", "a21", "a22", "a23", "a24", "a25", "a26", "a27", "a28", "a29", "a30", "a31", "a32", "a33", "a34", "a35", "a36", "a37", "a38", "a39", "a40", "a41", "a42", "a43", "a44", "a45", "a46", "a47", "a48", "a49", "a50", "a51", "a52", "a53", "a54", "a55", "a56", "a57", "a58", "a59", "a60", "a61", "a62", "a63", "a64", "a65", "a66", "a67", "a68", "a69", "a70", "a71", "a72", "a73", "a74", "a75", "a76", "a77", "a78", "a79", "a80", "a81", "a82", "a83", "a84", "a85", "a86", "a87", "a88", "a89", "a90", "a91", "a92", "a93", "a94", "a95", "a96", "a97", "a98", "a99", "a100", "a101", "a102", "a103", "a104", "a105", "a106", "a107", "a108", "a109", "a110", "a111", "a112", "a113", "a114", "a115", "a116", "a117", "a118", "a119", "a120", "a121", "a122", "a123", "a124", "a125", "a126", "a127", "a128", "a129", "a130", "a131", "a132", "a133", "a134", "a135", "a136", "a137", "a138", "a139", "a140", "a141", "a142", "a143", "a144", "a145", "a146", "a147", "a148", "a149", "a150", "a151", "a152", "a153", "a154", "a155", "a156", "a157", "a158", "a159", "a160", "a161", "a162", "a163", "a164", "a165", "a166", "a167", "a168", "a169", "a170", "a171", "a172", "a173", "a174", "a175", "a176", "a177", "a178", "a179", "a180", "a181", "a182", "a183", "a184", "a185", "a186", "a187", "a188", "a189", "a190", "a191", "a192", "a193", "a194", "a195", "a196", "a197", "a198", "a199", "a200", "a201", "a202", "a203", "a204", "a205", "a206", "a207", "a208", "a209", "a210", "a211", "a212", "a213", "a214", "a215", "a216", "a217", "a218", "a219", "a220", "a221", "a222", "a223", "a224", "a225", "a226", "a227", "a228", "a229", "a230", "a231", "a232", "a233", "a234", "a235", "a236", "a237", "a238", "a239", "a240", "a241", "a242", "a243", "a244", "a245", "a246", "a247", "a248", "a249", "a250", "a251", "a252", "a253", "a254", "a255");
}

extern "C" void kernel_launch(void* const* d_in, const int* in_sizes, int n_in, void* d_out, int out_size, void* d_ws, size_t ws_size, hipStream_t stream) {
    (void)in_sizes; (void)n_in; (void)out_size; (void)ws_size;
    char* ws = (char*)d_ws;
    (void)hipFuncSetAttribute((const void*)k1, hipFuncAttributeMaxDynamicSharedMemorySize, 144384);
    (void)hipFuncSetAttribute((const void*)k2, hipFuncAttributeMaxDynamicSharedMemorySize, 148480);
    (void)hipFuncSetAttribute((const void*)k3, hipFuncAttributeMaxDynamicSharedMemorySize, 163840);
    { K1Args a{d_in[1], d_in[3], d_in[4], ws + WS_K, ws + WS_V, d_in[0], ws + WS_XQ, ws + WS_FLAG, d_in[8], d_in[5], d_in[10], d_in[2], d_in[7], ws + WS_ST};
      k1<<<256, 256, 144384, stream>>>(a); }
    { K2Args a{ws + WS_XQ, d_in[2], ws + WS_K, ws + WS_V, ws + WS_CY, d_in[7], ws + WS_Z1, ws + WS_Z2, ws + WS_Z2N, ws + WS_FLAG, ws + WS_ST + 16384, d_in[8], d_in[5], d_in[10]};
      k2<<<256, 256, 148480, stream>>>(a); }
    { K3Args a{d_in[8], ws + WS_Z2N, d_in[9], ws + WS_Z1, ws + WS_CY, d_in[5], d_in[10], ws + WS_PART, ws + WS_FLAG, d_in[0], d_out, ws + WS_ST + 32768, ws + WS_SPX};
      k3<<<240, 256, 163840, stream>>>(a); }
}
```
